```python
import torch

def _softmax(x, axis=-1):
    return torch.softmax(x, dim=axis)
def _gelu(x):
    return torch.nn.functional.gelu(x, approximate="tanh")
def _silu(x):
    return torch.nn.functional.silu(x)
def _sigmoid(x):
    return torch.sigmoid(x)
def _layernorm(x, axis=-1, eps=1e-5):
    m = x.mean(dim=axis, keepdim=True)
    v = x.var(dim=axis, keepdim=True, unbiased=False)
    return (x - m) / torch.sqrt(v + eps)
def _scatter_add(tgt, idx, src, axis=0):
    return tgt.clone().index_add_(axis, idx, src)
def _segment_sum(src, seg, n):
    out = torch.zeros((n,) + src.shape[1:], dtype=src.dtype)
    return out.index_add_(0, seg, src)
def _pad(x, pw):
    flat = tuple(v for pair in reversed(pw) for v in pair)
    return torch.nn.functional.pad(x, flat)

kernel_name = 'recurrent_scan__sliding_local_dilated__mixer_token_mlp__prompt'

def setup_inputs():

    _w = (lambda a: (lambda t: t.to(torch.bfloat16) if t.is_floating_point() else t)(torch.as_tensor(a)))
    def _A_setup():
        B, S, D = 1, 2048, 448
        return {
            "x": _w((lambda *s: torch.randn(*s, dtype=torch.bfloat16) * 0.05)(B, S, D)),
            "wqkv": _w((lambda *s: torch.randn(*s, dtype=torch.bfloat16) * 0.2)(D, 3 * D)),
            "wo": _w((lambda *s: torch.randn(*s, dtype=torch.bfloat16) * 0.2)(D, D)),
        }
    _A_in = _A_setup()

    def _B_setup():
        B, S, D, F = 1, 2048, 448, 1792
        return {
            "x": _w((lambda *s: torch.randn(*s, dtype=torch.bfloat16) * 0.05)(B, S, D)),
            "w1": _w((lambda *s: torch.randn(*s, dtype=torch.bfloat16) * 0.8)(S, F) * (S ** -0.5)),
            "b1": _w((lambda *s: torch.randn(*s, dtype=torch.bfloat16) * 0.8)(F) * 0.02),
            "w2": _w((lambda *s: torch.randn(*s, dtype=torch.bfloat16) * 0.1)(F, S) * (F ** -0.5)),
            "b2": _w((lambda *s: torch.randn(*s, dtype=torch.bfloat16) * 0.1)(S) * 0.02),
        }
    _B_in = _B_setup()

    return {
        "A_x": _A_in["x"],
        "A_wqkv": _A_in["wqkv"],
        "A_wo": _A_in["wo"],
        "B_x": _B_in["x"],
        "B_w1": _B_in["w1"],
        "B_b1": _B_in["b1"],
        "B_w2": _B_in["w2"],
        "B_b2": _B_in["b2"],
    }

def reference(A_x, A_wqkv, A_wo, B_x, B_w1, B_b1, B_w2, B_b2):
    def _A_ref(x, wqkv, wo):
        B, S, D = x.shape
        H = 16; Dh = D // H
        W, dil = 8, 2
        qkv = (lambda x, *d: x.permute(*d))((x @ wqkv).reshape(B, S, 3, H, Dh), 2, 0, 3, 1, 4)
        q, k, v = qkv[0], qkv[1], qkv[2]
        scale = Dh ** -0.5
        pos = torch.arange(S)
        off = torch.arange(W) * dil
        idx = pos[:, None] - off[None, :]
        mask = idx >= 0
        idx_c = torch.where(mask, idx, 0).reshape(-1)
        kg = k[:, :, idx_c, :].reshape(B, H, S, W, Dh)
        vg = v[:, :, idx_c, :].reshape(B, H, S, W, Dh)
        logits = torch.einsum("bhsd,bhswd->bhsw", q, kg) * scale
        logits = torch.where(mask[None, None, :, :], logits, -1e9)
        attn = _softmax(logits, axis=-1)
        o = torch.einsum("bhsw,bhswd->bhsd", attn, vg)
        return (lambda x, *d: x.permute(*d))(o, 0, 2, 1, 3).reshape(B, S, D) @ wo

    def _B_ref(x, w1, b1, w2, b2):
        B, S, D = x.shape
        h = _layernorm(x, axis=-1)
        u = torch.einsum("bsd,sf->bdf", h, w1) + b1
        u = _gelu(u)
        y = torch.einsum("bdf,fs->bds", u, w2) + b2
        return (lambda x, *d: x.permute(*d))(y, 0, 2, 1)

    x = A_x
    xn = _layernorm(x, axis=-1)
    s = _A_ref(xn, A_wqkv, A_wo)
    g = _silu(_B_ref(xn, B_w1, B_b1, B_w2, B_b2))
    return x + s * g
```

```cpp
#include <hip/hip_runtime.h>
#include <hip/hip_bf16.h>
#include <stdint.h>

typedef short bf16x8 __attribute__((ext_vector_type(8)));
typedef short s16x4 __attribute__((ext_vector_type(4)));
typedef float f32x4 __attribute__((ext_vector_type(4)));
typedef float f32x16 __attribute__((ext_vector_type(16)));
typedef unsigned u32x4 __attribute__((ext_vector_type(4)));
typedef unsigned u32x2 __attribute__((ext_vector_type(2)));
typedef __bf16 bf16x2_t __attribute__((ext_vector_type(2)));
typedef float f32x2_t __attribute__((ext_vector_type(2)));
#define LDS_AS __attribute__((address_space(3)))
#define DEV __device__ __forceinline__

#define STAMP(i)
#define STAMP_DUMP()

#define SB() __builtin_amdgcn_sched_barrier(0)
#define WAITV(n) asm volatile("s_waitcnt vmcnt(" #n ")" ::: "memory")
#define WAITV1(n, a) asm volatile("s_waitcnt vmcnt(" #n ")" : "+v"(a) :: "memory")
#define WAITV2(n, a, b) asm volatile("s_waitcnt vmcnt(" #n ")" : "+v"(a), "+v"(b) :: "memory")
#define WAITV3(n, a, b, c) asm volatile("s_waitcnt vmcnt(" #n ")" : "+v"(a), "+v"(b), "+v"(c) :: "memory")
#define WAITV4(n, a, b, c, d) asm volatile("s_waitcnt vmcnt(" #n ")" : "+v"(a), "+v"(b), "+v"(c), "+v"(d) :: "memory")
#define WAITL(n) do { asm volatile("" ::: "memory"); __builtin_amdgcn_s_waitcnt(0xc07f | ((n) << 8)); asm volatile("" ::: "memory"); } while (0)
#define BAR() do { WAITL(0); __builtin_amdgcn_s_barrier(); asm volatile("" ::: "memory"); } while (0)
#define BAR0() do { asm volatile("s_barrier" ::: "memory"); } while (0)
#define OPQ(p) asm volatile("" : "+s"(p))
#define PINV(p) asm volatile("" : "+v"(p))
#define PINF(p) asm("" : "+v"(p))
#define PRIO(n) __builtin_amdgcn_s_setprio(n)
#define LD16(dst, voff, sb, imm, bits) asm volatile("global_load_dwordx4 %0, %1, %2 offset:" #imm " " bits : "=&v"(dst) : "v"(voff), "s"(sb) : "memory")
#define LD8(dst, voff, sb, imm, bits) asm volatile("global_load_dwordx2 %0, %1, %2 offset:" #imm " " bits : "=&v"(dst) : "v"(voff), "s"(sb) : "memory")
#define LD4(dst, voff, sb, imm, bits) asm volatile("global_load_dword %0, %1, %2 offset:" #imm " " bits : "=&v"(dst) : "v"(voff), "s"(sb) : "memory")
#define LD2(dst, voff, sb, imm, bits) asm volatile("global_load_ushort %0, %1, %2 offset:" #imm " " bits : "=&v"(dst) : "v"(voff), "s"(sb) : "memory")
#define DMA1(v0, sb, lds, limm, bits) \
  asm volatile("s_add_u32 m0, %2, %3\n\ts_nop 0\n\tglobal_load_lds_dwordx4 %0, %1 " bits :: "v"(v0), "s"(sb), "s"(lds), "n"(limm) : "memory", "scc")
#define DMA2(v0, v1, sb, lds, limm, bits) \
  asm volatile("s_add_u32 m0, %3, %4\n\ts_nop 0\n\tglobal_load_lds_dwordx4 %0, %2 " bits "\n\tglobal_load_lds_dwordx4 %1, %2 offset:1024 " bits \
               :: "v"(v0), "v"(v1), "s"(sb), "s"(lds), "n"(limm) : "memory", "scc")
#define TOUCH(v0, sb, lds) asm volatile("s_mov_b32 m0, %2\n\ts_nop 0\n\tglobal_load_lds_dword %0, %1 sc1" :: "v"(v0), "s"(sb), "s"(lds) : "memory")
#define ST16(voff, sb, imm, v) do { const u32x4 v_ = (v); asm volatile("global_store_dwordx4 %0, %1, %2 offset:" #imm " sc1\n\ts_nop 1" :: "v"(voff), "v"(v_), "s"(sb) : "memory"); } while (0)
#define ST16M(mask, voff, sb, v) do { const u32x4 v_ = (v); \
  asm volatile("s_mov_b32 exec_lo, %3\n\ts_mov_b32 exec_hi, %3\n\tglobal_store_dwordx4 %0, %1, %2 sc1\n\ts_mov_b64 exec, -1\n\ts_nop 0" :: "v"(voff), "v"(v_), "s"(sb), "s"(mask) : "memory"); } while (0)
#define ST16E(mask, voff, sb, v) do { const u32x4 v_ = (v); \
  asm volatile("s_mov_b32 exec_lo, " #mask "\n\ts_mov_b32 exec_hi, " #mask "\n\tglobal_store_dwordx4 %0, %1, %2 sc1\n\ts_mov_b64 exec, -1\n\ts_nop 0" :: "v"(voff), "v"(v_), "s"(sb) : "memory"); } while (0)
#define ST16L56(voff, sb, v) do { const u32x4 v_ = (v); \
  asm volatile("s_mov_b32 exec_hi, 0xffffff\n\tglobal_store_dwordx4 %0, %1, %2 sc1\n\ts_mov_b32 exec_hi, -1\n\ts_nop 0" :: "v"(voff), "v"(v_), "s"(sb) : "memory"); } while (0)
#define ST16L48(voff, sb, imm, v) do { const u32x4 v_ = (v); \
  asm volatile("s_mov_b32 exec_hi, 0xffff\n\tglobal_store_dwordx4 %0, %1, %2 offset:" #imm " sc1\n\ts_mov_b32 exec_hi, -1\n\ts_nop 0" :: "v"(voff), "v"(v_), "s"(sb) : "memory"); } while (0)
#define ST4(voff, sb, v) do { const unsigned v_ = (v); asm volatile("global_store_dword %0, %1, %2 sc1" :: "v"(voff), "v"(v_), "s"(sb) : "memory"); } while (0)
#define ST4X(voff, sb, v) do { const unsigned v_ = (v); asm volatile("s_mov_b64 exec, 1\n\tglobal_store_dword %0, %1, %2 sc1\n\ts_mov_b64 exec, -1" :: "v"(voff), "v"(v_), "s"(sb) : "memory"); } while (0)
#define ST16X(mask, voff, sb, v) do { const u32x4 v_ = {(v), (v), (v), (v)}; asm volatile("s_mov_b64 exec, " #mask "\n\tglobal_store_dwordx4 %0, %1, %2 sc1\n\ts_mov_b64 exec, -1\n\ts_nop 0" :: "v"(voff), "v"(v_), "s"(sb) : "memory"); } while (0)

DEV float lo2f(unsigned v) { return __uint_as_float(v << 16); }
DEV float hi2f(unsigned v) { return __uint_as_float(v & 0xffff0000u); }
DEV unsigned pack2(float a, float b) { f32x2_t v = {a, b}; return __builtin_bit_cast(unsigned, __builtin_convertvector(v, bf16x2_t)); }
DEV float rbf(float a) { return __uint_as_float(pack2(0.f, a)); }
DEV f32x2_t rbf2(f32x2_t a) { return (f32x2_t){rbf(a[0]), rbf(a[1])}; }
DEV float dot2(unsigned a, unsigned b, float c) {
  return __builtin_amdgcn_fdot2_f32_bf16(__builtin_bit_cast(bf16x2_t, a), __builtin_bit_cast(bf16x2_t, b), c, false);
}
DEV bf16x8 cat8(s16x4 a, s16x4 b) { return __builtin_shufflevector(a, b, 0, 1, 2, 3, 4, 5, 6, 7); }
DEV bf16x8 tr8u(unsigned p, int second) {
  return cat8(__builtin_amdgcn_ds_read_tr16_b64_v4i16((LDS_AS s16x4*)p), __builtin_amdgcn_ds_read_tr16_b64_v4i16((LDS_AS s16x4*)(p + second)));
}
template <int CTRL> DEV float dpp_add(float v) {
  return v + __builtin_bit_cast(float, __builtin_amdgcn_update_dpp(0, __builtin_bit_cast(int, v), CTRL, 0xf, 0xf, true));
}
DEV float red32(float v) {
  v = dpp_add<0x140>(dpp_add<0x141>(dpp_add<0x4E>(dpp_add<0xB1>(v))));
  const auto r = __builtin_amdgcn_permlane16_swap(__float_as_uint(v), __float_as_uint(v), false, false);
  return __uint_as_float(r[0]) + __uint_as_float(r[1]);
}
DEV float fmx(float a, float b) { return __builtin_elementwise_maximum(a, b); }
DEV float xmax2(float a, float b) {
  const auto r = __builtin_amdgcn_permlane16_swap(__float_as_uint(a), __float_as_uint(b), false, false);
  const float v = fmx(__uint_as_float(r[0]), __uint_as_float(r[1]));
  const auto s = __builtin_amdgcn_permlane32_swap(__float_as_uint(v), __float_as_uint(v), false, false);
  return fmx(__uint_as_float(s[0]), __uint_as_float(s[1]));
}
DEV float xsum2(float a, float b) {
  const auto r = __builtin_amdgcn_permlane16_swap(__float_as_uint(a), __float_as_uint(b), false, false);
  const float v = __uint_as_float(r[0]) + __uint_as_float(r[1]);
  const auto s = __builtin_amdgcn_permlane32_swap(__float_as_uint(v), __float_as_uint(v), false, false);
  return __uint_as_float(s[0]) + __uint_as_float(s[1]);
}
DEV f32x2_t xbcast2(float v) {
  const auto r = __builtin_amdgcn_permlane16_swap(__float_as_uint(v), __float_as_uint(v), false, false);
  return (f32x2_t){__uint_as_float(r[0]), __uint_as_float(r[1])};
}
#define MFMA32(a, b, c) __builtin_amdgcn_mfma_f32_32x32x16_bf16(a, b, c, 0, 0, 0)
#define MFMA16(a, b, c) __builtin_amdgcn_mfma_f32_16x16x32_bf16(a, b, c, 0, 0, 0)
#define MV16(d, a, b) asm volatile("v_mfma_f32_16x16x32_bf16 %0, %1, %2, %0" : "+v"(d) : "v"(a), "v"(b))
#define MV16Z(d, a, b) asm volatile("v_mfma_f32_16x16x32_bf16 %0, %1, %2, 0" : "=&v"(d) : "v"(a), "v"(b))
#define MV32(d, a, b) asm volatile("v_mfma_f32_32x32x16_bf16 %0, %1, %2, %0" : "+v"(d) : "v"(a), "v"(b))
#define MV32Z(d, a, b) asm volatile("v_mfma_f32_32x32x16_bf16 %0, %1, %2, 0" : "=&v"(d) : "v"(a), "v"(b))
#define NOP(n) asm volatile("s_nop " #n)
DEV int lane_now() { int l; asm volatile("v_mbcnt_lo_u32_b32 %0, -1, 0\n\tv_mbcnt_hi_u32_b32 %0, -1, %0" : "=v"(l)); __builtin_assume(l >= 0 && l < 64); return l; }
#define SEL(h, i) __uint_as_float((m1[i] & __float_as_uint(sc[h][1][i])) | (~m1[i] & __float_as_uint(sc[h][0][i])))
#define Q4(a, i) (f32x4){a[4 * (i)], a[4 * (i) + 1], a[4 * (i) + 2], a[4 * (i) + 3]}

constexpr int kSpinCap = 20000;
template <int SLEEP> DEV void poll1(const char* sb, unsigned voff, unsigned nonce) {
#pragma unroll 1
  for (int spin = 0; spin < kSpinCap; ++spin) {
    __builtin_amdgcn_s_sleep(SLEEP);
    unsigned v;
    asm volatile("global_load_dword %0, %1, %2 sc1\n\ts_waitcnt vmcnt(0)" : "=&v"(v) : "v"(voff), "s"(sb) : "memory");
    if (__ballot(v != nonce) == 0ull) return;
  }
}
DEV unsigned bad4(const u32x4& v, unsigned n) { return (v[0] ^ n) | (v[1] ^ n) | (v[2] ^ n) | (v[3] ^ n); }
template <int SLEEP> DEV void poll4(const char* sb, unsigned voff, unsigned nonce) {
#pragma unroll 1
  for (int spin = 0; spin < kSpinCap; ++spin) {
    __builtin_amdgcn_s_sleep(SLEEP);
    u32x4 a;
    asm volatile("global_load_dwordx4 %0, %1, %2 sc1\n\ts_waitcnt vmcnt(0)" : "=&v"(a) : "v"(voff), "s"(sb) : "memory");
    if (__ballot(bad4(a, nonce) != 0u) == 0ull) return;
  }
}
#define LDS_ARRIVE(old, addr) do { unsigned v_; \
  asm volatile("s_mov_b64 exec, 1\n\tds_add_rtn_u32 %0, %2, %3\n\ts_mov_b64 exec, -1\n\ts_waitcnt lgkmcnt(0)\n\tv_readfirstlane_b32 %1, %0" \
               : "=&v"(v_), "=s"(old) : "v"(addr), "v"(1u) : "memory"); old = __builtin_amdgcn_readfirstlane(old); } while (0)
DEV unsigned frag(int h, int L) {
  return 512 + 1152 * h + L * 16 + (L >= 56 - 8 * h ? 128 : 0);
}

DEV unsigned line7(unsigned l) { return (l + ((l + 4) * 9363 >> 16)) * 128; }

DEV int pair3(int p) { const int g = (p * 11) >> 5, r = p - 3 * g; return g * 1024 + (r ? 256 + 256 * r : 0); }

DEV float ln_inv(float var) {
  const float sd = rbf(__builtin_amdgcn_sqrtf(rbf(rbf(fmaxf(var, 0.f)) + 1e-5f)));
  return __builtin_amdgcn_rcpf(sd);
}
DEV float red64(float v) {
  v = red32(v);
  const auto r = __builtin_amdgcn_permlane32_swap(__float_as_uint(v), __float_as_uint(v), false, false);
  return __uint_as_float(r[0]) + __uint_as_float(r[1]);
}
DEV void ln_1(u32x4& p, f32x2_t (&d)[4], bool act) {
  const float s = dot2(p[0], 0x3f803f80u, dot2(p[1], 0x3f803f80u, 0.f)) + dot2(p[2], 0x3f803f80u, dot2(p[3], 0x3f803f80u, 0.f));
  const float mean = red64(act ? s : 0.f) * (1.0f / 448.0f), mb = rbf(mean), dm = mean - mb;
  f32x2_t q = {0.f, 0.f};
#pragma unroll
  for (int i = 0; i < 4; ++i) { d[i] = (f32x2_t){lo2f(p[i]), hi2f(p[i])} - (f32x2_t){mb, mb}; q = d[i] * d[i] + q; }
  const float inv = ln_inv(red64(act ? q[0] + q[1] : 0.f) * (1.0f / 448.0f) - dm * dm);
#pragma unroll
  for (int i = 0; i < 4; ++i) { d[i] = rbf2(d[i]) * (f32x2_t){inv, inv}; p[i] = pack2(d[i][0], d[i][1]); }
}
template <bool EXACT> DEV void ln_2(u32x4& p, const f32x2_t (&d)[4], bool act) {
  const float s = dot2(p[0], 0x3f803f80u, dot2(p[1], 0x3f803f80u, 0.f)) + dot2(p[2], 0x3f803f80u, dot2(p[3], 0x3f803f80u, 0.f));
  const float q = dot2(p[0], p[0], dot2(p[1], p[1], 0.f)) + dot2(p[2], p[2], dot2(p[3], p[3], 0.f));
  const float mean = red64(act ? s : 0.f) * (1.0f / 448.0f), msq = red64(act ? q : 0.f) * (1.0f / 448.0f);
  const float inv = ln_inv(msq - mean * mean), mb = rbf(mean), c = -mb * inv;
#pragma unroll
  for (int i = 0; i < 4; ++i) {
    const f32x2_t r = EXACT ? rbf2((f32x2_t){lo2f(p[i]), hi2f(p[i])} - (f32x2_t){mb, mb}) * (f32x2_t){inv, inv}
                            : __builtin_elementwise_fma(d[i], (f32x2_t){inv, inv}, (f32x2_t){c, c});
    p[i] = pack2(r[0], r[1]);
  }
}
#define OP4_3(op, d, a, b) asm volatile(op " %0, %4, %8\n\t" op " %1, %5, %9\n\t" op " %2, %6, %10\n\t" op " %3, %7, %11" \
  : "=&v"(d[0]), "=&v"(d[1]), "=&v"(d[2]), "=v"(d[3]) : "v"(a[0]), "v"(a[1]), "v"(a[2]), "v"(a[3]), "v"(b[0]), "v"(b[1]), "v"(b[2]), "v"(b[3]))
#define ACC4(op, d, a) asm volatile(op " %0, %4, %0\n\t" op " %1, %5, %1\n\t" op " %2, %6, %2\n\t" op " %3, %7, %3" \
  : "+v"(d[0]), "+v"(d[1]), "+v"(d[2]), "+v"(d[3]) : "v"(a[0]), "v"(a[1]), "v"(a[2]), "v"(a[3]))
#define UN4(op, pre, post, d) asm volatile(op " %0, " pre "%0" post "\n\t" op " %1, " pre "%1" post "\n\t" op " %2, " pre "%2" post "\n\t" op " %3, " pre "%3" post \
  : "+v"(d[0]), "+v"(d[1]), "+v"(d[2]), "+v"(d[3]))
#define POLY4(d, c) asm volatile("v_fmamk_f32 %0, %0, 0x3dd2d3e8, %4\n\tv_fmamk_f32 %1, %1, 0x3dd2d3e8, %4\n\tv_fmamk_f32 %2, %2, 0x3dd2d3e8, %4\n\tv_fmamk_f32 %3, %3, 0x3dd2d3e8, %4" \
  : "+v"(d[0]), "+v"(d[1]), "+v"(d[2]), "+v"(d[3]) : "v"(c))
#define NFMA4(d, a) asm volatile("v_fma_f32 %0, -%0, %4, %0\n\tv_fma_f32 %1, -%1, %5, %1\n\tv_fma_f32 %2, -%2, %6, %2\n\tv_fma_f32 %3, -%3, %7, %3" \
  : "+v"(d[0]), "+v"(d[1]), "+v"(d[2]), "+v"(d[3]) : "v"(a[0]), "v"(a[1]), "v"(a[2]), "v"(a[3]))
#define SCL4(d, a) asm volatile("v_mul_f32 %0, 0xbfb8aa3b, %4\n\tv_mul_f32 %1, 0xbfb8aa3b, %5\n\tv_mul_f32 %2, 0xbfb8aa3b, %6\n\tv_mul_f32 %3, 0xbfb8aa3b, %7" \
  : "=&v"(d[0]), "=&v"(d[1]), "=&v"(d[2]), "=v"(d[3]) : "v"(a[0]), "v"(a[1]), "v"(a[2]), "v"(a[3]))
#define WAITVB(n) __builtin_amdgcn_s_waitcnt(0x0f70 | ((n) & 15) | (((n) >> 4) << 14))
DEV void xchg4(float (&a)[4], float (&b)[4]) {
#pragma unroll
  for (int i = 0; i < 4; ++i) {
    const int x = __float_as_int(a[i]), y = __float_as_int(b[i]);
    a[i] = __builtin_bit_cast(float, __builtin_amdgcn_update_dpp(x, y, 0x114, 0xf, 0xa, false));
    b[i] = __builtin_bit_cast(float, __builtin_amdgcn_update_dpp(y, x, 0x104, 0xf, 0x5, false));
  }
}
constexpr int FA = 4, FU = 16, FO = 64, FS = 64;
constexpr size_t WS_FLAGA = 0;
constexpr size_t WS_FLAGU = WS_FLAGA + 256 * 128;
constexpr size_t WS_FLAGO = WS_FLAGU + 32 * 128 * 128;
constexpr size_t WS_FLAGS = WS_FLAGO + 1024 * 128;
constexpr size_t HT_W = 14 * 5120, UT_W = 13 * 5120, WQ_S = 24576, OT_T = 65536, WO_B = 4 * 5120, ST_T = 24576;
constexpr size_t WS_HT = WS_FLAGS + 1024 * 128, WS_XN = WS_HT + 32 * HT_W, WS_WQ = WS_XN + 2049 * 1024 + 3072, WS_UT = WS_WQ + 56 * WQ_S;
constexpr size_t WS_OT = WS_UT + 32 * UT_W, WS_WO = WS_OT + 32 * OT_T, WS_ST = WS_WO + 28 * WO_B;
static_assert(WS_HT % 1024 == 0 && WS_XN % 1024 == 0 && WS_WQ % 1024 == 0 && WS_UT % 1024 == 0 && WS_OT % 1024 == 0 && WS_WO % 1024 == 0 && WS_ST % 1024 == 0, "alignment");
constexpr int KROT = 2, KROT2 = 2;
constexpr int PW = 16384;
constexpr int TH = 8 * PW, TP = 960, TQ = TH + 8 * TP, TQP = 1088;
constexpr int L_CNT = TH + 896, L_DUMP = TQ + 8 * TQP;
constexpr int SG = 39936, SGX = 21504;
constexpr int TB = 3 * SG;
constexpr int WU = 8 * PW;
constexpr int SVB = 4608, SKH = 9216, SK = 4 * SVB, SQ = SK + 2 * SKH - 512;
constexpr int LDS_BYTES = 163840;

struct Args { const char *x, *wqkv, *wo, *w1, *b1, *w2, *b2; char *ws0, *ws, *out; };

__global__ __launch_bounds__(512, 2) void fused_kernel(const char* __restrict__ x, const char* __restrict__ wqkv, const char* __restrict__ wo,
                                                       const char* __restrict__ w1, const char* __restrict__ b1,
                                                       const char* __restrict__ w2, const char* __restrict__ b2,
                                                       char* __restrict__ ws, char* __restrict__ out) {
  __shared__ __attribute__((aligned(256))) char smem[LDS_BYTES];
  const int w = __builtin_amdgcn_readfirstlane(threadIdx.x >> 6);
  const int b = blockIdx.x, jb = b >> 3, q = (b >> 1) & 3, side = b & 1;
  STAMP(0);
  const unsigned lds0 = (unsigned)(size_t)(LDS_AS char*)smem;
  { const char* pc_; unsigned vo_ = (threadIdx.x * 2 + ((b >> 3) & 1)) * 128;
    asm volatile("s_getpc_b64 %0\n.Lcpf_b:\n\ts_mov_b32 m0, %2\n\tv_min_u32 %1, .Lcpf_e-.Lcpf_b-4, %1\n\tglobal_load_lds_dword %1, %0"
                 : "=&s"(pc_), "+v"(vo_) : "s"(lds0 + L_DUMP) : "memory"); }
  Args A = {x, wqkv, wo, w1, b1, w2, b2, ws, ws, out};
  const int lane = threadIdx.x & 63, ci = (b & 7) * 32 + (b >> 3);
  const unsigned vQ = (lane < 56 ? lane : 55) * 16;
  unsigned oX = (8 * ci + w) * 896;
  asm volatile("" : "+s"(oX) : "v"(vQ));
  asm volatile("" :: "s"(A.x), "s"(A.wqkv), "s"(A.wo), "s"(A.w1), "s"(A.b1), "s"(A.w2), "s"(A.b2), "s"(A.out), "s"(A.ws0));
  u32x4 xr;
  const char* sX = A.x + oX;
  LD16(xr, vQ, sX, 0, "");
  SB();
  PRIO(3);
  const int n16 = lane & 15, g4 = lane >> 4, q4 = n16 >> 2, pq = lane & 3, r8 = lane >> 3; const unsigned vL16 = lane * 16;
  const int dg = jb & 3, pk = jb < 4 ? 0 : (2 * q + side) * 28 + jb - 4, qp = (pk * 293) >> 14, kg = pk - 56 * qp;
  const unsigned ldsP = lds0 + w * PW;
  { const int oh = (kg * 37) >> 8, oj = kg - 7 * oh;
    int oq_ = (8 * kg + w) * 2688 + qp * 896, oo_ = (56 * oh + 4 * oj + (w & 3) + 28 * (w >> 2)) * 896; OPQ(oq_); OPQ(oo_);
    const char* sQ = (qp < 3 ? A.wqkv : A.wo) + (qp < 3 ? oq_ : oo_);
    DMA1(vQ, sQ, lds0 + w * TQP, TQ, ""); }
  unsigned nonce; asm volatile("s_load_dword %0, %1, 0x0" : "=&s"(nonce) : "s"(A.ws0) : "memory");
  const int t7 = (jb < 28 ? jb >> 2 : 1) + ((3 - (int)((size_t)A.w1 >> 7) - 7 * q) & 3), ft = 7 * q + (t7 >= 7 ? t7 - 7 : t7);
  const int st = 8 * ((q + (side ? 0 : 0)) & 3) + (((jb >> 2) + 3 - (int)((size_t)A.w2 >> 7)) & 7);
  const int rot = side ? KROT2 * dg : KROT * dg + ((jb + 4) >> 5), ld = 3584 + 512 * side;
  int wrap = 7 - side - rot, dW = 16 * ld;
  unsigned vWa = r8 * ld + ((lane & 7) ^ (r8 & 2)) * 16, vWb = (vWa ^ 64) + (8 * ld - 1024);
  const char* bW = (side ? A.w2 : A.w1) + ((side ? st : ft) * 128u + w * (256u * 3584u));
  const char* sW = bW + (unsigned)(rot * 2 * dW);
  asm volatile("" : "+s"(wrap), "+s"(dW), "+s"(bW), "+s"(sW) : "v"(vWa), "v"(vWb));
  OPQ(A.ws); A.ws = (char*)(((size_t)A.ws + 8191) & ~(size_t)4095);
  *(LDS_AS unsigned*)(lds0 + L_CNT) = 0u;
  unsigned vH, tH, vq, tq, hw = lds0 + TH + w * TP + vQ;
  const int ks = kg >> 2;
  { const int task = w < 7 ? w : 6, bk = 4 * task + g4, d4 = (bk * 37) >> 8, b7 = bk - 7 * d4;
    const int pos = ((ci >> 2) - KROT * d4) & 7, ix = (b7 < 6 ? 8 * b7 + 8 : 0) + pos;
    vH = d4 * (8 * (unsigned)HT_W) + (ix >> 2) * 5120 + frag(ix & 3, 16 * (ci & 3) + n16);
    tH = lds0 + TH + q4 * TP + bk * 32 + pq * 8;
    const int ti = w ? w - 1 : 0, c0 = 64 * ti + lane, hp0 = (c0 * 1171) >> 16, cc = c0 - 56 * hp0, kk = (kg >> 2) & 1, g_ = kg & 3;
    const unsigned off = (cc >> 4) * 6144 + qp * 1024 + (cc < 48 ? kk * 3072 + g_ * 256 : kk * 512 + g_ * 128) + (cc & 15) * 16;
    unsigned a_ = hp0 * (7 * (unsigned)WQ_S) + line7(off >> 7) + (off & 127), b_ = (4 * ti + g4) * (unsigned)WO_B + frag(ks & 3, 16 * g_ + n16);
    PINV(a_); PINV(b_);
    vq = qp < 3 ? a_ : b_;
    tq = lds0 + TQ + q4 * TQP + ti * 128 + g4 * 32 + pq * 8; }
  unsigned wq_ = WS_WQ + (kg >> 3) * WQ_S, wo_ = WS_WO + (ks >> 2) * 5120; OPQ(wq_); OPQ(wo_);
  const char* sN = A.ws + WS_XN + 512 + (size_t)(8 * ci + w) * 1024;
  const char* sH = A.ws + WS_HT + (size_t)(ci >> 5) * HT_W;
  const char* sWq = A.ws + (qp < 3 ? wq_ : wo_);
  const char* sFA = A.ws + WS_FLAGA + ci * FA;
  int j_ = 3 - jb, n_ = -w; OPQ(j_); OPQ(n_);
  int mH = (w - 7) >> 31, mQ = (j_ & n_) >> 31;
  asm volatile("" : "+s"(mH), "+s"(mQ), "+s"(sN), "+s"(sH), "+s"(sWq), "+s"(sFA)); asm volatile("" : "+v"(vH), "+v"(tH), "+v"(vq), "+v"(tq), "+v"(hw));
  u32x2 bv; { const char* sB = side ? A.b2 + st * 128 : A.b1 + ft * 128; const unsigned vB = side ? n16 * 8 : ((w & 3) * 16 + 4 * g4) * 2;
    LD8(bv, vB, sB, 0, ""); }
  PRIO(0);
  DMA2(vWa, vWb, sW, ldsP, 0, ""); sW += dW; OPQ(sW);
  DMA2(vWa, vWb, sW, ldsP, 2048, ""); sW = wrap == 0 ? bW : sW + dW; OPQ(sW);
  PRIO(3);
  WAITV1(6, xr);
  STAMP(24);
  f32x2_t dd[4];
  ln_1(xr, dd, lane < 56);
  ST16L56(vQ, sN, xr);
  ln_2<true>(xr, dd, lane < 56);
  *(LDS_AS u32x4*)hw = xr;
  WAITV(6);
  asm volatile("s_waitcnt lgkmcnt(0)" : "+s"(nonce) :: "memory"); nonce += 1u;
  BAR();
  STAMP(25);
  { const bf16x8 h0 = tr8u(tH, 4 * TP), q0 = tr8u(tq, 4 * TQP);
    ST16M(mH, vH, sH, __builtin_bit_cast(u32x4, h0));
    ST16M(mQ, vq, sWq, __builtin_bit_cast(u32x4, q0)); }
  SB();
  PRIO(0);
  DMA2(vWa, vWb, sW, ldsP, 4096, ""); sW += dW; OPQ(sW);
  PRIO(3);
  STAMP(26);
  unsigned fh; const char* sFH = A.ws + WS_FLAGA + w * (32 * FA); unsigned vFH = (lane & 31) * FA;
  asm volatile("" : "+s"(sFH)); asm volatile("" : "+v"(vFH));
  WAITV1(2, bv);
  BAR0(); if (w == 0) ST4X(0u, sFA, nonce);
  PRIO(0);
  STAMP(27);

  if (__builtin_expect(side, 1)) {
  const unsigned ldsU = lds0 + WU + w * 4096; bf16x8 fw[7][4];
  DMA2(vWa, vWb, sW, ldsP, 6144, ""); sW = wrap == 1 ? bW : sW + dW; OPQ(sW);
  unsigned rW0 = ldsP + (8 * g4 + q4) * 128 + (((q4 >> 1) | (2 * (g4 & 1))) * 32) + pq * 8, rW1 = rW0 ^ 32, rW2 = rW0 ^ 64, rW3 = rW0 ^ 96;
  asm volatile("" : "+v"(rW0), "+v"(rW1), "+v"(rW2), "+v"(rW3));
  unsigned rU0 = ldsU + (8 * g4 + q4) * 128 + (((q4 >> 1) | (2 * (g4 & 1))) * 32) + pq * 8, rU1 = rU0 ^ 32, rU2 = rU0 ^ 64, rU3 = rU0 ^ 96;
  asm volatile("" : "+v"(rU0), "+v"(rU1), "+v"(rU2), "+v"(rU3));
  const int tt = 4 * q + (jb >> 3), hp = jb & 7, t0 = 128 * tt, cb = w & 3;
  unsigned vD[5];
#pragma unroll
  for (int m = 0; m < 5; ++m) {
    const int i = m == 4 && w == 7 ? 31 : w + 8 * m, row = 8 * (i - 21) + r8, tok = t0 - 16 + row;
    unsigned a_ = line7(8 * i + r8) + (lane & 7) * 16, b_ = (tok < 0 ? 0 : tok) * 1024 + ((lane & 7) ^ ((row >> 1) & 7)) * 16;
    vD[m] = m < 2 ? a_ : m > 2 ? b_ : i < 21 ? a_ : b_;
    PINV(vD[m]);
  }
  const char* sDq = A.ws + WS_WQ + (size_t)hp * (7 * WQ_S); const char* sDx = A.ws + WS_XN + 512; const char* sDm = w < 5 ? sDq : sDx;
  int dm = w < 5 ? (int)WQ_S : 128;
  unsigned ldsD = lds0 + w * 1024, ldsE = w == 7 ? lds0 + 31 * 1024 : ldsD + 32768;
  asm volatile("" : "+s"(sDq), "+s"(sDx), "+s"(sDm), "+s"(dm), "+s"(ldsD), "+s"(ldsE));
  WAITV(5);
  fw[0][0] = tr8u(rW0 + 0, 512);
  fw[0][1] = tr8u(rW1 + 0, 512);
  fw[0][2] = tr8u(rW2 + 0, 512);
  fw[0][3] = tr8u(rW3 + 0, 512);
  DMA2(vWa, vWb, sW, ldsP, 8192, ""); sW += dW; OPQ(sW);
  DMA2(vWa, vWb, sW, ldsP, 10240, ""); sW = wrap == 2 ? bW : sW + dW; OPQ(sW);
  u32x4 tb0; u32x2 tb4; unsigned tba = lds0 + TB + (w < 4 ? w : w + 4) * 1024 + vL16, tbb = lds0 + TB + 4096 + w * 512 + lane * 8;
  { const int dk = 4 * g4 - n16;
    u32x4 m_;
#pragma unroll
    for (int i = 0; i < 4; ++i) m_[i] = (unsigned)((dk + i + 7) >> 31);
    tb0 = m_;
    if ((w & 3) == 0) {
#pragma unroll
      for (int i = 0; i < 4; ++i) tb0[i] = ~m_[i] & ((unsigned)(dk + i - 1) < 8u ? (w && lane < 32 ? 0xce6e0000u : 0u) : 0xff800000u);
    } else if (w == 2) {
      tb0 = (u32x4){frag(0, lane), frag(1, lane), frag(2, lane), frag(3, lane)};
    } else if (w == 3) {
      tb0 = (u32x4){(m_[0] & 0xffffu) | (m_[1] & 0xffff0000u), (m_[2] & 0xffffu) | (m_[3] & 0xffff0000u),
                    lds0 + SG + n16 * 64 + ((g4 ^ q4) << 4), lds0 + SG + (4 * g4 + q4) * 32 + pq * 8};
    } else if (w == 5) {
      tb0 = (u32x4){n16 * 128u + ((g4 ^ (n16 & 7)) << 4), r8 * 128u + (((2 * pq + ((lane >> 2) & 1)) ^ r8) << 4), r8 * 896u + (lane & 7) * 16,
                    ((lane & 7) >> 1) * 3072u + r8 * 32 + (lane & 1) * 16};
    }
    if (w < 4) {
      const int ga = 7 * hp + g4;
      const unsigned l0 = ((ga >> 2) * 4 + cb) * 8 + (ga & 3) * 2 + (n16 >> 3);
      tb4 = (u32x2){line7(l0) + (n16 & 7) * 16, line7(l0 + 32) + (n16 & 7) * 16};
    } else {
      const int c = 16 * cb + 4 * g4;
      const bool ok = c < 56;
      const int hd = ok ? (c >= 28) : (g4 & 1), d = ok ? c - 28 * hd : 28, row = (n16 >> 1) + 72 * (n16 & 1);
      tb4 = (u32x2){lds0 + SG + hd * SKH + row * 64 + (((d >> 3) ^ (n16 >> 3) ^ (2 * (n16 & 1))) << 4) + ((d >> 2) & 1) * 8,
                    lds0 + SG + (2 * hd + (d >> 4)) * SVB + row * 32 + (d & 15) * 2};
    } }
  unsigned xr0 = lds0 + SGX + n16 * 128 + ((g4 ^ (n16 >> 1)) << 4) + (w >> 2) * 8192, xr1 = xr0 ^ 64;
  unsigned wr0 = lds0 + cb * 6144 + (cb < 3 ? vL16 : g4 * 128 + (n16 & 7) * 16), wr1 = wr0 + (cb < 3 ? 3072 : 512);
  asm volatile("" : "+v"(tb0), "+v"(tb4), "+v"(tba), "+v"(tbb), "+v"(xr0), "+v"(xr1), "+v"(wr0), "+v"(wr1));
  const char* sP = A.ws + WS_UT + (size_t)(dg * 8 + w) * UT_W;
  const char* sFU = A.ws + WS_FLAGU + (dg * 8 + w) * (128 * FU);
  const char* sO = A.ws + WS_OT + (size_t)(2 * tt + (w >> 2)) * OT_T;
  const char* sFO = A.ws + WS_FLAGO + ((tt * 8 + hp) * 8 + w) * FO;
  const char* sS = A.ws + WS_ST + (size_t)(st * 4 + dg) * ST_T + pair3(w);
  const char* sFS = A.ws + WS_FLAGS + (st * 4 + dg) * (8 * FS);
  const char* sXo = A.x + ((64 * st + 8 * w) * 896 + dg * 224); const char* sOut = A.out + ((64 * st + 8 * w) * 896 + dg * 224);
  int kx = 32 * (w & 1), r0 = 72 * (w & 1) + 16 * (w >> 1), cmo = t0 + (w >> 1) == 0 ? 8192 : 0, rao = lds0 + w * 1024, bio = lds0 + TB + 10496 + 32 * w;
  asm volatile("" : "+s"(sP), "+s"(sFU), "+s"(sO), "+s"(sFO), "+s"(sS), "+s"(sFS), "+s"(sXo), "+s"(sOut), "+s"(kx), "+s"(r0), "+s"(cmo), "+s"(rao), "+s"(bio));
  u32x4 t2, t3, m1; f32x4 cm; u32x2 vS; unsigned rk, rv, mq_, vFU;
  WAITV(4);
  fw[1][0] = tr8u(rW0 + 4096, 512);
  fw[1][1] = tr8u(rW1 + 4096, 512);
  fw[1][2] = tr8u(rW2 + 4096, 512);
  fw[1][3] = tr8u(rW3 + 4096, 512);
  DMA2(vWa, vWb, sW, ldsP, 12288, ""); sW += dW; OPQ(sW);
  DMA2(vWa, vWb, sW, ldsP, 14336, ""); sW = wrap == 3 ? bW : sW + dW; OPQ(sW);
  WAITV(4);
  fw[2][0] = tr8u(rW0 + 8192, 512);
  fw[2][1] = tr8u(rW1 + 8192, 512);
  fw[2][2] = tr8u(rW2 + 8192, 512);
  fw[2][3] = tr8u(rW3 + 8192, 512);
  DMA2(vWa, vWb, sW, ldsU, 0, ""); sW += dW; OPQ(sW);
  WAITL(0);
  DMA2(vWa, vWb, sW, ldsU, 2048, ""); sW = wrap == 4 ? bW : sW + dW; OPQ(sW);
  WAITV(4);
  fw[3][0] = tr8u(rW0 + 12288, 512);
  fw[3][1] = tr8u(rW1 + 12288, 512);
  fw[3][2] = tr8u(rW2 + 12288, 512);
  fw[3][3] = tr8u(rW3 + 12288, 512);
  STAMP(33);
  LD4(fh, vFH, sFH, 0, "sc1");
  WAITV1(0, fh);
  if (__ballot(fh != nonce) != 0ull) poll1<4>(sFH, vFH, nonce);
  BAR();
  STAMP(34);
  DMA1(vD[0], sDq, ldsD, 0, "sc1");
  DMA1(vD[1], sDq, ldsD, 8192, "sc1"); sDq += WQ_S; OPQ(sDq);
  DMA1(vD[2], sDm, ldsD, 16384, "sc1"); sDm += dm; OPQ(sDm);
  DMA1(vD[3], sDx, ldsD, 24576, "sc1");
  DMA1(vD[4], sDx, ldsE, 0, "sc1"); sDx += 128; OPQ(sDx);
  BAR0();
  DMA1(vD[0], sDq, ldsD, 39936, "sc1");
  DMA1(vD[1], sDq, ldsD, 48128, "sc1"); sDq += WQ_S; OPQ(sDq);
  DMA1(vD[2], sDm, ldsD, 56320, "sc1"); sDm += dm; OPQ(sDm);
  DMA1(vD[3], sDx, ldsD, 64512, "sc1");
  DMA1(vD[4], sDx, ldsE, 39936, "sc1"); sDx += 128; OPQ(sDx);
  *(LDS_AS u32x2*)(tbb) = tb4;
  if (w < 6) *(LDS_AS u32x4*)(tba) = tb0; else *(LDS_AS u32x4*)(lds0 + TB + 10496 + n16 * 16) = (u32x4){bv[0] << 16, bv[0] & 0xffff0000u, bv[1] << 16, bv[1] & 0xffff0000u};
  SB();
  WAITV(11);
  fw[4][0] = tr8u(rU0 + 0, 512);
  fw[4][1] = tr8u(rU1 + 0, 512);
  fw[4][2] = tr8u(rU2 + 0, 512);
  fw[4][3] = tr8u(rU3 + 0, 512);
  if (w < 4) {
  f32x4 aq[4], ak[5], av[5];
  bf16x8 xf[2][5], wf[2][3];
  unsigned sk0, sk1, sv;
  const unsigned kz = t0 == 0 ? 0u : ~0u;
  WAITV(5);
  BAR();
  STAMP(35);
  wf[0][1] = *(LDS_AS const bf16x8*)(wr0 + 1024);
  xf[0][0] = *(LDS_AS const bf16x8*)(xr0 + 0);
  wf[0][2] = *(LDS_AS const bf16x8*)(wr0 + 2048);
  wf[0][0] = *(LDS_AS const bf16x8*)(wr0 + 0);
  xf[0][1] = *(LDS_AS const bf16x8*)(xr0 + 2048);
  xf[0][2] = *(LDS_AS const bf16x8*)(xr0 + 4096);
  xf[0][3] = *(LDS_AS const bf16x8*)(xr0 + 6144);
  xf[0][4] = *(LDS_AS const bf16x8*)(xr0 + 8192);
  SB();
  MV16Z(ak[0], wf[0][1], xf[0][0]); SB();
  wf[1][1] = *(LDS_AS const bf16x8*)(wr1 + 1024);
  SB();
  MV16Z(av[0], wf[0][2], xf[0][0]); SB();
  xf[1][0] = *(LDS_AS const bf16x8*)(xr1 + 0);
  SB();
  MV16Z(ak[1], wf[0][1], xf[0][1]); SB();
  wf[1][2] = *(LDS_AS const bf16x8*)(wr1 + 2048);
  SB();
  MV16Z(av[1], wf[0][2], xf[0][1]); SB();
  wf[1][0] = *(LDS_AS const bf16x8*)(wr1 + 0);
  SB();
  MV16Z(aq[0], wf[0][0], xf[0][1]); SB();
  xf[1][1] = *(LDS_AS const bf16x8*)(xr1 + 2048);
  SB();
  MV16Z(ak[2], wf[0][1], xf[0][2]); SB();
  xf[1][2] = *(LDS_AS const bf16x8*)(xr1 + 4096);
  SB();
  MV16Z(av[2], wf[0][2], xf[0][2]); SB();
  xf[1][3] = *(LDS_AS const bf16x8*)(xr1 + 6144);
  SB();
  MV16Z(aq[1], wf[0][0], xf[0][2]); SB();
  xf[1][4] = *(LDS_AS const bf16x8*)(xr1 + 8192);
  SB();
  MV16Z(ak[3], wf[0][1], xf[0][3]); SB();
  DMA1(vD[0], sDq, ldsD, 79872, "sc1");
  SB();
  MV16Z(av[3], wf[0][2], xf[0][3]); SB();
  DMA1(vD[1], sDq, ldsD, 88064, "sc1"); sDq += WQ_S; OPQ(sDq);
  SB();
  MV16Z(aq[2], wf[0][0], xf[0][3]); SB();
  DMA1(vD[2], sDm, ldsD, 96256, "sc1"); sDm += dm; OPQ(sDm);
  SB();
  MV16Z(ak[4], wf[0][1], xf[0][4]); SB();
  DMA1(vD[3], sDx, ldsD, 104448, "sc1");
  SB();
  MV16Z(av[4], wf[0][2], xf[0][4]); SB();
  DMA1(vD[4], sDx, ldsE, 79872, "sc1"); sDx += 128; OPQ(sDx);
  SB();
  MV16Z(aq[3], wf[0][0], xf[0][4]); SB();
  SB();
  MV16(ak[0], wf[1][1], xf[1][0]); SB();
  SB();
  MV16(av[0], wf[1][2], xf[1][0]); SB();
  WAITV(5);
  BAR();
  xr0 += 39936; wr0 += 39936; asm volatile("" : "+v"(xr0), "+v"(wr0));
  SB();
  MV16(ak[1], wf[1][1], xf[1][1]); SB();
  wf[0][1] = *(LDS_AS const bf16x8*)(wr0 + 1024);
  SB();
  MV16(av[1], wf[1][2], xf[1][1]); SB();
  xf[0][0] = *(LDS_AS const bf16x8*)(xr0 + 0);
  SB();
  MV16(aq[0], wf[1][0], xf[1][1]); SB();
  wf[0][2] = *(LDS_AS const bf16x8*)(wr0 + 2048);
  SB();
  MV16(ak[2], wf[1][1], xf[1][2]); SB();
  wf[0][0] = *(LDS_AS const bf16x8*)(wr0 + 0);
  SB();
  MV16(av[2], wf[1][2], xf[1][2]); SB();
  xf[0][1] = *(LDS_AS const bf16x8*)(xr0 + 2048);
  SB();
  MV16(aq[1], wf[1][0], xf[1][2]); SB();
  xf[0][2] = *(LDS_AS const bf16x8*)(xr0 + 4096);
  SB();
  MV16(ak[3], wf[1][1], xf[1][3]); SB();
  xf[0][3] = *(LDS_AS const bf16x8*)(xr0 + 6144);
  SB();
  MV16(av[3], wf[1][2], xf[1][3]); SB();
  xf[0][4] = *(LDS_AS const bf16x8*)(xr0 + 8192);
  SB();
  MV16(aq[2], wf[1][0], xf[1][3]); SB();
  MV16(ak[4], wf[1][1], xf[1][4]); SB();
  MV16(av[4], wf[1][2], xf[1][4]); SB();
  MV16(aq[3], wf[1][0], xf[1][4]); SB();
  SB();
  MV16(ak[0], wf[0][1], xf[0][0]); SB();
  xr1 += 39936; wr1 += 39936; asm volatile("" : "+v"(xr1), "+v"(wr1));
  SB();
  MV16(av[0], wf[0][2], xf[0][0]); SB();
  wf[1][1] = *(LDS_AS const bf16x8*)(wr1 + 1024);
  SB();
  MV16(ak[1], wf[0][1], xf[0][1]); SB();
  xf[1][0] = *(LDS_AS const bf16x8*)(xr1 + 0);
  SB();
  MV16(av[1], wf[0][2], xf[0][1]); SB();
  wf[1][2] = *(LDS_AS const bf16x8*)(wr1 + 2048);
  SB();
  MV16(aq[0], wf[0][0], xf[0][1]); SB();
  wf[1][0] = *(LDS_AS const bf16x8*)(wr1 + 0);
  SB();
  MV16(ak[2], wf[0][1], xf[0][2]); SB();
  xf[1][1] = *(LDS_AS const bf16x8*)(xr1 + 2048);
  SB();
  MV16(av[2], wf[0][2], xf[0][2]); SB();
  xf[1][2] = *(LDS_AS const bf16x8*)(xr1 + 4096);
  SB();
  MV16(aq[1], wf[0][0], xf[0][2]); SB();
  xf[1][3] = *(LDS_AS const bf16x8*)(xr1 + 6144);
  SB();
  MV16(ak[3], wf[0][1], xf[0][3]); SB();
  xf[1][4] = *(LDS_AS const bf16x8*)(xr1 + 8192);
  SB();
  MV16(av[3], wf[0][2], xf[0][3]); SB();
  DMA1(vD[0], sDq, ldsD, 0, "sc1");
  SB();
  MV16(aq[2], wf[0][0], xf[0][3]); SB();
  DMA1(vD[1], sDq, ldsD, 8192, "sc1"); sDq += WQ_S; OPQ(sDq);
  SB();
  MV16(ak[4], wf[0][1], xf[0][4]); SB();
  DMA1(vD[2], sDm, ldsD, 16384, "sc1"); sDm += dm; OPQ(sDm);
  SB();
  MV16(av[4], wf[0][2], xf[0][4]); SB();
  DMA1(vD[3], sDx, ldsD, 24576, "sc1");
  SB();
  MV16(aq[3], wf[0][0], xf[0][4]); SB();
  DMA1(vD[4], sDx, ldsE, 0, "sc1"); sDx += 128; OPQ(sDx);
  SB();
  SB();
  MV16(ak[0], wf[1][1], xf[1][0]); SB();
  SB();
  MV16(av[0], wf[1][2], xf[1][0]); SB();
  WAITV(5);
  BAR();
  xr0 += 39936; wr0 += 39936; asm volatile("" : "+v"(xr0), "+v"(wr0));
  STAMP(60);
  SB();
  MV16(ak[1], wf[1][1], xf[1][1]); SB();
  wf[0][1] = *(LDS_AS const bf16x8*)(wr0 + 1024);
  SB();
  MV16(av[1], wf[1][2], xf[1][1]); SB();
  xf[0][0] = *(LDS_AS const bf16x8*)(xr0 + 0);
  SB();
  MV16(aq[0], wf[1][0], xf[1][1]); SB();
  wf[0][2] = *(LDS_AS const bf16x8*)(wr0 + 2048);
  SB();
  MV16(ak[2], wf[1][1], xf[1][2]); SB();
  wf[0][0] = *(LDS_AS const bf16x8*)(wr0 + 0);
  SB();
  MV16(av[2], wf[1][2], xf[1][2]); SB();
  xf[0][1] = *(LDS_AS const bf16x8*)(xr0 + 2048);
  SB();
  MV16(aq[1], wf[1][0], xf[1][2]); SB();
  xf[0][2] = *(LDS_AS const bf16x8*)(xr0 + 4096);
  SB();
  MV16(ak[3], wf[1][1], xf[1][3]); SB();
  xf[0][3] = *(LDS_AS const bf16x8*)(xr0 + 6144);
  SB();
  MV16(av[3], wf[1][2], xf[1][3]); SB();
  xf[0][4] = *(LDS_AS const bf16x8*)(xr0 + 8192);
  SB();
  MV16(aq[2], wf[1][0], xf[1][3]); SB();
  MV16(ak[4], wf[1][1], xf[1][4]); SB();
  MV16(av[4], wf[1][2], xf[1][4]); SB();
  MV16(aq[3], wf[1][0], xf[1][4]); SB();
  SB();
  MV16(ak[0], wf[0][1], xf[0][0]); SB();
  xr1 += 39936; wr1 += 39936; asm volatile("" : "+v"(xr1), "+v"(wr1));
  SB();
  MV16(av[0], wf[0][2], xf[0][0]); SB();
  wf[1][1] = *(LDS_AS const bf16x8*)(wr1 + 1024);
  SB();
  MV16(ak[1], wf[0][1], xf[0][1]); SB();
  xf[1][0] = *(LDS_AS const bf16x8*)(xr1 + 0);
  SB();
  MV16(av[1], wf[0][2], xf[0][1]); SB();
  wf[1][2] = *(LDS_AS const bf16x8*)(wr1 + 2048);
  SB();
  MV16(aq[0], wf[0][0], xf[0][1]); SB();
  wf[1][0] = *(LDS_AS const bf16x8*)(wr1 + 0);
  SB();
  MV16(ak[2], wf[0][1], xf[0][2]); SB();
  xf[1][1] = *(LDS_AS const bf16x8*)(xr1 + 2048);
  SB();
  MV16(av[2], wf[0][2], xf[0][2]); SB();
  xf[1][2] = *(LDS_AS const bf16x8*)(xr1 + 4096);
  SB();
  MV16(aq[1], wf[0][0], xf[0][2]); SB();
  xf[1][3] = *(LDS_AS const bf16x8*)(xr1 + 6144);
  SB();
  MV16(ak[3], wf[0][1], xf[0][3]); SB();
  xf[1][4] = *(LDS_AS const bf16x8*)(xr1 + 8192);
  SB();
  MV16(av[3], wf[0][2], xf[0][3]); SB();
  DMA1(vD[0], sDq, ldsD, 39936, "sc1");
  SB();
  MV16(aq[2], wf[0][0], xf[0][3]); SB();
  DMA1(vD[1], sDq, ldsD, 48128, "sc1"); sDq += WQ_S; OPQ(sDq);
  SB();
  MV16(ak[4], wf[0][1], xf[0][4]); SB();
  DMA1(vD[2], sDm, ldsD, 56320, "sc1"); sDm += dm; OPQ(sDm);
  SB();
  MV16(av[4], wf[0][2], xf[0][4]); SB();
  DMA1(vD[3], sDx, ldsD, 64512, "sc1");
  SB();
  MV16(aq[3], wf[0][0], xf[0][4]); SB();
  DMA1(vD[4], sDx, ldsE, 39936, "sc1"); sDx += 128; OPQ(sDx);
  SB();
  SB();
  MV16(ak[0], wf[1][1], xf[1][0]); SB();
  SB();
  MV16(av[0], wf[1][2], xf[1][0]); SB();
  WAITV(5);
  BAR();
  xr0 += -79872; wr0 += -79872; asm volatile("" : "+v"(xr0), "+v"(wr0));
  SB();
  MV16(ak[1], wf[1][1], xf[1][1]); SB();
  wf[0][1] = *(LDS_AS const bf16x8*)(wr0 + 1024);
  SB();
  MV16(av[1], wf[1][2], xf[1][1]); SB();
  xf[0][0] = *(LDS_AS const bf16x8*)(xr0 + 0);
  SB();
  MV16(aq[0], wf[1][0], xf[1][1]); SB();
  wf[0][2] = *(LDS_AS const bf16x8*)(wr0 + 2048);
  SB();
  MV16(ak[2], wf[1][1], xf[1][2]); SB();
  wf[0][0] = *(LDS_AS const bf16x8*)(wr0 + 0);
  SB();
  MV16(av[2], wf[1][2], xf[1][2]); SB();
  xf[0][1] = *(LDS_AS const bf16x8*)(xr0 + 2048);
  SB();
  MV16(aq[1], wf[1][0], xf[1][2]); SB();
  xf[0][2] = *(LDS_AS const bf16x8*)(xr0 + 4096);
  SB();
  MV16(ak[3], wf[1][1], xf[1][3]); SB();
  xf[0][3] = *(LDS_AS const bf16x8*)(xr0 + 6144);
  SB();
  MV16(av[3], wf[1][2], xf[1][3]); SB();
  xf[0][4] = *(LDS_AS const bf16x8*)(xr0 + 8192);
  SB();
  MV16(aq[2], wf[1][0], xf[1][3]); SB();
  MV16(ak[4], wf[1][1], xf[1][4]); SB();
  MV16(av[4], wf[1][2], xf[1][4]); SB();
  MV16(aq[3], wf[1][0], xf[1][4]); SB();
  SB();
  MV16(ak[0], wf[0][1], xf[0][0]); SB();
  xr1 += -79872; wr1 += -79872; asm volatile("" : "+v"(xr1), "+v"(wr1));
  SB();
  MV16(av[0], wf[0][2], xf[0][0]); SB();
  wf[1][1] = *(LDS_AS const bf16x8*)(wr1 + 1024);
  SB();
  MV16(ak[1], wf[0][1], xf[0][1]); SB();
  xf[1][0] = *(LDS_AS const bf16x8*)(xr1 + 0);
  SB();
  MV16(av[1], wf[0][2], xf[0][1]); SB();
  wf[1][2] = *(LDS_AS const bf16x8*)(wr1 + 2048);
  SB();
  MV16(aq[0], wf[0][0], xf[0][1]); SB();
  wf[1][0] = *(LDS_AS const bf16x8*)(wr1 + 0);
  SB();
  MV16(ak[2], wf[0][1], xf[0][2]); SB();
  xf[1][1] = *(LDS_AS const bf16x8*)(xr1 + 2048);
  SB();
  MV16(av[2], wf[0][2], xf[0][2]); SB();
  xf[1][2] = *(LDS_AS const bf16x8*)(xr1 + 4096);
  SB();
  MV16(aq[1], wf[0][0], xf[0][2]); SB();
  xf[1][3] = *(LDS_AS const bf16x8*)(xr1 + 6144);
  SB();
  MV16(ak[3], wf[0][1], xf[0][3]); SB();
  xf[1][4] = *(LDS_AS const bf16x8*)(xr1 + 8192);
  SB();
  MV16(av[3], wf[0][2], xf[0][3]); SB();
  DMA1(vD[0], sDq, ldsD, 79872, "sc1");
  SB();
  MV16(aq[2], wf[0][0], xf[0][3]); SB();
  DMA1(vD[1], sDq, ldsD, 88064, "sc1"); sDq += WQ_S; OPQ(sDq);
  SB();
  MV16(ak[4], wf[0][1], xf[0][4]); SB();
  DMA1(vD[2], sDm, ldsD, 96256, "sc1"); sDm += dm; OPQ(sDm);
  SB();
  MV16(av[4], wf[0][2], xf[0][4]); SB();
  DMA1(vD[3], sDx, ldsD, 104448, "sc1");
  SB();
  MV16(aq[3], wf[0][0], xf[0][4]); SB();
  DMA1(vD[4], sDx, ldsE, 79872, "sc1"); sDx += 128; OPQ(sDx);
  SB();
  SB();
  MV16(ak[0], wf[1][1], xf[1][0]); SB();
  SB();
  MV16(av[0], wf[1][2], xf[1][0]); SB();
  WAITV(5);
  BAR();
  xr0 += 39936; wr0 += 39936; asm volatile("" : "+v"(xr0), "+v"(wr0));
  STAMP(61);
  SB();
  MV16(ak[1], wf[1][1], xf[1][1]); SB();
  wf[0][1] = *(LDS_AS const bf16x8*)(wr0 + 1024);
  SB();
  MV16(av[1], wf[1][2], xf[1][1]); SB();
  xf[0][0] = *(LDS_AS const bf16x8*)(xr0 + 0);
  SB();
  MV16(aq[0], wf[1][0], xf[1][1]); SB();
  wf[0][2] = *(LDS_AS const bf16x8*)(wr0 + 2048);
  SB();
  MV16(ak[2], wf[1][1], xf[1][2]); SB();
  wf[0][0] = *(LDS_AS const bf16x8*)(wr0 + 0);
  SB();
  MV16(av[2], wf[1][2], xf[1][2]); SB();
  xf[0][1] = *(LDS_AS const bf16x8*)(xr0 + 2048);
  SB();
  MV16(aq[1], wf[1][0], xf[1][2]); SB();
  xf[0][2] = *(LDS_AS const bf16x8*)(xr0 + 4096);
  SB();
  MV16(ak[3], wf[1][1], xf[1][3]); SB();
  xf[0][3] = *(LDS_AS const bf16x8*)(xr0 + 6144);
  SB();
  MV16(av[3], wf[1][2], xf[1][3]); SB();
  xf[0][4] = *(LDS_AS const bf16x8*)(xr0 + 8192);
  SB();
  MV16(aq[2], wf[1][0], xf[1][3]); SB();
  MV16(ak[4], wf[1][1], xf[1][4]); SB();
  MV16(av[4], wf[1][2], xf[1][4]); SB();
  MV16(aq[3], wf[1][0], xf[1][4]); SB();
  SB();
  MV16(ak[0], wf[0][1], xf[0][0]); SB();
  xr1 += 39936; wr1 += 39936; asm volatile("" : "+v"(xr1), "+v"(wr1));
  SB();
  MV16(av[0], wf[0][2], xf[0][0]); SB();
  wf[1][1] = *(LDS_AS const bf16x8*)(wr1 + 1024);
  SB();
  MV16(ak[1], wf[0][1], xf[0][1]); SB();
  xf[1][0] = *(LDS_AS const bf16x8*)(xr1 + 0);
  SB();
  MV16(av[1], wf[0][2], xf[0][1]); SB();
  wf[1][2] = *(LDS_AS const bf16x8*)(wr1 + 2048);
  SB();
  MV16(aq[0], wf[0][0], xf[0][1]); SB();
  wf[1][0] = *(LDS_AS const bf16x8*)(wr1 + 0);
  SB();
  MV16(ak[2], wf[0][1], xf[0][2]); SB();
  xf[1][1] = *(LDS_AS const bf16x8*)(xr1 + 2048);
  SB();
  MV16(av[2], wf[0][2], xf[0][2]); SB();
  xf[1][2] = *(LDS_AS const bf16x8*)(xr1 + 4096);
  SB();
  MV16(aq[1], wf[0][0], xf[0][2]); SB();
  xf[1][3] = *(LDS_AS const bf16x8*)(xr1 + 6144);
  SB();
  MV16(ak[3], wf[0][1], xf[0][3]); SB();
  xf[1][4] = *(LDS_AS const bf16x8*)(xr1 + 8192);
  SB();
  MV16(av[3], wf[0][2], xf[0][3]); SB();
  DMA1(vD[0], sDq, ldsD, 0, "sc1");
  SB();
  MV16(aq[2], wf[0][0], xf[0][3]); SB();
  DMA1(vD[1], sDq, ldsD, 8192, "sc1"); sDq += WQ_S; OPQ(sDq);
  SB();
  MV16(ak[4], wf[0][1], xf[0][4]); SB();
  DMA1(vD[2], sDm, ldsD, 16384, "sc1"); sDm += dm; OPQ(sDm);
  SB();
  MV16(av[4], wf[0][2], xf[0][4]); SB();
  DMA1(vD[3], sDx, ldsD, 24576, "sc1");
  SB();
  MV16(aq[3], wf[0][0], xf[0][4]); SB();
  DMA1(vD[4], sDx, ldsE, 0, "sc1"); sDx += 128; OPQ(sDx);
  SB();
  SB();
  MV16(ak[0], wf[1][1], xf[1][0]); SB();
  SB();
  MV16(av[0], wf[1][2], xf[1][0]); SB();
  WAITV(5);
  BAR();
  xr0 += 39936; wr0 += 39936; asm volatile("" : "+v"(xr0), "+v"(wr0));
  SB();
  MV16(ak[1], wf[1][1], xf[1][1]); SB();
  wf[0][1] = *(LDS_AS const bf16x8*)(wr0 + 1024);
  SB();
  MV16(av[1], wf[1][2], xf[1][1]); SB();
  xf[0][0] = *(LDS_AS const bf16x8*)(xr0 + 0);
  SB();
  MV16(aq[0], wf[1][0], xf[1][1]); SB();
  wf[0][2] = *(LDS_AS const bf16x8*)(wr0 + 2048);
  SB();
  MV16(ak[2], wf[1][1], xf[1][2]); SB();
  wf[0][0] = *(LDS_AS const bf16x8*)(wr0 + 0);
  SB();
  MV16(av[2], wf[1][2], xf[1][2]); SB();
  xf[0][1] = *(LDS_AS const bf16x8*)(xr0 + 2048);
  SB();
  MV16(aq[1], wf[1][0], xf[1][2]); SB();
  xf[0][2] = *(LDS_AS const bf16x8*)(xr0 + 4096);
  SB();
  MV16(ak[3], wf[1][1], xf[1][3]); SB();
  xf[0][3] = *(LDS_AS const bf16x8*)(xr0 + 6144);
  SB();
  MV16(av[3], wf[1][2], xf[1][3]); SB();
  xf[0][4] = *(LDS_AS const bf16x8*)(xr0 + 8192);
  SB();
  MV16(aq[2], wf[1][0], xf[1][3]); SB();
  unsigned f0; { const int l_ = lane_now(); vFU = (l_ < 14 ? l_ : 13) * FU; PINV(vFU); }
  LD4(f0, vFU, sFU, 0, "sc1");
  SB();
  MV16(ak[4], wf[1][1], xf[1][4]); SB();
  MV16(av[4], wf[1][2], xf[1][4]); SB();
  MV16(aq[3], wf[1][0], xf[1][4]); SB();
  SB();
  MV16(ak[0], wf[0][1], xf[0][0]); SB();
  xr1 += 39936; wr1 += 39936; asm volatile("" : "+v"(xr1), "+v"(wr1));
  SB();
  MV16(av[0], wf[0][2], xf[0][0]); SB();
  wf[1][1] = *(LDS_AS const bf16x8*)(wr1 + 1024);
  SB();
  MV16(ak[1], wf[0][1], xf[0][1]); SB();
  xf[1][0] = *(LDS_AS const bf16x8*)(xr1 + 0);
  SB();
  MV16(av[1], wf[0][2], xf[0][1]); SB();
  wf[1][2] = *(LDS_AS const bf16x8*)(wr1 + 2048);
  SB();
  MV16(aq[0], wf[0][0], xf[0][1]); SB();
  wf[1][0] = *(LDS_AS const bf16x8*)(wr1 + 0);
  SB();
  MV16(ak[2], wf[0][1], xf[0][2]); SB();
  xf[1][1] = *(LDS_AS const bf16x8*)(xr1 + 2048);
  SB();
  MV16(av[2], wf[0][2], xf[0][2]); SB();
  xf[1][2] = *(LDS_AS const bf16x8*)(xr1 + 4096);
  SB();
  MV16(aq[1], wf[0][0], xf[0][2]); SB();
  xf[1][3] = *(LDS_AS const bf16x8*)(xr1 + 6144);
  SB();
  MV16(ak[3], wf[0][1], xf[0][3]); SB();
  xf[1][4] = *(LDS_AS const bf16x8*)(xr1 + 8192);
  SB();
  MV16(av[3], wf[0][2], xf[0][3]); SB();
  DMA2(vWa, vWb, sW, ldsU, 0, ""); sW += dW; OPQ(sW);
  SB();
  MV16(aq[2], wf[0][0], xf[0][3]); SB();
  DMA2(vWa, vWb, sW, ldsU, 2048, ""); sW = wrap == 5 ? bW : sW + dW; OPQ(sW);
  SB();
  MV16(ak[4], wf[0][1], xf[0][4]); SB();
  MV16(av[4], wf[0][2], xf[0][4]); SB();
  MV16(aq[3], wf[0][0], xf[0][4]); SB();
  SB();
  MV16(ak[0], wf[1][1], xf[1][0]); SB();
  SB();
  MV16(av[0], wf[1][2], xf[1][0]); SB();
  WAITV(5);
  BAR();
  xr0 += -79872; wr0 += -79872; asm volatile("" : "+v"(xr0), "+v"(wr0));
  STAMP(62);
  SB();
  MV16(ak[1], wf[1][1], xf[1][1]); SB();
  wf[0][1] = *(LDS_AS const bf16x8*)(wr0 + 1024);
  SB();
  MV16(av[1], wf[1][2], xf[1][1]); SB();
  xf[0][0] = *(LDS_AS const bf16x8*)(xr0 + 0);
  SB();
  MV16(aq[0], wf[1][0], xf[1][1]); SB();
  wf[0][2] = *(LDS_AS const bf16x8*)(wr0 + 2048);
  SB();
  MV16(ak[2], wf[1][1], xf[1][2]); SB();
  wf[0][0] = *(LDS_AS const bf16x8*)(wr0 + 0);
  SB();
  MV16(av[2], wf[1][2], xf[1][2]); SB();
  xf[0][1] = *(LDS_AS const bf16x8*)(xr0 + 2048);
  SB();
  MV16(aq[1], wf[1][0], xf[1][2]); SB();
  xf[0][2] = *(LDS_AS const bf16x8*)(xr0 + 4096);
  SB();
  MV16(ak[3], wf[1][1], xf[1][3]); SB();
  xf[0][3] = *(LDS_AS const bf16x8*)(xr0 + 6144);
  SB();
  MV16(av[3], wf[1][2], xf[1][3]); SB();
  xf[0][4] = *(LDS_AS const bf16x8*)(xr0 + 8192);
  SB();
  MV16(aq[2], wf[1][0], xf[1][3]); SB();
  unsigned tl; { const int l_ = lane_now(); tl = lds0 + TB + l_ * 16; const u32x2 t_ = *(LDS_AS const u32x2*)(lds0 + TB + 6144 + cb * 512 + l_ * 8); sk0 = t_[0]; sv = t_[1];
    vS = *(LDS_AS const u32x2*)(lds0 + TB + 4096 + cb * 512 + l_ * 8); PINV(tl); }
  SB();
  MV16(ak[4], wf[1][1], xf[1][4]); SB();
  MV16(av[4], wf[1][2], xf[1][4]); SB();
  MV16(aq[3], wf[1][0], xf[1][4]); SB();
  xr1 += -79872; wr1 += -79872; asm volatile("" : "+v"(xr1), "+v"(wr1));
  MV16(ak[0], wf[0][1], xf[0][0]); SB();
  wf[1][1] = *(LDS_AS const bf16x8*)(wr1 + 1024);
  xf[1][0] = *(LDS_AS const bf16x8*)(xr1 + 0);
  wf[1][2] = *(LDS_AS const bf16x8*)(wr1 + 2048);
  SB();
  MV16(av[0], wf[0][2], xf[0][0]); SB();
  wf[1][0] = *(LDS_AS const bf16x8*)(wr1 + 0);
  xf[1][1] = *(LDS_AS const bf16x8*)(xr1 + 2048);
  xf[1][2] = *(LDS_AS const bf16x8*)(xr1 + 4096);
  SB();
  MV16(ak[1], wf[0][1], xf[0][1]); SB();
  xf[1][3] = *(LDS_AS const bf16x8*)(xr1 + 6144);
  xf[1][4] = *(LDS_AS const bf16x8*)(xr1 + 8192);
  SB();
  MV16(av[1], wf[0][2], xf[0][1]); SB();
  WAITV(0);
  fw[5][0] = tr8u(rU0 + 0, 512);
  fw[5][1] = tr8u(rU1 + 0, 512);
  fw[5][2] = tr8u(rU2 + 0, 512);
  fw[5][3] = tr8u(rU3 + 0, 512);
  WAITL(0);
  DMA2(vWa, vWb, sW, ldsU, 0, ""); sW += dW; OPQ(sW);
  SB();
  MV16(aq[0], wf[0][0], xf[0][1]); SB();
  DMA2(vWa, vWb, sW, ldsU, 2048, "");
  sk1 = sk0 ^ 32; asm volatile("" : "+v"(sk0), "+v"(sk1), "+v"(sv));
  SB();
  MV16(ak[0], wf[1][1], xf[1][0]); SB();
  WAITV1(8, f0);
  if (__ballot(f0 != nonce) != 0ull) poll1<8>(sFU, vFU, nonce);
  STAMP(44);
  { const int l_ = lane_now(); vFU = (14 + (l_ < 28 ? l_ : 27)) * FU; mq_ = l_ >= 48 ? 0u : ~0u; asm volatile("" : "+v"(vFU), "+v"(mq_)); }
  SB();
  MV16(av[0], wf[1][2], xf[1][0]); SB();
  SB();
  MV16(ak[2], wf[0][1], xf[0][2]); SB();
  SB();
  MV16(av[2], wf[0][2], xf[0][2]); SB();
  SB();
  MV16(aq[1], wf[0][0], xf[0][2]); SB();
  SB();
  MV16(ak[1], wf[1][1], xf[1][1]); SB();
  SB();
  MV16(av[1], wf[1][2], xf[1][1]); SB();
  SB();
  MV16(aq[0], wf[1][0], xf[1][1]); SB();
  SB();
  MV16(ak[3], wf[0][1], xf[0][3]); SB();
  SB();
  MV16(av[3], wf[0][2], xf[0][3]); SB();
  *(LDS_AS u32x2*)(sk0 + SK + 0) = (u32x2){pack2(ak[0][0], ak[0][1]) & kz, pack2(ak[0][2], ak[0][3]) & kz};
  SB();
  MV16(aq[2], wf[0][0], xf[0][3]); SB();
  *(LDS_AS u32x2*)(sv + 0) = (u32x2){pack2(av[0][0], av[0][1]), pack2(av[0][2], av[0][3])};
  SB();
  MV16(ak[2], wf[1][1], xf[1][2]); SB();
  t3 = *(LDS_AS const u32x4*)(tl + 3072); t2 = *(LDS_AS const u32x4*)(tl + 2048);
  SB();
  MV16(av[2], wf[1][2], xf[1][2]); SB();
  SB();
  MV16(aq[1], wf[1][0], xf[1][2]); SB();
  SB();
  MV16(ak[4], wf[0][1], xf[0][4]); SB();
  *(LDS_AS u32x2*)(sk1 + SK + 512) = (u32x2){pack2(ak[1][0], ak[1][1]), pack2(ak[1][2], ak[1][3])};
  SB();
  MV16(av[4], wf[0][2], xf[0][4]); SB();
  *(LDS_AS u32x2*)(sv + 256) = (u32x2){pack2(av[1][0], av[1][1]), pack2(av[1][2], av[1][3])};
  SB();
  MV16(aq[3], wf[0][0], xf[0][4]); SB();
  *(LDS_AS u32x2*)(sk0 + SQ + 512) = (u32x2){pack2(aq[0][0], aq[0][1]), pack2(aq[0][2], aq[0][3])};
  SB();
  MV16(ak[3], wf[1][1], xf[1][3]); SB();
  m1 = *(LDS_AS const u32x4*)(tl + 1024); cm = *(LDS_AS const f32x4*)(tl + cmo);
  SB();
  MV16(av[3], wf[1][2], xf[1][3]); SB();
  SB();
  MV16(aq[2], wf[1][0], xf[1][3]); SB();
  SB();
  MV16(ak[4], wf[1][1], xf[1][4]); SB();
  rk = (t3[2] ^ kx) + r0 * 64; rv = t3[3] + r0 * 32; asm volatile("" : "+v"(rk), "+v"(rv));
  *(LDS_AS u32x2*)(sk0 + SK + 1024) = (u32x2){pack2(ak[2][0], ak[2][1]), pack2(ak[2][2], ak[2][3])};
  SB();
  MV16(av[4], wf[1][2], xf[1][4]); SB();
  *(LDS_AS u32x2*)(sv + 512) = (u32x2){pack2(av[2][0], av[2][1]), pack2(av[2][2], av[2][3])};
  SB();
  MV16(aq[3], wf[1][0], xf[1][4]); SB();
  *(LDS_AS u32x2*)(sk1 + SQ + 1024) = (u32x2){pack2(aq[1][0], aq[1][1]), pack2(aq[1][2], aq[1][3])};
  SB();
  STAMP(36);
  *(LDS_AS u32x2*)(sk1 + SK + 1536) = (u32x2){pack2(ak[3][0], ak[3][1]), pack2(ak[3][2], ak[3][3])};
  *(LDS_AS u32x2*)(sv + 768) = (u32x2){pack2(av[3][0], av[3][1]), pack2(av[3][2], av[3][3])};
  *(LDS_AS u32x2*)(sk0 + SQ + 1536) = (u32x2){pack2(aq[2][0], aq[2][1]), pack2(aq[2][2], aq[2][3])};
  SB();
  *(LDS_AS u32x2*)(sk0 + SK + 2048) = (u32x2){pack2(ak[4][0], ak[4][1]), pack2(ak[4][2], ak[4][3])};
  *(LDS_AS u32x2*)(sv + 1024) = (u32x2){pack2(av[4][0], av[4][1]), pack2(av[4][2], av[4][3])};
  *(LDS_AS u32x2*)(sk1 + SQ + 2048) = (u32x2){pack2(aq[3][0], aq[3][1]), pack2(aq[3][2], aq[3][3])};
  SB();

  } else {
  f32x4 aq[4], ak[5], av[5];
  bf16x8 xf[2][5], wf[2][3];
  unsigned sk0, sk1, sv;
  WAITV(5);
  BAR();
  STAMP(35);
  wf[0][1] = *(LDS_AS const bf16x8*)(wr0 + 1024);
  xf[0][1] = *(LDS_AS const bf16x8*)(xr0 + 2048);
  wf[0][2] = *(LDS_AS const bf16x8*)(wr0 + 2048);
  wf[0][0] = *(LDS_AS const bf16x8*)(wr0 + 0);
  xf[0][2] = *(LDS_AS const bf16x8*)(xr0 + 4096);
  xf[0][3] = *(LDS_AS const bf16x8*)(xr0 + 6144);
  xf[0][4] = *(LDS_AS const bf16x8*)(xr0 + 8192);
  SB();
  MV16Z(ak[1], wf[0][1], xf[0][1]); SB();
  wf[1][1] = *(LDS_AS const bf16x8*)(wr1 + 1024);
  SB();
  MV16Z(av[1], wf[0][2], xf[0][1]); SB();
  xf[1][1] = *(LDS_AS const bf16x8*)(xr1 + 2048);
  SB();
  MV16Z(aq[0], wf[0][0], xf[0][1]); SB();
  wf[1][2] = *(LDS_AS const bf16x8*)(wr1 + 2048);
  SB();
  MV16Z(ak[2], wf[0][1], xf[0][2]); SB();
  wf[1][0] = *(LDS_AS const bf16x8*)(wr1 + 0);
  SB();
  MV16Z(av[2], wf[0][2], xf[0][2]); SB();
  xf[1][2] = *(LDS_AS const bf16x8*)(xr1 + 4096);
  SB();
  MV16Z(aq[1], wf[0][0], xf[0][2]); SB();
  xf[1][3] = *(LDS_AS const bf16x8*)(xr1 + 6144);
  SB();
  MV16Z(ak[3], wf[0][1], xf[0][3]); SB();
  xf[1][4] = *(LDS_AS const bf16x8*)(xr1 + 8192);
  SB();
  MV16Z(av[3], wf[0][2], xf[0][3]); SB();
  DMA1(vD[0], sDq, ldsD, 79872, "sc1");
  SB();
  MV16Z(aq[2], wf[0][0], xf[0][3]); SB();
  DMA1(vD[1], sDq, ldsD, 88064, "sc1"); sDq += WQ_S; OPQ(sDq);
  SB();
  MV16Z(ak[4], wf[0][1], xf[0][4]); SB();
  DMA1(vD[2], sDm, ldsD, 96256, "sc1"); sDm += dm; OPQ(sDm);
  SB();
  MV16Z(av[4], wf[0][2], xf[0][4]); SB();
  DMA1(vD[3], sDx, ldsD, 104448, "sc1");
  SB();
  MV16Z(aq[3], wf[0][0], xf[0][4]); SB();
  DMA1(vD[4], sDx, ldsE, 79872, "sc1"); sDx += 128; OPQ(sDx);
  SB();
  SB();
  MV16(ak[1], wf[1][1], xf[1][1]); SB();
  SB();
  MV16(av[1], wf[1][2], xf[1][1]); SB();
  WAITV(5);
  BAR();
  xr0 += 39936; wr0 += 39936; asm volatile("" : "+v"(xr0), "+v"(wr0));
  DMA1(vD[0], sDq, ldsD, 0, "sc1");
  DMA1(vD[1], sDq, ldsD, 8192, "sc1"); sDq += WQ_S; OPQ(sDq);
  DMA1(vD[2], sDm, ldsD, 16384, "sc1"); sDm += dm; OPQ(sDm);
  DMA1(vD[3], sDx, ldsD, 24576, "sc1");
  DMA1(vD[4], sDx, ldsE, 0, "sc1"); sDx += 128; OPQ(sDx);
  SB();
  MV16(aq[0], wf[1][0], xf[1][1]); SB();
  wf[0][1] = *(LDS_AS const bf16x8*)(wr0 + 1024);
  SB();
  MV16(ak[2], wf[1][1], xf[1][2]); SB();
  xf[0][1] = *(LDS_AS const bf16x8*)(xr0 + 2048);
  SB();
  MV16(av[2], wf[1][2], xf[1][2]); SB();
  wf[0][2] = *(LDS_AS const bf16x8*)(wr0 + 2048);
  SB();
  MV16(aq[1], wf[1][0], xf[1][2]); SB();
  wf[0][0] = *(LDS_AS const bf16x8*)(wr0 + 0);
  SB();
  MV16(ak[3], wf[1][1], xf[1][3]); SB();
  xf[0][2] = *(LDS_AS const bf16x8*)(xr0 + 4096);
  SB();
  MV16(av[3], wf[1][2], xf[1][3]); SB();
  xf[0][3] = *(LDS_AS const bf16x8*)(xr0 + 6144);
  SB();
  MV16(aq[2], wf[1][0], xf[1][3]); SB();
  xf[0][4] = *(LDS_AS const bf16x8*)(xr0 + 8192);
  SB();
  MV16(ak[4], wf[1][1], xf[1][4]); SB();
  MV16(av[4], wf[1][2], xf[1][4]); SB();
  MV16(aq[3], wf[1][0], xf[1][4]); SB();
  SB();
  MV16(ak[1], wf[0][1], xf[0][1]); SB();
  xr1 += 39936; wr1 += 39936; asm volatile("" : "+v"(xr1), "+v"(wr1));
  SB();
  MV16(av[1], wf[0][2], xf[0][1]); SB();
  wf[1][1] = *(LDS_AS const bf16x8*)(wr1 + 1024);
  SB();
  MV16(aq[0], wf[0][0], xf[0][1]); SB();
  xf[1][1] = *(LDS_AS const bf16x8*)(xr1 + 2048);
  SB();
  MV16(ak[2], wf[0][1], xf[0][2]); SB();
  wf[1][2] = *(LDS_AS const bf16x8*)(wr1 + 2048);
  SB();
  MV16(av[2], wf[0][2], xf[0][2]); SB();
  wf[1][0] = *(LDS_AS const bf16x8*)(wr1 + 0);
  SB();
  MV16(aq[1], wf[0][0], xf[0][2]); SB();
  xf[1][2] = *(LDS_AS const bf16x8*)(xr1 + 4096);
  SB();
  MV16(ak[3], wf[0][1], xf[0][3]); SB();
  xf[1][3] = *(LDS_AS const bf16x8*)(xr1 + 6144);
  SB();
  MV16(av[3], wf[0][2], xf[0][3]); SB();
  xf[1][4] = *(LDS_AS const bf16x8*)(xr1 + 8192);
  SB();
  MV16(aq[2], wf[0][0], xf[0][3]); SB();
  MV16(ak[4], wf[0][1], xf[0][4]); SB();
  MV16(av[4], wf[0][2], xf[0][4]); SB();
  MV16(aq[3], wf[0][0], xf[0][4]); SB();
  SB();
  MV16(ak[1], wf[1][1], xf[1][1]); SB();
  SB();
  MV16(av[1], wf[1][2], xf[1][1]); SB();
  WAITV(5);
  BAR();
  xr0 += 39936; wr0 += 39936; asm volatile("" : "+v"(xr0), "+v"(wr0));
  STAMP(60);
  DMA1(vD[0], sDq, ldsD, 39936, "sc1");
  DMA1(vD[1], sDq, ldsD, 48128, "sc1"); sDq += WQ_S; OPQ(sDq);
  DMA1(vD[2], sDm, ldsD, 56320, "sc1"); sDm += dm; OPQ(sDm);
  DMA1(vD[3], sDx, ldsD, 64512, "sc1");
  DMA1(vD[4], sDx, ldsE, 39936, "sc1"); sDx += 128; OPQ(sDx);
  SB();
  MV16(aq[0], wf[1][0], xf[1][1]); SB();
  wf[0][1] = *(LDS_AS const bf16x8*)(wr0 + 1024);
  SB();
  MV16(ak[2], wf[1][1], xf[1][2]); SB();
  xf[0][1] = *(LDS_AS const bf16x8*)(xr0 + 2048);
  SB();
  MV16(av[2], wf[1][2], xf[1][2]); SB();
  wf[0][2] = *(LDS_AS const bf16x8*)(wr0 + 2048);
  SB();
  MV16(aq[1], wf[1][0], xf[1][2]); SB();
  wf[0][0] = *(LDS_AS const bf16x8*)(wr0 + 0);
  SB();
  MV16(ak[3], wf[1][1], xf[1][3]); SB();
  xf[0][2] = *(LDS_AS const bf16x8*)(xr0 + 4096);
  SB();
  MV16(av[3], wf[1][2], xf[1][3]); SB();
  xf[0][3] = *(LDS_AS const bf16x8*)(xr0 + 6144);
  SB();
  MV16(aq[2], wf[1][0], xf[1][3]); SB();
  xf[0][4] = *(LDS_AS const bf16x8*)(xr0 + 8192);
  SB();
  MV16(ak[4], wf[1][1], xf[1][4]); SB();
  MV16(av[4], wf[1][2], xf[1][4]); SB();
  MV16(aq[3], wf[1][0], xf[1][4]); SB();
  SB();
  MV16(ak[1], wf[0][1], xf[0][1]); SB();
  xr1 += 39936; wr1 += 39936; asm volatile("" : "+v"(xr1), "+v"(wr1));
  SB();
  MV16(av[1], wf[0][2], xf[0][1]); SB();
  wf[1][1] = *(LDS_AS const bf16x8*)(wr1 + 1024);
  SB();
  MV16(aq[0], wf[0][0], xf[0][1]); SB();
  xf[1][1] = *(LDS_AS const bf16x8*)(xr1 + 2048);
  SB();
  MV16(ak[2], wf[0][1], xf[0][2]); SB();
  wf[1][2] = *(LDS_AS const bf16x8*)(wr1 + 2048);
  SB();
  MV16(av[2], wf[0][2], xf[0][2]); SB();
  wf[1][0] = *(LDS_AS const bf16x8*)(wr1 + 0);
  SB();
  MV16(aq[1], wf[0][0], xf[0][2]); SB();
  xf[1][2] = *(LDS_AS const bf16x8*)(xr1 + 4096);
  SB();
  MV16(ak[3], wf[0][1], xf[0][3]); SB();
  xf[1][3] = *(LDS_AS const bf16x8*)(xr1 + 6144);
  SB();
  MV16(av[3], wf[0][2], xf[0][3]); SB();
  xf[1][4] = *(LDS_AS const bf16x8*)(xr1 + 8192);
  SB();
  MV16(aq[2], wf[0][0], xf[0][3]); SB();
  MV16(ak[4], wf[0][1], xf[0][4]); SB();
  MV16(av[4], wf[0][2], xf[0][4]); SB();
  MV16(aq[3], wf[0][0], xf[0][4]); SB();
  SB();
  MV16(ak[1], wf[1][1], xf[1][1]); SB();
  SB();
  MV16(av[1], wf[1][2], xf[1][1]); SB();
  WAITV(5);
  BAR();
  xr0 += -79872; wr0 += -79872; asm volatile("" : "+v"(xr0), "+v"(wr0));
  DMA1(vD[0], sDq, ldsD, 79872, "sc1");
  DMA1(vD[1], sDq, ldsD, 88064, "sc1"); sDq += WQ_S; OPQ(sDq);
  DMA1(vD[2], sDm, ldsD, 96256, "sc1"); sDm += dm; OPQ(sDm);
  DMA1(vD[3], sDx, ldsD, 104448, "sc1");
  DMA1(vD[4], sDx, ldsE, 79872, "sc1"); sDx += 128; OPQ(sDx);
  SB();
  MV16(aq[0], wf[1][0], xf[1][1]); SB();
  wf[0][1] = *(LDS_AS const bf16x8*)(wr0 + 1024);
  SB();
  MV16(ak[2], wf[1][1], xf[1][2]); SB();
  xf[0][1] = *(LDS_AS const bf16x8*)(xr0 + 2048);
  SB();
  MV16(av[2], wf[1][2], xf[1][2]); SB();
  wf[0][2] = *(LDS_AS const bf16x8*)(wr0 + 2048);
  SB();
  MV16(aq[1], wf[1][0], xf[1][2]); SB();
  wf[0][0] = *(LDS_AS const bf16x8*)(wr0 + 0);
  SB();
  MV16(ak[3], wf[1][1], xf[1][3]); SB();
  xf[0][2] = *(LDS_AS const bf16x8*)(xr0 + 4096);
  SB();
  MV16(av[3], wf[1][2], xf[1][3]); SB();
  xf[0][3] = *(LDS_AS const bf16x8*)(xr0 + 6144);
  SB();
  MV16(aq[2], wf[1][0], xf[1][3]); SB();
  xf[0][4] = *(LDS_AS const bf16x8*)(xr0 + 8192);
  SB();
  MV16(ak[4], wf[1][1], xf[1][4]); SB();
  MV16(av[4], wf[1][2], xf[1][4]); SB();
  MV16(aq[3], wf[1][0], xf[1][4]); SB();
  SB();
  MV16(ak[1], wf[0][1], xf[0][1]); SB();
  xr1 += -79872; wr1 += -79872; asm volatile("" : "+v"(xr1), "+v"(wr1));
  SB();
  MV16(av[1], wf[0][2], xf[0][1]); SB();
  wf[1][1] = *(LDS_AS const bf16x8*)(wr1 + 1024);
  SB();
  MV16(aq[0], wf[0][0], xf[0][1]); SB();
  xf[1][1] = *(LDS_AS const bf16x8*)(xr1 + 2048);
  SB();
  MV16(ak[2], wf[0][1], xf[0][2]); SB();
  wf[1][2] = *(LDS_AS const bf16x8*)(wr1 + 2048);
  SB();
  MV16(av[2], wf[0][2], xf[0][2]); SB();
  wf[1][0] = *(LDS_AS const bf16x8*)(wr1 + 0);
  SB();
  MV16(aq[1], wf[0][0], xf[0][2]); SB();
  xf[1][2] = *(LDS_AS const bf16x8*)(xr1 + 4096);
  SB();
  MV16(ak[3], wf[0][1], xf[0][3]); SB();
  xf[1][3] = *(LDS_AS const bf16x8*)(xr1 + 6144);
  SB();
  MV16(av[3], wf[0][2], xf[0][3]); SB();
  xf[1][4] = *(LDS_AS const bf16x8*)(xr1 + 8192);
  SB();
  MV16(aq[2], wf[0][0], xf[0][3]); SB();
  MV16(ak[4], wf[0][1], xf[0][4]); SB();
  MV16(av[4], wf[0][2], xf[0][4]); SB();
  MV16(aq[3], wf[0][0], xf[0][4]); SB();
  SB();
  MV16(ak[1], wf[1][1], xf[1][1]); SB();
  SB();
  MV16(av[1], wf[1][2], xf[1][1]); SB();
  WAITV(5);
  BAR();
  xr0 += 39936; wr0 += 39936; asm volatile("" : "+v"(xr0), "+v"(wr0));
  STAMP(61);
  DMA1(vD[0], sDq, ldsD, 0, "sc1");
  DMA1(vD[1], sDq, ldsD, 8192, "sc1"); sDq += WQ_S; OPQ(sDq);
  DMA1(vD[2], sDm, ldsD, 16384, "sc1"); sDm += dm; OPQ(sDm);
  DMA1(vD[3], sDx, ldsD, 24576, "sc1");
  DMA1(vD[4], sDx, ldsE, 0, "sc1"); sDx += 128; OPQ(sDx);
  SB();
  MV16(aq[0], wf[1][0], xf[1][1]); SB();
  wf[0][1] = *(LDS_AS const bf16x8*)(wr0 + 1024);
  SB();
  MV16(ak[2], wf[1][1], xf[1][2]); SB();
  xf[0][1] = *(LDS_AS const bf16x8*)(xr0 + 2048);
  SB();
  MV16(av[2], wf[1][2], xf[1][2]); SB();
  wf[0][2] = *(LDS_AS const bf16x8*)(wr0 + 2048);
  SB();
  MV16(aq[1], wf[1][0], xf[1][2]); SB();
  wf[0][0] = *(LDS_AS const bf16x8*)(wr0 + 0);
  SB();
  MV16(ak[3], wf[1][1], xf[1][3]); SB();
  xf[0][2] = *(LDS_AS const bf16x8*)(xr0 + 4096);
  SB();
  MV16(av[3], wf[1][2], xf[1][3]); SB();
  xf[0][3] = *(LDS_AS const bf16x8*)(xr0 + 6144);
  SB();
  MV16(aq[2], wf[1][0], xf[1][3]); SB();
  xf[0][4] = *(LDS_AS const bf16x8*)(xr0 + 8192);
  SB();
  MV16(ak[4], wf[1][1], xf[1][4]); SB();
  MV16(av[4], wf[1][2], xf[1][4]); SB();
  MV16(aq[3], wf[1][0], xf[1][4]); SB();
  SB();
  MV16(ak[1], wf[0][1], xf[0][1]); SB();
  xr1 += 39936; wr1 += 39936; asm volatile("" : "+v"(xr1), "+v"(wr1));
  SB();
  MV16(av[1], wf[0][2], xf[0][1]); SB();
  wf[1][1] = *(LDS_AS const bf16x8*)(wr1 + 1024);
  SB();
  MV16(aq[0], wf[0][0], xf[0][1]); SB();
  xf[1][1] = *(LDS_AS const bf16x8*)(xr1 + 2048);
  SB();
  MV16(ak[2], wf[0][1], xf[0][2]); SB();
  wf[1][2] = *(LDS_AS const bf16x8*)(wr1 + 2048);
  SB();
  MV16(av[2], wf[0][2], xf[0][2]); SB();
  wf[1][0] = *(LDS_AS const bf16x8*)(wr1 + 0);
  SB();
  MV16(aq[1], wf[0][0], xf[0][2]); SB();
  xf[1][2] = *(LDS_AS const bf16x8*)(xr1 + 4096);
  SB();
  MV16(ak[3], wf[0][1], xf[0][3]); SB();
  xf[1][3] = *(LDS_AS const bf16x8*)(xr1 + 6144);
  SB();
  MV16(av[3], wf[0][2], xf[0][3]); SB();
  xf[1][4] = *(LDS_AS const bf16x8*)(xr1 + 8192);
  SB();
  MV16(aq[2], wf[0][0], xf[0][3]); SB();
  MV16(ak[4], wf[0][1], xf[0][4]); SB();
  MV16(av[4], wf[0][2], xf[0][4]); SB();
  MV16(aq[3], wf[0][0], xf[0][4]); SB();
  SB();
  MV16(ak[1], wf[1][1], xf[1][1]); SB();
  SB();
  MV16(av[1], wf[1][2], xf[1][1]); SB();
  WAITV(5);
  BAR();
  xr0 += 39936; wr0 += 39936; asm volatile("" : "+v"(xr0), "+v"(wr0));
  DMA2(vWa, vWb, sW, ldsU, 0, ""); sW += dW; OPQ(sW);
  DMA2(vWa, vWb, sW, ldsU, 2048, ""); sW = wrap == 5 ? bW : sW + dW; OPQ(sW);
  SB();
  MV16(aq[0], wf[1][0], xf[1][1]); SB();
  wf[0][1] = *(LDS_AS const bf16x8*)(wr0 + 1024);
  SB();
  MV16(ak[2], wf[1][1], xf[1][2]); SB();
  xf[0][1] = *(LDS_AS const bf16x8*)(xr0 + 2048);
  SB();
  MV16(av[2], wf[1][2], xf[1][2]); SB();
  wf[0][2] = *(LDS_AS const bf16x8*)(wr0 + 2048);
  SB();
  MV16(aq[1], wf[1][0], xf[1][2]); SB();
  wf[0][0] = *(LDS_AS const bf16x8*)(wr0 + 0);
  SB();
  MV16(ak[3], wf[1][1], xf[1][3]); SB();
  xf[0][2] = *(LDS_AS const bf16x8*)(xr0 + 4096);
  SB();
  MV16(av[3], wf[1][2], xf[1][3]); SB();
  xf[0][3] = *(LDS_AS const bf16x8*)(xr0 + 6144);
  SB();
  MV16(aq[2], wf[1][0], xf[1][3]); SB();
  xf[0][4] = *(LDS_AS const bf16x8*)(xr0 + 8192);
  SB();
  MV16(ak[4], wf[1][1], xf[1][4]); SB();
  unsigned f0; { const int l_ = lane_now(); vFU = (l_ < 14 ? l_ : 13) * FU; PINV(vFU); }
  LD4(f0, vFU, sFU, 0, "sc1");
  SB();
  MV16(av[4], wf[1][2], xf[1][4]); SB();
  MV16(aq[3], wf[1][0], xf[1][4]); SB();
  SB();
  MV16(ak[1], wf[0][1], xf[0][1]); SB();
  xr1 += 39936; wr1 += 39936; asm volatile("" : "+v"(xr1), "+v"(wr1));
  SB();
  MV16(av[1], wf[0][2], xf[0][1]); SB();
  wf[1][1] = *(LDS_AS const bf16x8*)(wr1 + 1024);
  SB();
  MV16(aq[0], wf[0][0], xf[0][1]); SB();
  xf[1][1] = *(LDS_AS const bf16x8*)(xr1 + 2048);
  SB();
  MV16(ak[2], wf[0][1], xf[0][2]); SB();
  wf[1][2] = *(LDS_AS const bf16x8*)(wr1 + 2048);
  SB();
  MV16(av[2], wf[0][2], xf[0][2]); SB();
  wf[1][0] = *(LDS_AS const bf16x8*)(wr1 + 0);
  SB();
  MV16(aq[1], wf[0][0], xf[0][2]); SB();
  xf[1][2] = *(LDS_AS const bf16x8*)(xr1 + 4096);
  SB();
  MV16(ak[3], wf[0][1], xf[0][3]); SB();
  xf[1][3] = *(LDS_AS const bf16x8*)(xr1 + 6144);
  SB();
  MV16(av[3], wf[0][2], xf[0][3]); SB();
  xf[1][4] = *(LDS_AS const bf16x8*)(xr1 + 8192);
  SB();
  MV16(aq[2], wf[0][0], xf[0][3]); SB();
  MV16(ak[4], wf[0][1], xf[0][4]); SB();
  MV16(av[4], wf[0][2], xf[0][4]); SB();
  MV16(aq[3], wf[0][0], xf[0][4]); SB();
  SB();
  MV16(ak[1], wf[1][1], xf[1][1]); SB();
  SB();
  MV16(av[1], wf[1][2], xf[1][1]); SB();
  WAITV(5);
  BAR();
  xr0 += -79872; wr0 += -79872; asm volatile("" : "+v"(xr0), "+v"(wr0));
  STAMP(62);
  WAITV(1);
  fw[5][0] = tr8u(rU0 + 0, 512);
  fw[5][1] = tr8u(rU1 + 0, 512);
  fw[5][2] = tr8u(rU2 + 0, 512);
  fw[5][3] = tr8u(rU3 + 0, 512);
  WAITL(0);
  DMA2(vWa, vWb, sW, ldsU, 0, ""); sW += dW; OPQ(sW);
  DMA2(vWa, vWb, sW, ldsU, 2048, "");
  SB();
  MV16(aq[0], wf[1][0], xf[1][1]); SB();
  wf[0][1] = *(LDS_AS const bf16x8*)(wr0 + 1024);
  SB();
  MV16(ak[2], wf[1][1], xf[1][2]); SB();
  xf[0][1] = *(LDS_AS const bf16x8*)(xr0 + 2048);
  SB();
  MV16(av[2], wf[1][2], xf[1][2]); SB();
  wf[0][2] = *(LDS_AS const bf16x8*)(wr0 + 2048);
  SB();
  MV16(aq[1], wf[1][0], xf[1][2]); SB();
  wf[0][0] = *(LDS_AS const bf16x8*)(wr0 + 0);
  SB();
  MV16(ak[3], wf[1][1], xf[1][3]); SB();
  xf[0][2] = *(LDS_AS const bf16x8*)(xr0 + 4096);
  SB();
  MV16(av[3], wf[1][2], xf[1][3]); SB();
  xf[0][3] = *(LDS_AS const bf16x8*)(xr0 + 6144);
  SB();
  MV16(aq[2], wf[1][0], xf[1][3]); SB();
  xf[0][4] = *(LDS_AS const bf16x8*)(xr0 + 8192);
  SB();
  MV16(ak[4], wf[1][1], xf[1][4]); SB();
  unsigned tl; { const int l_ = lane_now(); tl = lds0 + TB + l_ * 16; const u32x2 t_ = *(LDS_AS const u32x2*)(lds0 + TB + 6144 + cb * 512 + l_ * 8); sk0 = t_[0]; sv = t_[1];
    vS = *(LDS_AS const u32x2*)(lds0 + TB + 4096 + cb * 512 + l_ * 8); PINV(tl); }
  SB();
  MV16(av[4], wf[1][2], xf[1][4]); SB();
  MV16(aq[3], wf[1][0], xf[1][4]); SB();
  xr1 += -79872; wr1 += -79872; asm volatile("" : "+v"(xr1), "+v"(wr1));
  MV16(ak[1], wf[0][1], xf[0][1]); SB();
  wf[1][1] = *(LDS_AS const bf16x8*)(wr1 + 1024);
  xf[1][1] = *(LDS_AS const bf16x8*)(xr1 + 2048);
  SB();
  MV16(av[1], wf[0][2], xf[0][1]); SB();
  wf[1][2] = *(LDS_AS const bf16x8*)(wr1 + 2048);
  SB();
  MV16(aq[0], wf[0][0], xf[0][1]); SB();
  wf[1][0] = *(LDS_AS const bf16x8*)(wr1 + 0);
  SB();
  MV16(ak[2], wf[0][1], xf[0][2]); SB();
  xf[1][2] = *(LDS_AS const bf16x8*)(xr1 + 4096);
  xf[1][3] = *(LDS_AS const bf16x8*)(xr1 + 6144);
  SB();
  MV16(av[2], wf[0][2], xf[0][2]); SB();
  xf[1][4] = *(LDS_AS const bf16x8*)(xr1 + 8192);
  SB();
  MV16(aq[1], wf[0][0], xf[0][2]); SB();
  sk1 = sk0 ^ 32; asm volatile("" : "+v"(sk0), "+v"(sk1), "+v"(sv));
  SB();
  MV16(ak[1], wf[1][1], xf[1][1]); SB();
  WAITV1(4, f0);
  if (__ballot(f0 != nonce) != 0ull) poll1<8>(sFU, vFU, nonce);
  STAMP(44);
  { const int l_ = lane_now(); vFU = (14 + (l_ < 28 ? l_ : 27)) * FU; mq_ = l_ >= 48 ? 0u : ~0u; asm volatile("" : "+v"(vFU), "+v"(mq_)); }
  SB();
  MV16(av[1], wf[1][2], xf[1][1]); SB();
  SB();
  MV16(aq[0], wf[1][0], xf[1][1]); SB();
  SB();
  MV16(ak[3], wf[0][1], xf[0][3]); SB();
  SB();
  MV16(av[3], wf[0][2], xf[0][3]); SB();
  SB();
  MV16(aq[2], wf[0][0], xf[0][3]); SB();
  SB();
  MV16(ak[2], wf[1][1], xf[1][2]); SB();
  t3 = *(LDS_AS const u32x4*)(tl + 3072); t2 = *(LDS_AS const u32x4*)(tl + 2048);
  SB();
  MV16(av[2], wf[1][2], xf[1][2]); SB();
  SB();
  MV16(aq[1], wf[1][0], xf[1][2]); SB();
  SB();
  MV16(ak[4], wf[0][1], xf[0][4]); SB();
  *(LDS_AS u32x2*)(sk1 + SK + 2560) = (u32x2){pack2(ak[1][0], ak[1][1]), pack2(ak[1][2], ak[1][3])};
  SB();
  MV16(av[4], wf[0][2], xf[0][4]); SB();
  *(LDS_AS u32x2*)(sv + 1280) = (u32x2){pack2(av[1][0], av[1][1]), pack2(av[1][2], av[1][3])};
  SB();
  MV16(aq[3], wf[0][0], xf[0][4]); SB();
  *(LDS_AS u32x2*)(sk0 + SQ + 2560) = (u32x2){pack2(aq[0][0], aq[0][1]), pack2(aq[0][2], aq[0][3])};
  SB();
  MV16(ak[3], wf[1][1], xf[1][3]); SB();
  m1 = *(LDS_AS const u32x4*)(tl + 1024); cm = *(LDS_AS const f32x4*)(tl + cmo);
  SB();
  MV16(av[3], wf[1][2], xf[1][3]); SB();
  SB();
  MV16(aq[2], wf[1][0], xf[1][3]); SB();
  SB();
  MV16(ak[4], wf[1][1], xf[1][4]); SB();
  rk = (t3[2] ^ kx) + r0 * 64; rv = t3[3] + r0 * 32; asm volatile("" : "+v"(rk), "+v"(rv));
  *(LDS_AS u32x2*)(sk0 + SK + 3072) = (u32x2){pack2(ak[2][0], ak[2][1]), pack2(ak[2][2], ak[2][3])};
  SB();
  MV16(av[4], wf[1][2], xf[1][4]); SB();
  *(LDS_AS u32x2*)(sv + 1536) = (u32x2){pack2(av[2][0], av[2][1]), pack2(av[2][2], av[2][3])};
  SB();
  MV16(aq[3], wf[1][0], xf[1][4]); SB();
  *(LDS_AS u32x2*)(sk1 + SQ + 3072) = (u32x2){pack2(aq[1][0], aq[1][1]), pack2(aq[1][2], aq[1][3])};
  SB();
  STAMP(36);
  *(LDS_AS u32x2*)(sk1 + SK + 3584) = (u32x2){pack2(ak[3][0], ak[3][1]), pack2(ak[3][2], ak[3][3])};
  *(LDS_AS u32x2*)(sv + 1792) = (u32x2){pack2(av[3][0], av[3][1]), pack2(av[3][2], av[3][3])};
  *(LDS_AS u32x2*)(sk0 + SQ + 3584) = (u32x2){pack2(aq[2][0], aq[2][1]), pack2(aq[2][2], aq[2][3])};
  SB();
  *(LDS_AS u32x2*)(sk0 + SK + 4096) = (u32x2){pack2(ak[4][0], ak[4][1]), pack2(ak[4][2], ak[4][3])};
  *(LDS_AS u32x2*)(sv + 2048) = (u32x2){pack2(av[4][0], av[4][1]), pack2(av[4][2], av[4][3])};
  *(LDS_AS u32x2*)(sk1 + SQ + 4096) = (u32x2){pack2(aq[3][0], aq[3][1]), pack2(aq[3][2], aq[3][3])};
  SB();

  }
  bf16x8 p[10]; unsigned fu;
  const unsigned vP0 = t2[0], vP1 = t2[1], vP2 = t2[2], vP3 = t2[3], vS0 = vS[0], vS1 = vS[1], mp[2] = {t3[0], t3[1]};
  PRIO(3);
  BAR();
  STAMP(37);
  u32x4 qf[2]; bf16x8 kf[2][2], vf[2][2];
  qf[0] = *(LDS_AS const u32x4*)(rk + SQ + 512); kf[0][0] = *(LDS_AS const bf16x8*)(rk + SK); kf[0][1] = *(LDS_AS const bf16x8*)(rk + SK + 1024);
  qf[1] = *(LDS_AS const u32x4*)(rk + SQ + 512 + SKH); kf[1][0] = *(LDS_AS const bf16x8*)(rk + SK + SKH); kf[1][1] = *(LDS_AS const bf16x8*)(rk + SK + SKH + 1024);
  SB();
  vf[0][0] = tr8u(rv, 512); vf[1][0] = tr8u(rv + 2 * SVB, 512); vf[0][1] = tr8u(rv + SVB, 512); vf[1][1] = tr8u(rv + 3 * SVB, 512);
  u32x4 t7; float bias[4];
  { const int l_ = lane_now(); t7 = *(LDS_AS const u32x4*)(lds0 + TB + 9216 + l_ * 16); bias[0] = bias[1] = bias[2] = bias[3] = *(LDS_AS const float*)(bio + (l_ >> 3) * 4); }
  SB();
  SB();
  LD16(p[0], vP0, sP, 0, "sc1");
  LD4(fu, vFU, sFU, 0, "sc1");
  SB();
  f32x4 sc[2][2];
  const f32x4 z4 = {0.f, 0.f, 0.f, 0.f};
  qf[0][2] &= mq_; qf[0][3] &= mq_; qf[1][2] &= mq_; qf[1][3] &= mq_;
  sc[0][0] = MFMA16(kf[0][0], __builtin_bit_cast(bf16x8, qf[0]), z4); sc[0][1] = MFMA16(kf[0][1], __builtin_bit_cast(bf16x8, qf[0]), z4);
  sc[1][0] = MFMA16(kf[1][0], __builtin_bit_cast(bf16x8, qf[1]), z4); sc[1][1] = MFMA16(kf[1][1], __builtin_bit_cast(bf16x8, qf[1]), z4);
  SB();
  LD16(p[1], vP1, sP, 0, "sc1");
  LD16(p[2], vP2, sP, 0, "sc1");
  SB();
  f32x2_t lg[2][2];
  const f32x2_t scale = {0.18898223650461362f, 0.18898223650461362f};
#pragma unroll
  for (int h = 0; h < 2; ++h) {
    lg[h][0] = (f32x2_t){SEL(h, 0), SEL(h, 1)}; lg[h][1] = (f32x2_t){SEL(h, 2), SEL(h, 3)};
    lg[h][0] = rbf2(__builtin_elementwise_fma(rbf2(lg[h][0]), scale, (f32x2_t){cm[0], cm[1]}));
    lg[h][1] = rbf2(__builtin_elementwise_fma(rbf2(lg[h][1]), scale, (f32x2_t){cm[2], cm[3]}));
  } asm volatile("" : "+v"(lg[0][0]), "+v"(lg[0][1]), "+v"(lg[1][0]), "+v"(lg[1][1]));
  SB();
  LD16(p[3], vP3, sP, 0, "sc1"); sP += 5120; OPQ(sP);
  LD16(p[4], vP0, sP, 0, "sc1");
  SB();
  f32x2_t mx = xbcast2(xmax2(fmx(fmx(lg[0][0][0], lg[0][0][1]), fmx(lg[0][1][0], lg[0][1][1])), fmx(fmx(lg[1][0][0], lg[1][0][1]), fmx(lg[1][1][0], lg[1][1][1])))); PINV(mx);
  SB();
  LD16(p[5], vP1, sP, 0, "sc1");
  LD16(p[6], vP2, sP, 0, "sc1");
  SB();
  #pragma unroll
  for (int h = 0; h < 2; ++h)
#pragma unroll
    for (int j = 0; j < 2; ++j) {
      const f32x2_t z = (lg[h][j] - (f32x2_t){mx[h], mx[h]}) * (f32x2_t){1.442695041f, 1.442695041f};
      lg[h][j] = (f32x2_t){__builtin_amdgcn_exp2f(z[0]), __builtin_amdgcn_exp2f(z[1])};
    } asm volatile("" : "+v"(lg[0][0]), "+v"(lg[0][1]), "+v"(lg[1][0]), "+v"(lg[1][1]));
  SB();
  SB();
  const f32x2_t e0 = lg[0][0] + lg[0][1], e1 = lg[1][0] + lg[1][1];
  f32x2_t rs = xbcast2(__builtin_amdgcn_rcpf(xsum2(e0[0] + e0[1], e1[0] + e1[1]))); PINV(rs);
  SB();
  SB();
  u32x4 pf[2];
#pragma unroll
  for (int h = 0; h < 2; ++h) {
    const f32x2_t pa = lg[h][0] * (f32x2_t){rs[h], rs[h]}, pb = lg[h][1] * (f32x2_t){rs[h], rs[h]};
    const unsigned wa = pack2(pa[0], pa[1]), wb = pack2(pb[0], pb[1]);
    pf[h] = (u32x4){wa & ~mp[0], wb & ~mp[1], wa & mp[0], wb & mp[1]};
  }
  const f32x4 oa0 = MFMA16(vf[0][0], __builtin_bit_cast(bf16x8, pf[0]), z4), ob0 = MFMA16(vf[1][0], __builtin_bit_cast(bf16x8, pf[1]), z4);
  const f32x4 oa1 = MFMA16(vf[0][1], __builtin_bit_cast(bf16x8, pf[0]), z4), ob1 = MFMA16(vf[1][1], __builtin_bit_cast(bf16x8, pf[1]), z4);
  SB();
  SB();
  ST16(vS0, sO, 0, ((u32x4){pack2(oa0[0], oa0[1]), pack2(oa0[2], oa0[3]), pack2(ob0[0], ob0[1]), pack2(ob0[2], ob0[3])}));
  ST16L48(vS1, sO, 0, ((u32x4){pack2(oa1[0], oa1[1]), pack2(oa1[2], oa1[3]), pack2(ob1[0], ob1[1]), pack2(ob1[2], ob1[3])}));
  STAMP(38);
  WAITV1(8, fu);
  if (__ballot(fu != nonce) != 0ull) { WAITV(0); { const unsigned l_ = lane_now() * 16; ST16X(15, l_, sFO, nonce); } poll1<2>(sFU, vFU, nonce); }
  STAMP(51);
  LD16(p[7], vP3, sP, 0, "sc1"); sP += 5120; OPQ(sP);
  LD16(p[8], vP0, sP, 0, "sc1");
  WAITV(12);
  fw[6][0] = tr8u(rU0 + 0, 512);
  fw[6][1] = tr8u(rU1 + 0, 512);
  fw[6][2] = tr8u(rU2 + 0, 512);
  fw[6][3] = tr8u(rU3 + 0, 512);
  WAITV(2);
  { const unsigned l_ = lane_now() * 16; ST16X(15, l_, sFO, nonce); }
  STAMP(39);
  unsigned wa0 = ldsP + t7[0], wa1 = wa0 ^ 64, ra = rao + t7[1], rah = ra + 65536, fs, vFS;
  const unsigned vOut = t7[2], vSt = t7[3];
  asm volatile("" : "+v"(wa0), "+v"(wa1), "+v"(ra), "+v"(rah));
  f32x4 acc[8], t4[8]; float gy[4][4], zz[4];
  u32x4 xv[2], sv4[2];
  SB();
  WAITVB(12); SB();
  STAMP(45);
  PRIO(2); SB();
  MV16Z(acc[0], p[0], fw[0][0]); SB();
  LD16(p[9], vP1, sP, 0, "sc1");
  SB();
  MV16Z(acc[1], p[0], fw[0][1]); SB();
  MV16Z(acc[2], p[0], fw[0][2]); SB();
  MV16Z(acc[3], p[0], fw[0][3]); SB();
  SB();
  WAITVB(11); SB();
  MV16(acc[0], p[1], fw[1][0]); SB();
  LD16(p[0], vP2, sP, 0, "sc1");
  SB();
  MV16(acc[1], p[1], fw[1][1]); SB();
  MV16(acc[2], p[1], fw[1][2]); SB();
  MV16(acc[3], p[1], fw[1][3]); SB();
  SB();
  WAITVB(11); SB();
  MV16(acc[0], p[2], fw[2][0]); SB();
  LD16(p[1], vP3, sP, 0, "sc1"); sP += 5120; OPQ(sP);
  SB();
  MV16(acc[1], p[2], fw[2][1]); SB();
  MV16(acc[2], p[2], fw[2][2]); SB();
  MV16(acc[3], p[2], fw[2][3]); SB();
  SB();
  WAITVB(11); SB();
  MV16(acc[0], p[3], fw[3][0]); SB();
  LD16(p[2], vP0, sP, 0, "sc1");
  SB();
  MV16(acc[1], p[3], fw[3][1]); SB();
  MV16(acc[2], p[3], fw[3][2]); SB();
  MV16(acc[3], p[3], fw[3][3]); SB();
  SB();
  WAITVB(11); SB();
  MV16(acc[0], p[4], fw[4][0]); SB();
  LD16(p[3], vP1, sP, 0, "sc1");
  SB();
  MV16(acc[1], p[4], fw[4][1]); SB();
  MV16(acc[2], p[4], fw[4][2]); SB();
  MV16(acc[3], p[4], fw[4][3]); SB();
  SB();
  WAITVB(11); SB();
  MV16(acc[0], p[5], fw[5][0]); SB();
  LD16(p[4], vP2, sP, 0, "sc1");
  SB();
  MV16(acc[1], p[5], fw[5][1]); SB();
  { const int l_ = lane_now(); vFU = (42 + (l_ < 28 ? l_ : 27)) * FU; PINV(vFU); }
  LD4(fu, vFU, sFU, 0, "sc1");
  SB();
  MV16(acc[2], p[5], fw[5][2]); SB();
  MV16(acc[3], p[5], fw[5][3]); SB();
  SB();
  WAITVB(12); SB();
  MV16(acc[0], p[6], fw[6][0]); SB();
  LD16(p[5], vP3, sP, 0, "sc1"); sP += 5120; OPQ(sP);
  SB();
  MV16(acc[1], p[6], fw[6][1]); SB();
  BAR0();
  SB();
  MV16(acc[2], p[6], fw[6][2]); SB();
  MV16(acc[3], p[6], fw[6][3]); SB();
  SB();
  WAITVB(10); SB();
  PRIO(1); SB();
  MV16Z(acc[4], p[7], fw[0][0]); SB();
  LD16(p[6], vP0, sP, 0, "sc1");
  SB();
  MV16Z(acc[5], p[7], fw[0][1]); SB();
  MV16Z(acc[6], p[7], fw[0][2]); SB();
  MV16Z(acc[7], p[7], fw[0][3]); SB();
  SB();
  WAITVB(10); SB();
  MV16(acc[4], p[8], fw[1][0]); SB();
  LD16(p[7], vP1, sP, 0, "sc1");
  SB();
  MV16(acc[5], p[8], fw[1][1]); SB();
  *(LDS_AS f32x4*)(wa0 + 0) = acc[0];
  SB();
  MV16(acc[6], p[8], fw[1][2]); SB();
  *(LDS_AS f32x4*)(wa0 + 2048) = acc[1];
  SB();
  MV16(acc[7], p[8], fw[1][3]); SB();
  SB();
  WAITVB(9); SB();
  PRIO(0); SB();
  MV16(acc[4], p[9], fw[2][0]); SB();
  LD16(p[8], vP2, sP, 0, "sc1");
  SB();
  MV16(acc[5], p[9], fw[2][1]); SB();
  *(LDS_AS f32x4*)(wa0 + 4096) = acc[2];
  SB();
  MV16(acc[6], p[9], fw[2][2]); SB();
  *(LDS_AS f32x4*)(wa0 + 6144) = acc[3];
  SB();
  MV16(acc[7], p[9], fw[2][3]); SB();
  SB();
  WAITVB(9); SB();
  MV16(acc[4], p[0], fw[3][0]); SB();
  LD16(p[9], vP3, sP, 0, "sc1"); sP += 5120; OPQ(sP);
  SB();
  MV16(acc[5], p[0], fw[3][1]); SB();
  BAR();
  STAMP(46);
  SB();
  MV16(acc[6], p[0], fw[3][2]); SB();
  MV16(acc[7], p[0], fw[3][3]); SB();
  SB();
  WAITVB(9); SB();
  PRIO(3); SB();
  MV16(acc[4], p[1], fw[4][0]); SB();
  LD16(p[0], vP0, sP, 0, "sc1");
  t4[0] = *(LDS_AS const f32x4*)(ra + 0);
  SB();
  MV16(acc[5], p[1], fw[4][1]); SB();
  t4[1] = *(LDS_AS const f32x4*)(ra + 16384);
  SB();
  MV16(acc[6], p[1], fw[4][2]); SB();
  t4[2] = *(LDS_AS const f32x4*)(ra + 32768);
  SB();
  MV16(acc[7], p[1], fw[4][3]); SB();
  t4[3] = *(LDS_AS const f32x4*)(ra + 49152);
  SB();
  SB();
  WAITVB(9); SB();
  MV16(acc[4], p[2], fw[5][0]); SB();
  WAITV1(6, fu);
  if (__ballot(fu != nonce) != 0ull) poll1<8>(sFU, vFU, nonce);
  STAMP(53);
  LD16(p[1], vP1, sP, 0, "sc1");
  t4[4] = *(LDS_AS const f32x4*)(rah + 0);
  SB();
  MV16(acc[5], p[2], fw[5][1]); SB();
  t4[5] = *(LDS_AS const f32x4*)(rah + 16384);
  SB();
  MV16(acc[6], p[2], fw[5][2]); SB();
  t4[6] = *(LDS_AS const f32x4*)(rah + 32768);
  SB();
  MV16(acc[7], p[2], fw[5][3]); SB();
  t4[7] = *(LDS_AS const f32x4*)(rah + 49152);
  SB();
  SB();
  WAITVB(9); SB();
  MV16(acc[4], p[3], fw[6][0]); SB();
  LD16(p[2], vP2, sP, 0, "sc1");
  SB();
  MV16(acc[5], p[3], fw[6][1]); SB();
  OP4_3("v_add_f32", gy[3], t4[0], bias);
  SB();
  MV16(acc[6], p[3], fw[6][2]); SB();
  MV16(acc[7], p[3], fw[6][3]); SB();
  SB();
  WAITVB(9); SB();
  MV16Z(acc[0], p[4], fw[0][0]); SB();
  LD16(p[3], vP3, sP, 0, "sc1"); sP += 5120; OPQ(sP);
  SB();
  MV16Z(acc[1], p[4], fw[0][1]); SB();
  OP4_3("v_add_f32", zz, t4[1], t4[2]);
  SB();
  MV16Z(acc[2], p[4], fw[0][2]); SB();
  MV16Z(acc[3], p[4], fw[0][3]); SB();
  SB();
  WAITVB(8); SB();
  MV16(acc[0], p[5], fw[1][0]); SB();
  LD16(p[4], vP0, sP, 0, "sc1");
  SB();
  MV16(acc[1], p[5], fw[1][1]); SB();
  *(LDS_AS f32x4*)(wa0 + 8192) = acc[4];
  SB();
  MV16(acc[2], p[5], fw[1][2]); SB();
  ACC4("v_add_f32", gy[3], t4[3]);
  SB();
  MV16(acc[3], p[5], fw[1][3]); SB();
  SB();
  WAITVB(8); SB();
  PRIO(2); SB();
  MV16(acc[0], p[6], fw[2][0]); SB();
  LD16(p[5], vP1, sP, 0, "sc1");
  SB();
  MV16(acc[1], p[6], fw[2][1]); SB();
  *(LDS_AS f32x4*)(wa0 + 10240) = acc[5];
  SB();
  MV16(acc[2], p[6], fw[2][2]); SB();
  ACC4("v_add_f32", zz, t4[4]);
  SB();
  MV16(acc[3], p[6], fw[2][3]); SB();
  SB();
  WAITVB(8); SB();
  MV16(acc[0], p[7], fw[3][0]); SB();
  LD16(p[6], vP2, sP, 0, "sc1");
  SB();
  MV16(acc[1], p[7], fw[3][1]); SB();
  *(LDS_AS f32x4*)(wa0 + 12288) = acc[6];
  SB();
  MV16(acc[2], p[7], fw[3][2]); SB();
  ACC4("v_add_f32", gy[3], t4[5]);
  SB();
  MV16(acc[3], p[7], fw[3][3]); SB();
  SB();
  WAITVB(8); SB();
  MV16(acc[0], p[8], fw[4][0]); SB();
  LD16(p[7], vP3, sP, 0, "sc1"); sP += 5120; OPQ(sP);
  SB();
  MV16(acc[1], p[8], fw[4][1]); SB();
  *(LDS_AS f32x4*)(wa0 + 14336) = acc[7];
  SB();
  MV16(acc[2], p[8], fw[4][2]); SB();
  ACC4("v_add_f32", zz, t4[6]);
  SB();
  MV16(acc[3], p[8], fw[4][3]); SB();
  SB();
  WAITVB(8); SB();
  MV16(acc[0], p[9], fw[5][0]); SB();
  LD16(p[8], vP0, sP, 0, "sc1");
  SB();
  MV16(acc[1], p[9], fw[5][1]); SB();
  { const int l_ = lane_now(); vFU = (70 + (l_ < 28 ? l_ : 27)) * FU; PINV(vFU); }
  LD4(fu, vFU, sFU, 0, "sc1");
  SB();
  MV16(acc[2], p[9], fw[5][2]); SB();
  ACC4("v_add_f32", gy[3], t4[7]);
  SB();
  MV16(acc[3], p[9], fw[5][3]); SB();
  SB();
  WAITVB(9); SB();
  MV16(acc[0], p[0], fw[6][0]); SB();
  LD16(p[9], vP1, sP, 0, "sc1");
  SB();
  MV16(acc[1], p[0], fw[6][1]); SB();
  ACC4("v_add_f32", gy[3], zz);
  SB();
  MV16(acc[2], p[0], fw[6][2]); SB();
  MV16(acc[3], p[0], fw[6][3]); SB();
  SB();
  WAITVB(9); SB();
  PRIO(1); SB();
  MV16Z(acc[4], p[1], fw[0][0]); SB();
  LD16(p[0], vP2, sP, 0, "sc1");
  SB();
  MV16Z(acc[5], p[1], fw[0][1]); SB();
  SCL4(zz, gy[3]);
  SB();
  MV16Z(acc[6], p[1], fw[0][2]); SB();
  MV16Z(acc[7], p[1], fw[0][3]); SB();
  SB();
  WAITVB(9); SB();
  MV16(acc[4], p[2], fw[1][0]); SB();
  LD16(p[1], vP3, sP, 0, "sc1"); sP += 5120; OPQ(sP);
  SB();
  MV16(acc[5], p[2], fw[1][1]); SB();
  *(LDS_AS f32x4*)(wa1 + 8192) = acc[0];
  SB();
  MV16(acc[6], p[2], fw[1][2]); SB();
  *(LDS_AS f32x4*)(wa1 + 10240) = acc[1];
  SB();
  MV16(acc[7], p[2], fw[1][3]); SB();
  UN4("v_exp_f32", "", "", zz);
  SB();
  SB();
  WAITVB(9); SB();
  PRIO(0); SB();
  MV16(acc[4], p[3], fw[2][0]); SB();
  LD16(p[2], vP0, sP, 0, "sc1");
  SB();
  MV16(acc[5], p[3], fw[2][1]); SB();
  *(LDS_AS f32x4*)(wa1 + 12288) = acc[2];
  SB();
  MV16(acc[6], p[3], fw[2][2]); SB();
  *(LDS_AS f32x4*)(wa1 + 14336) = acc[3];
  SB();
  MV16(acc[7], p[3], fw[2][3]); SB();
  UN4("v_add_f32", "1.0, ", "", zz);
  SB();
  SB();
  WAITVB(9); SB();
  MV16(acc[4], p[4], fw[3][0]); SB();
  LD16(p[3], vP1, sP, 0, "sc1");
  SB();
  MV16(acc[5], p[4], fw[3][1]); SB();
  UN4("v_rcp_f32", "", "", zz);
  SB();
  MV16(acc[6], p[4], fw[3][2]); SB();
  BAR();
  STAMP(47);
  SB();
  MV16(acc[7], p[4], fw[3][3]); SB();
  SB();
  WAITVB(9); SB();
  PRIO(3); SB();
  MV16(acc[4], p[5], fw[4][0]); SB();
  LD16(p[4], vP2, sP, 0, "sc1");
  ACC4("v_mul_f32", gy[3], zz);
  SB();
  MV16(acc[5], p[5], fw[4][1]); SB();
  t4[0] = *(LDS_AS const f32x4*)(ra + 8192);
  SB();
  MV16(acc[6], p[5], fw[4][2]); SB();
  t4[1] = *(LDS_AS const f32x4*)(ra + 24576);
  t4[2] = *(LDS_AS const f32x4*)(ra + 40960);
  SB();
  MV16(acc[7], p[5], fw[4][3]); SB();
  t4[3] = *(LDS_AS const f32x4*)(ra + 57344);
  SB();
  SB();
  WAITVB(9); SB();
  MV16(acc[4], p[6], fw[5][0]); SB();
  WAITV1(6, fu);
  if (__ballot(fu != nonce) != 0ull) poll1<8>(sFU, vFU, nonce);
  STAMP(43);
  LD16(p[5], vP3, sP, 0, "sc1"); sP += 5120; OPQ(sP);
  t4[4] = *(LDS_AS const f32x4*)(rah + 8192);
  SB();
  MV16(acc[5], p[6], fw[5][1]); SB();
  t4[5] = *(LDS_AS const f32x4*)(rah + 24576);
  SB();
  MV16(acc[6], p[6], fw[5][2]); SB();
  t4[6] = *(LDS_AS const f32x4*)(rah + 40960);
  SB();
  MV16(acc[7], p[6], fw[5][3]); SB();
  t4[7] = *(LDS_AS const f32x4*)(rah + 57344);
  SB();
  SB();
  WAITVB(9); SB();
  MV16(acc[4], p[7], fw[6][0]); SB();
  LD16(p[6], vP0, sP, 0, "sc1");
  SB();
  MV16(acc[5], p[7], fw[6][1]); SB();
  OP4_3("v_add_f32", gy[0], t4[0], bias);
  SB();
  MV16(acc[6], p[7], fw[6][2]); SB();
  MV16(acc[7], p[7], fw[6][3]); SB();
  SB();
  WAITVB(9); SB();
  MV16Z(acc[0], p[8], fw[0][0]); SB();
  LD16(p[7], vP1, sP, 0, "sc1");
  SB();
  MV16Z(acc[1], p[8], fw[0][1]); SB();
  OP4_3("v_add_f32", zz, t4[1], t4[2]);
  SB();
  MV16Z(acc[2], p[8], fw[0][2]); SB();
  MV16Z(acc[3], p[8], fw[0][3]); SB();
  SB();
  WAITVB(8); SB();
  MV16(acc[0], p[9], fw[1][0]); SB();
  LD16(p[8], vP2, sP, 0, "sc1");
  SB();
  MV16(acc[1], p[9], fw[1][1]); SB();
  *(LDS_AS f32x4*)(wa0 + 0) = acc[4];
  SB();
  MV16(acc[2], p[9], fw[1][2]); SB();
  ACC4("v_add_f32", gy[0], t4[3]);
  SB();
  MV16(acc[3], p[9], fw[1][3]); SB();
  SB();
  WAITVB(8); SB();
  PRIO(2); SB();
  MV16(acc[0], p[0], fw[2][0]); SB();
  LD16(p[9], vP3, sP, 0, "sc1"); sP += 5120; OPQ(sP);
  SB();
  MV16(acc[1], p[0], fw[2][1]); SB();
  *(LDS_AS f32x4*)(wa0 + 2048) = acc[5];
  SB();
  MV16(acc[2], p[0], fw[2][2]); SB();
  ACC4("v_add_f32", zz, t4[4]);
  SB();
  MV16(acc[3], p[0], fw[2][3]); SB();
  SB();
  WAITVB(8); SB();
  MV16(acc[0], p[1], fw[3][0]); SB();
  LD16(p[0], vP0, sP, 0, "sc1");
  SB();
  MV16(acc[1], p[1], fw[3][1]); SB();
  *(LDS_AS f32x4*)(wa0 + 4096) = acc[6];
  SB();
  MV16(acc[2], p[1], fw[3][2]); SB();
  ACC4("v_add_f32", gy[0], t4[5]);
  SB();
  MV16(acc[3], p[1], fw[3][3]); SB();
  SB();
  WAITVB(8); SB();
  MV16(acc[0], p[2], fw[4][0]); SB();
  LD16(p[1], vP1, sP, 0, "sc1");
  SB();
  MV16(acc[1], p[2], fw[4][1]); SB();
  *(LDS_AS f32x4*)(wa0 + 6144) = acc[7];
  SB();
  MV16(acc[2], p[2], fw[4][2]); SB();
  ACC4("v_add_f32", zz, t4[6]);
  SB();
  MV16(acc[3], p[2], fw[4][3]); SB();
  SB();
  WAITVB(8); SB();
  MV16(acc[0], p[3], fw[5][0]); SB();
  LD16(p[2], vP2, sP, 0, "sc1");
  SB();
  MV16(acc[1], p[3], fw[5][1]); SB();
  ACC4("v_add_f32", gy[0], t4[7]);
  SB();
  MV16(acc[2], p[3], fw[5][2]); SB();
  MV16(acc[3], p[3], fw[5][3]); SB();
  SB();
  WAITVB(8); SB();
  MV16(acc[0], p[4], fw[6][0]); SB();
  LD16(p[3], vP3, sP, 0, "sc1"); sP += 5120; OPQ(sP);
  SB();
  MV16(acc[1], p[4], fw[6][1]); SB();
  ACC4("v_add_f32", gy[0], zz);
  SB();
  MV16(acc[2], p[4], fw[6][2]); SB();
  MV16(acc[3], p[4], fw[6][3]); SB();
  SB();
  WAITVB(8); SB();
  PRIO(1); SB();
  MV16Z(acc[4], p[5], fw[0][0]); SB();
  LD16(p[4], vP0, sP, 0, "sc1");
  SB();
  MV16Z(acc[5], p[5], fw[0][1]); SB();
  SCL4(zz, gy[0]);
  SB();
  MV16Z(acc[6], p[5], fw[0][2]); SB();
  MV16Z(acc[7], p[5], fw[0][3]); SB();
  SB();
  WAITVB(8); SB();
  MV16(acc[4], p[6], fw[1][0]); SB();
  LD16(p[5], vP1, sP, 0, "sc1");
  SB();
  MV16(acc[5], p[6], fw[1][1]); SB();
  *(LDS_AS f32x4*)(wa1 + 0) = acc[0];
  SB();
  MV16(acc[6], p[6], fw[1][2]); SB();
  *(LDS_AS f32x4*)(wa1 + 2048) = acc[1];
  SB();
  MV16(acc[7], p[6], fw[1][3]); SB();
  UN4("v_exp_f32", "", "", zz);
  SB();
  SB();
  WAITVB(8); SB();
  PRIO(0); SB();
  MV16(acc[4], p[7], fw[2][0]); SB();
  LD16(p[6], vP2, sP, 0, "sc1");
  SB();
  MV16(acc[5], p[7], fw[2][1]); SB();
  *(LDS_AS f32x4*)(wa1 + 4096) = acc[2];
  SB();
  MV16(acc[6], p[7], fw[2][2]); SB();
  *(LDS_AS f32x4*)(wa1 + 6144) = acc[3];
  SB();
  MV16(acc[7], p[7], fw[2][3]); SB();
  UN4("v_add_f32", "1.0, ", "", zz);
  SB();
  SB();
  WAITVB(8); SB();
  MV16(acc[4], p[8], fw[3][0]); SB();
  LD16(p[7], vP3, sP, 0, "sc1"); sP += 5120; OPQ(sP);
  SB();
  MV16(acc[5], p[8], fw[3][1]); SB();
  UN4("v_rcp_f32", "", "", zz);
  SB();
  MV16(acc[6], p[8], fw[3][2]); SB();
  BAR();
  STAMP(48);
  SB();
  MV16(acc[7], p[8], fw[3][3]); SB();
  SB();
  WAITVB(8); SB();
  PRIO(3); SB();
  MV16(acc[4], p[9], fw[4][0]); SB();
  LD16(p[8], vP0, sP, 0, "sc1");
  ACC4("v_mul_f32", gy[0], zz);
  SB();
  MV16(acc[5], p[9], fw[4][1]); SB();
  t4[0] = *(LDS_AS const f32x4*)(ra + 0);
  SB();
  MV16(acc[6], p[9], fw[4][2]); SB();
  t4[1] = *(LDS_AS const f32x4*)(ra + 16384);
  t4[2] = *(LDS_AS const f32x4*)(ra + 32768);
  SB();
  MV16(acc[7], p[9], fw[4][3]); SB();
  t4[3] = *(LDS_AS const f32x4*)(ra + 49152);
  SB();
  SB();
  WAITVB(8); SB();
  PRIO(2); SB();
  MV16(acc[4], p[0], fw[5][0]); SB();
  unsigned c6; { const int l_ = lane_now(); c6 = (l_ & 6) == 6 ? 32 : 0; vFS = (l_ < 7 ? l_ : 6) * FS; asm volatile("" : "+v"(c6), "+v"(vFS)); }
  LD16(xv[0], vOut, sXo, 0, "");
  LD16(xv[1], vOut - c6, sXo, 128, "");
  t4[4] = *(LDS_AS const f32x4*)(rah + 0);
  SB();
  MV16(acc[5], p[0], fw[5][1]); SB();
  t4[5] = *(LDS_AS const f32x4*)(rah + 16384);
  SB();
  MV16(acc[6], p[0], fw[5][2]); SB();
  t4[6] = *(LDS_AS const f32x4*)(rah + 32768);
  SB();
  MV16(acc[7], p[0], fw[5][3]); SB();
  t4[7] = *(LDS_AS const f32x4*)(rah + 49152);
  SB();
  SB();
  WAITVB(9); SB();
  MV16(acc[4], p[1], fw[6][0]); SB();
  LD4(fs, vFS, sFS, 0, "sc1");
  SB();
  MV16(acc[5], p[1], fw[6][1]); SB();
  OP4_3("v_add_f32", gy[1], t4[0], bias);
  SB();
  MV16(acc[6], p[1], fw[6][2]); SB();
  MV16(acc[7], p[1], fw[6][3]); SB();
  SB();
  WAITVB(9); SB();
  MV16Z(acc[0], p[2], fw[0][0]); SB();
  OP4_3("v_add_f32", zz, t4[1], t4[2]);
  SB();
  MV16Z(acc[1], p[2], fw[0][1]); SB();
  ACC4("v_add_f32", gy[1], t4[3]);
  SB();
  MV16Z(acc[2], p[2], fw[0][2]); SB();
  MV16Z(acc[3], p[2], fw[0][3]); SB();
  SB();
  WAITVB(8); SB();
  MV16(acc[0], p[3], fw[1][0]); SB();
  *(LDS_AS f32x4*)(wa0 + 8192) = acc[4];
  SB();
  MV16(acc[1], p[3], fw[1][1]); SB();
  ACC4("v_add_f32", zz, t4[4]);
  SB();
  MV16(acc[2], p[3], fw[1][2]); SB();
  ACC4("v_add_f32", gy[1], t4[5]);
  SB();
  MV16(acc[3], p[3], fw[1][3]); SB();
  SB();
  WAITVB(7); SB();
  MV16(acc[0], p[4], fw[2][0]); SB();
  *(LDS_AS f32x4*)(wa0 + 10240) = acc[5];
  SB();
  MV16(acc[1], p[4], fw[2][1]); SB();
  ACC4("v_add_f32", zz, t4[6]);
  SB();
  MV16(acc[2], p[4], fw[2][2]); SB();
  ACC4("v_add_f32", gy[1], t4[7]);
  SB();
  MV16(acc[3], p[4], fw[2][3]); SB();
  SB();
  WAITVB(6); SB();
  PRIO(1); SB();
  MV16(acc[0], p[5], fw[3][0]); SB();
  *(LDS_AS f32x4*)(wa0 + 12288) = acc[6];
  SB();
  MV16(acc[1], p[5], fw[3][1]); SB();
  ACC4("v_add_f32", gy[1], zz);
  SB();
  MV16(acc[2], p[5], fw[3][2]); SB();
  MV16(acc[3], p[5], fw[3][3]); SB();
  SB();
  WAITVB(5); SB();
  MV16(acc[0], p[6], fw[4][0]); SB();
  *(LDS_AS f32x4*)(wa0 + 14336) = acc[7];
  SB();
  MV16(acc[1], p[6], fw[4][1]); SB();
  SCL4(zz, gy[1]);
  SB();
  MV16(acc[2], p[6], fw[4][2]); SB();
  UN4("v_exp_f32", "", "", zz);
  SB();
  MV16(acc[3], p[6], fw[4][3]); SB();
  SB();
  WAITVB(4); SB();
  PRIO(0); SB();
  MV16(acc[0], p[7], fw[5][0]); SB();
  WAITV1(0, fs);
  if (__ballot(fs != nonce) != 0ull) poll1<4>(sFS, vFS, nonce);
  STAMP(40);
  LD16(sv4[0], vSt, sS, 0, "sc1");
  { const char* s_ = sS + 12288; LD16(sv4[1], vSt - c6 * 96, s_, 0, "sc1"); }
  SB();
  MV16(acc[1], p[7], fw[5][1]); SB();
  UN4("v_add_f32", "1.0, ", "", zz);
  SB();
  MV16(acc[2], p[7], fw[5][2]); SB();
  UN4("v_rcp_f32", "", "", zz);
  SB();
  MV16(acc[3], p[7], fw[5][3]); SB();
  SB();
  WAITVB(5); SB();
  MV16(acc[0], p[8], fw[6][0]); SB();
  ACC4("v_mul_f32", gy[1], zz);
  SB();
  MV16(acc[1], p[8], fw[6][1]); SB();
  xchg4(gy[0], gy[1]);
  SB();
  MV16(acc[2], p[8], fw[6][2]); SB();
  MV16(acc[3], p[8], fw[6][3]); SB();
  NOP(4); SB();
  *(LDS_AS f32x4*)(wa1 + 8192) = acc[0];
  SB();
  *(LDS_AS f32x4*)(wa1 + 10240) = acc[1];
  SB();
  *(LDS_AS f32x4*)(wa1 + 12288) = acc[2];
  SB();
  *(LDS_AS f32x4*)(wa1 + 14336) = acc[3];
  SB();
  BAR();
  STAMP(49);
  t4[0] = *(LDS_AS const f32x4*)(ra + 8192);
  t4[1] = *(LDS_AS const f32x4*)(ra + 24576);
  t4[2] = *(LDS_AS const f32x4*)(ra + 40960);
  t4[3] = *(LDS_AS const f32x4*)(ra + 57344);
  t4[4] = *(LDS_AS const f32x4*)(rah + 8192);
  t4[5] = *(LDS_AS const f32x4*)(rah + 24576);
  t4[6] = *(LDS_AS const f32x4*)(rah + 40960);
  t4[7] = *(LDS_AS const f32x4*)(rah + 57344);
  SB();
  WAITV2(1, xv[0], sv4[0]);
  ST16(vOut, sOut, 0, ((u32x4){pack2(__builtin_fmaf(lo2f(sv4[0][0]), gy[0][0], lo2f(xv[0][0])), __builtin_fmaf(hi2f(sv4[0][0]), gy[0][1], hi2f(xv[0][0]))), pack2(__builtin_fmaf(lo2f(sv4[0][1]), gy[0][2], lo2f(xv[0][1])), __builtin_fmaf(hi2f(sv4[0][1]), gy[0][3], hi2f(xv[0][1]))), pack2(__builtin_fmaf(lo2f(sv4[0][2]), gy[1][0], lo2f(xv[0][2])), __builtin_fmaf(hi2f(sv4[0][2]), gy[1][1], hi2f(xv[0][2]))), pack2(__builtin_fmaf(lo2f(sv4[0][3]), gy[1][2], lo2f(xv[0][3])), __builtin_fmaf(hi2f(sv4[0][3]), gy[1][3], hi2f(xv[0][3])))}));
  SB();
  OP4_3("v_add_f32", gy[2], t4[0], bias);
  OP4_3("v_add_f32", zz, t4[1], t4[2]);
  ACC4("v_add_f32", gy[2], t4[3]);
  ACC4("v_add_f32", zz, t4[4]);
  ACC4("v_add_f32", gy[2], t4[5]);
  ACC4("v_add_f32", zz, t4[6]);
  ACC4("v_add_f32", gy[2], t4[7]);
  ACC4("v_add_f32", gy[2], zz);
  SCL4(zz, gy[2]);
  UN4("v_exp_f32", "", "", zz);
  UN4("v_add_f32", "1.0, ", "", zz);
  UN4("v_rcp_f32", "", "", zz);
  ACC4("v_mul_f32", gy[2], zz);
  xchg4(gy[2], gy[3]);
  STAMP(41);
  WAITV2(1, xv[1], sv4[1]);
  { const char* o_ = sOut + 128; ST16E(0x3f3f3f3f, vOut, o_, ((u32x4){pack2(__builtin_fmaf(lo2f(sv4[1][0]), gy[2][0], lo2f(xv[1][0])), __builtin_fmaf(hi2f(sv4[1][0]), gy[2][1], hi2f(xv[1][0]))), pack2(__builtin_fmaf(lo2f(sv4[1][1]), gy[2][2], lo2f(xv[1][1])), __builtin_fmaf(hi2f(sv4[1][1]), gy[2][3], hi2f(xv[1][1]))), pack2(__builtin_fmaf(lo2f(sv4[1][2]), gy[3][0], lo2f(xv[1][2])), __builtin_fmaf(hi2f(sv4[1][2]), gy[3][1], hi2f(xv[1][2]))), pack2(__builtin_fmaf(lo2f(sv4[1][3]), gy[3][2], lo2f(xv[1][3])), __builtin_fmaf(hi2f(sv4[1][3]), gy[3][3], hi2f(xv[1][3])))})); }
  STAMP(42);
  STAMP(50);
  STAMP_DUMP();

  } else {
  if (__builtin_expect(jb < 28, 1)) {
  DMA2(vWa, vWb, sW, ldsP, 6144, ""); sW = wrap == 1 ? bW : sW + dW; OPQ(sW);
  bf16x8 fw[8][4], p[9]; f32x4 acc[8], t4[8]; float mm[4], zz[4], bias[4], c23 = 2.3022082f;
  unsigned rW0 = ldsP + (8 * g4 + q4) * 128 + (((q4 >> 1) | (2 * (g4 & 1))) * 32) + pq * 8, rW1 = rW0 ^ 32, rW2 = rW0 ^ 64, rW3 = rW0 ^ 96;
  asm volatile("" : "+v"(rW0), "+v"(rW1), "+v"(rW2), "+v"(rW3));
  unsigned vP0 = frag(0, lane), vP1 = frag(1, lane), vP2 = frag(2, lane), vP3 = frag(3, lane);
  const char* sP = A.ws + WS_HT + (size_t)(dg * 8 + w) * HT_W;
  asm volatile("" : "+s"(sP)); asm volatile("" : "+v"(vP0), "+v"(vP1), "+v"(vP2), "+v"(vP3));
  WAITV(5);
  fw[0][0] = tr8u(rW0 + 0, 512);
  fw[0][1] = tr8u(rW1 + 0, 512);
  fw[0][2] = tr8u(rW2 + 0, 512);
  fw[0][3] = tr8u(rW3 + 0, 512);
  DMA2(vWa, vWb, sW, ldsP, 8192, ""); sW += dW; OPQ(sW);
  DMA2(vWa, vWb, sW, ldsP, 10240, ""); sW = wrap == 2 ? bW : sW + dW; OPQ(sW);
  WAITV(4);
  fw[1][0] = tr8u(rW0 + 4096, 512);
  fw[1][1] = tr8u(rW1 + 4096, 512);
  fw[1][2] = tr8u(rW2 + 4096, 512);
  fw[1][3] = tr8u(rW3 + 4096, 512);
  DMA2(vWa, vWb, sW, ldsP, 12288, ""); sW += dW; OPQ(sW);
  DMA2(vWa, vWb, sW, ldsP, 14336, ""); sW = wrap == 3 ? bW : sW + dW; OPQ(sW);
  WAITV(4);
  fw[2][0] = tr8u(rW0 + 8192, 512);
  fw[2][1] = tr8u(rW1 + 8192, 512);
  fw[2][2] = tr8u(rW2 + 8192, 512);
  fw[2][3] = tr8u(rW3 + 8192, 512);
  WAITL(0);
  DMA2(vWa, vWb, sW, ldsP, 0, ""); sW += dW; OPQ(sW);
  DMA2(vWa, vWb, sW, ldsP, 2048, ""); sW = wrap == 4 ? bW : sW + dW; OPQ(sW);
  WAITV(4);
  fw[3][0] = tr8u(rW0 + 12288, 512);
  fw[3][1] = tr8u(rW1 + 12288, 512);
  fw[3][2] = tr8u(rW2 + 12288, 512);
  fw[3][3] = tr8u(rW3 + 12288, 512);
  STAMP(1);
  WAITL(0);
  DMA2(vWa, vWb, sW, ldsP, 4096, ""); sW += dW; OPQ(sW);
  DMA2(vWa, vWb, sW, ldsP, 6144, ""); sW = wrap == 5 ? bW : sW + dW; OPQ(sW);
  bias[0] = lo2f(bv[0]); bias[1] = hi2f(bv[0]); bias[2] = lo2f(bv[1]); bias[3] = hi2f(bv[1]);
  unsigned wa0 = ldsP + n16 * 256 + ((g4 ^ n16) << 4), wa1 = wa0 ^ 64, wa2 = wa0 ^ 128, wa3 = wa0 ^ 192;
  unsigned ra = lds0 + (16 * (w >> 2) + n16) * 256 + (((4 * (w & 3) + g4) ^ n16) << 4), rah = ra + 65536, uL = 32 * (w & 1) + 16 * (g4 >> 1) + n16, vU;
  const int kap = 2 * ft + ((w >> 1) & 1), wv = (kap * 37) >> 8, us = kap - 7 * wv, up_ = us - KROT2 * dg;
  int up = up_ < 0 ? up_ + 7 : up_, mU = ((w - 4) >> 31) & 0xffff;
  const char* sU = A.ws + WS_UT + (size_t)(dg * 8 + wv) * UT_W;
  const char* sFU = A.ws + WS_FLAGU + ((dg * 8 + wv) * 128 + (w >> 2) * 14 + us * 2 + (w & 1)) * FU;
  asm volatile("" : "+s"(sU), "+s"(sFU), "+s"(up), "+s"(mU)); asm volatile("" : "+v"(wa0), "+v"(wa1), "+v"(wa2), "+v"(wa3), "+v"(ra), "+v"(rah), "+v"(uL), "+v"(bias[0]), "+v"(bias[1]), "+v"(bias[2]), "+v"(bias[3]), "+v"(c23));
  WAITV(4);
  fw[4][0] = tr8u(rW0 + 0, 512);
  fw[4][1] = tr8u(rW1 + 0, 512);
  fw[4][2] = tr8u(rW2 + 0, 512);
  fw[4][3] = tr8u(rW3 + 0, 512);
  WAITL(0);
  DMA2(vWa, vWb, sW, ldsP, 8192, ""); sW += dW; OPQ(sW);
  LD4(fh, vFH, sFH, 0, "sc1");
  DMA2(vWa, vWb, sW, ldsP, 10240, ""); sW = wrap == 6 ? bW : sW + dW; OPQ(sW);
  WAITV(5);
  fw[5][0] = tr8u(rW0 + 4096, 512);
  fw[5][1] = tr8u(rW1 + 4096, 512);
  fw[5][2] = tr8u(rW2 + 4096, 512);
  fw[5][3] = tr8u(rW3 + 4096, 512);
  WAITL(0);
  DMA2(vWa, vWb, sW, ldsP, 12288, ""); sW += dW; OPQ(sW);
  DMA2(vWa, vWb, sW, ldsP, 14336, "");
  WAITV(4);
  fw[6][0] = tr8u(rW0 + 8192, 512);
  fw[6][1] = tr8u(rW1 + 8192, 512);
  fw[6][2] = tr8u(rW2 + 8192, 512);
  fw[6][3] = tr8u(rW3 + 8192, 512);
  WAITV1(6, fh);
  if (__ballot(fh != nonce) != 0ull) poll1<16>(sFH, vFH, nonce);
  STAMP(3);
  LD16(p[0], vP0, sP, 0, "sc1");
  LD16(p[1], vP1, sP, 0, "sc1");
  LD16(p[2], vP2, sP, 0, "sc1");
  LD16(p[3], vP3, sP, 0, "sc1"); sP += 5120; OPQ(sP);
  WAITV(4);
  fw[7][0] = tr8u(rW0 + 12288, 512);
  fw[7][1] = tr8u(rW1 + 12288, 512);
  fw[7][2] = tr8u(rW2 + 12288, 512);
  fw[7][3] = tr8u(rW3 + 12288, 512);
  STAMP(2);
  LD16(p[4], vP0, sP, 0, "sc1");
  LD16(p[5], vP1, sP, 0, "sc1");
  LD16(p[6], vP2, sP, 0, "sc1");
  LD16(p[7], vP3, sP, 0, "sc1"); sP += 5120; OPQ(sP);
  SB();
  WAITVB(7); SB();
  STAMP(4);
  PRIO(3); SB();
  MV16Z(acc[0], fw[0][0], p[0]); SB();
  LD16(p[8], vP0, sP, 0, "sc1");
  SB();
  MV16Z(acc[1], fw[0][1], p[0]); SB();
  MV16Z(acc[2], fw[0][2], p[0]); SB();
  MV16Z(acc[3], fw[0][3], p[0]); SB();
  SB();
  WAITVB(7); SB();
  MV16(acc[0], fw[1][0], p[1]); SB();
  LD16(p[0], vP1, sP, 0, "sc1");
  SB();
  MV16(acc[1], fw[1][1], p[1]); SB();
  MV16(acc[2], fw[1][2], p[1]); SB();
  MV16(acc[3], fw[1][3], p[1]); SB();
  SB();
  WAITVB(7); SB();
  PRIO(2); SB();
  MV16(acc[0], fw[2][0], p[2]); SB();
  LD16(p[1], vP2, sP, 0, "sc1");
  SB();
  MV16(acc[1], fw[2][1], p[2]); SB();
  MV16(acc[2], fw[2][2], p[2]); SB();
  MV16(acc[3], fw[2][3], p[2]); SB();
  SB();
  WAITVB(7); SB();
  MV16(acc[0], fw[3][0], p[3]); SB();
  LD16(p[2], vP3, sP, 0, "sc1"); sP += 5120; OPQ(sP);
  SB();
  MV16(acc[1], fw[3][1], p[3]); SB();
  MV16(acc[2], fw[3][2], p[3]); SB();
  MV16(acc[3], fw[3][3], p[3]); SB();
  SB();
  WAITVB(7); SB();
  MV16(acc[0], fw[4][0], p[4]); SB();
  LD16(p[3], vP0, sP, 0, "sc1");
  SB();
  MV16(acc[1], fw[4][1], p[4]); SB();
  MV16(acc[2], fw[4][2], p[4]); SB();
  MV16(acc[3], fw[4][3], p[4]); SB();
  SB();
  WAITVB(7); SB();
  MV16(acc[0], fw[5][0], p[5]); SB();
  LD16(p[4], vP1, sP, 0, "sc1");
  SB();
  MV16(acc[1], fw[5][1], p[5]); SB();
  MV16(acc[2], fw[5][2], p[5]); SB();
  MV16(acc[3], fw[5][3], p[5]); SB();
  SB();
  WAITVB(7); SB();
  MV16(acc[0], fw[6][0], p[6]); SB();
  LD16(p[5], vP2, sP, 0, "sc1");
  SB();
  MV16(acc[1], fw[6][1], p[6]); SB();
  MV16(acc[2], fw[6][2], p[6]); SB();
  MV16(acc[3], fw[6][3], p[6]); SB();
  SB();
  WAITVB(7); SB();
  PRIO(1); SB();
  MV16(acc[0], fw[7][0], p[7]); SB();
  LD16(p[6], vP3, sP, 0, "sc1"); sP += 5120; OPQ(sP);
  SB();
  MV16(acc[1], fw[7][1], p[7]); SB();
  MV16(acc[2], fw[7][2], p[7]); SB();
  MV16(acc[3], fw[7][3], p[7]); SB();
  SB();
  WAITVB(7); SB();
  MV16Z(acc[4], fw[0][0], p[8]); SB();
  LD16(p[7], vP0, sP, 0, "sc1");
  SB();
  MV16Z(acc[5], fw[0][1], p[8]); SB();
  MV16Z(acc[6], fw[0][2], p[8]); SB();
  MV16Z(acc[7], fw[0][3], p[8]); SB();
  SB();
  WAITVB(7); SB();
  PRIO(0); SB();
  MV16(acc[4], fw[1][0], p[0]); SB();
  LD16(p[8], vP1, sP, 0, "sc1");
  SB();
  MV16(acc[5], fw[1][1], p[0]); SB();
  *(LDS_AS f32x4*)(wa0 + 0) = acc[0];
  SB();
  MV16(acc[6], fw[1][2], p[0]); SB();
  *(LDS_AS f32x4*)(wa1 + 0) = acc[1];
  SB();
  MV16(acc[7], fw[1][3], p[0]); SB();
  SB();
  WAITVB(7); SB();
  MV16(acc[4], fw[2][0], p[1]); SB();
  LD16(p[0], vP2, sP, 0, "sc1");
  SB();
  MV16(acc[5], fw[2][1], p[1]); SB();
  *(LDS_AS f32x4*)(wa2 + 0) = acc[2];
  SB();
  MV16(acc[6], fw[2][2], p[1]); SB();
  *(LDS_AS f32x4*)(wa3 + 0) = acc[3];
  SB();
  MV16(acc[7], fw[2][3], p[1]); SB();
  BAR();
  STAMP(5);
  SB();
  SB();
  WAITVB(7); SB();
  PRIO(3); SB();
  MV16(acc[4], fw[3][0], p[2]); SB();
  LD16(p[1], vP3, sP, 0, "sc1"); sP += 5120; OPQ(sP);
  t4[0] = *(LDS_AS const f32x4*)(ra + 0);
  SB();
  MV16(acc[5], fw[3][1], p[2]); SB();
  t4[1] = *(LDS_AS const f32x4*)(ra + 16384);
  SB();
  MV16(acc[6], fw[3][2], p[2]); SB();
  t4[2] = *(LDS_AS const f32x4*)(ra + 32768);
  SB();
  MV16(acc[7], fw[3][3], p[2]); SB();
  t4[3] = *(LDS_AS const f32x4*)(ra + 49152);
  SB();
  SB();
  WAITVB(7); SB();
  MV16(acc[4], fw[4][0], p[3]); SB();
  LD16(p[2], vP0, sP, 0, "sc1");
  t4[4] = *(LDS_AS const f32x4*)(rah + 0);
  SB();
  MV16(acc[5], fw[4][1], p[3]); SB();
  t4[5] = *(LDS_AS const f32x4*)(rah + 16384);
  SB();
  MV16(acc[6], fw[4][2], p[3]); SB();
  t4[6] = *(LDS_AS const f32x4*)(rah + 32768);
  t4[7] = *(LDS_AS const f32x4*)(rah + 49152);
  SB();
  MV16(acc[7], fw[4][3], p[3]); SB();
  { const int ix = up; vU = (ix >> 2) * 5120 + frag(ix & 3, uL); PINV(vU); }
  SB();
  SB();
  WAITVB(7); SB();
  MV16(acc[4], fw[5][0], p[4]); SB();
  LD16(p[3], vP1, sP, 0, "sc1");
  OP4_3("v_add_f32", mm, t4[0], bias);
  SB();
  MV16(acc[5], fw[5][1], p[4]); SB();
  OP4_3("v_add_f32", zz, t4[1], t4[2]);
  SB();
  MV16(acc[6], fw[5][2], p[4]); SB();
  ACC4("v_add_f32", mm, t4[3]);
  SB();
  MV16(acc[7], fw[5][3], p[4]); SB();
  ACC4("v_add_f32", zz, t4[4]);
  SB();
  SB();
  WAITVB(7); SB();
  MV16(acc[4], fw[6][0], p[5]); SB();
  LD16(p[4], vP2, sP, 0, "sc1");
  ACC4("v_add_f32", mm, t4[5]);
  SB();
  MV16(acc[5], fw[6][1], p[5]); SB();
  ACC4("v_add_f32", zz, t4[6]);
  SB();
  MV16(acc[6], fw[6][2], p[5]); SB();
  ACC4("v_add_f32", mm, t4[7]);
  SB();
  MV16(acc[7], fw[6][3], p[5]); SB();
  ACC4("v_add_f32", mm, zz);
  SB();
  SB();
  WAITVB(7); SB();
  MV16(acc[4], fw[7][0], p[6]); SB();
  LD16(p[5], vP3, sP, 0, "sc1"); sP += 5120; OPQ(sP);
  OP4_3("v_mul_f32", zz, mm, mm);
  SB();
  MV16(acc[5], fw[7][1], p[6]); SB();
  POLY4(zz, c23);
  SB();
  MV16(acc[6], fw[7][2], p[6]); SB();
  ACC4("v_mul_f32", zz, mm);
  SB();
  MV16(acc[7], fw[7][3], p[6]); SB();
  UN4("v_exp_f32", "", "", zz);
  SB();
  SB();
  WAITVB(7); SB();
  MV16Z(acc[0], fw[0][0], p[7]); SB();
  LD16(p[6], vP0, sP, 0, "sc1");
  UN4("v_add_f32", "1.0, ", "", zz);
  SB();
  MV16Z(acc[1], fw[0][1], p[7]); SB();
  UN4("v_rcp_f32", "", "", zz);
  SB();
  MV16Z(acc[2], fw[0][2], p[7]); SB();
  NFMA4(mm, zz);
  SB();
  MV16Z(acc[3], fw[0][3], p[7]); SB();
  { const unsigned x0 = pack2(mm[0], mm[1]), x1 = pack2(mm[2], mm[3]);
    const auto r0 = __builtin_amdgcn_permlane16_swap(x0, x0, false, false), r1 = __builtin_amdgcn_permlane16_swap(x1, x1, false, false);
    ST16M(mU, vU, sU, ((u32x4){r0[0], r1[0], r0[1], r1[1]})); }
  SB();
  SB();
  WAITVB(7); SB();
  MV16(acc[0], fw[1][0], p[8]); SB();
  LD16(p[7], vP1, sP, 0, "sc1");
  SB();
  MV16(acc[1], fw[1][1], p[8]); SB();
  *(LDS_AS f32x4*)(wa0 + 8192) = acc[4];
  SB();
  MV16(acc[2], fw[1][2], p[8]); SB();
  MV16(acc[3], fw[1][3], p[8]); SB();
  SB();
  WAITVB(7); SB();
  PRIO(2); SB();
  MV16(acc[0], fw[2][0], p[0]); SB();
  LD16(p[8], vP2, sP, 0, "sc1");
  SB();
  MV16(acc[1], fw[2][1], p[0]); SB();
  *(LDS_AS f32x4*)(wa1 + 8192) = acc[5];
  SB();
  MV16(acc[2], fw[2][2], p[0]); SB();
  MV16(acc[3], fw[2][3], p[0]); SB();
  SB();
  WAITVB(7); SB();
  MV16(acc[0], fw[3][0], p[1]); SB();
  LD16(p[0], vP3, sP, 0, "sc1"); sP += 5120; OPQ(sP);
  SB();
  MV16(acc[1], fw[3][1], p[1]); SB();
  *(LDS_AS f32x4*)(wa2 + 8192) = acc[6];
  SB();
  MV16(acc[2], fw[3][2], p[1]); SB();
  MV16(acc[3], fw[3][3], p[1]); SB();
  SB();
  WAITVB(7); SB();
  MV16(acc[0], fw[4][0], p[2]); SB();
  LD16(p[1], vP0, sP, 0, "sc1");
  SB();
  MV16(acc[1], fw[4][1], p[2]); SB();
  *(LDS_AS f32x4*)(wa3 + 8192) = acc[7];
  SB();
  MV16(acc[2], fw[4][2], p[2]); SB();
  MV16(acc[3], fw[4][3], p[2]); SB();
  SB();
  WAITVB(7); SB();
  MV16(acc[0], fw[5][0], p[3]); SB();
  LD16(p[2], vP1, sP, 0, "sc1");
  SB();
  MV16(acc[1], fw[5][1], p[3]); SB();
  MV16(acc[2], fw[5][2], p[3]); SB();
  MV16(acc[3], fw[5][3], p[3]); SB();
  SB();
  WAITVB(7); SB();
  MV16(acc[0], fw[6][0], p[4]); SB();
  LD16(p[3], vP2, sP, 0, "sc1");
  SB();
  MV16(acc[1], fw[6][1], p[4]); SB();
  MV16(acc[2], fw[6][2], p[4]); SB();
  MV16(acc[3], fw[6][3], p[4]); SB();
  SB();
  WAITVB(7); SB();
  PRIO(1); SB();
  MV16(acc[0], fw[7][0], p[5]); SB();
  LD16(p[4], vP3, sP, 0, "sc1"); sP += 5120; OPQ(sP);
  SB();
  MV16(acc[1], fw[7][1], p[5]); SB();
  MV16(acc[2], fw[7][2], p[5]); SB();
  MV16(acc[3], fw[7][3], p[5]); SB();
  SB();
  WAITVB(7); SB();
  MV16Z(acc[4], fw[0][0], p[6]); SB();
  LD16(p[5], vP0, sP, 0, "sc1");
  SB();
  MV16Z(acc[5], fw[0][1], p[6]); SB();
  MV16Z(acc[6], fw[0][2], p[6]); SB();
  MV16Z(acc[7], fw[0][3], p[6]); SB();
  SB();
  WAITVB(7); SB();
  PRIO(0); SB();
  MV16(acc[4], fw[1][0], p[7]); SB();
  LD16(p[6], vP1, sP, 0, "sc1");
  SB();
  MV16(acc[5], fw[1][1], p[7]); SB();
  *(LDS_AS f32x4*)(wa0 + 12288) = acc[0];
  SB();
  MV16(acc[6], fw[1][2], p[7]); SB();
  *(LDS_AS f32x4*)(wa1 + 12288) = acc[1];
  SB();
  MV16(acc[7], fw[1][3], p[7]); SB();
  if (w < 4) { const unsigned l_ = lane_now() * 16; ST16X(1, l_, sFU, nonce); }
  SB();
  SB();
  WAITVB(7); SB();
  MV16(acc[4], fw[2][0], p[8]); SB();
  LD16(p[7], vP2, sP, 0, "sc1");
  SB();
  MV16(acc[5], fw[2][1], p[8]); SB();
  *(LDS_AS f32x4*)(wa2 + 12288) = acc[2];
  SB();
  MV16(acc[6], fw[2][2], p[8]); SB();
  *(LDS_AS f32x4*)(wa3 + 12288) = acc[3];
  SB();
  MV16(acc[7], fw[2][3], p[8]); SB();
  BAR();
  STAMP(6);
  SB();
  SB();
  WAITVB(7); SB();
  PRIO(3); SB();
  MV16(acc[4], fw[3][0], p[0]); SB();
  LD16(p[8], vP3, sP, 0, "sc1"); sP += 5120; OPQ(sP);
  t4[0] = *(LDS_AS const f32x4*)(ra + 8192);
  SB();
  MV16(acc[5], fw[3][1], p[0]); SB();
  t4[1] = *(LDS_AS const f32x4*)(ra + 24576);
  SB();
  MV16(acc[6], fw[3][2], p[0]); SB();
  t4[2] = *(LDS_AS const f32x4*)(ra + 40960);
  SB();
  MV16(acc[7], fw[3][3], p[0]); SB();
  t4[3] = *(LDS_AS const f32x4*)(ra + 57344);
  SB();
  SB();
  WAITVB(7); SB();
  MV16(acc[4], fw[4][0], p[1]); SB();
  LD16(p[0], vP0, sP, 0, "sc1");
  t4[4] = *(LDS_AS const f32x4*)(rah + 8192);
  SB();
  MV16(acc[5], fw[4][1], p[1]); SB();
  t4[5] = *(LDS_AS const f32x4*)(rah + 24576);
  SB();
  MV16(acc[6], fw[4][2], p[1]); SB();
  t4[6] = *(LDS_AS const f32x4*)(rah + 40960);
  t4[7] = *(LDS_AS const f32x4*)(rah + 57344);
  SB();
  MV16(acc[7], fw[4][3], p[1]); SB();
  { const int ix = 7 + up + 7 * (w >> 2); vU = (ix >> 2) * 5120 + frag(ix & 3, uL); PINV(vU); }
  SB();
  SB();
  WAITVB(7); SB();
  MV16(acc[4], fw[5][0], p[2]); SB();
  LD16(p[1], vP1, sP, 0, "sc1");
  OP4_3("v_add_f32", mm, t4[0], bias);
  SB();
  MV16(acc[5], fw[5][1], p[2]); SB();
  OP4_3("v_add_f32", zz, t4[1], t4[2]);
  SB();
  MV16(acc[6], fw[5][2], p[2]); SB();
  ACC4("v_add_f32", mm, t4[3]);
  SB();
  MV16(acc[7], fw[5][3], p[2]); SB();
  ACC4("v_add_f32", zz, t4[4]);
  SB();
  SB();
  WAITVB(7); SB();
  MV16(acc[4], fw[6][0], p[3]); SB();
  LD16(p[2], vP2, sP, 0, "sc1");
  ACC4("v_add_f32", mm, t4[5]);
  SB();
  MV16(acc[5], fw[6][1], p[3]); SB();
  ACC4("v_add_f32", zz, t4[6]);
  SB();
  MV16(acc[6], fw[6][2], p[3]); SB();
  ACC4("v_add_f32", mm, t4[7]);
  SB();
  MV16(acc[7], fw[6][3], p[3]); SB();
  ACC4("v_add_f32", mm, zz);
  SB();
  SB();
  WAITVB(7); SB();
  MV16(acc[4], fw[7][0], p[4]); SB();
  LD16(p[3], vP3, sP, 0, "sc1"); sP += 5120; OPQ(sP);
  OP4_3("v_mul_f32", zz, mm, mm);
  SB();
  MV16(acc[5], fw[7][1], p[4]); SB();
  POLY4(zz, c23);
  SB();
  MV16(acc[6], fw[7][2], p[4]); SB();
  ACC4("v_mul_f32", zz, mm);
  SB();
  MV16(acc[7], fw[7][3], p[4]); SB();
  UN4("v_exp_f32", "", "", zz);
  SB();
  SB();
  WAITVB(7); SB();
  MV16Z(acc[0], fw[0][0], p[5]); SB();
  LD16(p[4], vP0, sP, 0, "sc1");
  UN4("v_add_f32", "1.0, ", "", zz);
  SB();
  MV16Z(acc[1], fw[0][1], p[5]); SB();
  UN4("v_rcp_f32", "", "", zz);
  SB();
  MV16Z(acc[2], fw[0][2], p[5]); SB();
  NFMA4(mm, zz);
  SB();
  MV16Z(acc[3], fw[0][3], p[5]); SB();
  { const unsigned x0 = pack2(mm[0], mm[1]), x1 = pack2(mm[2], mm[3]);
    const auto r0 = __builtin_amdgcn_permlane16_swap(x0, x0, false, false), r1 = __builtin_amdgcn_permlane16_swap(x1, x1, false, false);
    ST16E(0xffff, vU, sU, ((u32x4){r0[0], r1[0], r0[1], r1[1]})); }
  SB();
  SB();
  WAITVB(8); SB();
  MV16(acc[0], fw[1][0], p[6]); SB();
  LD16(p[5], vP1, sP, 0, "sc1");
  SB();
  MV16(acc[1], fw[1][1], p[6]); SB();
  *(LDS_AS f32x4*)(wa0 + 0) = acc[4];
  SB();
  MV16(acc[2], fw[1][2], p[6]); SB();
  MV16(acc[3], fw[1][3], p[6]); SB();
  SB();
  WAITVB(8); SB();
  PRIO(2); SB();
  MV16(acc[0], fw[2][0], p[7]); SB();
  LD16(p[6], vP2, sP, 0, "sc1");
  SB();
  MV16(acc[1], fw[2][1], p[7]); SB();
  *(LDS_AS f32x4*)(wa1 + 0) = acc[5];
  SB();
  MV16(acc[2], fw[2][2], p[7]); SB();
  MV16(acc[3], fw[2][3], p[7]); SB();
  SB();
  WAITVB(8); SB();
  MV16(acc[0], fw[3][0], p[8]); SB();
  LD16(p[7], vP3, sP, 0, "sc1"); sP += 5120; OPQ(sP);
  SB();
  MV16(acc[1], fw[3][1], p[8]); SB();
  *(LDS_AS f32x4*)(wa2 + 0) = acc[6];
  SB();
  MV16(acc[2], fw[3][2], p[8]); SB();
  MV16(acc[3], fw[3][3], p[8]); SB();
  SB();
  WAITVB(8); SB();
  MV16(acc[0], fw[4][0], p[0]); SB();
  LD16(p[8], vP0, sP, 0, "sc1");
  SB();
  MV16(acc[1], fw[4][1], p[0]); SB();
  *(LDS_AS f32x4*)(wa3 + 0) = acc[7];
  SB();
  MV16(acc[2], fw[4][2], p[0]); SB();
  MV16(acc[3], fw[4][3], p[0]); SB();
  SB();
  WAITVB(8); SB();
  MV16(acc[0], fw[5][0], p[1]); SB();
  LD16(p[0], vP1, sP, 0, "sc1");
  SB();
  MV16(acc[1], fw[5][1], p[1]); SB();
  MV16(acc[2], fw[5][2], p[1]); SB();
  MV16(acc[3], fw[5][3], p[1]); SB();
  SB();
  WAITVB(8); SB();
  MV16(acc[0], fw[6][0], p[2]); SB();
  LD16(p[1], vP2, sP, 0, "sc1");
  SB();
  MV16(acc[1], fw[6][1], p[2]); SB();
  MV16(acc[2], fw[6][2], p[2]); SB();
  MV16(acc[3], fw[6][3], p[2]); SB();
  SB();
  WAITVB(8); SB();
  PRIO(1); SB();
  MV16(acc[0], fw[7][0], p[3]); SB();
  LD16(p[2], vP3, sP, 0, "sc1"); sP += 5120; OPQ(sP);
  SB();
  MV16(acc[1], fw[7][1], p[3]); SB();
  MV16(acc[2], fw[7][2], p[3]); SB();
  MV16(acc[3], fw[7][3], p[3]); SB();
  SB();
  WAITVB(8); SB();
  MV16Z(acc[4], fw[0][0], p[4]); SB();
  LD16(p[3], vP0, sP, 0, "sc1");
  SB();
  MV16Z(acc[5], fw[0][1], p[4]); SB();
  MV16Z(acc[6], fw[0][2], p[4]); SB();
  MV16Z(acc[7], fw[0][3], p[4]); SB();
  SB();
  WAITVB(7); SB();
  PRIO(0); SB();
  MV16(acc[4], fw[1][0], p[5]); SB();
  LD16(p[4], vP1, sP, 0, "sc1");
  SB();
  MV16(acc[5], fw[1][1], p[5]); SB();
  *(LDS_AS f32x4*)(wa0 + 4096) = acc[0];
  SB();
  MV16(acc[6], fw[1][2], p[5]); SB();
  *(LDS_AS f32x4*)(wa1 + 4096) = acc[1];
  SB();
  MV16(acc[7], fw[1][3], p[5]); SB();
  { const char* f_ = sFU + 14 * FU; const unsigned l_ = lane_now() * 16; ST16X(1, l_, f_, nonce); }
  SB();
  SB();
  WAITVB(8); SB();
  MV16(acc[4], fw[2][0], p[6]); SB();
  LD16(p[5], vP2, sP, 0, "sc1");
  SB();
  MV16(acc[5], fw[2][1], p[6]); SB();
  *(LDS_AS f32x4*)(wa2 + 4096) = acc[2];
  SB();
  MV16(acc[6], fw[2][2], p[6]); SB();
  *(LDS_AS f32x4*)(wa3 + 4096) = acc[3];
  SB();
  MV16(acc[7], fw[2][3], p[6]); SB();
  BAR();
  STAMP(7);
  SB();
  SB();
  WAITVB(8); SB();
  PRIO(3); SB();
  MV16(acc[4], fw[3][0], p[7]); SB();
  LD16(p[6], vP3, sP, 0, "sc1"); sP += 5120; OPQ(sP);
  t4[0] = *(LDS_AS const f32x4*)(ra + 0);
  SB();
  MV16(acc[5], fw[3][1], p[7]); SB();
  t4[1] = *(LDS_AS const f32x4*)(ra + 16384);
  SB();
  MV16(acc[6], fw[3][2], p[7]); SB();
  t4[2] = *(LDS_AS const f32x4*)(ra + 32768);
  SB();
  MV16(acc[7], fw[3][3], p[7]); SB();
  t4[3] = *(LDS_AS const f32x4*)(ra + 49152);
  SB();
  SB();
  WAITVB(8); SB();
  MV16(acc[4], fw[4][0], p[8]); SB();
  LD16(p[7], vP0, sP, 0, "sc1");
  t4[4] = *(LDS_AS const f32x4*)(rah + 0);
  SB();
  MV16(acc[5], fw[4][1], p[8]); SB();
  t4[5] = *(LDS_AS const f32x4*)(rah + 16384);
  SB();
  MV16(acc[6], fw[4][2], p[8]); SB();
  t4[6] = *(LDS_AS const f32x4*)(rah + 32768);
  t4[7] = *(LDS_AS const f32x4*)(rah + 49152);
  SB();
  MV16(acc[7], fw[4][3], p[8]); SB();
  { const int ix = 21 + up + 7 * (w >> 2); vU = (ix >> 2) * 5120 + frag(ix & 3, uL); PINV(vU); }
  SB();
  SB();
  WAITVB(8); SB();
  MV16(acc[4], fw[5][0], p[0]); SB();
  LD16(p[8], vP1, sP, 0, "sc1");
  OP4_3("v_add_f32", mm, t4[0], bias);
  SB();
  MV16(acc[5], fw[5][1], p[0]); SB();
  OP4_3("v_add_f32", zz, t4[1], t4[2]);
  SB();
  MV16(acc[6], fw[5][2], p[0]); SB();
  ACC4("v_add_f32", mm, t4[3]);
  SB();
  MV16(acc[7], fw[5][3], p[0]); SB();
  ACC4("v_add_f32", zz, t4[4]);
  SB();
  SB();
  WAITVB(8); SB();
  MV16(acc[4], fw[6][0], p[1]); SB();
  LD16(p[0], vP2, sP, 0, "sc1");
  ACC4("v_add_f32", mm, t4[5]);
  SB();
  MV16(acc[5], fw[6][1], p[1]); SB();
  ACC4("v_add_f32", zz, t4[6]);
  SB();
  MV16(acc[6], fw[6][2], p[1]); SB();
  ACC4("v_add_f32", mm, t4[7]);
  SB();
  MV16(acc[7], fw[6][3], p[1]); SB();
  ACC4("v_add_f32", mm, zz);
  SB();
  SB();
  WAITVB(8); SB();
  PRIO(2); SB();
  MV16(acc[4], fw[7][0], p[2]); SB();
  LD16(p[1], vP3, sP, 0, "sc1");
  OP4_3("v_mul_f32", zz, mm, mm);
  SB();
  MV16(acc[5], fw[7][1], p[2]); SB();
  POLY4(zz, c23);
  SB();
  MV16(acc[6], fw[7][2], p[2]); SB();
  ACC4("v_mul_f32", zz, mm);
  SB();
  MV16(acc[7], fw[7][3], p[2]); SB();
  UN4("v_exp_f32", "", "", zz);
  SB();
  SB();
  WAITVB(8); SB();
  MV16Z(acc[0], fw[0][0], p[3]); SB();
  bf16x8 fo[14];
  const char* sWo = A.ws + WS_WO + (size_t)(dg * 7 + (w < 7 ? w : 6)) * WO_B; OPQ(sWo);
  UN4("v_add_f32", "1.0, ", "", zz);
  SB();
  MV16Z(acc[1], fw[0][1], p[3]); SB();
  UN4("v_rcp_f32", "", "", zz);
  SB();
  MV16Z(acc[2], fw[0][2], p[3]); SB();
  NFMA4(mm, zz);
  SB();
  MV16Z(acc[3], fw[0][3], p[3]); SB();
  { const unsigned x0 = pack2(mm[0], mm[1]), x1 = pack2(mm[2], mm[3]);
    const auto r0 = __builtin_amdgcn_permlane16_swap(x0, x0, false, false), r1 = __builtin_amdgcn_permlane16_swap(x1, x1, false, false);
    ST16E(0xffff, vU, sU, ((u32x4){r0[0], r1[0], r0[1], r1[1]})); }
  SB();
  SB();
  WAITVB(8); SB();
  MV16(acc[0], fw[1][0], p[4]); SB();
  *(LDS_AS f32x4*)(wa0 + 8192) = acc[4];
  SB();
  MV16(acc[1], fw[1][1], p[4]); SB();
  MV16(acc[2], fw[1][2], p[4]); SB();
  MV16(acc[3], fw[1][3], p[4]); SB();
  SB();
  WAITVB(6); SB();
  MV16(acc[0], fw[2][0], p[5]); SB();
  *(LDS_AS f32x4*)(wa1 + 8192) = acc[5];
  SB();
  MV16(acc[1], fw[2][1], p[5]); SB();
  MV16(acc[2], fw[2][2], p[5]); SB();
  MV16(acc[3], fw[2][3], p[5]); SB();
  SB();
  WAITVB(5); SB();
  MV16(acc[0], fw[3][0], p[6]); SB();
  *(LDS_AS f32x4*)(wa2 + 8192) = acc[6];
  SB();
  MV16(acc[1], fw[3][1], p[6]); SB();
  MV16(acc[2], fw[3][2], p[6]); SB();
  MV16(acc[3], fw[3][3], p[6]); SB();
  SB();
  WAITVB(4); SB();
  PRIO(1); SB();
  MV16(acc[0], fw[4][0], p[7]); SB();
  *(LDS_AS f32x4*)(wa3 + 8192) = acc[7];
  SB();
  MV16(acc[1], fw[4][1], p[7]); SB();
  LD16(fo[0], vP0, sWo, 0, "sc1");
  LD16(fo[1], vP1, sWo, 0, "sc1");
  SB();
  MV16(acc[2], fw[4][2], p[7]); SB();
  MV16(acc[3], fw[4][3], p[7]); SB();
  SB();
  WAITVB(5); SB();
  MV16(acc[0], fw[5][0], p[8]); SB();
  LD16(fo[2], vP2, sWo, 0, "sc1");
  LD16(fo[3], vP3, sWo, 0, "sc1"); sWo += 5120; OPQ(sWo);
  SB();
  MV16(acc[1], fw[5][1], p[8]); SB();
  MV16(acc[2], fw[5][2], p[8]); SB();
  MV16(acc[3], fw[5][3], p[8]); SB();
  SB();
  WAITVB(6); SB();
  PRIO(0); SB();
  MV16(acc[0], fw[6][0], p[0]); SB();
  LD16(fo[4], vP0, sWo, 0, "sc1");
  LD16(fo[5], vP1, sWo, 0, "sc1");
  SB();
  MV16(acc[1], fw[6][1], p[0]); SB();
  MV16(acc[2], fw[6][2], p[0]); SB();
  MV16(acc[3], fw[6][3], p[0]); SB();
  SB();
  WAITVB(7); SB();
  MV16(acc[0], fw[7][0], p[1]); SB();
  LD16(fo[6], vP2, sWo, 0, "sc1");
  LD16(fo[7], vP3, sWo, 0, "sc1"); sWo += 5120; OPQ(sWo);
  SB();
  MV16(acc[1], fw[7][1], p[1]); SB();
  MV16(acc[2], fw[7][2], p[1]); SB();
  MV16(acc[3], fw[7][3], p[1]); SB();
  NOP(4); SB();
  *(LDS_AS f32x4*)(wa0 + 12288) = acc[0];
  SB();
  *(LDS_AS f32x4*)(wa1 + 12288) = acc[1];
  SB();
  *(LDS_AS f32x4*)(wa2 + 12288) = acc[2];
  SB();
  *(LDS_AS f32x4*)(wa3 + 12288) = acc[3];
  SB();
  BAR();
  STAMP(8);
  t4[0] = *(LDS_AS const f32x4*)(ra + 8192);
  t4[1] = *(LDS_AS const f32x4*)(ra + 24576);
  t4[2] = *(LDS_AS const f32x4*)(ra + 40960);
  t4[3] = *(LDS_AS const f32x4*)(ra + 57344);
  t4[4] = *(LDS_AS const f32x4*)(rah + 8192);
  t4[5] = *(LDS_AS const f32x4*)(rah + 24576);
  t4[6] = *(LDS_AS const f32x4*)(rah + 40960);
  t4[7] = *(LDS_AS const f32x4*)(rah + 57344);
  { const int ix = 35 + up + 7 * (w >> 2); vU = (ix >> 2) * 5120 + frag(ix & 3, uL); PINV(vU); }
  WAITV(8);
  { const char* f_ = sFU + 42 * FU; const unsigned l_ = lane_now() * 16; ST16X(1, l_, f_, nonce); }
  OP4_3("v_add_f32", mm, t4[0], bias);
  OP4_3("v_add_f32", zz, t4[1], t4[2]);
  ACC4("v_add_f32", mm, t4[3]);
  ACC4("v_add_f32", zz, t4[4]);
  ACC4("v_add_f32", mm, t4[5]);
  ACC4("v_add_f32", zz, t4[6]);
  ACC4("v_add_f32", mm, t4[7]);
  ACC4("v_add_f32", mm, zz);
  OP4_3("v_mul_f32", zz, mm, mm);
  POLY4(zz, c23);
  ACC4("v_mul_f32", zz, mm);
  UN4("v_exp_f32", "", "", zz);
  UN4("v_add_f32", "1.0, ", "", zz);
  UN4("v_rcp_f32", "", "", zz);
  NFMA4(mm, zz);
  { const unsigned x0 = pack2(mm[0], mm[1]), x1 = pack2(mm[2], mm[3]);
    const auto r0 = __builtin_amdgcn_permlane16_swap(x0, x0, false, false), r1 = __builtin_amdgcn_permlane16_swap(x1, x1, false, false);
    ST16E(0xffff, vU, sU, ((u32x4){r0[0], r1[0], r0[1], r1[1]})); }
  STAMP(9);
  LD16(fo[8], vP0, sWo, 0, "sc1");
  LD16(fo[9], vP1, sWo, 0, "sc1");
  LD16(fo[10], vP2, sWo, 0, "sc1");
  LD16(fo[11], vP3, sWo, 0, "sc1"); sWo += 5120; OPQ(sWo);
  WAITV(4);
  { const char* f_ = sFU + 70 * FU; const unsigned l_ = lane_now() * 16; ST16X(1, l_, f_, nonce); }
  LD16(fo[12], vP0, sWo, 0, "sc1");
  LD16(fo[13], vP1, sWo, 0, "sc1");
  { int w_ = w; OPQ(w_); { const int w = w_ & 7, lane = lane_now(); const int n16 = lane & 15, g4 = lane >> 4, q4 = n16 >> 2, pq = lane & 3, r8 = lane >> 3; const unsigned vL16 = lane * 16;
  unsigned vO[7];
#pragma unroll
  for (int i = 0; i < 7; ++i) { vO[i] = line7(64 * i + 8 * w + r8) + (lane & 7) * 16; PINV(vO[i]); }
  const char* sO = A.ws + WS_OT + (size_t)st * OT_T;
  unsigned vFO = (((lane >> 2) & 7) * 8 + (lane & 3)) * FO, ro = lds0 + vL16, roh = ro + 65536;
  unsigned vS0 = pair3(q4) + (2 * (n16 & 3) + (g4 & 1)) * 32 + (g4 >> 1) * 16, vS1 = vS0 - pair3(q4) + pair3(4 + q4);
  const char* sFO = A.ws + WS_FLAGO + ((st >> 1) * 64 + (st & 1) * 4) * FO;
  const char* sS = A.ws + WS_ST + (size_t)(st * 4 + dg) * ST_T + w * 3072;
  const char* sFS = A.ws + WS_FLAGS + ((st * 4 + dg) * 8 + w) * FS;
  asm volatile("" : "+s"(sO), "+s"(sFO), "+s"(sS), "+s"(sFS)); asm volatile("" : "+v"(vFO), "+v"(ro), "+v"(roh), "+v"(vS0), "+v"(vS1));
  STAMP(10);
  STAMP(11);
  WAITV(0);
  asm volatile("" : "+v"(fo[0]), "+v"(fo[1]), "+v"(fo[2]), "+v"(fo[3]), "+v"(fo[4]), "+v"(fo[5]), "+v"(fo[6]), "+v"(fo[7]), "+v"(fo[8]), "+v"(fo[9]), "+v"(fo[10]), "+v"(fo[11]), "+v"(fo[12]), "+v"(fo[13]));
  if (w == 7) poll1<6>(sFO, vFO, nonce);
  BAR();
  STAMP(12);
  bf16x8 of[3][4]; f32x4 as[4];
  DMA1(vO[0], sO, ldsP, 0, "sc1");
  DMA1(vO[1], sO, ldsP, 1024, "sc1");
  DMA1(vO[2], sO, ldsP, 2048, "sc1");
  DMA1(vO[3], sO, ldsP, 3072, "sc1");
  DMA1(vO[4], sO, ldsP, 4096, "sc1");
  DMA1(vO[5], sO, ldsP, 5120, "sc1");
  DMA1(vO[6], sO, ldsP, 6144, "sc1");
  if (w == 7) {
  WAITV(4);
  BAR0();
  WAITV(0);
  BAR0();
  } else {
  WAITV(4);
  BAR0();
  of[0][0] = *(LDS_AS const bf16x8*)(ro + 0);
  of[0][1] = *(LDS_AS const bf16x8*)(ro + 16384);
  of[0][2] = *(LDS_AS const bf16x8*)(ro + 32768);
  of[0][3] = *(LDS_AS const bf16x8*)(ro + 49152);
  STAMP(13);
  of[1][0] = *(LDS_AS const bf16x8*)(roh + 0);
  of[1][1] = *(LDS_AS const bf16x8*)(roh + 16384);
  of[1][2] = *(LDS_AS const bf16x8*)(roh + 32768);
  of[1][3] = *(LDS_AS const bf16x8*)(roh + 49152);
  SB();
  MV16Z(as[0], fo[0], of[0][0]); SB();
  of[2][0] = *(LDS_AS const bf16x8*)(ro + 1024);
  SB();
  MV16Z(as[1], fo[0], of[0][1]); SB();
  of[2][1] = *(LDS_AS const bf16x8*)(ro + 17408);
  SB();
  MV16Z(as[2], fo[0], of[0][2]); SB();
  of[2][2] = *(LDS_AS const bf16x8*)(ro + 33792);
  SB();
  MV16Z(as[3], fo[0], of[0][3]); SB();
  of[2][3] = *(LDS_AS const bf16x8*)(ro + 50176);
  SB();
  SB();
  MV16(as[0], fo[1], of[1][0]); SB();
  of[0][0] = *(LDS_AS const bf16x8*)(roh + 1024);
  SB();
  MV16(as[1], fo[1], of[1][1]); SB();
  of[0][1] = *(LDS_AS const bf16x8*)(roh + 17408);
  SB();
  MV16(as[2], fo[1], of[1][2]); SB();
  of[0][2] = *(LDS_AS const bf16x8*)(roh + 33792);
  SB();
  MV16(as[3], fo[1], of[1][3]); SB();
  of[0][3] = *(LDS_AS const bf16x8*)(roh + 50176);
  SB();
  SB();
  MV16(as[0], fo[2], of[2][0]); SB();
  of[1][0] = *(LDS_AS const bf16x8*)(ro + 2048);
  SB();
  MV16(as[1], fo[2], of[2][1]); SB();
  of[1][1] = *(LDS_AS const bf16x8*)(ro + 18432);
  SB();
  MV16(as[2], fo[2], of[2][2]); SB();
  of[1][2] = *(LDS_AS const bf16x8*)(ro + 34816);
  SB();
  MV16(as[3], fo[2], of[2][3]); SB();
  of[1][3] = *(LDS_AS const bf16x8*)(ro + 51200);
  SB();
  SB();
  MV16(as[0], fo[3], of[0][0]); SB();
  of[2][0] = *(LDS_AS const bf16x8*)(roh + 2048);
  SB();
  MV16(as[1], fo[3], of[0][1]); SB();
  of[2][1] = *(LDS_AS const bf16x8*)(roh + 18432);
  SB();
  MV16(as[2], fo[3], of[0][2]); SB();
  of[2][2] = *(LDS_AS const bf16x8*)(roh + 34816);
  SB();
  MV16(as[3], fo[3], of[0][3]); SB();
  of[2][3] = *(LDS_AS const bf16x8*)(roh + 51200);
  SB();
  SB();
  MV16(as[0], fo[4], of[1][0]); SB();
  WAITV(0);
  BAR0();
  of[0][0] = *(LDS_AS const bf16x8*)(ro + 3072);
  SB();
  MV16(as[1], fo[4], of[1][1]); SB();
  of[0][1] = *(LDS_AS const bf16x8*)(ro + 19456);
  SB();
  MV16(as[2], fo[4], of[1][2]); SB();
  of[0][2] = *(LDS_AS const bf16x8*)(ro + 35840);
  SB();
  MV16(as[3], fo[4], of[1][3]); SB();
  of[0][3] = *(LDS_AS const bf16x8*)(ro + 52224);
  SB();
  SB();
  MV16(as[0], fo[5], of[2][0]); SB();
  of[1][0] = *(LDS_AS const bf16x8*)(roh + 3072);
  SB();
  MV16(as[1], fo[5], of[2][1]); SB();
  of[1][1] = *(LDS_AS const bf16x8*)(roh + 19456);
  SB();
  MV16(as[2], fo[5], of[2][2]); SB();
  of[1][2] = *(LDS_AS const bf16x8*)(roh + 35840);
  SB();
  MV16(as[3], fo[5], of[2][3]); SB();
  of[1][3] = *(LDS_AS const bf16x8*)(roh + 52224);
  SB();
  SB();
  MV16(as[0], fo[6], of[0][0]); SB();
  of[2][0] = *(LDS_AS const bf16x8*)(ro + 4096);
  SB();
  MV16(as[1], fo[6], of[0][1]); SB();
  of[2][1] = *(LDS_AS const bf16x8*)(ro + 20480);
  SB();
  MV16(as[2], fo[6], of[0][2]); SB();
  of[2][2] = *(LDS_AS const bf16x8*)(ro + 36864);
  SB();
  MV16(as[3], fo[6], of[0][3]); SB();
  of[2][3] = *(LDS_AS const bf16x8*)(ro + 53248);
  SB();
  SB();
  MV16(as[0], fo[7], of[1][0]); SB();
  of[0][0] = *(LDS_AS const bf16x8*)(roh + 4096);
  SB();
  MV16(as[1], fo[7], of[1][1]); SB();
  of[0][1] = *(LDS_AS const bf16x8*)(roh + 20480);
  SB();
  MV16(as[2], fo[7], of[1][2]); SB();
  of[0][2] = *(LDS_AS const bf16x8*)(roh + 36864);
  SB();
  MV16(as[3], fo[7], of[1][3]); SB();
  of[0][3] = *(LDS_AS const bf16x8*)(roh + 53248);
  SB();
  SB();
  MV16(as[0], fo[8], of[2][0]); SB();
  of[1][0] = *(LDS_AS const bf16x8*)(ro + 5120);
  SB();
  MV16(as[1], fo[8], of[2][1]); SB();
  of[1][1] = *(LDS_AS const bf16x8*)(ro + 21504);
  SB();
  MV16(as[2], fo[8], of[2][2]); SB();
  of[1][2] = *(LDS_AS const bf16x8*)(ro + 37888);
  SB();
  MV16(as[3], fo[8], of[2][3]); SB();
  of[1][3] = *(LDS_AS const bf16x8*)(ro + 54272);
  SB();
  SB();
  MV16(as[0], fo[9], of[0][0]); SB();
  of[2][0] = *(LDS_AS const bf16x8*)(roh + 5120);
  SB();
  MV16(as[1], fo[9], of[0][1]); SB();
  of[2][1] = *(LDS_AS const bf16x8*)(roh + 21504);
  SB();
  MV16(as[2], fo[9], of[0][2]); SB();
  of[2][2] = *(LDS_AS const bf16x8*)(roh + 37888);
  SB();
  MV16(as[3], fo[9], of[0][3]); SB();
  of[2][3] = *(LDS_AS const bf16x8*)(roh + 54272);
  SB();
  SB();
  MV16(as[0], fo[10], of[1][0]); SB();
  of[0][0] = *(LDS_AS const bf16x8*)(ro + 6144);
  SB();
  MV16(as[1], fo[10], of[1][1]); SB();
  of[0][1] = *(LDS_AS const bf16x8*)(ro + 22528);
  SB();
  MV16(as[2], fo[10], of[1][2]); SB();
  of[0][2] = *(LDS_AS const bf16x8*)(ro + 38912);
  SB();
  MV16(as[3], fo[10], of[1][3]); SB();
  of[0][3] = *(LDS_AS const bf16x8*)(ro + 55296);
  SB();
  SB();
  MV16(as[0], fo[11], of[2][0]); SB();
  of[1][0] = *(LDS_AS const bf16x8*)(roh + 6144);
  SB();
  MV16(as[1], fo[11], of[2][1]); SB();
  of[1][1] = *(LDS_AS const bf16x8*)(roh + 22528);
  SB();
  MV16(as[2], fo[11], of[2][2]); SB();
  of[1][2] = *(LDS_AS const bf16x8*)(roh + 38912);
  SB();
  MV16(as[3], fo[11], of[2][3]); SB();
  of[1][3] = *(LDS_AS const bf16x8*)(roh + 55296);
  SB();
  SB();
  MV16(as[0], fo[12], of[0][0]); SB();
  MV16(as[1], fo[12], of[0][1]); SB();
  MV16(as[2], fo[12], of[0][2]); SB();
  MV16(as[3], fo[12], of[0][3]); SB();
  SB();
  MV16(as[0], fo[13], of[1][0]); SB();
  MV16(as[1], fo[13], of[1][1]); SB();
  MV16(as[2], fo[13], of[1][2]); SB();
  MV16(as[3], fo[13], of[1][3]); SB();
  NOP(5); SB();
  { const auto r0 = __builtin_amdgcn_permlane16_swap(pack2(as[0][0], as[0][1]), pack2(as[1][0], as[1][1]), false, false);
    const auto r1 = __builtin_amdgcn_permlane16_swap(pack2(as[0][2], as[0][3]), pack2(as[1][2], as[1][3]), false, false);
    ST16(vS0, sS, 0, ((u32x4){r0[0], r1[0], r0[1], r1[1]})); } SB();
  { const auto r0 = __builtin_amdgcn_permlane16_swap(pack2(as[2][0], as[2][1]), pack2(as[3][0], as[3][1]), false, false);
    const auto r1 = __builtin_amdgcn_permlane16_swap(pack2(as[2][2], as[2][3]), pack2(as[3][2], as[3][3]), false, false);
    ST16(vS1, sS, 0, ((u32x4){r0[0], r1[0], r0[1], r1[1]})); } SB();
  STAMP(14);
  WAITV(0);
  ST16X(15, vL16, sFS, nonce);
  STAMP(15);
  }
  STAMP_DUMP();
  }}
  } else {
  WAITV(0);
  __builtin_amdgcn_s_sleep(40);
  poll1<16>(sFH, vFH, nonce);
  STAMP(3);
  __builtin_amdgcn_s_sleep(64);
  BAR();
  const unsigned vP0 = frag(0, lane), vP1 = frag(1, lane), vP2 = frag(2, lane), vP3 = frag(3, lane);
  bf16x8 fo[14];
  const char* sWo = A.ws + WS_WO + (size_t)(dg * 7 + (w < 7 ? w : 6)) * WO_B; OPQ(sWo);
  LD16(fo[0], vP0, sWo, 0, "sc1");
  LD16(fo[1], vP1, sWo, 0, "sc1");
  LD16(fo[2], vP2, sWo, 0, "sc1");
  LD16(fo[3], vP3, sWo, 0, "sc1"); sWo += 5120; OPQ(sWo);
  LD16(fo[4], vP0, sWo, 0, "sc1");
  LD16(fo[5], vP1, sWo, 0, "sc1");
  LD16(fo[6], vP2, sWo, 0, "sc1");
  LD16(fo[7], vP3, sWo, 0, "sc1"); sWo += 5120; OPQ(sWo);
  LD16(fo[8], vP0, sWo, 0, "sc1");
  LD16(fo[9], vP1, sWo, 0, "sc1");
  LD16(fo[10], vP2, sWo, 0, "sc1");
  LD16(fo[11], vP3, sWo, 0, "sc1"); sWo += 5120; OPQ(sWo);
  LD16(fo[12], vP0, sWo, 0, "sc1");
  LD16(fo[13], vP1, sWo, 0, "sc1");
  { int w_ = w; OPQ(w_); { const int w = w_ & 7, lane = lane_now(); const int n16 = lane & 15, g4 = lane >> 4, q4 = n16 >> 2, pq = lane & 3, r8 = lane >> 3; const unsigned vL16 = lane * 16;
  unsigned vO[7];
#pragma unroll
  for (int i = 0; i < 7; ++i) { vO[i] = line7(64 * i + 8 * w + r8) + (lane & 7) * 16; PINV(vO[i]); }
  const char* sO = A.ws + WS_OT + (size_t)st * OT_T;
  unsigned vFO = (((lane >> 2) & 7) * 8 + (lane & 3)) * FO, ro = lds0 + vL16, roh = ro + 65536;
  unsigned vS0 = pair3(q4) + (2 * (n16 & 3) + (g4 & 1)) * 32 + (g4 >> 1) * 16, vS1 = vS0 - pair3(q4) + pair3(4 + q4);
  const char* sFO = A.ws + WS_FLAGO + ((st >> 1) * 64 + (st & 1) * 4) * FO;
  const char* sS = A.ws + WS_ST + (size_t)(st * 4 + dg) * ST_T + w * 3072;
  const char* sFS = A.ws + WS_FLAGS + ((st * 4 + dg) * 8 + w) * FS;
  asm volatile("" : "+s"(sO), "+s"(sFO), "+s"(sS), "+s"(sFS)); asm volatile("" : "+v"(vFO), "+v"(ro), "+v"(roh), "+v"(vS0), "+v"(vS1));
  STAMP(11);
  WAITV(0);
  asm volatile("" : "+v"(fo[0]), "+v"(fo[1]), "+v"(fo[2]), "+v"(fo[3]), "+v"(fo[4]), "+v"(fo[5]), "+v"(fo[6]), "+v"(fo[7]), "+v"(fo[8]), "+v"(fo[9]), "+v"(fo[10]), "+v"(fo[11]), "+v"(fo[12]), "+v"(fo[13]));
  if (w == 7) poll1<6>(sFO, vFO, nonce);
  BAR();
  STAMP(12);
  bf16x8 of[3][4]; f32x4 as[4];
  DMA1(vO[0], sO, ldsP, 0, "sc1");
  DMA1(vO[1], sO, ldsP, 1024, "sc1");
  DMA1(vO[2], sO, ldsP, 2048, "sc1");
  DMA1(vO[3], sO, ldsP, 3072, "sc1");
  DMA1(vO[4], sO, ldsP, 4096, "sc1");
  DMA1(vO[5], sO, ldsP, 5120, "sc1");
  DMA1(vO[6], sO, ldsP, 6144, "sc1");
  if (w == 7) {
  WAITV(4);
  BAR0();
  WAITV(0);
  BAR0();
  } else {
  WAITV(4);
  BAR0();
  of[0][0] = *(LDS_AS const bf16x8*)(ro + 0);
  of[0][1] = *(LDS_AS const bf16x8*)(ro + 16384);
  of[0][2] = *(LDS_AS const bf16x8*)(ro + 32768);
  of[0][3] = *(LDS_AS const bf16x8*)(ro + 49152);
  STAMP(13);
  of[1][0] = *(LDS_AS const bf16x8*)(roh + 0);
  of[1][1] = *(LDS_AS const bf16x8*)(roh + 16384);
  of[1][2] = *(LDS_AS const bf16x8*)(roh + 32768);
  of[1][3] = *(LDS_AS const bf16x8*)(roh + 49152);
  SB();
  MV16Z(as[0], fo[0], of[0][0]); SB();
  of[2][0] = *(LDS_AS const bf16x8*)(ro + 1024);
  SB();
  MV16Z(as[1], fo[0], of[0][1]); SB();
  of[2][1] = *(LDS_AS const bf16x8*)(ro + 17408);
  SB();
  MV16Z(as[2], fo[0], of[0][2]); SB();
  of[2][2] = *(LDS_AS const bf16x8*)(ro + 33792);
  SB();
  MV16Z(as[3], fo[0], of[0][3]); SB();
  of[2][3] = *(LDS_AS const bf16x8*)(ro + 50176);
  SB();
  SB();
  MV16(as[0], fo[1], of[1][0]); SB();
  of[0][0] = *(LDS_AS const bf16x8*)(roh + 1024);
  SB();
  MV16(as[1], fo[1], of[1][1]); SB();
  of[0][1] = *(LDS_AS const bf16x8*)(roh + 17408);
  SB();
  MV16(as[2], fo[1], of[1][2]); SB();
  of[0][2] = *(LDS_AS const bf16x8*)(roh + 33792);
  SB();
  MV16(as[3], fo[1], of[1][3]); SB();
  of[0][3] = *(LDS_AS const bf16x8*)(roh + 50176);
  SB();
  SB();
  MV16(as[0], fo[2], of[2][0]); SB();
  of[1][0] = *(LDS_AS const bf16x8*)(ro + 2048);
  SB();
  MV16(as[1], fo[2], of[2][1]); SB();
  of[1][1] = *(LDS_AS const bf16x8*)(ro + 18432);
  SB();
  MV16(as[2], fo[2], of[2][2]); SB();
  of[1][2] = *(LDS_AS const bf16x8*)(ro + 34816);
  SB();
  MV16(as[3], fo[2], of[2][3]); SB();
  of[1][3] = *(LDS_AS const bf16x8*)(ro + 51200);
  SB();
  SB();
  MV16(as[0], fo[3], of[0][0]); SB();
  of[2][0] = *(LDS_AS const bf16x8*)(roh + 2048);
  SB();
  MV16(as[1], fo[3], of[0][1]); SB();
  of[2][1] = *(LDS_AS const bf16x8*)(roh + 18432);
  SB();
  MV16(as[2], fo[3], of[0][2]); SB();
  of[2][2] = *(LDS_AS const bf16x8*)(roh + 34816);
  SB();
  MV16(as[3], fo[3], of[0][3]); SB();
  of[2][3] = *(LDS_AS const bf16x8*)(roh + 51200);
  SB();
  SB();
  MV16(as[0], fo[4], of[1][0]); SB();
  WAITV(0);
  BAR0();
  of[0][0] = *(LDS_AS const bf16x8*)(ro + 3072);
  SB();
  MV16(as[1], fo[4], of[1][1]); SB();
  of[0][1] = *(LDS_AS const bf16x8*)(ro + 19456);
  SB();
  MV16(as[2], fo[4], of[1][2]); SB();
  of[0][2] = *(LDS_AS const bf16x8*)(ro + 35840);
  SB();
  MV16(as[3], fo[4], of[1][3]); SB();
  of[0][3] = *(LDS_AS const bf16x8*)(ro + 52224);
  SB();
  SB();
  MV16(as[0], fo[5], of[2][0]); SB();
  of[1][0] = *(LDS_AS const bf16x8*)(roh + 3072);
  SB();
  MV16(as[1], fo[5], of[2][1]); SB();
  of[1][1] = *(LDS_AS const bf16x8*)(roh + 19456);
  SB();
  MV16(as[2], fo[5], of[2][2]); SB();
  of[1][2] = *(LDS_AS const bf16x8*)(roh + 35840);
  SB();
  MV16(as[3], fo[5], of[2][3]); SB();
  of[1][3] = *(LDS_AS const bf16x8*)(roh + 52224);
  SB();
  SB();
  MV16(as[0], fo[6], of[0][0]); SB();
  of[2][0] = *(LDS_AS const bf16x8*)(ro + 4096);
  SB();
  MV16(as[1], fo[6], of[0][1]); SB();
  of[2][1] = *(LDS_AS const bf16x8*)(ro + 20480);
  SB();
  MV16(as[2], fo[6], of[0][2]); SB();
  of[2][2] = *(LDS_AS const bf16x8*)(ro + 36864);
  SB();
  MV16(as[3], fo[6], of[0][3]); SB();
  of[2][3] = *(LDS_AS const bf16x8*)(ro + 53248);
  SB();
  SB();
  MV16(as[0], fo[7], of[1][0]); SB();
  of[0][0] = *(LDS_AS const bf16x8*)(roh + 4096);
  SB();
  MV16(as[1], fo[7], of[1][1]); SB();
  of[0][1] = *(LDS_AS const bf16x8*)(roh + 20480);
  SB();
  MV16(as[2], fo[7], of[1][2]); SB();
  of[0][2] = *(LDS_AS const bf16x8*)(roh + 36864);
  SB();
  MV16(as[3], fo[7], of[1][3]); SB();
  of[0][3] = *(LDS_AS const bf16x8*)(roh + 53248);
  SB();
  SB();
  MV16(as[0], fo[8], of[2][0]); SB();
  of[1][0] = *(LDS_AS const bf16x8*)(ro + 5120);
  SB();
  MV16(as[1], fo[8], of[2][1]); SB();
  of[1][1] = *(LDS_AS const bf16x8*)(ro + 21504);
  SB();
  MV16(as[2], fo[8], of[2][2]); SB();
  of[1][2] = *(LDS_AS const bf16x8*)(ro + 37888);
  SB();
  MV16(as[3], fo[8], of[2][3]); SB();
  of[1][3] = *(LDS_AS const bf16x8*)(ro + 54272);
  SB();
  SB();
  MV16(as[0], fo[9], of[0][0]); SB();
  of[2][0] = *(LDS_AS const bf16x8*)(roh + 5120);
  SB();
  MV16(as[1], fo[9], of[0][1]); SB();
  of[2][1] = *(LDS_AS const bf16x8*)(roh + 21504);
  SB();
  MV16(as[2], fo[9], of[0][2]); SB();
  of[2][2] = *(LDS_AS const bf16x8*)(roh + 37888);
  SB();
  MV16(as[3], fo[9], of[0][3]); SB();
  of[2][3] = *(LDS_AS const bf16x8*)(roh + 54272);
  SB();
  SB();
  MV16(as[0], fo[10], of[1][0]); SB();
  of[0][0] = *(LDS_AS const bf16x8*)(ro + 6144);
  SB();
  MV16(as[1], fo[10], of[1][1]); SB();
  of[0][1] = *(LDS_AS const bf16x8*)(ro + 22528);
  SB();
  MV16(as[2], fo[10], of[1][2]); SB();
  of[0][2] = *(LDS_AS const bf16x8*)(ro + 38912);
  SB();
  MV16(as[3], fo[10], of[1][3]); SB();
  of[0][3] = *(LDS_AS const bf16x8*)(ro + 55296);
  SB();
  SB();
  MV16(as[0], fo[11], of[2][0]); SB();
  of[1][0] = *(LDS_AS const bf16x8*)(roh + 6144);
  SB();
  MV16(as[1], fo[11], of[2][1]); SB();
  of[1][1] = *(LDS_AS const bf16x8*)(roh + 22528);
  SB();
  MV16(as[2], fo[11], of[2][2]); SB();
  of[1][2] = *(LDS_AS const bf16x8*)(roh + 38912);
  SB();
  MV16(as[3], fo[11], of[2][3]); SB();
  of[1][3] = *(LDS_AS const bf16x8*)(roh + 55296);
  SB();
  SB();
  MV16(as[0], fo[12], of[0][0]); SB();
  MV16(as[1], fo[12], of[0][1]); SB();
  MV16(as[2], fo[12], of[0][2]); SB();
  MV16(as[3], fo[12], of[0][3]); SB();
  SB();
  MV16(as[0], fo[13], of[1][0]); SB();
  MV16(as[1], fo[13], of[1][1]); SB();
  MV16(as[2], fo[13], of[1][2]); SB();
  MV16(as[3], fo[13], of[1][3]); SB();
  NOP(5); SB();
  { const auto r0 = __builtin_amdgcn_permlane16_swap(pack2(as[0][0], as[0][1]), pack2(as[1][0], as[1][1]), false, false);
    const auto r1 = __builtin_amdgcn_permlane16_swap(pack2(as[0][2], as[0][3]), pack2(as[1][2], as[1][3]), false, false);
    ST16(vS0, sS, 0, ((u32x4){r0[0], r1[0], r0[1], r1[1]})); } SB();
  { const auto r0 = __builtin_amdgcn_permlane16_swap(pack2(as[2][0], as[2][1]), pack2(as[3][0], as[3][1]), false, false);
    const auto r1 = __builtin_amdgcn_permlane16_swap(pack2(as[2][2], as[2][3]), pack2(as[3][2], as[3][3]), false, false);
    ST16(vS1, sS, 0, ((u32x4){r0[0], r1[0], r0[1], r1[1]})); } SB();
  STAMP(14);
  WAITV(0);
  ST16X(15, vL16, sFS, nonce);
  STAMP(15);
  }
  if (q == 0 && jb == 31 && w == 7) ST4X(0u, A.ws0, nonce);
  STAMP_DUMP();
  asm volatile(".Lcpf_e:");
  }}
  }
  }
}

extern "C" void kernel_launch(void* const* d_in, const int* in_sizes, int n_in,
                              void* d_out, int out_size, void* d_ws, size_t ws_size,
                              hipStream_t stream) {
  fused_kernel<<<256, 512, 0, stream>>>((const char*)d_in[0], (const char*)d_in[1], (const char*)d_in[2], (const char*)d_in[4],
                                        (const char*)d_in[5], (const char*)d_in[6], (const char*)d_in[7], (char*)d_ws, (char*)d_out);
}
```

```cpp
#include <hip/hip_runtime.h>
#include <hip/hip_bf16.h>
#include <stdint.h>

typedef short bf16x8 __attribute__((ext_vector_type(8)));
typedef short s16x4 __attribute__((ext_vector_type(4)));
typedef float f32x4 __attribute__((ext_vector_type(4)));
typedef float f32x16 __attribute__((ext_vector_type(16)));
typedef unsigned u32x4 __attribute__((ext_vector_type(4)));
typedef unsigned u32x2 __attribute__((ext_vector_type(2)));
typedef __bf16 bf16x2_t __attribute__((ext_vector_type(2)));
typedef float f32x2_t __attribute__((ext_vector_type(2)));
#define LDS_AS __attribute__((address_space(3)))
#define DEV __device__ __forceinline__

#define STAMP(i)
#define STAMP_DUMP()

#define SB() __builtin_amdgcn_sched_barrier(0)
#define WAITV(n) asm volatile("s_waitcnt vmcnt(" #n ")" ::: "memory")
#define WAITV1(n, a) asm volatile("s_waitcnt vmcnt(" #n ")" : "+v"(a) :: "memory")
#define WAITV2(n, a, b) asm volatile("s_waitcnt vmcnt(" #n ")" : "+v"(a), "+v"(b) :: "memory")
#define WAITV3(n, a, b, c) asm volatile("s_waitcnt vmcnt(" #n ")" : "+v"(a), "+v"(b), "+v"(c) :: "memory")
#define WAITV4(n, a, b, c, d) asm volatile("s_waitcnt vmcnt(" #n ")" : "+v"(a), "+v"(b), "+v"(c), "+v"(d) :: "memory")
#define WAITL(n) do { asm volatile("" ::: "memory"); __builtin_amdgcn_s_waitcnt(0xc07f | ((n) << 8)); asm volatile("" ::: "memory"); } while (0)
#define BAR() do { WAITL(0); __builtin_amdgcn_s_barrier(); asm volatile("" ::: "memory"); } while (0)
#define BAR0() do { asm volatile("s_barrier" ::: "memory"); } while (0)
#define OPQ(p) asm volatile("" : "+s"(p))
#define PINV(p) asm volatile("" : "+v"(p))
#define PINF(p) asm("" : "+v"(p))
#define PRIO(n) __builtin_amdgcn_s_setprio(n)
#define LD16(dst, voff, sb, imm, bits) asm volatile("global_load_dwordx4 %0, %1, %2 offset:" #imm " " bits : "=&v"(dst) : "v"(voff), "s"(sb) : "memory")
#define LD16M(dst, voff, sb, m) asm volatile("s_mov_b64 exec, %3\n\tglobal_load_dwordx4 %0, %1, %2 sc1\n\ts_mov_b64 exec, -1" : "=&v"(dst) : "v"(voff), "s"(sb), "s"(m) : "memory")
#define LD8(dst, voff, sb, imm, bits) asm volatile("global_load_dwordx2 %0, %1, %2 offset:" #imm " " bits : "=&v"(dst) : "v"(voff), "s"(sb) : "memory")
#define LD4(dst, voff, sb, imm, bits) asm volatile("global_load_dword %0, %1, %2 offset:" #imm " " bits : "=&v"(dst) : "v"(voff), "s"(sb) : "memory")
#define LD2(dst, voff, sb, imm, bits) asm volatile("global_load_ushort %0, %1, %2 offset:" #imm " " bits : "=&v"(dst) : "v"(voff), "s"(sb) : "memory")
#define DMA1(v0, sb, lds, limm, bits) \
  asm volatile("s_add_u32 m0, %2, %3\n\ts_nop 0\n\tglobal_load_lds_dwordx4 %0, %1 " bits :: "v"(v0), "s"(sb), "s"(lds), "n"(limm) : "memory", "scc")
#define DMA2(v0, v1, sb, lds, limm, bits) \
  asm volatile("s_add_u32 m0, %3, %4\n\ts_nop 0\n\tglobal_load_lds_dwordx4 %0, %2 " bits "\n\tglobal_load_lds_dwordx4 %1, %2 offset:1024 " bits \
               :: "v"(v0), "v"(v1), "s"(sb), "s"(lds), "n"(limm) : "memory", "scc")
#define TOUCH(v0, sb, lds) asm volatile("s_mov_b32 m0, %2\n\ts_nop 0\n\tglobal_load_lds_dword %0, %1 sc1" :: "v"(v0), "s"(sb), "s"(lds) : "memory")
#define ST16(voff, sb, imm, v) do { const u32x4 v_ = (v); asm volatile("global_store_dwordx4 %0, %1, %2 offset:" #imm " sc1\n\ts_nop 1" :: "v"(voff), "v"(v_), "s"(sb) : "memory"); } while (0)
#define ST16M(mask, voff, sb, v) do { const u32x4 v_ = (v); \
  asm volatile("s_mov_b32 exec_lo, %3\n\ts_mov_b32 exec_hi, %3\n\tglobal_store_dwordx4 %0, %1, %2 sc1\n\ts_mov_b64 exec, -1\n\ts_nop 0" :: "v"(voff), "v"(v_), "s"(sb), "s"(mask) : "memory"); } while (0)
#define ST16E(mask, voff, sb, v) do { const u32x4 v_ = (v); \
  asm volatile("s_mov_b32 exec_lo, " #mask "\n\ts_mov_b32 exec_hi, " #mask "\n\tglobal_store_dwordx4 %0, %1, %2 sc1\n\ts_mov_b64 exec, -1\n\ts_nop 0" :: "v"(voff), "v"(v_), "s"(sb) : "memory"); } while (0)
#define ST16L56(voff, sb, v) do { const u32x4 v_ = (v); \
  asm volatile("s_mov_b32 exec_hi, 0xffffff\n\tglobal_store_dwordx4 %0, %1, %2 sc1\n\ts_mov_b32 exec_hi, -1\n\ts_nop 0" :: "v"(voff), "v"(v_), "s"(sb) : "memory"); } while (0)
#define ST16L48(voff, sb, imm, v) do { const u32x4 v_ = (v); \
  asm volatile("s_mov_b32 exec_hi, 0xffff\n\tglobal_store_dwordx4 %0, %1, %2 offset:" #imm " sc1\n\ts_mov_b32 exec_hi, -1\n\ts_nop 0" :: "v"(voff), "v"(v_), "s"(sb) : "memory"); } while (0)
#define ST4(voff, sb, v) do { const unsigned v_ = (v); asm volatile("global_store_dword %0, %1, %2 sc1" :: "v"(voff), "v"(v_), "s"(sb) : "memory"); } while (0)
#define ST4M(mask, voff, sb) asm volatile("s_mov_b32 exec_lo, %2\n\ts_mov_b32 exec_hi, %2\n\tglobal_store_dword %0, %0, %1 sc1\n\ts_mov_b64 exec, -1" :: "v"(voff), "s"(sb), "s"(mask) : "memory")
#define ST4X(voff, sb, v) do { const unsigned v_ = (v); asm volatile("s_mov_b64 exec, 1\n\tglobal_store_dword %0, %1, %2 sc1\n\ts_mov_b64 exec, -1" :: "v"(voff), "v"(v_), "s"(sb) : "memory"); } while (0)
#define ST16X(mask, voff, sb, v) do { const u32x4 v_ = {(v), (v), (v), (v)}; asm volatile("s_mov_b64 exec, " #mask "\n\tglobal_store_dwordx4 %0, %1, %2 sc1\n\ts_mov_b64 exec, -1\n\ts_nop 0" :: "v"(voff), "v"(v_), "s"(sb) : "memory"); } while (0)

DEV float lo2f(unsigned v) { return __uint_as_float(v << 16); }
DEV float hi2f(unsigned v) { return __uint_as_float(v & 0xffff0000u); }
DEV unsigned pack2(float a, float b) { f32x2_t v = {a, b}; return __builtin_bit_cast(unsigned, __builtin_convertvector(v, bf16x2_t)); }
DEV float rbf(float a) { return __uint_as_float(pack2(0.f, a)); }
DEV f32x2_t rbf2(f32x2_t a) { return (f32x2_t){rbf(a[0]), rbf(a[1])}; }
DEV float dot2(unsigned a, unsigned b, float c) {
  return __builtin_amdgcn_fdot2_f32_bf16(__builtin_bit_cast(bf16x2_t, a), __builtin_bit_cast(bf16x2_t, b), c, false);
}
DEV bf16x8 cat8(s16x4 a, s16x4 b) { return __builtin_shufflevector(a, b, 0, 1, 2, 3, 4, 5, 6, 7); }
DEV bf16x8 tr8u(unsigned p, int second) {
  return cat8(__builtin_amdgcn_ds_read_tr16_b64_v4i16((LDS_AS s16x4*)p), __builtin_amdgcn_ds_read_tr16_b64_v4i16((LDS_AS s16x4*)(p + second)));
}
template <int CTRL> DEV float dpp_add(float v) {
  return v + __builtin_bit_cast(float, __builtin_amdgcn_update_dpp(0, __builtin_bit_cast(int, v), CTRL, 0xf, 0xf, true));
}
DEV float red32(float v) {
  v = dpp_add<0x140>(dpp_add<0x141>(dpp_add<0x4E>(dpp_add<0xB1>(v))));
  const auto r = __builtin_amdgcn_permlane16_swap(__float_as_uint(v), __float_as_uint(v), false, false);
  return __uint_as_float(r[0]) + __uint_as_float(r[1]);
}
DEV float fmx(float a, float b) { return __builtin_elementwise_maximum(a, b); }
DEV float xmax2(float a, float b) {
  const auto r = __builtin_amdgcn_permlane16_swap(__float_as_uint(a), __float_as_uint(b), false, false);
  const float v = fmx(__uint_as_float(r[0]), __uint_as_float(r[1]));
  const auto s = __builtin_amdgcn_permlane32_swap(__float_as_uint(v), __float_as_uint(v), false, false);
  return fmx(__uint_as_float(s[0]), __uint_as_float(s[1]));
}
DEV float xsum2(float a, float b) {
  const auto r = __builtin_amdgcn_permlane16_swap(__float_as_uint(a), __float_as_uint(b), false, false);
  const float v = __uint_as_float(r[0]) + __uint_as_float(r[1]);
  const auto s = __builtin_amdgcn_permlane32_swap(__float_as_uint(v), __float_as_uint(v), false, false);
  return __uint_as_float(s[0]) + __uint_as_float(s[1]);
}
DEV f32x2_t xbcast2(float v) {
  const auto r = __builtin_amdgcn_permlane16_swap(__float_as_uint(v), __float_as_uint(v), false, false);
  return (f32x2_t){__uint_as_float(r[0]), __uint_as_float(r[1])};
}
#define MFMA32(a, b, c) __builtin_amdgcn_mfma_f32_32x32x16_bf16(a, b, c, 0, 0, 0)
#define MFMA16(a, b, c) __builtin_amdgcn_mfma_f32_16x16x32_bf16(a, b, c, 0, 0, 0)
#define MV16(d, a, b) asm volatile("v_mfma_f32_16x16x32_bf16 %0, %1, %2, %0" : "+v"(d) : "v"(a), "v"(b))
#define MV16Z(d, a, b) asm volatile("v_mfma_f32_16x16x32_bf16 %0, %1, %2, 0" : "=&v"(d) : "v"(a), "v"(b))
#define MV32(d, a, b) asm volatile("v_mfma_f32_32x32x16_bf16 %0, %1, %2, %0" : "+v"(d) : "v"(a), "v"(b))
#define MV32Z(d, a, b) asm volatile("v_mfma_f32_32x32x16_bf16 %0, %1, %2, 0" : "=&v"(d) : "v"(a), "v"(b))
#define NOP(n) asm volatile("s_nop " #n)
DEV int lane_now() { int l; asm volatile("v_mbcnt_lo_u32_b32 %0, -1, 0\n\tv_mbcnt_hi_u32_b32 %0, -1, %0" : "=v"(l)); __builtin_assume(l >= 0 && l < 64); return l; }
#define SEL(h, i) __uint_as_float((m1[i] & __float_as_uint(sc[h][1][i])) | (~m1[i] & __float_as_uint(sc[h][0][i])))
#define Q4(a, i) (f32x4){a[4 * (i)], a[4 * (i) + 1], a[4 * (i) + 2], a[4 * (i) + 3]}

constexpr int kSpinCap = 20000;
template <int SLEEP> DEV void poll1(const char* sb, unsigned voff, unsigned nonce) {
#pragma unroll 1
  for (int spin = 0; spin < kSpinCap; ++spin) {
    __builtin_amdgcn_s_sleep(SLEEP);
    unsigned v;
    asm volatile("global_load_dword %0, %1, %2 sc1\n\ts_waitcnt vmcnt(0)" : "=&v"(v) : "v"(voff), "s"(sb) : "memory");
    if (__ballot(v != nonce) == 0ull) return;
  }
}
DEV unsigned bad4(const u32x4& v, unsigned n) { return (v[0] ^ n) | (v[1] ^ n) | (v[2] ^ n) | (v[3] ^ n); }
template <int SLEEP> DEV void poll4(const char* sb, unsigned voff, unsigned nonce) {
#pragma unroll 1
  for (int spin = 0; spin < kSpinCap; ++spin) {
    __builtin_amdgcn_s_sleep(SLEEP);
    u32x4 a;
    asm volatile("global_load_dwordx4 %0, %1, %2 sc1\n\ts_waitcnt vmcnt(0)" : "=&v"(a) : "v"(voff), "s"(sb) : "memory");
    if (__ballot(bad4(a, nonce) != 0u) == 0ull) return;
  }
}
#define LDS_ARRIVE(old, addr) do { unsigned v_; \
  asm volatile("s_mov_b64 exec, 1\n\tds_add_rtn_u32 %0, %2, %3\n\ts_mov_b64 exec, -1\n\ts_waitcnt lgkmcnt(0)\n\tv_readfirstlane_b32 %1, %0" \
               : "=&v"(v_), "=s"(old) : "v"(addr), "v"(1u) : "memory"); old = __builtin_amdgcn_readfirstlane(old); } while (0)
DEV unsigned frag(int h, int L) {
  return 512 + 1152 * h + L * 16 + (L >= 56 - 8 * h ? 128 : 0);
}

DEV unsigned line7(unsigned l) { return (l + ((l + 4) * 9363 >> 16)) * 128; }

DEV int pair3(int p) { const int g = (p * 11) >> 5, r = p - 3 * g; return g * 1024 + (r ? 256 + 256 * r : 0); }

DEV float ln_inv(float var) {
  const float sd = rbf(__builtin_amdgcn_sqrtf(rbf(rbf(fmaxf(var, 0.f)) + 1e-5f)));
  return __builtin_amdgcn_rcpf(sd);
}
DEV float red64(float v) {
  v = red32(v);
  const auto r = __builtin_amdgcn_permlane32_swap(__float_as_uint(v), __float_as_uint(v), false, false);
  return __uint_as_float(r[0]) + __uint_as_float(r[1]);
}
DEV void ln_1(u32x4& p, f32x2_t (&d)[4], bool act) {
  const float s = dot2(p[0], 0x3f803f80u, dot2(p[1], 0x3f803f80u, 0.f)) + dot2(p[2], 0x3f803f80u, dot2(p[3], 0x3f803f80u, 0.f));
  const float mean = red64(act ? s : 0.f) * (1.0f / 448.0f), mb = rbf(mean), dm = mean - mb;
  f32x2_t q = {0.f, 0.f};
#pragma unroll
  for (int i = 0; i < 4; ++i) { d[i] = (f32x2_t){lo2f(p[i]), hi2f(p[i])} - (f32x2_t){mb, mb}; q = d[i] * d[i] + q; }
  const float inv = ln_inv(red64(act ? q[0] + q[1] : 0.f) * (1.0f / 448.0f) - dm * dm);
#pragma unroll
  for (int i = 0; i < 4; ++i) { d[i] = rbf2(d[i]) * (f32x2_t){inv, inv}; p[i] = pack2(d[i][0], d[i][1]); }
}
template <bool EXACT> DEV void ln_2(u32x4& p, const f32x2_t (&d)[4], bool act) {
  const float s = dot2(p[0], 0x3f803f80u, dot2(p[1], 0x3f803f80u, 0.f)) + dot2(p[2], 0x3f803f80u, dot2(p[3], 0x3f803f80u, 0.f));
  const float q = dot2(p[0], p[0], dot2(p[1], p[1], 0.f)) + dot2(p[2], p[2], dot2(p[3], p[3], 0.f));
  const float mean = red64(act ? s : 0.f) * (1.0f / 448.0f), msq = red64(act ? q : 0.f) * (1.0f / 448.0f);
  const float inv = ln_inv(msq - mean * mean), mb = rbf(mean), c = -mb * inv;
#pragma unroll
  for (int i = 0; i < 4; ++i) {
    const f32x2_t r = EXACT ? rbf2((f32x2_t){lo2f(p[i]), hi2f(p[i])} - (f32x2_t){mb, mb}) * (f32x2_t){inv, inv}
                            : __builtin_elementwise_fma(d[i], (f32x2_t){inv, inv}, (f32x2_t){c, c});
    p[i] = pack2(r[0], r[1]);
  }
}
#define OP4_3(op, d, a, b) asm volatile(op " %0, %4, %8\n\t" op " %1, %5, %9\n\t" op " %2, %6, %10\n\t" op " %3, %7, %11" \
  : "=&v"(d[0]), "=&v"(d[1]), "=&v"(d[2]), "=v"(d[3]) : "v"(a[0]), "v"(a[1]), "v"(a[2]), "v"(a[3]), "v"(b[0]), "v"(b[1]), "v"(b[2]), "v"(b[3]))
#define ACC4(op, d, a) asm volatile(op " %0, %4, %0\n\t" op " %1, %5, %1\n\t" op " %2, %6, %2\n\t" op " %3, %7, %3" \
  : "+v"(d[0]), "+v"(d[1]), "+v"(d[2]), "+v"(d[3]) : "v"(a[0]), "v"(a[1]), "v"(a[2]), "v"(a[3]))
#define UN4(op, pre, post, d) asm volatile(op " %0, " pre "%0" post "\n\t" op " %1, " pre "%1" post "\n\t" op " %2, " pre "%2" post "\n\t" op " %3, " pre "%3" post \
  : "+v"(d[0]), "+v"(d[1]), "+v"(d[2]), "+v"(d[3]))
#define POLY4(d, c) asm volatile("v_fmamk_f32 %0, %0, 0x3dd2d3e8, %4\n\tv_fmamk_f32 %1, %1, 0x3dd2d3e8, %4\n\tv_fmamk_f32 %2, %2, 0x3dd2d3e8, %4\n\tv_fmamk_f32 %3, %3, 0x3dd2d3e8, %4" \
  : "+v"(d[0]), "+v"(d[1]), "+v"(d[2]), "+v"(d[3]) : "v"(c))
#define NFMA4(d, a) asm volatile("v_fma_f32 %0, -%0, %4, %0\n\tv_fma_f32 %1, -%1, %5, %1\n\tv_fma_f32 %2, -%2, %6, %2\n\tv_fma_f32 %3, -%3, %7, %3" \
  : "+v"(d[0]), "+v"(d[1]), "+v"(d[2]), "+v"(d[3]) : "v"(a[0]), "v"(a[1]), "v"(a[2]), "v"(a[3]))
#define SCL4(d, a) asm volatile("v_mul_f32 %0, 0xbfb8aa3b, %4\n\tv_mul_f32 %1, 0xbfb8aa3b, %5\n\tv_mul_f32 %2, 0xbfb8aa3b, %6\n\tv_mul_f32 %3, 0xbfb8aa3b, %7" \
  : "=&v"(d[0]), "=&v"(d[1]), "=&v"(d[2]), "=v"(d[3]) : "v"(a[0]), "v"(a[1]), "v"(a[2]), "v"(a[3]))
#define WAITVB(n) __builtin_amdgcn_s_waitcnt(0x0f70 | ((n) & 15) | (((n) >> 4) << 14))
DEV void xchg4(float (&a)[4], float (&b)[4]) {
#pragma unroll
  for (int i = 0; i < 4; ++i) {
    const int x = __float_as_int(a[i]), y = __float_as_int(b[i]);
    a[i] = __builtin_bit_cast(float, __builtin_amdgcn_update_dpp(x, y, 0x114, 0xf, 0xa, false));
    b[i] = __builtin_bit_cast(float, __builtin_amdgcn_update_dpp(y, x, 0x104, 0xf, 0x5, false));
  }
}
DEV unsigned fin2(unsigned s, float g0, float g1, f32x2_t x) {
  const f32x2_t r = __builtin_elementwise_fma((f32x2_t){lo2f(s), hi2f(s)}, (f32x2_t){g0, g1}, x);
  return pack2(r[0], r[1]);
}

constexpr int FA = 16, FU = 16, FO = 64, FS = 64;
constexpr size_t WS_FLAGA = 0;
constexpr size_t WS_FLAGU = WS_FLAGA + 256 * 128;
constexpr size_t WS_FLAGO = WS_FLAGU + 32 * 128 * 128;
constexpr size_t WS_FLAGS = WS_FLAGO + 1024 * 128;
constexpr size_t HT_W = 14 * 5120, UT_W = 13 * 5120, WQ_S = 24576, OT_T = 65536, WO_B = 4 * 5120, ST_T = 24576;
constexpr size_t WS_HT = WS_FLAGS + 1024 * 128, WS_XN = WS_HT + 32 * HT_W, WS_WQ = WS_XN + 2049 * 1024 + 3072, WS_UT = WS_WQ + 56 * WQ_S;
constexpr size_t WS_OT = WS_UT + 32 * UT_W, WS_WO = WS_OT + 32 * OT_T, WS_ST = WS_WO + 28 * WO_B;
static_assert(WS_HT % 1024 == 0 && WS_XN % 1024 == 0 && WS_WQ % 1024 == 0 && WS_UT % 1024 == 0 && WS_OT % 1024 == 0 && WS_WO % 1024 == 0 && WS_ST % 1024 == 0, "alignment");
constexpr int KROT = 2, KROT2 = 2;
constexpr int PW = 16384;
constexpr int TH = 8 * PW, TP = 960, TQ = TH + 8 * TP, TQP = 1088;
constexpr int L_CNT = TH + 896, L_DUMP = TQ + 8 * TQP;
constexpr int SG = 39936, SGX = 21504;
constexpr int TB = 3 * SG;
constexpr int WU = 8 * PW;
constexpr int SVB = 4608, SKH = 9216, SK = 4 * SVB, SQ = SK + 2 * SKH - 512;
constexpr int LDS_BYTES = 163840;

struct Args { const char *x, *wqkv, *wo, *w1, *b1, *w2, *b2; char *ws0, *ws, *out; };

__global__ __launch_bounds__(512, 2) void fused_kernel(const char* __restrict__ x, const char* __restrict__ wqkv, const char* __restrict__ wo,
                                                       const char* __restrict__ w1, const char* __restrict__ b1,
                                                       const char* __restrict__ w2, const char* __restrict__ b2,
                                                       char* __restrict__ ws, char* __restrict__ out) {
  __shared__ __attribute__((aligned(256))) char smem[LDS_BYTES];
  const int w = __builtin_amdgcn_readfirstlane(threadIdx.x >> 6);
  const int b = blockIdx.x, jb = b >> 3, q = (b >> 1) & 3, side = b & 1;
  STAMP(0);
  const unsigned lds0 = (unsigned)(size_t)(LDS_AS char*)smem;
  { const char* pc_; unsigned vo_ = (threadIdx.x * 32 + ((b >> 3) & 31)) * 128;
    asm volatile("s_getpc_b64 %0\n.Lcpf_b:\n\ts_mov_b32 m0, %2\n\tv_min_u32 %1, .Lcpf_e-.Lcpf_b-4, %1\n\tglobal_load_lds_dword %1, %0"
                 : "=&s"(pc_), "+v"(vo_) : "s"(lds0 + L_DUMP) : "memory"); }
  Args A = {x, wqkv, wo, w1, b1, w2, b2, ws, ws, out};
  const int lane = threadIdx.x & 63, ci = (b & 7) * 32 + (b >> 3);
  const unsigned vQ = (lane < 56 ? lane : 55) * 16;
  unsigned oX = (8 * ci + w) * 896;
  asm volatile("" : "+s"(oX) : "v"(vQ));
  asm volatile("" :: "s"(A.x), "s"(A.wqkv), "s"(A.wo), "s"(A.w1), "s"(A.b1), "s"(A.w2), "s"(A.b2), "s"(A.out), "s"(A.ws0));
  u32x4 xr;
  const char* sX = A.x + oX;
  LD16(xr, vQ, sX, 0, "");
  SB();
  PRIO(3);
  const int n16 = lane & 15, g4 = lane >> 4, q4 = n16 >> 2, pq = lane & 3, r8 = lane >> 3; const unsigned vL16 = lane * 16;
  const int dg = jb & 3, pk = jb < 4 ? 0 : (2 * q + side) * 28 + jb - 4, qp = (pk * 293) >> 14, kg = pk - 56 * qp;
  const unsigned ldsP = lds0 + w * PW;
  { const int oh = (kg * 37) >> 8, oj = kg - 7 * oh;
    int oq_ = (8 * kg + w) * 2688 + qp * 896, oo_ = (56 * oh + 4 * oj + (w & 3) + 28 * (w >> 2)) * 896; OPQ(oq_); OPQ(oo_);
    const char* sQ = (qp < 3 ? A.wqkv : A.wo) + (qp < 3 ? oq_ : oo_);
    DMA1(vQ, sQ, lds0 + w * TQP, TQ, ""); }
  unsigned nonce; asm volatile("s_load_dword %0, %1, 0x0" : "=&s"(nonce) : "s"(A.ws0) : "memory");
  const int t7 = (jb < 28 ? jb >> 2 : 1) + ((3 - (int)((size_t)A.w1 >> 7) - 7 * q) & 3), ft = 7 * q + (t7 >= 7 ? t7 - 7 : t7);
  const int swp = 0 * (((side ? 35 - jb : jb + 4) >> 5) & 1), is2 = side | swp;
  const int st = 8 * ((q + (side ? 0 : 0) + swp) & 3) + ((((jb >> 2) ^ (7 * swp)) + 3 - (int)((size_t)A.w2 >> 7)) & 7);
  const int rot = side ? KROT2 * dg : KROT * dg + ((jb + 4) >> 5), ld = 3584 + 512 * side;
  int wrap = 7 - side - rot, dW = 16 * ld;
  unsigned vWa = r8 * ld + ((lane & 7) ^ (r8 & 2)) * 16, vWb = (vWa ^ 64) + (8 * ld - 1024);
  vWa &= (side & swp) - 1; vWb &= (side & swp) - 1;
  const char* bW = (side ? A.w2 : A.w1) + ((side ? st : ft) * 128u + w * (256u * 3584u));
  const char* sW = bW + (unsigned)(rot * 2 * dW);
  asm volatile("" : "+s"(wrap), "+s"(dW), "+s"(bW), "+s"(sW) : "v"(vWa), "v"(vWb));
  OPQ(A.ws); A.ws = (char*)(((size_t)A.ws + 8191) & ~(size_t)4095);
  *(LDS_AS unsigned*)(lds0 + L_CNT) = 0u;
  unsigned vH, tH, vq, tq, hw = lds0 + TH + w * TP + vQ;
  const int ks = kg >> 2;
  { const int task = w < 7 ? w : 6, bk = 4 * task + g4, d4 = (bk * 37) >> 8, b7 = bk - 7 * d4;
    const int pos = ((ci >> 2) - KROT * d4) & 7, ix = (b7 < 6 ? 8 * b7 + 8 : 0) + pos;
    vH = d4 * (8 * (unsigned)HT_W) + (ix >> 2) * 5120 + frag(ix & 3, 16 * (ci & 3) + n16);
    tH = lds0 + TH + q4 * TP + bk * 32 + pq * 8;
    const int ti = w ? w - 1 : 0, c0 = 64 * ti + lane, hp0 = (c0 * 1171) >> 16, cc = c0 - 56 * hp0, kk = (kg >> 2) & 1, g_ = kg & 3;
    const unsigned off = (cc >> 4) * 6144 + qp * 1024 + (cc < 48 ? kk * 3072 + g_ * 256 : kk * 512 + g_ * 128) + (cc & 15) * 16;
    unsigned a_ = hp0 * (7 * (unsigned)WQ_S) + line7(off >> 7) + (off & 127), b_ = (4 * ti + g4) * (unsigned)WO_B + frag(ks & 3, 16 * g_ + n16);
    PINV(a_); PINV(b_);
    vq = qp < 3 ? a_ : b_;
    tq = lds0 + TQ + q4 * TQP + ti * 128 + g4 * 32 + pq * 8; }
  unsigned wq_ = WS_WQ + (kg >> 3) * WQ_S, wo_ = WS_WO + (ks >> 2) * 5120; OPQ(wq_); OPQ(wo_);
  const char* sN = A.ws + WS_XN + 512 + (size_t)(8 * ci + w) * 1024;
  const char* sH = A.ws + WS_HT + (size_t)(ci >> 5) * HT_W;
  const char* sWq = A.ws + (qp < 3 ? wq_ : wo_);
  const char* sFA = A.ws + WS_FLAGA + ci * FA;
  int j_ = 3 - jb, n_ = -w; OPQ(j_); OPQ(n_);
  int mH = (w - 7) >> 31, mQ = (j_ & n_) >> 31;
  asm volatile("" : "+s"(mH), "+s"(mQ), "+s"(sN), "+s"(sH), "+s"(sWq), "+s"(sFA)); asm volatile("" : "+v"(vH), "+v"(tH), "+v"(vq), "+v"(tq), "+v"(hw));
  u32x2 bv; { const char* sB = is2 ? A.b2 + st * 128 : A.b1 + ft * 128; const unsigned vB = is2 ? n16 * 8 : ((w & 3) * 16 + 4 * g4) * 2;
    LD8(bv, vB, sB, 0, ""); }
  PRIO(0);
  DMA2(vWa, vWb, sW, ldsP, 0, ""); sW += dW; OPQ(sW);
  DMA2(vWa, vWb, sW, ldsP, 2048, ""); sW = wrap == 0 ? bW : sW + dW; OPQ(sW);
  PRIO(3);
  WAITV1(6, xr);
  STAMP(24);
  f32x2_t dd[4];
  ln_1(xr, dd, lane < 56);
  ST16L56(vQ, sN, xr);
  ln_2<true>(xr, dd, lane < 56);
  *(LDS_AS u32x4*)hw = xr;
  WAITV(6);
  asm volatile("s_waitcnt lgkmcnt(0)" : "+s"(nonce) :: "memory"); nonce += 1u;
  BAR();
  STAMP(25);
  { const bf16x8 h0 = tr8u(tH, 4 * TP), q0 = tr8u(tq, 4 * TQP);
    ST16M(mH, vH, sH, __builtin_bit_cast(u32x4, h0));
    ST16M(mQ, vq, sWq, __builtin_bit_cast(u32x4, q0)); }
  SB();
  PRIO(0);
  DMA2(vWa, vWb, sW, ldsP, 4096, ""); sW += dW; OPQ(sW);
  PRIO(3);
  STAMP(26);
  unsigned fh; const char* sFH = A.ws + WS_FLAGA + w * (32 * FA); unsigned vFH = (lane & 31) * FA;
  asm volatile("" : "+s"(sFH)); asm volatile("" : "+v"(vFH));
  WAITV1(2, bv);
  BAR0(); if (w == 0) ST16X(1, vL16, sFA, nonce);
  PRIO(0);
  STAMP(27);

  if (__builtin_expect(side, 1)) {
  const unsigned ldsU = lds0 + WU + w * 4096; bf16x8 fw[7][4];
  DMA2(vWa, vWb, sW, ldsP, 6144, ""); sW = wrap == 1 ? bW : sW + dW; OPQ(sW);
  unsigned rW0 = ldsP + (8 * g4 + q4) * 128 + (((q4 >> 1) | (2 * (g4 & 1))) * 32) + pq * 8, rW1 = rW0 ^ 32, rW2 = rW0 ^ 64, rW3 = rW0 ^ 96;
  asm volatile("" : "+v"(rW0), "+v"(rW1), "+v"(rW2), "+v"(rW3));
  unsigned rU0 = ldsU + (8 * g4 + q4) * 128 + (((q4 >> 1) | (2 * (g4 & 1))) * 32) + pq * 8, rU1 = rU0 ^ 32, rU2 = rU0 ^ 64, rU3 = rU0 ^ 96;
  asm volatile("" : "+v"(rU0), "+v"(rU1), "+v"(rU2), "+v"(rU3));
  const int tt = 4 * q + (jb >> 3), hp = jb & 7, t0 = 128 * tt, cb = w & 3;
  unsigned vD[5];
#pragma unroll
  for (int m = 0; m < 5; ++m) {
    const int i = m == 4 && w == 7 ? 31 : w + 8 * m, row = 8 * (i - 21) + r8, tok = t0 - 16 + row;
    unsigned a_ = line7(8 * i + r8) + (lane & 7) * 16, b_ = (tok < 0 ? 0 : tok) * 1024 + ((lane & 7) ^ ((row >> 1) & 7)) * 16;
    vD[m] = m < 2 ? a_ : m > 2 ? b_ : i < 21 ? a_ : b_;
    PINV(vD[m]);
  }
  const char* sDq = A.ws + WS_WQ + (size_t)hp * (7 * WQ_S); const char* sDx = A.ws + WS_XN + 512; const char* sDm = w < 5 ? sDq : sDx;
  int dm = w < 5 ? (int)WQ_S : 128;
  unsigned ldsD = lds0 + w * 1024, ldsE = w == 7 ? lds0 + 31 * 1024 : ldsD + 32768;
  asm volatile("" : "+s"(sDq), "+s"(sDx), "+s"(sDm), "+s"(dm), "+s"(ldsD), "+s"(ldsE));
  WAITV(5);
  fw[0][0] = tr8u(rW0 + 0, 512);
  fw[0][1] = tr8u(rW1 + 0, 512);
  fw[0][2] = tr8u(rW2 + 0, 512);
  fw[0][3] = tr8u(rW3 + 0, 512);
  DMA2(vWa, vWb, sW, ldsP, 8192, ""); sW += dW; OPQ(sW);
  DMA2(vWa, vWb, sW, ldsP, 10240, ""); sW = wrap == 2 ? bW : sW + dW; OPQ(sW);
  u32x4 tb0; u32x2 tb4; unsigned tba = lds0 + TB + (w < 4 ? w : w + 4) * 1024 + vL16, tbb = lds0 + TB + 4096 + w * 512 + lane * 8;
  { const int dk = 4 * g4 - n16;
    u32x4 m_;
#pragma unroll
    for (int i = 0; i < 4; ++i) m_[i] = (unsigned)((dk + i + 7) >> 31);
    tb0 = m_;
    if ((w & 3) == 0) {
#pragma unroll
      for (int i = 0; i < 4; ++i) tb0[i] = ~m_[i] & ((unsigned)(dk + i - 1) < 8u ? (w && lane < 32 ? 0xce6e0000u : 0u) : 0xff800000u);
    } else if (w == 2) {
      tb0 = (u32x4){frag(0, lane), frag(1, lane), frag(2, lane), frag(3, lane)};
    } else if (w == 3) {
      tb0 = (u32x4){(m_[0] & 0xffffu) | (m_[1] & 0xffff0000u), (m_[2] & 0xffffu) | (m_[3] & 0xffff0000u),
                    lds0 + SG + n16 * 64 + ((g4 ^ q4) << 4), lds0 + SG + (4 * g4 + q4) * 32 + pq * 8};
    } else if (w == 5) {
      tb0 = (u32x4){n16 * 128u + ((g4 ^ (n16 & 7)) << 4), r8 * 128u + (((2 * pq + ((lane >> 2) & 1)) ^ r8) << 4), r8 * 896u + (lane & 7) * 16,
                    ((lane & 7) >> 1) * 3072u + r8 * 32 + (lane & 1) * 16};
    }
    if (w < 4) {
      const int ga = 7 * hp + g4;
      const unsigned l0 = ((ga >> 2) * 4 + cb) * 8 + (ga & 3) * 2 + (n16 >> 3);
      tb4 = (u32x2){line7(l0) + (n16 & 7) * 16, line7(l0 + 32) + (n16 & 7) * 16};
    } else {
      const int c = 16 * cb + 4 * g4;
      const bool ok = c < 56;
      const int hd = ok ? (c >= 28) : (g4 & 1), d = ok ? c - 28 * hd : 28, row = (n16 >> 1) + 72 * (n16 & 1);
      tb4 = (u32x2){lds0 + SG + hd * SKH + row * 64 + (((d >> 3) ^ (n16 >> 3) ^ (2 * (n16 & 1))) << 4) + ((d >> 2) & 1) * 8,
                    lds0 + SG + (2 * hd + (d >> 4)) * SVB + row * 32 + (d & 15) * 2};
    } }
  unsigned xr0 = lds0 + SGX + n16 * 128 + ((g4 ^ (n16 >> 1)) << 4), xr1 = xr0 ^ 64;
  unsigned wr0 = lds0 + cb * 6144 + (cb < 3 ? vL16 : g4 * 128 + (n16 & 7) * 16);
  asm volatile("" : "+v"(tb0), "+v"(tb4), "+v"(tba), "+v"(tbb), "+v"(xr0), "+v"(xr1), "+v"(wr0));
  unsigned oP_ = WS_UT + (dg * 8 + w) * UT_W, oW_ = WS_WO + (dg * 7 + (w < 7 ? w : 6)) * WO_B; OPQ(oP_); OPQ(oW_);
  const char* sP = A.ws + (swp & 1 ? oW_ : oP_);
  const char* sFU = A.ws + WS_FLAGU + (dg * 8 + w) * (128 * FU);
  const char* sO = A.ws + WS_OT + (size_t)(2 * tt + (w >> 2)) * OT_T;
  const char* sFO = A.ws + WS_FLAGO + ((tt * 8 + hp) * 8 + w) * FO;
  const char* sS = A.ws + WS_ST + (size_t)(st * 4 + dg) * ST_T + pair3(w);
  const char* sFS = A.ws + WS_FLAGS + (st * 4 + dg) * (8 * FS);
  const char* sXo = A.x + ((64 * st + 8 * w) * 896 + dg * 224); const char* sOut = A.out + ((64 * st + 8 * w) * 896 + dg * 224);
  int kx = 32 * (w & 1), r0 = 72 * (w & 1) + 16 * (w >> 1), cmo = t0 + (w >> 1) == 0 ? 8192 : 0, rao = lds0 + w * 1024, bio = lds0 + TB + 10496 + 32 * w;
  asm volatile("" : "+s"(sP), "+s"(sFU), "+s"(sO), "+s"(sFO), "+s"(sS), "+s"(sFS), "+s"(sXo), "+s"(sOut), "+s"(kx), "+s"(r0), "+s"(cmo), "+s"(rao), "+s"(bio));
  u32x4 t2, t3, m1; f32x4 cm; u32x2 vS; unsigned rk, rv, mq_, vFU;
  WAITV(4);
  fw[1][0] = tr8u(rW0 + 4096, 512);
  fw[1][1] = tr8u(rW1 + 4096, 512);
  fw[1][2] = tr8u(rW2 + 4096, 512);
  fw[1][3] = tr8u(rW3 + 4096, 512);
  DMA2(vWa, vWb, sW, ldsP, 12288, ""); sW += dW; OPQ(sW);
  DMA2(vWa, vWb, sW, ldsP, 14336, ""); sW = wrap == 3 ? bW : sW + dW; OPQ(sW);
  WAITV(4);
  fw[2][0] = tr8u(rW0 + 8192, 512);
  fw[2][1] = tr8u(rW1 + 8192, 512);
  fw[2][2] = tr8u(rW2 + 8192, 512);
  fw[2][3] = tr8u(rW3 + 8192, 512);
  DMA2(vWa, vWb, sW, ldsU, 0, ""); sW += dW; OPQ(sW);
  WAITL(0);
  DMA2(vWa, vWb, sW, ldsU, 2048, ""); sW = wrap == 4 ? bW : sW + dW; OPQ(sW);
  WAITV(4);
  fw[3][0] = tr8u(rW0 + 12288, 512);
  fw[3][1] = tr8u(rW1 + 12288, 512);
  fw[3][2] = tr8u(rW2 + 12288, 512);
  fw[3][3] = tr8u(rW3 + 12288, 512);
  STAMP(33);
  LD4(fh, vFH, sFH, 0, "sc1");
  WAITV1(0, fh);
  if (__ballot(fh != nonce) != 0ull) poll1<4>(sFH, vFH, nonce);
  BAR();
  STAMP(34);
  DMA1(vD[0], sDq, ldsD, 0, "sc1");
  DMA1(vD[1], sDq, ldsD, 8192, "sc1"); sDq += WQ_S; OPQ(sDq);
  DMA1(vD[2], sDm, ldsD, 16384, "sc1"); sDm += dm; OPQ(sDm);
  DMA1(vD[3], sDx, ldsD, 24576, "sc1");
  DMA1(vD[4], sDx, ldsE, 0, "sc1"); sDx += 128; OPQ(sDx);
  BAR0();
  DMA1(vD[0], sDq, ldsD, 39936, "sc1");
  DMA1(vD[1], sDq, ldsD, 48128, "sc1"); sDq += WQ_S; OPQ(sDq);
  DMA1(vD[2], sDm, ldsD, 56320, "sc1"); sDm += dm; OPQ(sDm);
  DMA1(vD[3], sDx, ldsD, 64512, "sc1");
  DMA1(vD[4], sDx, ldsE, 39936, "sc1"); sDx += 128; OPQ(sDx);
  *(LDS_AS u32x2*)(tbb) = tb4;
  if (w < 6) *(LDS_AS u32x4*)(tba) = tb0; else *(LDS_AS u32x4*)(lds0 + TB + 10496 + n16 * 16) = (u32x4){bv[0] << 16, bv[0] & 0xffff0000u, bv[1] << 16, bv[1] & 0xffff0000u};
  SB();
  WAITV(11);
  fw[4][0] = tr8u(rU0 + 0, 512);
  fw[4][1] = tr8u(rU1 + 0, 512);
  fw[4][2] = tr8u(rU2 + 0, 512);
  fw[4][3] = tr8u(rU3 + 0, 512);
  if (w < 3) {
  f32x4 aq[5], ak[5], av[5];
  bf16x8 xf[2][5], wf[2][5];
  unsigned sk0, sk1, sv;
  WAITV(5);
  BAR();
  STAMP(35);
  wf[0][1] = *(LDS_AS const bf16x8*)(wr0 + 1024);
  xf[0][1] = *(LDS_AS const bf16x8*)(xr0 + 2048);
  wf[0][2] = *(LDS_AS const bf16x8*)(wr0 + 2048);
  wf[0][0] = *(LDS_AS const bf16x8*)(wr0 + 0);
  xf[0][2] = *(LDS_AS const bf16x8*)(xr0 + 4096);
  xf[0][3] = *(LDS_AS const bf16x8*)(xr0 + 6144);
  xf[0][4] = *(LDS_AS const bf16x8*)(xr0 + 8192);
  SB();
  MV16Z(ak[1], wf[0][1], xf[0][1]); SB();
  wf[1][1] = *(LDS_AS const bf16x8*)(wr0 + 4096);
  SB();
  MV16Z(av[1], wf[0][2], xf[0][1]); SB();
  xf[1][1] = *(LDS_AS const bf16x8*)(xr1 + 2048);
  SB();
  MV16Z(aq[1], wf[0][0], xf[0][1]); SB();
  wf[1][2] = *(LDS_AS const bf16x8*)(wr0 + 5120);
  SB();
  MV16Z(ak[2], wf[0][1], xf[0][2]); SB();
  wf[1][0] = *(LDS_AS const bf16x8*)(wr0 + 3072);
  SB();
  MV16Z(av[2], wf[0][2], xf[0][2]); SB();
  xf[1][2] = *(LDS_AS const bf16x8*)(xr1 + 4096);
  SB();
  MV16Z(aq[2], wf[0][0], xf[0][2]); SB();
  xf[1][3] = *(LDS_AS const bf16x8*)(xr1 + 6144);
  SB();
  MV16Z(ak[3], wf[0][1], xf[0][3]); SB();
  xf[1][4] = *(LDS_AS const bf16x8*)(xr1 + 8192);
  SB();
  MV16Z(av[3], wf[0][2], xf[0][3]); SB();
  DMA1(vD[0], sDq, ldsD, 79872, "sc1");
  SB();
  MV16Z(aq[3], wf[0][0], xf[0][3]); SB();
  DMA1(vD[1], sDq, ldsD, 88064, "sc1"); sDq += WQ_S; OPQ(sDq);
  SB();
  MV16Z(ak[4], wf[0][1], xf[0][4]); SB();
  DMA1(vD[2], sDm, ldsD, 96256, "sc1"); sDm += dm; OPQ(sDm);
  SB();
  MV16Z(av[4], wf[0][2], xf[0][4]); SB();
  DMA1(vD[3], sDx, ldsD, 104448, "sc1");
  SB();
  MV16Z(aq[4], wf[0][0], xf[0][4]); SB();
  DMA1(vD[4], sDx, ldsE, 79872, "sc1"); sDx += 128; OPQ(sDx);
  SB();
  SB();
  MV16(ak[1], wf[1][1], xf[1][1]); SB();
  SB();
  MV16(av[1], wf[1][2], xf[1][1]); SB();
  WAITV(5);
  BAR();
  xr0 += 39936; wr0 += 39936; asm volatile("" : "+v"(xr0), "+v"(wr0));
  SB();
  MV16(aq[1], wf[1][0], xf[1][1]); SB();
  wf[0][1] = *(LDS_AS const bf16x8*)(wr0 + 1024);
  SB();
  MV16(ak[2], wf[1][1], xf[1][2]); SB();
  xf[0][1] = *(LDS_AS const bf16x8*)(xr0 + 2048);
  SB();
  MV16(av[2], wf[1][2], xf[1][2]); SB();
  wf[0][2] = *(LDS_AS const bf16x8*)(wr0 + 2048);
  SB();
  MV16(aq[2], wf[1][0], xf[1][2]); SB();
  wf[0][0] = *(LDS_AS const bf16x8*)(wr0 + 0);
  SB();
  MV16(ak[3], wf[1][1], xf[1][3]); SB();
  xf[0][2] = *(LDS_AS const bf16x8*)(xr0 + 4096);
  SB();
  MV16(av[3], wf[1][2], xf[1][3]); SB();
  xf[0][3] = *(LDS_AS const bf16x8*)(xr0 + 6144);
  SB();
  MV16(aq[3], wf[1][0], xf[1][3]); SB();
  xf[0][4] = *(LDS_AS const bf16x8*)(xr0 + 8192);
  SB();
  MV16(ak[4], wf[1][1], xf[1][4]); SB();
  MV16(av[4], wf[1][2], xf[1][4]); SB();
  MV16(aq[4], wf[1][0], xf[1][4]); SB();
  SB();
  MV16(ak[1], wf[0][1], xf[0][1]); SB();
  xr1 += 39936; PINV(xr1);
  wf[1][1] = *(LDS_AS const bf16x8*)(wr0 + 4096);
  SB();
  MV16(av[1], wf[0][2], xf[0][1]); SB();
  xf[1][1] = *(LDS_AS const bf16x8*)(xr1 + 2048);
  SB();
  MV16(aq[1], wf[0][0], xf[0][1]); SB();
  wf[1][2] = *(LDS_AS const bf16x8*)(wr0 + 5120);
  SB();
  MV16(ak[2], wf[0][1], xf[0][2]); SB();
  wf[1][0] = *(LDS_AS const bf16x8*)(wr0 + 3072);
  SB();
  MV16(av[2], wf[0][2], xf[0][2]); SB();
  xf[1][2] = *(LDS_AS const bf16x8*)(xr1 + 4096);
  SB();
  MV16(aq[2], wf[0][0], xf[0][2]); SB();
  xf[1][3] = *(LDS_AS const bf16x8*)(xr1 + 6144);
  SB();
  MV16(ak[3], wf[0][1], xf[0][3]); SB();
  xf[1][4] = *(LDS_AS const bf16x8*)(xr1 + 8192);
  SB();
  MV16(av[3], wf[0][2], xf[0][3]); SB();
  DMA1(vD[0], sDq, ldsD, 0, "sc1");
  SB();
  MV16(aq[3], wf[0][0], xf[0][3]); SB();
  DMA1(vD[1], sDq, ldsD, 8192, "sc1"); sDq += WQ_S; OPQ(sDq);
  SB();
  MV16(ak[4], wf[0][1], xf[0][4]); SB();
  DMA1(vD[2], sDm, ldsD, 16384, "sc1"); sDm += dm; OPQ(sDm);
  SB();
  MV16(av[4], wf[0][2], xf[0][4]); SB();
  DMA1(vD[3], sDx, ldsD, 24576, "sc1");
  SB();
  MV16(aq[4], wf[0][0], xf[0][4]); SB();
  DMA1(vD[4], sDx, ldsE, 0, "sc1"); sDx += 128; OPQ(sDx);
  SB();
  SB();
  MV16(ak[1], wf[1][1], xf[1][1]); SB();
  SB();
  MV16(av[1], wf[1][2], xf[1][1]); SB();
  WAITV(5);
  BAR();
  xr0 += 39936; wr0 += 39936; asm volatile("" : "+v"(xr0), "+v"(wr0));
  STAMP(60);
  SB();
  MV16(aq[1], wf[1][0], xf[1][1]); SB();
  wf[0][1] = *(LDS_AS const bf16x8*)(wr0 + 1024);
  SB();
  MV16(ak[2], wf[1][1], xf[1][2]); SB();
  xf[0][1] = *(LDS_AS const bf16x8*)(xr0 + 2048);
  SB();
  MV16(av[2], wf[1][2], xf[1][2]); SB();
  wf[0][2] = *(LDS_AS const bf16x8*)(wr0 + 2048);
  SB();
  MV16(aq[2], wf[1][0], xf[1][2]); SB();
  wf[0][0] = *(LDS_AS const bf16x8*)(wr0 + 0);
  SB();
  MV16(ak[3], wf[1][1], xf[1][3]); SB();
  xf[0][2] = *(LDS_AS const bf16x8*)(xr0 + 4096);
  SB();
  MV16(av[3], wf[1][2], xf[1][3]); SB();
  xf[0][3] = *(LDS_AS const bf16x8*)(xr0 + 6144);
  SB();
  MV16(aq[3], wf[1][0], xf[1][3]); SB();
  xf[0][4] = *(LDS_AS const bf16x8*)(xr0 + 8192);
  SB();
  MV16(ak[4], wf[1][1], xf[1][4]); SB();
  MV16(av[4], wf[1][2], xf[1][4]); SB();
  MV16(aq[4], wf[1][0], xf[1][4]); SB();
  SB();
  MV16(ak[1], wf[0][1], xf[0][1]); SB();
  xr1 += 39936; PINV(xr1);
  wf[1][1] = *(LDS_AS const bf16x8*)(wr0 + 4096);
  SB();
  MV16(av[1], wf[0][2], xf[0][1]); SB();
  xf[1][1] = *(LDS_AS const bf16x8*)(xr1 + 2048);
  SB();
  MV16(aq[1], wf[0][0], xf[0][1]); SB();
  wf[1][2] = *(LDS_AS const bf16x8*)(wr0 + 5120);
  SB();
  MV16(ak[2], wf[0][1], xf[0][2]); SB();
  wf[1][0] = *(LDS_AS const bf16x8*)(wr0 + 3072);
  SB();
  MV16(av[2], wf[0][2], xf[0][2]); SB();
  xf[1][2] = *(LDS_AS const bf16x8*)(xr1 + 4096);
  SB();
  MV16(aq[2], wf[0][0], xf[0][2]); SB();
  xf[1][3] = *(LDS_AS const bf16x8*)(xr1 + 6144);
  SB();
  MV16(ak[3], wf[0][1], xf[0][3]); SB();
  xf[1][4] = *(LDS_AS const bf16x8*)(xr1 + 8192);
  SB();
  MV16(av[3], wf[0][2], xf[0][3]); SB();
  DMA1(vD[0], sDq, ldsD, 39936, "sc1");
  SB();
  MV16(aq[3], wf[0][0], xf[0][3]); SB();
  DMA1(vD[1], sDq, ldsD, 48128, "sc1"); sDq += WQ_S; OPQ(sDq);
  SB();
  MV16(ak[4], wf[0][1], xf[0][4]); SB();
  DMA1(vD[2], sDm, ldsD, 56320, "sc1"); sDm += dm; OPQ(sDm);
  SB();
  MV16(av[4], wf[0][2], xf[0][4]); SB();
  DMA1(vD[3], sDx, ldsD, 64512, "sc1");
  SB();
  MV16(aq[4], wf[0][0], xf[0][4]); SB();
  DMA1(vD[4], sDx, ldsE, 39936, "sc1"); sDx += 128; OPQ(sDx);
  SB();
  SB();
  MV16(ak[1], wf[1][1], xf[1][1]); SB();
  SB();
  MV16(av[1], wf[1][2], xf[1][1]); SB();
  WAITV(5);
  BAR();
  xr0 += -79872; wr0 += -79872; asm volatile("" : "+v"(xr0), "+v"(wr0));
  SB();
  MV16(aq[1], wf[1][0], xf[1][1]); SB();
  wf[0][1] = *(LDS_AS const bf16x8*)(wr0 + 1024);
  SB();
  MV16(ak[2], wf[1][1], xf[1][2]); SB();
  xf[0][1] = *(LDS_AS const bf16x8*)(xr0 + 2048);
  SB();
  MV16(av[2], wf[1][2], xf[1][2]); SB();
  wf[0][2] = *(LDS_AS const bf16x8*)(wr0 + 2048);
  SB();
  MV16(aq[2], wf[1][0], xf[1][2]); SB();
  wf[0][0] = *(LDS_AS const bf16x8*)(wr0 + 0);
  SB();
  MV16(ak[3], wf[1][1], xf[1][3]); SB();
  xf[0][2] = *(LDS_AS const bf16x8*)(xr0 + 4096);
  SB();
  MV16(av[3], wf[1][2], xf[1][3]); SB();
  xf[0][3] = *(LDS_AS const bf16x8*)(xr0 + 6144);
  SB();
  MV16(aq[3], wf[1][0], xf[1][3]); SB();
  xf[0][4] = *(LDS_AS const bf16x8*)(xr0 + 8192);
  SB();
  MV16(ak[4], wf[1][1], xf[1][4]); SB();
  MV16(av[4], wf[1][2], xf[1][4]); SB();
  MV16(aq[4], wf[1][0], xf[1][4]); SB();
  SB();
  MV16(ak[1], wf[0][1], xf[0][1]); SB();
  xr1 += -79872; PINV(xr1);
  wf[1][1] = *(LDS_AS const bf16x8*)(wr0 + 4096);
  SB();
  MV16(av[1], wf[0][2], xf[0][1]); SB();
  xf[1][1] = *(LDS_AS const bf16x8*)(xr1 + 2048);
  SB();
  MV16(aq[1], wf[0][0], xf[0][1]); SB();
  wf[1][2] = *(LDS_AS const bf16x8*)(wr0 + 5120);
  SB();
  MV16(ak[2], wf[0][1], xf[0][2]); SB();
  wf[1][0] = *(LDS_AS const bf16x8*)(wr0 + 3072);
  SB();
  MV16(av[2], wf[0][2], xf[0][2]); SB();
  xf[1][2] = *(LDS_AS const bf16x8*)(xr1 + 4096);
  SB();
  MV16(aq[2], wf[0][0], xf[0][2]); SB();
  xf[1][3] = *(LDS_AS const bf16x8*)(xr1 + 6144);
  SB();
  MV16(ak[3], wf[0][1], xf[0][3]); SB();
  xf[1][4] = *(LDS_AS const bf16x8*)(xr1 + 8192);
  SB();
  MV16(av[3], wf[0][2], xf[0][3]); SB();
  DMA1(vD[0], sDq, ldsD, 79872, "sc1");
  SB();
  MV16(aq[3], wf[0][0], xf[0][3]); SB();
  DMA1(vD[1], sDq, ldsD, 88064, "sc1"); sDq += WQ_S; OPQ(sDq);
  SB();
  MV16(ak[4], wf[0][1], xf[0][4]); SB();
  DMA1(vD[2], sDm, ldsD, 96256, "sc1"); sDm += dm; OPQ(sDm);
  SB();
  MV16(av[4], wf[0][2], xf[0][4]); SB();
  DMA1(vD[3], sDx, ldsD, 104448, "sc1");
  SB();
  MV16(aq[4], wf[0][0], xf[0][4]); SB();
  DMA1(vD[4], sDx, ldsE, 79872, "sc1"); sDx += 128; OPQ(sDx);
  SB();
  SB();
  MV16(ak[1], wf[1][1], xf[1][1]); SB();
  SB();
  MV16(av[1], wf[1][2], xf[1][1]); SB();
  WAITV(5);
  BAR();
  xr0 += 39936; wr0 += 39936; asm volatile("" : "+v"(xr0), "+v"(wr0));
  STAMP(61);
  SB();
  MV16(aq[1], wf[1][0], xf[1][1]); SB();
  wf[0][1] = *(LDS_AS const bf16x8*)(wr0 + 1024);
  SB();
  MV16(ak[2], wf[1][1], xf[1][2]); SB();
  xf[0][1] = *(LDS_AS const bf16x8*)(xr0 + 2048);
  SB();
  MV16(av[2], wf[1][2], xf[1][2]); SB();
  wf[0][2] = *(LDS_AS const bf16x8*)(wr0 + 2048);
  SB();
  MV16(aq[2], wf[1][0], xf[1][2]); SB();
  wf[0][0] = *(LDS_AS const bf16x8*)(wr0 + 0);
  SB();
  MV16(ak[3], wf[1][1], xf[1][3]); SB();
  xf[0][2] = *(LDS_AS const bf16x8*)(xr0 + 4096);
  SB();
  MV16(av[3], wf[1][2], xf[1][3]); SB();
  xf[0][3] = *(LDS_AS const bf16x8*)(xr0 + 6144);
  SB();
  MV16(aq[3], wf[1][0], xf[1][3]); SB();
  xf[0][4] = *(LDS_AS const bf16x8*)(xr0 + 8192);
  SB();
  MV16(ak[4], wf[1][1], xf[1][4]); SB();
  MV16(av[4], wf[1][2], xf[1][4]); SB();
  MV16(aq[4], wf[1][0], xf[1][4]); SB();
  SB();
  MV16(ak[1], wf[0][1], xf[0][1]); SB();
  xr1 += 39936; PINV(xr1);
  wf[1][1] = *(LDS_AS const bf16x8*)(wr0 + 4096);
  SB();
  MV16(av[1], wf[0][2], xf[0][1]); SB();
  xf[1][1] = *(LDS_AS const bf16x8*)(xr1 + 2048);
  SB();
  MV16(aq[1], wf[0][0], xf[0][1]); SB();
  wf[1][2] = *(LDS_AS const bf16x8*)(wr0 + 5120);
  SB();
  MV16(ak[2], wf[0][1], xf[0][2]); SB();
  wf[1][0] = *(LDS_AS const bf16x8*)(wr0 + 3072);
  SB();
  MV16(av[2], wf[0][2], xf[0][2]); SB();
  xf[1][2] = *(LDS_AS const bf16x8*)(xr1 + 4096);
  SB();
  MV16(aq[2], wf[0][0], xf[0][2]); SB();
  xf[1][3] = *(LDS_AS const bf16x8*)(xr1 + 6144);
  SB();
  MV16(ak[3], wf[0][1], xf[0][3]); SB();
  xf[1][4] = *(LDS_AS const bf16x8*)(xr1 + 8192);
  SB();
  MV16(av[3], wf[0][2], xf[0][3]); SB();
  DMA1(vD[0], sDq, ldsD, 0, "sc1");
  SB();
  MV16(aq[3], wf[0][0], xf[0][3]); SB();
  DMA1(vD[1], sDq, ldsD, 8192, "sc1"); sDq += WQ_S; OPQ(sDq);
  SB();
  MV16(ak[4], wf[0][1], xf[0][4]); SB();
  DMA1(vD[2], sDm, ldsD, 16384, "sc1"); sDm += dm; OPQ(sDm);
  SB();
  MV16(av[4], wf[0][2], xf[0][4]); SB();
  DMA1(vD[3], sDx, ldsD, 24576, "sc1");
  SB();
  MV16(aq[4], wf[0][0], xf[0][4]); SB();
  DMA1(vD[4], sDx, ldsE, 0, "sc1"); sDx += 128; OPQ(sDx);
  SB();
  SB();
  MV16(ak[1], wf[1][1], xf[1][1]); SB();
  SB();
  MV16(av[1], wf[1][2], xf[1][1]); SB();
  WAITV(5);
  BAR();
  xr0 += 39936; wr0 += 39936; asm volatile("" : "+v"(xr0), "+v"(wr0));
  SB();
  MV16(aq[1], wf[1][0], xf[1][1]); SB();
  wf[0][1] = *(LDS_AS const bf16x8*)(wr0 + 1024);
  SB();
  MV16(ak[2], wf[1][1], xf[1][2]); SB();
  xf[0][1] = *(LDS_AS const bf16x8*)(xr0 + 2048);
  SB();
  MV16(av[2], wf[1][2], xf[1][2]); SB();
  wf[0][2] = *(LDS_AS const bf16x8*)(wr0 + 2048);
  SB();
  MV16(aq[2], wf[1][0], xf[1][2]); SB();
  wf[0][0] = *(LDS_AS const bf16x8*)(wr0 + 0);
  SB();
  MV16(ak[3], wf[1][1], xf[1][3]); SB();
  xf[0][2] = *(LDS_AS const bf16x8*)(xr0 + 4096);
  SB();
  MV16(av[3], wf[1][2], xf[1][3]); SB();
  xf[0][3] = *(LDS_AS const bf16x8*)(xr0 + 6144);
  SB();
  MV16(aq[3], wf[1][0], xf[1][3]); SB();
  xf[0][4] = *(LDS_AS const bf16x8*)(xr0 + 8192);
  SB();
  MV16(ak[4], wf[1][1], xf[1][4]); SB();
  MV16(av[4], wf[1][2], xf[1][4]); SB();
  MV16(aq[4], wf[1][0], xf[1][4]); SB();
  SB();
  MV16(ak[1], wf[0][1], xf[0][1]); SB();
  xr1 += 39936; PINV(xr1);
  SB();
  MV16(av[1], wf[0][2], xf[0][1]); SB();
  wf[1][1] = *(LDS_AS const bf16x8*)(wr0 + 4096);
  SB();
  MV16(aq[1], wf[0][0], xf[0][1]); SB();
  xf[1][1] = *(LDS_AS const bf16x8*)(xr1 + 2048);
  SB();
  MV16(ak[2], wf[0][1], xf[0][2]); SB();
  wf[1][2] = *(LDS_AS const bf16x8*)(wr0 + 5120);
  SB();
  MV16(av[2], wf[0][2], xf[0][2]); SB();
  wf[1][0] = *(LDS_AS const bf16x8*)(wr0 + 3072);
  SB();
  MV16(aq[2], wf[0][0], xf[0][2]); SB();
  xf[1][2] = *(LDS_AS const bf16x8*)(xr1 + 4096);
  SB();
  MV16(ak[3], wf[0][1], xf[0][3]); SB();
  xf[1][3] = *(LDS_AS const bf16x8*)(xr1 + 6144);
  SB();
  MV16(av[3], wf[0][2], xf[0][3]); SB();
  xf[1][4] = *(LDS_AS const bf16x8*)(xr1 + 8192);
  SB();
  MV16(aq[3], wf[0][0], xf[0][3]); SB();
  DMA2(vWa, vWb, sW, ldsU, 0, ""); sW += dW; OPQ(sW);
  SB();
  MV16(ak[4], wf[0][1], xf[0][4]); SB();
  DMA2(vWa, vWb, sW, ldsU, 2048, ""); sW = wrap == 5 ? bW : sW + dW; OPQ(sW);
  SB();
  MV16(av[4], wf[0][2], xf[0][4]); SB();
  MV16(aq[4], wf[0][0], xf[0][4]); SB();
  SB();
  MV16(ak[1], wf[1][1], xf[1][1]); SB();
  SB();
  MV16(av[1], wf[1][2], xf[1][1]); SB();
  WAITV(4);
  BAR();
  xr0 += -79872; wr0 += -79872; asm volatile("" : "+v"(xr0), "+v"(wr0));
  STAMP(62);
  SB();
  MV16(aq[1], wf[1][0], xf[1][1]); SB();
  wf[0][1] = *(LDS_AS const bf16x8*)(wr0 + 1024);
  SB();
  MV16(ak[2], wf[1][1], xf[1][2]); SB();
  xf[0][1] = *(LDS_AS const bf16x8*)(xr0 + 2048);
  SB();
  MV16(av[2], wf[1][2], xf[1][2]); SB();
  wf[0][2] = *(LDS_AS const bf16x8*)(wr0 + 2048);
  SB();
  MV16(aq[2], wf[1][0], xf[1][2]); SB();
  wf[0][0] = *(LDS_AS const bf16x8*)(wr0 + 0);
  SB();
  MV16(ak[3], wf[1][1], xf[1][3]); SB();
  xf[0][2] = *(LDS_AS const bf16x8*)(xr0 + 4096);
  SB();
  MV16(av[3], wf[1][2], xf[1][3]); SB();
  xf[0][3] = *(LDS_AS const bf16x8*)(xr0 + 6144);
  SB();
  MV16(aq[3], wf[1][0], xf[1][3]); SB();
  xf[0][4] = *(LDS_AS const bf16x8*)(xr0 + 8192);
  SB();
  MV16(ak[4], wf[1][1], xf[1][4]); SB();
  unsigned f0; { const int l_ = lane_now(); vFU = (l_ < 14 ? l_ : 13) * FU; PINV(vFU); }
  LD4(f0, vFU, sFU, 0, "sc1");
  SB();
  MV16(av[4], wf[1][2], xf[1][4]); SB();
  unsigned tl; { const int l_ = lane_now(); tl = lds0 + TB + l_ * 16; const u32x2 t_ = *(LDS_AS const u32x2*)(lds0 + TB + 6144 + cb * 512 + l_ * 8); sk0 = t_[0]; sv = t_[1];
    vS = *(LDS_AS const u32x2*)(lds0 + TB + 4096 + cb * 512 + l_ * 8); PINV(tl); }
  SB();
  MV16(aq[4], wf[1][0], xf[1][4]); SB();
  xr1 += -79872; PINV(xr1);
  MV16(ak[1], wf[0][1], xf[0][1]); SB();
  wf[1][1] = *(LDS_AS const bf16x8*)(wr0 + 4096);
  xf[1][1] = *(LDS_AS const bf16x8*)(xr1 + 2048);
  SB();
  MV16(av[1], wf[0][2], xf[0][1]); SB();
  wf[1][2] = *(LDS_AS const bf16x8*)(wr0 + 5120);
  wf[1][0] = *(LDS_AS const bf16x8*)(wr0 + 3072);
  SB();
  MV16(aq[1], wf[0][0], xf[0][1]); SB();
  xf[1][2] = *(LDS_AS const bf16x8*)(xr1 + 4096);
  xf[1][3] = *(LDS_AS const bf16x8*)(xr1 + 6144);
  SB();
  MV16(ak[2], wf[0][1], xf[0][2]); SB();
  xf[1][4] = *(LDS_AS const bf16x8*)(xr1 + 8192);
  WAITV(1);
  fw[5][0] = tr8u(rU0 + 0, 512);
  fw[5][1] = tr8u(rU1 + 0, 512);
  fw[5][2] = tr8u(rU2 + 0, 512);
  fw[5][3] = tr8u(rU3 + 0, 512);
  SB();
  MV16(av[2], wf[0][2], xf[0][2]); SB();
  WAITL(0);
  DMA2(vWa, vWb, sW, ldsU, 0, ""); sW += dW; OPQ(sW);
  SB();
  MV16(aq[2], wf[0][0], xf[0][2]); SB();
  DMA2(vWa, vWb, sW, ldsU, 2048, "");
  sk1 = sk0 ^ 32; asm volatile("" : "+v"(sk0), "+v"(sk1), "+v"(sv));
  SB();
  MV16(ak[1], wf[1][1], xf[1][1]); SB();
  SB();
  MV16(av[1], wf[1][2], xf[1][1]); SB();
  SB();
  MV16(aq[1], wf[1][0], xf[1][1]); SB();
  SB();
  MV16(ak[3], wf[0][1], xf[0][3]); SB();
  SB();
  MV16(av[3], wf[0][2], xf[0][3]); SB();
  SB();
  MV16(aq[3], wf[0][0], xf[0][3]); SB();
  SB();
  MV16(ak[2], wf[1][1], xf[1][2]); SB();
  t3 = *(LDS_AS const u32x4*)(tl + 3072); t2 = *(LDS_AS const u32x4*)(tl + 2048);
  SB();
  MV16(av[2], wf[1][2], xf[1][2]); SB();
  SB();
  MV16(aq[2], wf[1][0], xf[1][2]); SB();
  SB();
  MV16(ak[4], wf[0][1], xf[0][4]); SB();
  *(LDS_AS u32x2*)(sk1 + SK + 512) = (u32x2){pack2(ak[1][0], ak[1][1]), pack2(ak[1][2], ak[1][3])};
  SB();
  MV16(av[4], wf[0][2], xf[0][4]); SB();
  *(LDS_AS u32x2*)(sv + 256) = (u32x2){pack2(av[1][0], av[1][1]), pack2(av[1][2], av[1][3])};
  SB();
  MV16(aq[4], wf[0][0], xf[0][4]); SB();
  *(LDS_AS u32x2*)(sk0 + SQ + 512) = (u32x2){pack2(aq[1][0], aq[1][1]), pack2(aq[1][2], aq[1][3])};
  SB();
  MV16(ak[3], wf[1][1], xf[1][3]); SB();
  m1 = *(LDS_AS const u32x4*)(tl + 1024); cm = *(LDS_AS const f32x4*)(tl + cmo);
  SB();
  MV16(av[3], wf[1][2], xf[1][3]); SB();
  SB();
  MV16(aq[3], wf[1][0], xf[1][3]); SB();
  SB();
  MV16(ak[4], wf[1][1], xf[1][4]); SB();
  WAITV1(4, f0);
  if (__ballot(f0 != nonce) != 0ull) poll1<2>(sFU, vFU, nonce);
  STAMP(44);
  { const int l_ = lane_now(); vFU = (14 + (l_ < 28 ? l_ : 27)) * FU; mq_ = l_ >= 48 ? 0u : ~0u; asm volatile("" : "+v"(vFU), "+v"(mq_)); }
  rk = (t3[2] ^ kx) + r0 * 64; rv = t3[3] + r0 * 32; asm volatile("" : "+v"(rk), "+v"(rv));
  SB();
  MV16(av[4], wf[1][2], xf[1][4]); SB();
  *(LDS_AS u32x2*)(sk0 + SK + 1024) = (u32x2){pack2(ak[2][0], ak[2][1]), pack2(ak[2][2], ak[2][3])};
  *(LDS_AS u32x2*)(sv + 512) = (u32x2){pack2(av[2][0], av[2][1]), pack2(av[2][2], av[2][3])};
  SB();
  MV16(aq[4], wf[1][0], xf[1][4]); SB();
  *(LDS_AS u32x2*)(sk1 + SQ + 1024) = (u32x2){pack2(aq[2][0], aq[2][1]), pack2(aq[2][2], aq[2][3])};
  SB();
  STAMP(36);
  *(LDS_AS u32x2*)(sk1 + SK + 1536) = (u32x2){pack2(ak[3][0], ak[3][1]), pack2(ak[3][2], ak[3][3])};
  *(LDS_AS u32x2*)(sv + 768) = (u32x2){pack2(av[3][0], av[3][1]), pack2(av[3][2], av[3][3])};
  *(LDS_AS u32x2*)(sk0 + SQ + 1536) = (u32x2){pack2(aq[3][0], aq[3][1]), pack2(aq[3][2], aq[3][3])};
  SB();
  *(LDS_AS u32x2*)(sk0 + SK + 2048) = (u32x2){pack2(ak[4][0], ak[4][1]), pack2(ak[4][2], ak[4][3])};
  *(LDS_AS u32x2*)(sv + 1024) = (u32x2){pack2(av[4][0], av[4][1]), pack2(av[4][2], av[4][3])};
  *(LDS_AS u32x2*)(sk1 + SQ + 2048) = (u32x2){pack2(aq[4][0], aq[4][1]), pack2(aq[4][2], aq[4][3])};
  SB();

  } else if (w == 3) {
  f32x4 aq[5], ak[5], av[5];
  bf16x8 xf[2][5], wf[2][5];
  unsigned sk0, sk1, sv;
  unsigned wk, wh, e0, e1, e2, hb[3]; { const int l_ = lane_now(); wk = wr0 + (l_ & 8) * 128; wh = lds0 + l_ * 16; asm volatile("" : "+v"(wk), "+v"(wh)); }
  const unsigned kz = t0 == 0 ? 0u : ~0u;
  WAITV(5);
  BAR();
  STAMP(35);
  xf[0][0] = *(LDS_AS const bf16x8*)(xr0 + 0);
  wf[0][2] = *(LDS_AS const bf16x8*)(wh + 1024);
  wf[0][3] = *(LDS_AS const bf16x8*)(wh + 7168);
  wf[0][4] = *(LDS_AS const bf16x8*)(wh + 13312);
  wf[0][1] = *(LDS_AS const bf16x8*)(wk + 1024);
  xf[0][1] = *(LDS_AS const bf16x8*)(xr0 + 2048);
  wf[0][0] = *(LDS_AS const bf16x8*)(wr0 + 0);
  xf[0][2] = *(LDS_AS const bf16x8*)(xr0 + 4096);
  xf[0][3] = *(LDS_AS const bf16x8*)(xr0 + 6144);
  xf[0][4] = *(LDS_AS const bf16x8*)(xr0 + 8192);
  SB();
  MV16Z(av[0], wf[0][2], xf[0][0]); SB();
  xf[1][0] = *(LDS_AS const bf16x8*)(xr1 + 0);
  wf[1][2] = *(LDS_AS const bf16x8*)(wh + 4096);
  SB();
  MV16Z(av[1], wf[0][3], xf[0][0]); SB();
  wf[1][3] = *(LDS_AS const bf16x8*)(wh + 10240);
  SB();
  MV16Z(av[2], wf[0][4], xf[0][0]); SB();
  wf[1][4] = *(LDS_AS const bf16x8*)(wh + 16384);
  SB();
  MV16Z(ak[0], wf[0][1], xf[0][0]); SB();
  wf[1][1] = *(LDS_AS const bf16x8*)(wk + 1536);
  SB();
  MV16Z(ak[1], wf[0][1], xf[0][1]); SB();
  xf[1][1] = *(LDS_AS const bf16x8*)(xr1 + 2048);
  wf[1][0] = *(LDS_AS const bf16x8*)(wr0 + 512);
  SB();
  MV16Z(aq[1], wf[0][0], xf[0][1]); SB();
  xf[1][2] = *(LDS_AS const bf16x8*)(xr1 + 4096);
  SB();
  MV16Z(ak[2], wf[0][1], xf[0][2]); SB();
  xf[1][3] = *(LDS_AS const bf16x8*)(xr1 + 6144);
  SB();
  MV16Z(aq[2], wf[0][0], xf[0][2]); SB();
  xf[1][4] = *(LDS_AS const bf16x8*)(xr1 + 8192);
  SB();
  MV16Z(ak[3], wf[0][1], xf[0][3]); SB();
  DMA1(vD[0], sDq, ldsD, 79872, "sc1");
  DMA1(vD[1], sDq, ldsD, 88064, "sc1"); sDq += WQ_S; OPQ(sDq);
  SB();
  MV16Z(aq[3], wf[0][0], xf[0][3]); SB();
  DMA1(vD[2], sDm, ldsD, 96256, "sc1"); sDm += dm; OPQ(sDm);
  SB();
  MV16Z(ak[4], wf[0][1], xf[0][4]); SB();
  DMA1(vD[3], sDx, ldsD, 104448, "sc1");
  SB();
  MV16Z(aq[4], wf[0][0], xf[0][4]); SB();
  DMA1(vD[4], sDx, ldsE, 79872, "sc1"); sDx += 128; OPQ(sDx);
  SB();
  SB();
  MV16(av[0], wf[1][2], xf[1][0]); SB();
  SB();
  MV16(av[1], wf[1][3], xf[1][0]); SB();
  WAITV(5);
  BAR();
  xr0 += 39936; wr0 += 39936; asm volatile("" : "+v"(xr0), "+v"(wr0));
  wk += 39936; wh += 39936; asm volatile("" : "+v"(wk), "+v"(wh));
  SB();
  MV16(av[2], wf[1][4], xf[1][0]); SB();
  xf[0][0] = *(LDS_AS const bf16x8*)(xr0 + 0);
  SB();
  MV16(ak[0], wf[1][1], xf[1][0]); SB();
  wf[0][2] = *(LDS_AS const bf16x8*)(wh + 1024);
  SB();
  MV16(ak[1], wf[1][1], xf[1][1]); SB();
  wf[0][3] = *(LDS_AS const bf16x8*)(wh + 7168);
  SB();
  MV16(aq[1], wf[1][0], xf[1][1]); SB();
  wf[0][4] = *(LDS_AS const bf16x8*)(wh + 13312);
  SB();
  MV16(ak[2], wf[1][1], xf[1][2]); SB();
  wf[0][1] = *(LDS_AS const bf16x8*)(wk + 1024);
  SB();
  MV16(aq[2], wf[1][0], xf[1][2]); SB();
  xf[0][1] = *(LDS_AS const bf16x8*)(xr0 + 2048);
  SB();
  MV16(ak[3], wf[1][1], xf[1][3]); SB();
  wf[0][0] = *(LDS_AS const bf16x8*)(wr0 + 0);
  SB();
  MV16(aq[3], wf[1][0], xf[1][3]); SB();
  xf[0][2] = *(LDS_AS const bf16x8*)(xr0 + 4096);
  SB();
  MV16(ak[4], wf[1][1], xf[1][4]); SB();
  xf[0][3] = *(LDS_AS const bf16x8*)(xr0 + 6144);
  SB();
  MV16(aq[4], wf[1][0], xf[1][4]); SB();
  xf[0][4] = *(LDS_AS const bf16x8*)(xr0 + 8192);
  SB();
  SB();
  MV16(av[0], wf[0][2], xf[0][0]); SB();
  xr1 += 39936; PINV(xr1);
  xf[1][0] = *(LDS_AS const bf16x8*)(xr1 + 0);
  SB();
  MV16(av[1], wf[0][3], xf[0][0]); SB();
  wf[1][2] = *(LDS_AS const bf16x8*)(wh + 4096);
  SB();
  MV16(av[2], wf[0][4], xf[0][0]); SB();
  wf[1][3] = *(LDS_AS const bf16x8*)(wh + 10240);
  SB();
  MV16(ak[0], wf[0][1], xf[0][0]); SB();
  wf[1][4] = *(LDS_AS const bf16x8*)(wh + 16384);
  wf[1][1] = *(LDS_AS const bf16x8*)(wk + 1536);
  SB();
  MV16(ak[1], wf[0][1], xf[0][1]); SB();
  xf[1][1] = *(LDS_AS const bf16x8*)(xr1 + 2048);
  SB();
  MV16(aq[1], wf[0][0], xf[0][1]); SB();
  wf[1][0] = *(LDS_AS const bf16x8*)(wr0 + 512);
  SB();
  MV16(ak[2], wf[0][1], xf[0][2]); SB();
  xf[1][2] = *(LDS_AS const bf16x8*)(xr1 + 4096);
  xf[1][3] = *(LDS_AS const bf16x8*)(xr1 + 6144);
  SB();
  MV16(aq[2], wf[0][0], xf[0][2]); SB();
  xf[1][4] = *(LDS_AS const bf16x8*)(xr1 + 8192);
  SB();
  MV16(ak[3], wf[0][1], xf[0][3]); SB();
  DMA1(vD[0], sDq, ldsD, 0, "sc1");
  SB();
  MV16(aq[3], wf[0][0], xf[0][3]); SB();
  DMA1(vD[1], sDq, ldsD, 8192, "sc1"); sDq += WQ_S; OPQ(sDq);
  DMA1(vD[2], sDm, ldsD, 16384, "sc1"); sDm += dm; OPQ(sDm);
  SB();
  MV16(ak[4], wf[0][1], xf[0][4]); SB();
  DMA1(vD[3], sDx, ldsD, 24576, "sc1");
  SB();
  MV16(aq[4], wf[0][0], xf[0][4]); SB();
  DMA1(vD[4], sDx, ldsE, 0, "sc1"); sDx += 128; OPQ(sDx);
  SB();
  SB();
  MV16(av[0], wf[1][2], xf[1][0]); SB();
  SB();
  MV16(av[1], wf[1][3], xf[1][0]); SB();
  WAITV(5);
  BAR();
  xr0 += 39936; wr0 += 39936; asm volatile("" : "+v"(xr0), "+v"(wr0));
  wk += 39936; wh += 39936; asm volatile("" : "+v"(wk), "+v"(wh));
  STAMP(60);
  SB();
  MV16(av[2], wf[1][4], xf[1][0]); SB();
  xf[0][0] = *(LDS_AS const bf16x8*)(xr0 + 0);
  SB();
  MV16(ak[0], wf[1][1], xf[1][0]); SB();
  wf[0][2] = *(LDS_AS const bf16x8*)(wh + 1024);
  SB();
  MV16(ak[1], wf[1][1], xf[1][1]); SB();
  wf[0][3] = *(LDS_AS const bf16x8*)(wh + 7168);
  SB();
  MV16(aq[1], wf[1][0], xf[1][1]); SB();
  wf[0][4] = *(LDS_AS const bf16x8*)(wh + 13312);
  SB();
  MV16(ak[2], wf[1][1], xf[1][2]); SB();
  wf[0][1] = *(LDS_AS const bf16x8*)(wk + 1024);
  SB();
  MV16(aq[2], wf[1][0], xf[1][2]); SB();
  xf[0][1] = *(LDS_AS const bf16x8*)(xr0 + 2048);
  SB();
  MV16(ak[3], wf[1][1], xf[1][3]); SB();
  wf[0][0] = *(LDS_AS const bf16x8*)(wr0 + 0);
  SB();
  MV16(aq[3], wf[1][0], xf[1][3]); SB();
  xf[0][2] = *(LDS_AS const bf16x8*)(xr0 + 4096);
  SB();
  MV16(ak[4], wf[1][1], xf[1][4]); SB();
  xf[0][3] = *(LDS_AS const bf16x8*)(xr0 + 6144);
  SB();
  MV16(aq[4], wf[1][0], xf[1][4]); SB();
  xf[0][4] = *(LDS_AS const bf16x8*)(xr0 + 8192);
  SB();
  SB();
  MV16(av[0], wf[0][2], xf[0][0]); SB();
  xr1 += 39936; PINV(xr1);
  xf[1][0] = *(LDS_AS const bf16x8*)(xr1 + 0);
  SB();
  MV16(av[1], wf[0][3], xf[0][0]); SB();
  wf[1][2] = *(LDS_AS const bf16x8*)(wh + 4096);
  SB();
  MV16(av[2], wf[0][4], xf[0][0]); SB();
  wf[1][3] = *(LDS_AS const bf16x8*)(wh + 10240);
  SB();
  MV16(ak[0], wf[0][1], xf[0][0]); SB();
  wf[1][4] = *(LDS_AS const bf16x8*)(wh + 16384);
  wf[1][1] = *(LDS_AS const bf16x8*)(wk + 1536);
  SB();
  MV16(ak[1], wf[0][1], xf[0][1]); SB();
  xf[1][1] = *(LDS_AS const bf16x8*)(xr1 + 2048);
  SB();
  MV16(aq[1], wf[0][0], xf[0][1]); SB();
  wf[1][0] = *(LDS_AS const bf16x8*)(wr0 + 512);
  SB();
  MV16(ak[2], wf[0][1], xf[0][2]); SB();
  xf[1][2] = *(LDS_AS const bf16x8*)(xr1 + 4096);
  xf[1][3] = *(LDS_AS const bf16x8*)(xr1 + 6144);
  SB();
  MV16(aq[2], wf[0][0], xf[0][2]); SB();
  xf[1][4] = *(LDS_AS const bf16x8*)(xr1 + 8192);
  SB();
  MV16(ak[3], wf[0][1], xf[0][3]); SB();
  DMA1(vD[0], sDq, ldsD, 39936, "sc1");
  SB();
  MV16(aq[3], wf[0][0], xf[0][3]); SB();
  DMA1(vD[1], sDq, ldsD, 48128, "sc1"); sDq += WQ_S; OPQ(sDq);
  DMA1(vD[2], sDm, ldsD, 56320, "sc1"); sDm += dm; OPQ(sDm);
  SB();
  MV16(ak[4], wf[0][1], xf[0][4]); SB();
  DMA1(vD[3], sDx, ldsD, 64512, "sc1");
  SB();
  MV16(aq[4], wf[0][0], xf[0][4]); SB();
  DMA1(vD[4], sDx, ldsE, 39936, "sc1"); sDx += 128; OPQ(sDx);
  SB();
  SB();
  MV16(av[0], wf[1][2], xf[1][0]); SB();
  SB();
  MV16(av[1], wf[1][3], xf[1][0]); SB();
  WAITV(5);
  BAR();
  xr0 += -79872; wr0 += -79872; asm volatile("" : "+v"(xr0), "+v"(wr0));
  wk += -79872; wh += -79872; asm volatile("" : "+v"(wk), "+v"(wh));
  SB();
  MV16(av[2], wf[1][4], xf[1][0]); SB();
  xf[0][0] = *(LDS_AS const bf16x8*)(xr0 + 0);
  SB();
  MV16(ak[0], wf[1][1], xf[1][0]); SB();
  wf[0][2] = *(LDS_AS const bf16x8*)(wh + 1024);
  SB();
  MV16(ak[1], wf[1][1], xf[1][1]); SB();
  wf[0][3] = *(LDS_AS const bf16x8*)(wh + 7168);
  SB();
  MV16(aq[1], wf[1][0], xf[1][1]); SB();
  wf[0][4] = *(LDS_AS const bf16x8*)(wh + 13312);
  SB();
  MV16(ak[2], wf[1][1], xf[1][2]); SB();
  wf[0][1] = *(LDS_AS const bf16x8*)(wk + 1024);
  SB();
  MV16(aq[2], wf[1][0], xf[1][2]); SB();
  xf[0][1] = *(LDS_AS const bf16x8*)(xr0 + 2048);
  SB();
  MV16(ak[3], wf[1][1], xf[1][3]); SB();
  wf[0][0] = *(LDS_AS const bf16x8*)(wr0 + 0);
  SB();
  MV16(aq[3], wf[1][0], xf[1][3]); SB();
  xf[0][2] = *(LDS_AS const bf16x8*)(xr0 + 4096);
  SB();
  MV16(ak[4], wf[1][1], xf[1][4]); SB();
  xf[0][3] = *(LDS_AS const bf16x8*)(xr0 + 6144);
  SB();
  MV16(aq[4], wf[1][0], xf[1][4]); SB();
  xf[0][4] = *(LDS_AS const bf16x8*)(xr0 + 8192);
  SB();
  SB();
  MV16(av[0], wf[0][2], xf[0][0]); SB();
  xr1 += -79872; PINV(xr1);
  xf[1][0] = *(LDS_AS const bf16x8*)(xr1 + 0);
  SB();
  MV16(av[1], wf[0][3], xf[0][0]); SB();
  wf[1][2] = *(LDS_AS const bf16x8*)(wh + 4096);
  SB();
  MV16(av[2], wf[0][4], xf[0][0]); SB();
  wf[1][3] = *(LDS_AS const bf16x8*)(wh + 10240);
  SB();
  MV16(ak[0], wf[0][1], xf[0][0]); SB();
  wf[1][4] = *(LDS_AS const bf16x8*)(wh + 16384);
  wf[1][1] = *(LDS_AS const bf16x8*)(wk + 1536);
  SB();
  MV16(ak[1], wf[0][1], xf[0][1]); SB();
  xf[1][1] = *(LDS_AS const bf16x8*)(xr1 + 2048);
  SB();
  MV16(aq[1], wf[0][0], xf[0][1]); SB();
  wf[1][0] = *(LDS_AS const bf16x8*)(wr0 + 512);
  SB();
  MV16(ak[2], wf[0][1], xf[0][2]); SB();
  xf[1][2] = *(LDS_AS const bf16x8*)(xr1 + 4096);
  xf[1][3] = *(LDS_AS const bf16x8*)(xr1 + 6144);
  SB();
  MV16(aq[2], wf[0][0], xf[0][2]); SB();
  xf[1][4] = *(LDS_AS const bf16x8*)(xr1 + 8192);
  SB();
  MV16(ak[3], wf[0][1], xf[0][3]); SB();
  DMA1(vD[0], sDq, ldsD, 79872, "sc1");
  SB();
  MV16(aq[3], wf[0][0], xf[0][3]); SB();
  DMA1(vD[1], sDq, ldsD, 88064, "sc1"); sDq += WQ_S; OPQ(sDq);
  DMA1(vD[2], sDm, ldsD, 96256, "sc1"); sDm += dm; OPQ(sDm);
  SB();
  MV16(ak[4], wf[0][1], xf[0][4]); SB();
  DMA1(vD[3], sDx, ldsD, 104448, "sc1");
  SB();
  MV16(aq[4], wf[0][0], xf[0][4]); SB();
  DMA1(vD[4], sDx, ldsE, 79872, "sc1"); sDx += 128; OPQ(sDx);
  SB();
  SB();
  MV16(av[0], wf[1][2], xf[1][0]); SB();
  SB();
  MV16(av[1], wf[1][3], xf[1][0]); SB();
  WAITV(5);
  BAR();
  xr0 += 39936; wr0 += 39936; asm volatile("" : "+v"(xr0), "+v"(wr0));
  wk += 39936; wh += 39936; asm volatile("" : "+v"(wk), "+v"(wh));
  STAMP(61);
  SB();
  MV16(av[2], wf[1][4], xf[1][0]); SB();
  xf[0][0] = *(LDS_AS const bf16x8*)(xr0 + 0);
  SB();
  MV16(ak[0], wf[1][1], xf[1][0]); SB();
  wf[0][2] = *(LDS_AS const bf16x8*)(wh + 1024);
  SB();
  MV16(ak[1], wf[1][1], xf[1][1]); SB();
  wf[0][3] = *(LDS_AS const bf16x8*)(wh + 7168);
  SB();
  MV16(aq[1], wf[1][0], xf[1][1]); SB();
  wf[0][4] = *(LDS_AS const bf16x8*)(wh + 13312);
  SB();
  MV16(ak[2], wf[1][1], xf[1][2]); SB();
  wf[0][1] = *(LDS_AS const bf16x8*)(wk + 1024);
  SB();
  MV16(aq[2], wf[1][0], xf[1][2]); SB();
  xf[0][1] = *(LDS_AS const bf16x8*)(xr0 + 2048);
  SB();
  MV16(ak[3], wf[1][1], xf[1][3]); SB();
  wf[0][0] = *(LDS_AS const bf16x8*)(wr0 + 0);
  SB();
  MV16(aq[3], wf[1][0], xf[1][3]); SB();
  xf[0][2] = *(LDS_AS const bf16x8*)(xr0 + 4096);
  SB();
  MV16(ak[4], wf[1][1], xf[1][4]); SB();
  xf[0][3] = *(LDS_AS const bf16x8*)(xr0 + 6144);
  SB();
  MV16(aq[4], wf[1][0], xf[1][4]); SB();
  xf[0][4] = *(LDS_AS const bf16x8*)(xr0 + 8192);
  SB();
  SB();
  MV16(av[0], wf[0][2], xf[0][0]); SB();
  xr1 += 39936; PINV(xr1);
  xf[1][0] = *(LDS_AS const bf16x8*)(xr1 + 0);
  SB();
  MV16(av[1], wf[0][3], xf[0][0]); SB();
  wf[1][2] = *(LDS_AS const bf16x8*)(wh + 4096);
  SB();
  MV16(av[2], wf[0][4], xf[0][0]); SB();
  wf[1][3] = *(LDS_AS const bf16x8*)(wh + 10240);
  SB();
  MV16(ak[0], wf[0][1], xf[0][0]); SB();
  wf[1][4] = *(LDS_AS const bf16x8*)(wh + 16384);
  wf[1][1] = *(LDS_AS const bf16x8*)(wk + 1536);
  SB();
  MV16(ak[1], wf[0][1], xf[0][1]); SB();
  xf[1][1] = *(LDS_AS const bf16x8*)(xr1 + 2048);
  SB();
  MV16(aq[1], wf[0][0], xf[0][1]); SB();
  wf[1][0] = *(LDS_AS const bf16x8*)(wr0 + 512);
  SB();
  MV16(ak[2], wf[0][1], xf[0][2]); SB();
  xf[1][2] = *(LDS_AS const bf16x8*)(xr1 + 4096);
  xf[1][3] = *(LDS_AS const bf16x8*)(xr1 + 6144);
  SB();
  MV16(aq[2], wf[0][0], xf[0][2]); SB();
  xf[1][4] = *(LDS_AS const bf16x8*)(xr1 + 8192);
  SB();
  MV16(ak[3], wf[0][1], xf[0][3]); SB();
  DMA1(vD[0], sDq, ldsD, 0, "sc1");
  SB();
  MV16(aq[3], wf[0][0], xf[0][3]); SB();
  DMA1(vD[1], sDq, ldsD, 8192, "sc1"); sDq += WQ_S; OPQ(sDq);
  DMA1(vD[2], sDm, ldsD, 16384, "sc1"); sDm += dm; OPQ(sDm);
  SB();
  MV16(ak[4], wf[0][1], xf[0][4]); SB();
  DMA1(vD[3], sDx, ldsD, 24576, "sc1");
  SB();
  MV16(aq[4], wf[0][0], xf[0][4]); SB();
  DMA1(vD[4], sDx, ldsE, 0, "sc1"); sDx += 128; OPQ(sDx);
  SB();
  SB();
  MV16(av[0], wf[1][2], xf[1][0]); SB();
  SB();
  MV16(av[1], wf[1][3], xf[1][0]); SB();
  WAITV(5);
  BAR();
  xr0 += 39936; wr0 += 39936; asm volatile("" : "+v"(xr0), "+v"(wr0));
  wk += 39936; wh += 39936; asm volatile("" : "+v"(wk), "+v"(wh));
  SB();
  MV16(av[2], wf[1][4], xf[1][0]); SB();
  xf[0][0] = *(LDS_AS const bf16x8*)(xr0 + 0);
  SB();
  MV16(ak[0], wf[1][1], xf[1][0]); SB();
  wf[0][2] = *(LDS_AS const bf16x8*)(wh + 1024);
  SB();
  MV16(ak[1], wf[1][1], xf[1][1]); SB();
  wf[0][3] = *(LDS_AS const bf16x8*)(wh + 7168);
  SB();
  MV16(aq[1], wf[1][0], xf[1][1]); SB();
  wf[0][4] = *(LDS_AS const bf16x8*)(wh + 13312);
  SB();
  MV16(ak[2], wf[1][1], xf[1][2]); SB();
  wf[0][1] = *(LDS_AS const bf16x8*)(wk + 1024);
  SB();
  MV16(aq[2], wf[1][0], xf[1][2]); SB();
  xf[0][1] = *(LDS_AS const bf16x8*)(xr0 + 2048);
  SB();
  MV16(ak[3], wf[1][1], xf[1][3]); SB();
  wf[0][0] = *(LDS_AS const bf16x8*)(wr0 + 0);
  SB();
  MV16(aq[3], wf[1][0], xf[1][3]); SB();
  xf[0][2] = *(LDS_AS const bf16x8*)(xr0 + 4096);
  SB();
  MV16(ak[4], wf[1][1], xf[1][4]); SB();
  xf[0][3] = *(LDS_AS const bf16x8*)(xr0 + 6144);
  SB();
  MV16(aq[4], wf[1][0], xf[1][4]); SB();
  xf[0][4] = *(LDS_AS const bf16x8*)(xr0 + 8192);
  SB();
  SB();
  MV16(av[0], wf[0][2], xf[0][0]); SB();
  xr1 += 39936; PINV(xr1);
  xf[1][0] = *(LDS_AS const bf16x8*)(xr1 + 0);
  SB();
  MV16(av[1], wf[0][3], xf[0][0]); SB();
  wf[1][2] = *(LDS_AS const bf16x8*)(wh + 4096);
  SB();
  MV16(av[2], wf[0][4], xf[0][0]); SB();
  wf[1][3] = *(LDS_AS const bf16x8*)(wh + 10240);
  SB();
  MV16(ak[0], wf[0][1], xf[0][0]); SB();
  wf[1][4] = *(LDS_AS const bf16x8*)(wh + 16384);
  SB();
  MV16(ak[1], wf[0][1], xf[0][1]); SB();
  wf[1][1] = *(LDS_AS const bf16x8*)(wk + 1536);
  SB();
  MV16(aq[1], wf[0][0], xf[0][1]); SB();
  xf[1][1] = *(LDS_AS const bf16x8*)(xr1 + 2048);
  SB();
  MV16(ak[2], wf[0][1], xf[0][2]); SB();
  wf[1][0] = *(LDS_AS const bf16x8*)(wr0 + 512);
  SB();
  MV16(aq[2], wf[0][0], xf[0][2]); SB();
  xf[1][2] = *(LDS_AS const bf16x8*)(xr1 + 4096);
  SB();
  MV16(ak[3], wf[0][1], xf[0][3]); SB();
  xf[1][3] = *(LDS_AS const bf16x8*)(xr1 + 6144);
  SB();
  MV16(aq[3], wf[0][0], xf[0][3]); SB();
  xf[1][4] = *(LDS_AS const bf16x8*)(xr1 + 8192);
  SB();
  MV16(ak[4], wf[0][1], xf[0][4]); SB();
  DMA2(vWa, vWb, sW, ldsU, 0, ""); sW += dW; OPQ(sW);
  SB();
  MV16(aq[4], wf[0][0], xf[0][4]); SB();
  DMA2(vWa, vWb, sW, ldsU, 2048, ""); sW = wrap == 5 ? bW : sW + dW; OPQ(sW);
  SB();
  SB();
  MV16(av[0], wf[1][2], xf[1][0]); SB();
  WAITV(4);
  BAR();
  xr0 += -79872; wr0 += -79872; asm volatile("" : "+v"(xr0), "+v"(wr0));
  wk += -79872; wh += -79872; asm volatile("" : "+v"(wk), "+v"(wh));
  STAMP(62);
  SB();
  MV16(av[1], wf[1][3], xf[1][0]); SB();
  xf[0][0] = *(LDS_AS const bf16x8*)(xr0 + 0);
  SB();
  MV16(av[2], wf[1][4], xf[1][0]); SB();
  wf[0][2] = *(LDS_AS const bf16x8*)(wh + 1024);
  SB();
  MV16(ak[0], wf[1][1], xf[1][0]); SB();
  wf[0][3] = *(LDS_AS const bf16x8*)(wh + 7168);
  SB();
  MV16(ak[1], wf[1][1], xf[1][1]); SB();
  wf[0][4] = *(LDS_AS const bf16x8*)(wh + 13312);
  wf[0][1] = *(LDS_AS const bf16x8*)(wk + 1024);
  SB();
  MV16(aq[1], wf[1][0], xf[1][1]); SB();
  xf[0][1] = *(LDS_AS const bf16x8*)(xr0 + 2048);
  SB();
  MV16(ak[2], wf[1][1], xf[1][2]); SB();
  wf[0][0] = *(LDS_AS const bf16x8*)(wr0 + 0);
  SB();
  MV16(aq[2], wf[1][0], xf[1][2]); SB();
  xf[0][2] = *(LDS_AS const bf16x8*)(xr0 + 4096);
  SB();
  MV16(ak[3], wf[1][1], xf[1][3]); SB();
  xf[0][3] = *(LDS_AS const bf16x8*)(xr0 + 6144);
  xf[0][4] = *(LDS_AS const bf16x8*)(xr0 + 8192);
  SB();
  MV16(aq[3], wf[1][0], xf[1][3]); SB();
  unsigned f0; { const int l_ = lane_now(); vFU = (l_ < 14 ? l_ : 13) * FU; PINV(vFU); }
  LD4(f0, vFU, sFU, 0, "sc1");
  SB();
  MV16(ak[4], wf[1][1], xf[1][4]); SB();
  unsigned tl; { const int l_ = lane_now(); tl = lds0 + TB + l_ * 16; const u32x2 t_ = *(LDS_AS const u32x2*)(lds0 + TB + 6144 + cb * 512 + l_ * 8); sk0 = t_[0]; sv = t_[1];
    vS = *(LDS_AS const u32x2*)(lds0 + TB + 4096 + cb * 512 + l_ * 8); PINV(tl); }
  SB();
  MV16(aq[4], wf[1][0], xf[1][4]); SB();
  { const int l_ = lane_now(); const unsigned a_ = lds0 + TB + 6144 + l_ * 8; sv = *(LDS_AS const unsigned*)(lds0 + TB + 7684 + (l_ & 31) * 8);
    hb[0] = *(LDS_AS const unsigned*)(a_ + 0); hb[1] = *(LDS_AS const unsigned*)(a_ + 512); hb[2] = *(LDS_AS const unsigned*)(a_ + 1024); }
  SB();
  xr1 += -79872; PINV(xr1);
  MV16(av[0], wf[0][2], xf[0][0]); SB();
  xf[1][0] = *(LDS_AS const bf16x8*)(xr1 + 0);
  wf[1][2] = *(LDS_AS const bf16x8*)(wh + 4096);
  wf[1][3] = *(LDS_AS const bf16x8*)(wh + 10240);
  SB();
  MV16(av[1], wf[0][3], xf[0][0]); SB();
  wf[1][4] = *(LDS_AS const bf16x8*)(wh + 16384);
  wf[1][1] = *(LDS_AS const bf16x8*)(wk + 1536);
  SB();
  MV16(av[2], wf[0][4], xf[0][0]); SB();
  xf[1][1] = *(LDS_AS const bf16x8*)(xr1 + 2048);
  wf[1][0] = *(LDS_AS const bf16x8*)(wr0 + 512);
  xf[1][2] = *(LDS_AS const bf16x8*)(xr1 + 4096);
  SB();
  MV16(ak[0], wf[0][1], xf[0][0]); SB();
  xf[1][3] = *(LDS_AS const bf16x8*)(xr1 + 6144);
  xf[1][4] = *(LDS_AS const bf16x8*)(xr1 + 8192);
  SB();
  MV16(ak[1], wf[0][1], xf[0][1]); SB();
  WAITV(1);
  fw[5][0] = tr8u(rU0 + 0, 512);
  fw[5][1] = tr8u(rU1 + 0, 512);
  fw[5][2] = tr8u(rU2 + 0, 512);
  fw[5][3] = tr8u(rU3 + 0, 512);
  WAITL(0);
  DMA2(vWa, vWb, sW, ldsU, 0, ""); sW += dW; OPQ(sW);
  SB();
  MV16(aq[1], wf[0][0], xf[0][1]); SB();
  DMA2(vWa, vWb, sW, ldsU, 2048, "");
  sk1 = sk0 ^ 32; asm volatile("" : "+v"(sk0), "+v"(sk1), "+v"(sv));
  const bool lo_ = lane_now() < 32;
  e0 = lo_ ? sk0 + SK : sv; e1 = lo_ ? sk1 + SK + 512 : sv + 256; e2 = lo_ ? 1024 : 512; asm volatile("" : "+v"(e0), "+v"(e1), "+v"(e2));
  const unsigned kl = lo_ ? kz : ~0u;
  SB();
  MV16(av[0], wf[1][2], xf[1][0]); SB();
  SB();
  MV16(av[1], wf[1][3], xf[1][0]); SB();
  SB();
  MV16(av[2], wf[1][4], xf[1][0]); SB();
  SB();
  MV16(ak[0], wf[1][1], xf[1][0]); SB();
  SB();
  MV16(ak[2], wf[0][1], xf[0][2]); SB();
  SB();
  MV16(aq[2], wf[0][0], xf[0][2]); SB();
  SB();
  MV16(ak[1], wf[1][1], xf[1][1]); SB();
  SB();
  MV16(aq[1], wf[1][0], xf[1][1]); SB();
  SB();
  MV16(ak[3], wf[0][1], xf[0][3]); SB();
  SB();
  MV16(aq[3], wf[0][0], xf[0][3]); SB();
  SB();
  MV16(ak[2], wf[1][1], xf[1][2]); SB();
  t3 = *(LDS_AS const u32x4*)(tl + 3072); t2 = *(LDS_AS const u32x4*)(tl + 2048);
  *(LDS_AS u32x2*)(hb[0] + SK) = (u32x2){pack2(av[0][0], av[0][1]) & kz, pack2(av[0][2], av[0][3]) & kz};
  SB();
  MV16(aq[2], wf[1][0], xf[1][2]); SB();
  *(LDS_AS u32x2*)(hb[1] + SK) = (u32x2){pack2(av[1][0], av[1][1]) & kz, pack2(av[1][2], av[1][3]) & kz};
  SB();
  MV16(ak[4], wf[0][1], xf[0][4]); SB();
  *(LDS_AS u32x2*)(hb[2] + SK) = (u32x2){pack2(av[2][0], av[2][1]) & kz, pack2(av[2][2], av[2][3]) & kz};
  SB();
  MV16(aq[4], wf[0][0], xf[0][4]); SB();
  *(LDS_AS u32x2*)(e0) = (u32x2){pack2(ak[0][0], ak[0][1]) & kl, pack2(ak[0][2], ak[0][3]) & kl};
  SB();
  MV16(ak[3], wf[1][1], xf[1][3]); SB();
  WAITV1(4, f0);
  if (__ballot(f0 != nonce) != 0ull) poll1<2>(sFU, vFU, nonce);
  STAMP(44);
  { const int l_ = lane_now(); vFU = (14 + (l_ < 28 ? l_ : 27)) * FU; mq_ = l_ >= 48 ? 0u : ~0u; asm volatile("" : "+v"(vFU), "+v"(mq_)); }
  SB();
  MV16(aq[3], wf[1][0], xf[1][3]); SB();
  m1 = *(LDS_AS const u32x4*)(tl + 1024); cm = *(LDS_AS const f32x4*)(tl + cmo);
  SB();
  MV16(ak[4], wf[1][1], xf[1][4]); SB();
  rk = (t3[2] ^ kx) + r0 * 64; rv = t3[3] + r0 * 32; asm volatile("" : "+v"(rk), "+v"(rv));
  *(LDS_AS u32x2*)(e1 + 0 * e2) = (u32x2){pack2(ak[1][0], ak[1][1]), pack2(ak[1][2], ak[1][3])};
  SB();
  MV16(aq[4], wf[1][0], xf[1][4]); SB();
  *(LDS_AS u32x2*)(sk0 + SQ + 512) = (u32x2){pack2(aq[1][0], aq[1][1]), pack2(aq[1][2], aq[1][3])};
  SB();
  STAMP(36);
  *(LDS_AS u32x2*)(e0 + 1 * e2) = (u32x2){pack2(ak[2][0], ak[2][1]), pack2(ak[2][2], ak[2][3])};
  *(LDS_AS u32x2*)(sk1 + SQ + 1024) = (u32x2){pack2(aq[2][0], aq[2][1]), pack2(aq[2][2], aq[2][3])};
  SB();
  *(LDS_AS u32x2*)(e1 + 1 * e2) = (u32x2){pack2(ak[3][0], ak[3][1]), pack2(ak[3][2], ak[3][3])};
  *(LDS_AS u32x2*)(sk0 + SQ + 1536) = (u32x2){pack2(aq[3][0], aq[3][1]), pack2(aq[3][2], aq[3][3])};
  SB();
  *(LDS_AS u32x2*)(e0 + 2 * e2) = (u32x2){pack2(ak[4][0], ak[4][1]), pack2(ak[4][2], ak[4][3])};
  *(LDS_AS u32x2*)(sk1 + SQ + 2048) = (u32x2){pack2(aq[4][0], aq[4][1]), pack2(aq[4][2], aq[4][3])};
  SB();

  } else if (w < 7) {
  f32x4 aq[5], ak[5], av[5];
  bf16x8 xf[2][5], wf[2][5];
  unsigned sk0, sk1, sv;
  WAITV(5);
  BAR();
  STAMP(35);
  wf[0][1] = *(LDS_AS const bf16x8*)(wr0 + 1024);
  xf[0][1] = *(LDS_AS const bf16x8*)(xr0 + 10240);
  wf[0][2] = *(LDS_AS const bf16x8*)(wr0 + 2048);
  wf[0][0] = *(LDS_AS const bf16x8*)(wr0 + 0);
  xf[0][2] = *(LDS_AS const bf16x8*)(xr0 + 12288);
  xf[0][3] = *(LDS_AS const bf16x8*)(xr0 + 14336);
  xf[0][4] = *(LDS_AS const bf16x8*)(xr0 + 16384);
  SB();
  MV16Z(ak[1], wf[0][1], xf[0][1]); SB();
  wf[1][1] = *(LDS_AS const bf16x8*)(wr0 + 4096);
  SB();
  MV16Z(av[1], wf[0][2], xf[0][1]); SB();
  xf[1][1] = *(LDS_AS const bf16x8*)(xr1 + 10240);
  SB();
  MV16Z(aq[1], wf[0][0], xf[0][1]); SB();
  wf[1][2] = *(LDS_AS const bf16x8*)(wr0 + 5120);
  SB();
  MV16Z(ak[2], wf[0][1], xf[0][2]); SB();
  wf[1][0] = *(LDS_AS const bf16x8*)(wr0 + 3072);
  SB();
  MV16Z(av[2], wf[0][2], xf[0][2]); SB();
  xf[1][2] = *(LDS_AS const bf16x8*)(xr1 + 12288);
  SB();
  MV16Z(aq[2], wf[0][0], xf[0][2]); SB();
  xf[1][3] = *(LDS_AS const bf16x8*)(xr1 + 14336);
  SB();
  MV16Z(ak[3], wf[0][1], xf[0][3]); SB();
  xf[1][4] = *(LDS_AS const bf16x8*)(xr1 + 16384);
  SB();
  MV16Z(av[3], wf[0][2], xf[0][3]); SB();
  DMA1(vD[0], sDq, ldsD, 79872, "sc1");
  SB();
  MV16Z(aq[3], wf[0][0], xf[0][3]); SB();
  DMA1(vD[1], sDq, ldsD, 88064, "sc1"); sDq += WQ_S; OPQ(sDq);
  SB();
  MV16Z(ak[4], wf[0][1], xf[0][4]); SB();
  DMA1(vD[2], sDm, ldsD, 96256, "sc1"); sDm += dm; OPQ(sDm);
  SB();
  MV16Z(av[4], wf[0][2], xf[0][4]); SB();
  DMA1(vD[3], sDx, ldsD, 104448, "sc1");
  SB();
  MV16Z(aq[4], wf[0][0], xf[0][4]); SB();
  DMA1(vD[4], sDx, ldsE, 79872, "sc1"); sDx += 128; OPQ(sDx);
  SB();
  SB();
  MV16(ak[1], wf[1][1], xf[1][1]); SB();
  SB();
  MV16(av[1], wf[1][2], xf[1][1]); SB();
  WAITV(5);
  BAR();
  xr0 += 39936; wr0 += 39936; asm volatile("" : "+v"(xr0), "+v"(wr0));
  DMA1(vD[0], sDq, ldsD, 0, "sc1");
  DMA1(vD[1], sDq, ldsD, 8192, "sc1"); sDq += WQ_S; OPQ(sDq);
  DMA1(vD[2], sDm, ldsD, 16384, "sc1"); sDm += dm; OPQ(sDm);
  DMA1(vD[3], sDx, ldsD, 24576, "sc1");
  DMA1(vD[4], sDx, ldsE, 0, "sc1"); sDx += 128; OPQ(sDx);
  SB();
  MV16(aq[1], wf[1][0], xf[1][1]); SB();
  wf[0][1] = *(LDS_AS const bf16x8*)(wr0 + 1024);
  SB();
  MV16(ak[2], wf[1][1], xf[1][2]); SB();
  xf[0][1] = *(LDS_AS const bf16x8*)(xr0 + 10240);
  SB();
  MV16(av[2], wf[1][2], xf[1][2]); SB();
  wf[0][2] = *(LDS_AS const bf16x8*)(wr0 + 2048);
  SB();
  MV16(aq[2], wf[1][0], xf[1][2]); SB();
  wf[0][0] = *(LDS_AS const bf16x8*)(wr0 + 0);
  SB();
  MV16(ak[3], wf[1][1], xf[1][3]); SB();
  xf[0][2] = *(LDS_AS const bf16x8*)(xr0 + 12288);
  SB();
  MV16(av[3], wf[1][2], xf[1][3]); SB();
  xf[0][3] = *(LDS_AS const bf16x8*)(xr0 + 14336);
  SB();
  MV16(aq[3], wf[1][0], xf[1][3]); SB();
  xf[0][4] = *(LDS_AS const bf16x8*)(xr0 + 16384);
  SB();
  MV16(ak[4], wf[1][1], xf[1][4]); SB();
  MV16(av[4], wf[1][2], xf[1][4]); SB();
  MV16(aq[4], wf[1][0], xf[1][4]); SB();
  SB();
  MV16(ak[1], wf[0][1], xf[0][1]); SB();
  xr1 += 39936; PINV(xr1);
  SB();
  MV16(av[1], wf[0][2], xf[0][1]); SB();
  wf[1][1] = *(LDS_AS const bf16x8*)(wr0 + 4096);
  SB();
  MV16(aq[1], wf[0][0], xf[0][1]); SB();
  xf[1][1] = *(LDS_AS const bf16x8*)(xr1 + 10240);
  SB();
  MV16(ak[2], wf[0][1], xf[0][2]); SB();
  wf[1][2] = *(LDS_AS const bf16x8*)(wr0 + 5120);
  SB();
  MV16(av[2], wf[0][2], xf[0][2]); SB();
  wf[1][0] = *(LDS_AS const bf16x8*)(wr0 + 3072);
  SB();
  MV16(aq[2], wf[0][0], xf[0][2]); SB();
  xf[1][2] = *(LDS_AS const bf16x8*)(xr1 + 12288);
  SB();
  MV16(ak[3], wf[0][1], xf[0][3]); SB();
  xf[1][3] = *(LDS_AS const bf16x8*)(xr1 + 14336);
  SB();
  MV16(av[3], wf[0][2], xf[0][3]); SB();
  xf[1][4] = *(LDS_AS const bf16x8*)(xr1 + 16384);
  SB();
  MV16(aq[3], wf[0][0], xf[0][3]); SB();
  MV16(ak[4], wf[0][1], xf[0][4]); SB();
  MV16(av[4], wf[0][2], xf[0][4]); SB();
  MV16(aq[4], wf[0][0], xf[0][4]); SB();
  SB();
  MV16(ak[1], wf[1][1], xf[1][1]); SB();
  SB();
  MV16(av[1], wf[1][2], xf[1][1]); SB();
  WAITV(5);
  BAR();
  xr0 += 39936; wr0 += 39936; asm volatile("" : "+v"(xr0), "+v"(wr0));
  STAMP(60);
  DMA1(vD[0], sDq, ldsD, 39936, "sc1");
  DMA1(vD[1], sDq, ldsD, 48128, "sc1"); sDq += WQ_S; OPQ(sDq);
  DMA1(vD[2], sDm, ldsD, 56320, "sc1"); sDm += dm; OPQ(sDm);
  DMA1(vD[3], sDx, ldsD, 64512, "sc1");
  DMA1(vD[4], sDx, ldsE, 39936, "sc1"); sDx += 128; OPQ(sDx);
  SB();
  MV16(aq[1], wf[1][0], xf[1][1]); SB();
  wf[0][1] = *(LDS_AS const bf16x8*)(wr0 + 1024);
  SB();
  MV16(ak[2], wf[1][1], xf[1][2]); SB();
  xf[0][1] = *(LDS_AS const bf16x8*)(xr0 + 10240);
  SB();
  MV16(av[2], wf[1][2], xf[1][2]); SB();
  wf[0][2] = *(LDS_AS const bf16x8*)(wr0 + 2048);
  SB();
  MV16(aq[2], wf[1][0], xf[1][2]); SB();
  wf[0][0] = *(LDS_AS const bf16x8*)(wr0 + 0);
  SB();
  MV16(ak[3], wf[1][1], xf[1][3]); SB();
  xf[0][2] = *(LDS_AS const bf16x8*)(xr0 + 12288);
  SB();
  MV16(av[3], wf[1][2], xf[1][3]); SB();
  xf[0][3] = *(LDS_AS const bf16x8*)(xr0 + 14336);
  SB();
  MV16(aq[3], wf[1][0], xf[1][3]); SB();
  xf[0][4] = *(LDS_AS const bf16x8*)(xr0 + 16384);
  SB();
  MV16(ak[4], wf[1][1], xf[1][4]); SB();
  MV16(av[4], wf[1][2], xf[1][4]); SB();
  MV16(aq[4], wf[1][0], xf[1][4]); SB();
  SB();
  MV16(ak[1], wf[0][1], xf[0][1]); SB();
  xr1 += 39936; PINV(xr1);
  SB();
  MV16(av[1], wf[0][2], xf[0][1]); SB();
  wf[1][1] = *(LDS_AS const bf16x8*)(wr0 + 4096);
  SB();
  MV16(aq[1], wf[0][0], xf[0][1]); SB();
  xf[1][1] = *(LDS_AS const bf16x8*)(xr1 + 10240);
  SB();
  MV16(ak[2], wf[0][1], xf[0][2]); SB();
  wf[1][2] = *(LDS_AS const bf16x8*)(wr0 + 5120);
  SB();
  MV16(av[2], wf[0][2], xf[0][2]); SB();
  wf[1][0] = *(LDS_AS const bf16x8*)(wr0 + 3072);
  SB();
  MV16(aq[2], wf[0][0], xf[0][2]); SB();
  xf[1][2] = *(LDS_AS const bf16x8*)(xr1 + 12288);
  SB();
  MV16(ak[3], wf[0][1], xf[0][3]); SB();
  xf[1][3] = *(LDS_AS const bf16x8*)(xr1 + 14336);
  SB();
  MV16(av[3], wf[0][2], xf[0][3]); SB();
  xf[1][4] = *(LDS_AS const bf16x8*)(xr1 + 16384);
  SB();
  MV16(aq[3], wf[0][0], xf[0][3]); SB();
  MV16(ak[4], wf[0][1], xf[0][4]); SB();
  MV16(av[4], wf[0][2], xf[0][4]); SB();
  MV16(aq[4], wf[0][0], xf[0][4]); SB();
  SB();
  MV16(ak[1], wf[1][1], xf[1][1]); SB();
  SB();
  MV16(av[1], wf[1][2], xf[1][1]); SB();
  WAITV(5);
  BAR();
  xr0 += -79872; wr0 += -79872; asm volatile("" : "+v"(xr0), "+v"(wr0));
  DMA1(vD[0], sDq, ldsD, 79872, "sc1");
  DMA1(vD[1], sDq, ldsD, 88064, "sc1"); sDq += WQ_S; OPQ(sDq);
  DMA1(vD[2], sDm, ldsD, 96256, "sc1"); sDm += dm; OPQ(sDm);
  DMA1(vD[3], sDx, ldsD, 104448, "sc1");
  DMA1(vD[4], sDx, ldsE, 79872, "sc1"); sDx += 128; OPQ(sDx);
  SB();
  MV16(aq[1], wf[1][0], xf[1][1]); SB();
  wf[0][1] = *(LDS_AS const bf16x8*)(wr0 + 1024);
  SB();
  MV16(ak[2], wf[1][1], xf[1][2]); SB();
  xf[0][1] = *(LDS_AS const bf16x8*)(xr0 + 10240);
  SB();
  MV16(av[2], wf[1][2], xf[1][2]); SB();
  wf[0][2] = *(LDS_AS const bf16x8*)(wr0 + 2048);
  SB();
  MV16(aq[2], wf[1][0], xf[1][2]); SB();
  wf[0][0] = *(LDS_AS const bf16x8*)(wr0 + 0);
  SB();
  MV16(ak[3], wf[1][1], xf[1][3]); SB();
  xf[0][2] = *(LDS_AS const bf16x8*)(xr0 + 12288);
  SB();
  MV16(av[3], wf[1][2], xf[1][3]); SB();
  xf[0][3] = *(LDS_AS const bf16x8*)(xr0 + 14336);
  SB();
  MV16(aq[3], wf[1][0], xf[1][3]); SB();
  xf[0][4] = *(LDS_AS const bf16x8*)(xr0 + 16384);
  SB();
  MV16(ak[4], wf[1][1], xf[1][4]); SB();
  MV16(av[4], wf[1][2], xf[1][4]); SB();
  MV16(aq[4], wf[1][0], xf[1][4]); SB();
  SB();
  MV16(ak[1], wf[0][1], xf[0][1]); SB();
  xr1 += -79872; PINV(xr1);
  SB();
  MV16(av[1], wf[0][2], xf[0][1]); SB();
  wf[1][1] = *(LDS_AS const bf16x8*)(wr0 + 4096);
  SB();
  MV16(aq[1], wf[0][0], xf[0][1]); SB();
  xf[1][1] = *(LDS_AS const bf16x8*)(xr1 + 10240);
  SB();
  MV16(ak[2], wf[0][1], xf[0][2]); SB();
  wf[1][2] = *(LDS_AS const bf16x8*)(wr0 + 5120);
  SB();
  MV16(av[2], wf[0][2], xf[0][2]); SB();
  wf[1][0] = *(LDS_AS const bf16x8*)(wr0 + 3072);
  SB();
  MV16(aq[2], wf[0][0], xf[0][2]); SB();
  xf[1][2] = *(LDS_AS const bf16x8*)(xr1 + 12288);
  SB();
  MV16(ak[3], wf[0][1], xf[0][3]); SB();
  xf[1][3] = *(LDS_AS const bf16x8*)(xr1 + 14336);
  SB();
  MV16(av[3], wf[0][2], xf[0][3]); SB();
  xf[1][4] = *(LDS_AS const bf16x8*)(xr1 + 16384);
  SB();
  MV16(aq[3], wf[0][0], xf[0][3]); SB();
  MV16(ak[4], wf[0][1], xf[0][4]); SB();
  MV16(av[4], wf[0][2], xf[0][4]); SB();
  MV16(aq[4], wf[0][0], xf[0][4]); SB();
  SB();
  MV16(ak[1], wf[1][1], xf[1][1]); SB();
  SB();
  MV16(av[1], wf[1][2], xf[1][1]); SB();
  WAITV(5);
  BAR();
  xr0 += 39936; wr0 += 39936; asm volatile("" : "+v"(xr0), "+v"(wr0));
  STAMP(61);
  DMA1(vD[0], sDq, ldsD, 0, "sc1");
  DMA1(vD[1], sDq, ldsD, 8192, "sc1"); sDq += WQ_S; OPQ(sDq);
  DMA1(vD[2], sDm, ldsD, 16384, "sc1"); sDm += dm; OPQ(sDm);
  DMA1(vD[3], sDx, ldsD, 24576, "sc1");
  DMA1(vD[4], sDx, ldsE, 0, "sc1"); sDx += 128; OPQ(sDx);
  SB();
  MV16(aq[1], wf[1][0], xf[1][1]); SB();
  wf[0][1] = *(LDS_AS const bf16x8*)(wr0 + 1024);
  SB();
  MV16(ak[2], wf[1][1], xf[1][2]); SB();
  xf[0][1] = *(LDS_AS const bf16x8*)(xr0 + 10240);
  SB();
  MV16(av[2], wf[1][2], xf[1][2]); SB();
  wf[0][2] = *(LDS_AS const bf16x8*)(wr0 + 2048);
  SB();
  MV16(aq[2], wf[1][0], xf[1][2]); SB();
  wf[0][0] = *(LDS_AS const bf16x8*)(wr0 + 0);
  SB();
  MV16(ak[3], wf[1][1], xf[1][3]); SB();
  xf[0][2] = *(LDS_AS const bf16x8*)(xr0 + 12288);
  SB();
  MV16(av[3], wf[1][2], xf[1][3]); SB();
  xf[0][3] = *(LDS_AS const bf16x8*)(xr0 + 14336);
  SB();
  MV16(aq[3], wf[1][0], xf[1][3]); SB();
  xf[0][4] = *(LDS_AS const bf16x8*)(xr0 + 16384);
  SB();
  MV16(ak[4], wf[1][1], xf[1][4]); SB();
  MV16(av[4], wf[1][2], xf[1][4]); SB();
  MV16(aq[4], wf[1][0], xf[1][4]); SB();
  SB();
  MV16(ak[1], wf[0][1], xf[0][1]); SB();
  xr1 += 39936; PINV(xr1);
  SB();
  MV16(av[1], wf[0][2], xf[0][1]); SB();
  wf[1][1] = *(LDS_AS const bf16x8*)(wr0 + 4096);
  SB();
  MV16(aq[1], wf[0][0], xf[0][1]); SB();
  xf[1][1] = *(LDS_AS const bf16x8*)(xr1 + 10240);
  SB();
  MV16(ak[2], wf[0][1], xf[0][2]); SB();
  wf[1][2] = *(LDS_AS const bf16x8*)(wr0 + 5120);
  SB();
  MV16(av[2], wf[0][2], xf[0][2]); SB();
  wf[1][0] = *(LDS_AS const bf16x8*)(wr0 + 3072);
  SB();
  MV16(aq[2], wf[0][0], xf[0][2]); SB();
  xf[1][2] = *(LDS_AS const bf16x8*)(xr1 + 12288);
  SB();
  MV16(ak[3], wf[0][1], xf[0][3]); SB();
  xf[1][3] = *(LDS_AS const bf16x8*)(xr1 + 14336);
  SB();
  MV16(av[3], wf[0][2], xf[0][3]); SB();
  xf[1][4] = *(LDS_AS const bf16x8*)(xr1 + 16384);
  SB();
  MV16(aq[3], wf[0][0], xf[0][3]); SB();
  MV16(ak[4], wf[0][1], xf[0][4]); SB();
  MV16(av[4], wf[0][2], xf[0][4]); SB();
  MV16(aq[4], wf[0][0], xf[0][4]); SB();
  SB();
  MV16(ak[1], wf[1][1], xf[1][1]); SB();
  SB();
  MV16(av[1], wf[1][2], xf[1][1]); SB();
  WAITV(5);
  BAR();
  xr0 += 39936; wr0 += 39936; asm volatile("" : "+v"(xr0), "+v"(wr0));
  DMA2(vWa, vWb, sW, ldsU, 0, ""); sW += dW; OPQ(sW);
  DMA2(vWa, vWb, sW, ldsU, 2048, ""); sW = wrap == 5 ? bW : sW + dW; OPQ(sW);
  SB();
  MV16(aq[1], wf[1][0], xf[1][1]); SB();
  wf[0][1] = *(LDS_AS const bf16x8*)(wr0 + 1024);
  SB();
  MV16(ak[2], wf[1][1], xf[1][2]); SB();
  xf[0][1] = *(LDS_AS const bf16x8*)(xr0 + 10240);
  SB();
  MV16(av[2], wf[1][2], xf[1][2]); SB();
  wf[0][2] = *(LDS_AS const bf16x8*)(wr0 + 2048);
  SB();
  MV16(aq[2], wf[1][0], xf[1][2]); SB();
  wf[0][0] = *(LDS_AS const bf16x8*)(wr0 + 0);
  SB();
  MV16(ak[3], wf[1][1], xf[1][3]); SB();
  xf[0][2] = *(LDS_AS const bf16x8*)(xr0 + 12288);
  SB();
  MV16(av[3], wf[1][2], xf[1][3]); SB();
  xf[0][3] = *(LDS_AS const bf16x8*)(xr0 + 14336);
  SB();
  MV16(aq[3], wf[1][0], xf[1][3]); SB();
  xf[0][4] = *(LDS_AS const bf16x8*)(xr0 + 16384);
  SB();
  MV16(ak[4], wf[1][1], xf[1][4]); SB();
  MV16(av[4], wf[1][2], xf[1][4]); SB();
  MV16(aq[4], wf[1][0], xf[1][4]); SB();
  SB();
  MV16(ak[1], wf[0][1], xf[0][1]); SB();
  xr1 += 39936; PINV(xr1);
  SB();
  MV16(av[1], wf[0][2], xf[0][1]); SB();
  wf[1][1] = *(LDS_AS const bf16x8*)(wr0 + 4096);
  SB();
  MV16(aq[1], wf[0][0], xf[0][1]); SB();
  xf[1][1] = *(LDS_AS const bf16x8*)(xr1 + 10240);
  SB();
  MV16(ak[2], wf[0][1], xf[0][2]); SB();
  wf[1][2] = *(LDS_AS const bf16x8*)(wr0 + 5120);
  SB();
  MV16(av[2], wf[0][2], xf[0][2]); SB();
  wf[1][0] = *(LDS_AS const bf16x8*)(wr0 + 3072);
  SB();
  MV16(aq[2], wf[0][0], xf[0][2]); SB();
  xf[1][2] = *(LDS_AS const bf16x8*)(xr1 + 12288);
  SB();
  MV16(ak[3], wf[0][1], xf[0][3]); SB();
  xf[1][3] = *(LDS_AS const bf16x8*)(xr1 + 14336);
  SB();
  MV16(av[3], wf[0][2], xf[0][3]); SB();
  xf[1][4] = *(LDS_AS const bf16x8*)(xr1 + 16384);
  SB();
  MV16(aq[3], wf[0][0], xf[0][3]); SB();
  MV16(ak[4], wf[0][1], xf[0][4]); SB();
  MV16(av[4], wf[0][2], xf[0][4]); SB();
  MV16(aq[4], wf[0][0], xf[0][4]); SB();
  SB();
  MV16(ak[1], wf[1][1], xf[1][1]); SB();
  SB();
  MV16(av[1], wf[1][2], xf[1][1]); SB();
  WAITV(4);
  BAR();
  xr0 += -79872; wr0 += -79872; asm volatile("" : "+v"(xr0), "+v"(wr0));
  STAMP(62);
  WAITV(0);
  fw[5][0] = tr8u(rU0 + 0, 512);
  fw[5][1] = tr8u(rU1 + 0, 512);
  fw[5][2] = tr8u(rU2 + 0, 512);
  fw[5][3] = tr8u(rU3 + 0, 512);
  WAITL(0);
  DMA2(vWa, vWb, sW, ldsU, 0, ""); sW += dW; OPQ(sW);
  DMA2(vWa, vWb, sW, ldsU, 2048, "");
  SB();
  MV16(aq[1], wf[1][0], xf[1][1]); SB();
  wf[0][1] = *(LDS_AS const bf16x8*)(wr0 + 1024);
  SB();
  MV16(ak[2], wf[1][1], xf[1][2]); SB();
  xf[0][1] = *(LDS_AS const bf16x8*)(xr0 + 10240);
  SB();
  MV16(av[2], wf[1][2], xf[1][2]); SB();
  wf[0][2] = *(LDS_AS const bf16x8*)(wr0 + 2048);
  SB();
  MV16(aq[2], wf[1][0], xf[1][2]); SB();
  wf[0][0] = *(LDS_AS const bf16x8*)(wr0 + 0);
  SB();
  MV16(ak[3], wf[1][1], xf[1][3]); SB();
  xf[0][2] = *(LDS_AS const bf16x8*)(xr0 + 12288);
  SB();
  MV16(av[3], wf[1][2], xf[1][3]); SB();
  xf[0][3] = *(LDS_AS const bf16x8*)(xr0 + 14336);
  SB();
  MV16(aq[3], wf[1][0], xf[1][3]); SB();
  xf[0][4] = *(LDS_AS const bf16x8*)(xr0 + 16384);
  SB();
  MV16(ak[4], wf[1][1], xf[1][4]); SB();
  unsigned f0; { const int l_ = lane_now(); vFU = (l_ < 14 ? l_ : 13) * FU; PINV(vFU); }
  LD4(f0, vFU, sFU, 0, "sc1");
  SB();
  MV16(av[4], wf[1][2], xf[1][4]); SB();
  unsigned tl; { const int l_ = lane_now(); tl = lds0 + TB + l_ * 16; const u32x2 t_ = *(LDS_AS const u32x2*)(lds0 + TB + 6144 + cb * 512 + l_ * 8); sk0 = t_[0]; sv = t_[1];
    vS = *(LDS_AS const u32x2*)(lds0 + TB + 4096 + cb * 512 + l_ * 8); PINV(tl); }
  SB();
  MV16(aq[4], wf[1][0], xf[1][4]); SB();
  xr1 += -79872; PINV(xr1);
  MV16(ak[1], wf[0][1], xf[0][1]); SB();
  wf[1][1] = *(LDS_AS const bf16x8*)(wr0 + 4096);
  xf[1][1] = *(LDS_AS const bf16x8*)(xr1 + 10240);
  SB();
  MV16(av[1], wf[0][2], xf[0][1]); SB();
  wf[1][2] = *(LDS_AS const bf16x8*)(wr0 + 5120);
  SB();
  MV16(aq[1], wf[0][0], xf[0][1]); SB();
  wf[1][0] = *(LDS_AS const bf16x8*)(wr0 + 3072);
  SB();
  MV16(ak[2], wf[0][1], xf[0][2]); SB();
  xf[1][2] = *(LDS_AS const bf16x8*)(xr1 + 12288);
  xf[1][3] = *(LDS_AS const bf16x8*)(xr1 + 14336);
  SB();
  MV16(av[2], wf[0][2], xf[0][2]); SB();
  xf[1][4] = *(LDS_AS const bf16x8*)(xr1 + 16384);
  SB();
  MV16(aq[2], wf[0][0], xf[0][2]); SB();
  sk1 = sk0 ^ 32; asm volatile("" : "+v"(sk0), "+v"(sk1), "+v"(sv));
  SB();
  MV16(ak[1], wf[1][1], xf[1][1]); SB();
  SB();
  MV16(av[1], wf[1][2], xf[1][1]); SB();
  SB();
  MV16(aq[1], wf[1][0], xf[1][1]); SB();
  SB();
  MV16(ak[3], wf[0][1], xf[0][3]); SB();
  SB();
  MV16(av[3], wf[0][2], xf[0][3]); SB();
  SB();
  MV16(aq[3], wf[0][0], xf[0][3]); SB();
  SB();
  MV16(ak[2], wf[1][1], xf[1][2]); SB();
  t3 = *(LDS_AS const u32x4*)(tl + 3072); t2 = *(LDS_AS const u32x4*)(tl + 2048);
  SB();
  MV16(av[2], wf[1][2], xf[1][2]); SB();
  SB();
  MV16(aq[2], wf[1][0], xf[1][2]); SB();
  SB();
  MV16(ak[4], wf[0][1], xf[0][4]); SB();
  *(LDS_AS u32x2*)(sk1 + SK + 2560) = (u32x2){pack2(ak[1][0], ak[1][1]), pack2(ak[1][2], ak[1][3])};
  SB();
  MV16(av[4], wf[0][2], xf[0][4]); SB();
  *(LDS_AS u32x2*)(sv + 1280) = (u32x2){pack2(av[1][0], av[1][1]), pack2(av[1][2], av[1][3])};
  SB();
  MV16(aq[4], wf[0][0], xf[0][4]); SB();
  *(LDS_AS u32x2*)(sk0 + SQ + 2560) = (u32x2){pack2(aq[1][0], aq[1][1]), pack2(aq[1][2], aq[1][3])};
  SB();
  MV16(ak[3], wf[1][1], xf[1][3]); SB();
  m1 = *(LDS_AS const u32x4*)(tl + 1024); cm = *(LDS_AS const f32x4*)(tl + cmo);
  SB();
  MV16(av[3], wf[1][2], xf[1][3]); SB();
  SB();
  MV16(aq[3], wf[1][0], xf[1][3]); SB();
  SB();
  MV16(ak[4], wf[1][1], xf[1][4]); SB();
  WAITV1(0, f0);
  if (__ballot(f0 != nonce) != 0ull) poll1<2>(sFU, vFU, nonce);
  STAMP(44);
  { const int l_ = lane_now(); vFU = (14 + (l_ < 28 ? l_ : 27)) * FU; mq_ = l_ >= 48 ? 0u : ~0u; asm volatile("" : "+v"(vFU), "+v"(mq_)); }
  rk = (t3[2] ^ kx) + r0 * 64; rv = t3[3] + r0 * 32; asm volatile("" : "+v"(rk), "+v"(rv));
  SB();
  MV16(av[4], wf[1][2], xf[1][4]); SB();
  *(LDS_AS u32x2*)(sk0 + SK + 3072) = (u32x2){pack2(ak[2][0], ak[2][1]), pack2(ak[2][2], ak[2][3])};
  *(LDS_AS u32x2*)(sv + 1536) = (u32x2){pack2(av[2][0], av[2][1]), pack2(av[2][2], av[2][3])};
  SB();
  MV16(aq[4], wf[1][0], xf[1][4]); SB();
  *(LDS_AS u32x2*)(sk1 + SQ + 3072) = (u32x2){pack2(aq[2][0], aq[2][1]), pack2(aq[2][2], aq[2][3])};
  SB();
  STAMP(36);
  *(LDS_AS u32x2*)(sk1 + SK + 3584) = (u32x2){pack2(ak[3][0], ak[3][1]), pack2(ak[3][2], ak[3][3])};
  *(LDS_AS u32x2*)(sv + 1792) = (u32x2){pack2(av[3][0], av[3][1]), pack2(av[3][2], av[3][3])};
  *(LDS_AS u32x2*)(sk0 + SQ + 3584) = (u32x2){pack2(aq[3][0], aq[3][1]), pack2(aq[3][2], aq[3][3])};
  SB();
  *(LDS_AS u32x2*)(sk0 + SK + 4096) = (u32x2){pack2(ak[4][0], ak[4][1]), pack2(ak[4][2], ak[4][3])};
  *(LDS_AS u32x2*)(sv + 2048) = (u32x2){pack2(av[4][0], av[4][1]), pack2(av[4][2], av[4][3])};
  *(LDS_AS u32x2*)(sk1 + SQ + 4096) = (u32x2){pack2(aq[4][0], aq[4][1]), pack2(aq[4][2], aq[4][3])};
  SB();

  } else {
  f32x4 aq[5], ak[5], av[5];
  bf16x8 xf[2][5], wf[2][5];
  unsigned sk0, sk1, sv;
  unsigned wk, wh, e0, e1, e2, hb[3]; { const int l_ = lane_now(); wk = wr0 + (l_ & 8) * 128; wh = lds0 + l_ * 16; asm volatile("" : "+v"(wk), "+v"(wh)); }
  WAITV(5);
  BAR();
  STAMP(35);
  xf[0][0] = *(LDS_AS const bf16x8*)(xr0 + 0);
  wf[0][2] = *(LDS_AS const bf16x8*)(wh + 2048);
  wf[0][3] = *(LDS_AS const bf16x8*)(wh + 8192);
  wf[0][4] = *(LDS_AS const bf16x8*)(wh + 14336);
  wf[0][1] = *(LDS_AS const bf16x8*)(wk + 1024);
  xf[0][1] = *(LDS_AS const bf16x8*)(xr0 + 10240);
  wf[0][0] = *(LDS_AS const bf16x8*)(wr0 + 0);
  xf[0][2] = *(LDS_AS const bf16x8*)(xr0 + 12288);
  xf[0][3] = *(LDS_AS const bf16x8*)(xr0 + 14336);
  xf[0][4] = *(LDS_AS const bf16x8*)(xr0 + 16384);
  SB();
  MV16Z(av[0], wf[0][2], xf[0][0]); SB();
  xf[1][0] = *(LDS_AS const bf16x8*)(xr1 + 0);
  wf[1][2] = *(LDS_AS const bf16x8*)(wh + 5120);
  SB();
  MV16Z(av[1], wf[0][3], xf[0][0]); SB();
  wf[1][3] = *(LDS_AS const bf16x8*)(wh + 11264);
  SB();
  MV16Z(av[2], wf[0][4], xf[0][0]); SB();
  wf[1][4] = *(LDS_AS const bf16x8*)(wh + 17408);
  wf[1][1] = *(LDS_AS const bf16x8*)(wk + 1536);
  SB();
  MV16Z(ak[1], wf[0][1], xf[0][1]); SB();
  xf[1][1] = *(LDS_AS const bf16x8*)(xr1 + 10240);
  SB();
  MV16Z(aq[1], wf[0][0], xf[0][1]); SB();
  wf[1][0] = *(LDS_AS const bf16x8*)(wr0 + 512);
  SB();
  MV16Z(ak[2], wf[0][1], xf[0][2]); SB();
  xf[1][2] = *(LDS_AS const bf16x8*)(xr1 + 12288);
  xf[1][3] = *(LDS_AS const bf16x8*)(xr1 + 14336);
  SB();
  MV16Z(aq[2], wf[0][0], xf[0][2]); SB();
  xf[1][4] = *(LDS_AS const bf16x8*)(xr1 + 16384);
  SB();
  MV16Z(ak[3], wf[0][1], xf[0][3]); SB();
  DMA1(vD[0], sDq, ldsD, 79872, "sc1");
  SB();
  MV16Z(aq[3], wf[0][0], xf[0][3]); SB();
  DMA1(vD[1], sDq, ldsD, 88064, "sc1"); sDq += WQ_S; OPQ(sDq);
  DMA1(vD[2], sDm, ldsD, 96256, "sc1"); sDm += dm; OPQ(sDm);
  SB();
  MV16Z(ak[4], wf[0][1], xf[0][4]); SB();
  DMA1(vD[3], sDx, ldsD, 104448, "sc1");
  SB();
  MV16Z(aq[4], wf[0][0], xf[0][4]); SB();
  DMA1(vD[4], sDx, ldsE, 79872, "sc1"); sDx += 128; OPQ(sDx);
  SB();
  SB();
  MV16(av[0], wf[1][2], xf[1][0]); SB();
  WAITV(5);
  BAR();
  xr0 += 39936; wr0 += 39936; asm volatile("" : "+v"(xr0), "+v"(wr0));
  wk += 39936; wh += 39936; asm volatile("" : "+v"(wk), "+v"(wh));
  DMA1(vD[0], sDq, ldsD, 0, "sc1");
  DMA1(vD[1], sDq, ldsD, 8192, "sc1"); sDq += WQ_S; OPQ(sDq);
  DMA1(vD[2], sDm, ldsD, 16384, "sc1"); sDm += dm; OPQ(sDm);
  DMA1(vD[3], sDx, ldsD, 24576, "sc1");
  DMA1(vD[4], sDx, ldsE, 0, "sc1"); sDx += 128; OPQ(sDx);
  SB();
  MV16(av[1], wf[1][3], xf[1][0]); SB();
  xf[0][0] = *(LDS_AS const bf16x8*)(xr0 + 0);
  SB();
  MV16(av[2], wf[1][4], xf[1][0]); SB();
  wf[0][2] = *(LDS_AS const bf16x8*)(wh + 2048);
  SB();
  MV16(ak[1], wf[1][1], xf[1][1]); SB();
  wf[0][3] = *(LDS_AS const bf16x8*)(wh + 8192);
  SB();
  MV16(aq[1], wf[1][0], xf[1][1]); SB();
  wf[0][4] = *(LDS_AS const bf16x8*)(wh + 14336);
  SB();
  MV16(ak[2], wf[1][1], xf[1][2]); SB();
  wf[0][1] = *(LDS_AS const bf16x8*)(wk + 1024);
  SB();
  MV16(aq[2], wf[1][0], xf[1][2]); SB();
  xf[0][1] = *(LDS_AS const bf16x8*)(xr0 + 10240);
  SB();
  MV16(ak[3], wf[1][1], xf[1][3]); SB();
  wf[0][0] = *(LDS_AS const bf16x8*)(wr0 + 0);
  SB();
  MV16(aq[3], wf[1][0], xf[1][3]); SB();
  xf[0][2] = *(LDS_AS const bf16x8*)(xr0 + 12288);
  SB();
  MV16(ak[4], wf[1][1], xf[1][4]); SB();
  xf[0][3] = *(LDS_AS const bf16x8*)(xr0 + 14336);
  SB();
  MV16(aq[4], wf[1][0], xf[1][4]); SB();
  xf[0][4] = *(LDS_AS const bf16x8*)(xr0 + 16384);
  SB();
  SB();
  MV16(av[0], wf[0][2], xf[0][0]); SB();
  xr1 += 39936; PINV(xr1);
  SB();
  MV16(av[1], wf[0][3], xf[0][0]); SB();
  xf[1][0] = *(LDS_AS const bf16x8*)(xr1 + 0);
  SB();
  MV16(av[2], wf[0][4], xf[0][0]); SB();
  wf[1][2] = *(LDS_AS const bf16x8*)(wh + 5120);
  SB();
  MV16(ak[1], wf[0][1], xf[0][1]); SB();
  wf[1][3] = *(LDS_AS const bf16x8*)(wh + 11264);
  SB();
  MV16(aq[1], wf[0][0], xf[0][1]); SB();
  wf[1][4] = *(LDS_AS const bf16x8*)(wh + 17408);
  SB();
  MV16(ak[2], wf[0][1], xf[0][2]); SB();
  wf[1][1] = *(LDS_AS const bf16x8*)(wk + 1536);
  SB();
  MV16(aq[2], wf[0][0], xf[0][2]); SB();
  xf[1][1] = *(LDS_AS const bf16x8*)(xr1 + 10240);
  SB();
  MV16(ak[3], wf[0][1], xf[0][3]); SB();
  wf[1][0] = *(LDS_AS const bf16x8*)(wr0 + 512);
  SB();
  MV16(aq[3], wf[0][0], xf[0][3]); SB();
  xf[1][2] = *(LDS_AS const bf16x8*)(xr1 + 12288);
  SB();
  MV16(ak[4], wf[0][1], xf[0][4]); SB();
  xf[1][3] = *(LDS_AS const bf16x8*)(xr1 + 14336);
  SB();
  MV16(aq[4], wf[0][0], xf[0][4]); SB();
  xf[1][4] = *(LDS_AS const bf16x8*)(xr1 + 16384);
  SB();
  SB();
  MV16(av[0], wf[1][2], xf[1][0]); SB();
  WAITV(5);
  BAR();
  xr0 += 39936; wr0 += 39936; asm volatile("" : "+v"(xr0), "+v"(wr0));
  wk += 39936; wh += 39936; asm volatile("" : "+v"(wk), "+v"(wh));
  STAMP(60);
  DMA1(vD[0], sDq, ldsD, 39936, "sc1");
  DMA1(vD[1], sDq, ldsD, 48128, "sc1"); sDq += WQ_S; OPQ(sDq);
  DMA1(vD[2], sDm, ldsD, 56320, "sc1"); sDm += dm; OPQ(sDm);
  DMA1(vD[3], sDx, ldsD, 64512, "sc1");
  DMA1(vD[4], sDx, ldsE, 39936, "sc1"); sDx += 128; OPQ(sDx);
  SB();
  MV16(av[1], wf[1][3], xf[1][0]); SB();
  xf[0][0] = *(LDS_AS const bf16x8*)(xr0 + 0);
  SB();
  MV16(av[2], wf[1][4], xf[1][0]); SB();
  wf[0][2] = *(LDS_AS const bf16x8*)(wh + 2048);
  SB();
  MV16(ak[1], wf[1][1], xf[1][1]); SB();
  wf[0][3] = *(LDS_AS const bf16x8*)(wh + 8192);
  SB();
  MV16(aq[1], wf[1][0], xf[1][1]); SB();
  wf[0][4] = *(LDS_AS const bf16x8*)(wh + 14336);
  SB();
  MV16(ak[2], wf[1][1], xf[1][2]); SB();
  wf[0][1] = *(LDS_AS const bf16x8*)(wk + 1024);
  SB();
  MV16(aq[2], wf[1][0], xf[1][2]); SB();
  xf[0][1] = *(LDS_AS const bf16x8*)(xr0 + 10240);
  SB();
  MV16(ak[3], wf[1][1], xf[1][3]); SB();
  wf[0][0] = *(LDS_AS const bf16x8*)(wr0 + 0);
  SB();
  MV16(aq[3], wf[1][0], xf[1][3]); SB();
  xf[0][2] = *(LDS_AS const bf16x8*)(xr0 + 12288);
  SB();
  MV16(ak[4], wf[1][1], xf[1][4]); SB();
  xf[0][3] = *(LDS_AS const bf16x8*)(xr0 + 14336);
  SB();
  MV16(aq[4], wf[1][0], xf[1][4]); SB();
  xf[0][4] = *(LDS_AS const bf16x8*)(xr0 + 16384);
  SB();
  SB();
  MV16(av[0], wf[0][2], xf[0][0]); SB();
  xr1 += 39936; PINV(xr1);
  SB();
  MV16(av[1], wf[0][3], xf[0][0]); SB();
  xf[1][0] = *(LDS_AS const bf16x8*)(xr1 + 0);
  SB();
  MV16(av[2], wf[0][4], xf[0][0]); SB();
  wf[1][2] = *(LDS_AS const bf16x8*)(wh + 5120);
  SB();
  MV16(ak[1], wf[0][1], xf[0][1]); SB();
  wf[1][3] = *(LDS_AS const bf16x8*)(wh + 11264);
  SB();
  MV16(aq[1], wf[0][0], xf[0][1]); SB();
  wf[1][4] = *(LDS_AS const bf16x8*)(wh + 17408);
  SB();
  MV16(ak[2], wf[0][1], xf[0][2]); SB();
  wf[1][1] = *(LDS_AS const bf16x8*)(wk + 1536);
  SB();
  MV16(aq[2], wf[0][0], xf[0][2]); SB();
  xf[1][1] = *(LDS_AS const bf16x8*)(xr1 + 10240);
  SB();
  MV16(ak[3], wf[0][1], xf[0][3]); SB();
  wf[1][0] = *(LDS_AS const bf16x8*)(wr0 + 512);
  SB();
  MV16(aq[3], wf[0][0], xf[0][3]); SB();
  xf[1][2] = *(LDS_AS const bf16x8*)(xr1 + 12288);
  SB();
  MV16(ak[4], wf[0][1], xf[0][4]); SB();
  xf[1][3] = *(LDS_AS const bf16x8*)(xr1 + 14336);
  SB();
  MV16(aq[4], wf[0][0], xf[0][4]); SB();
  xf[1][4] = *(LDS_AS const bf16x8*)(xr1 + 16384);
  SB();
  SB();
  MV16(av[0], wf[1][2], xf[1][0]); SB();
  WAITV(5);
  BAR();
  xr0 += -79872; wr0 += -79872; asm volatile("" : "+v"(xr0), "+v"(wr0));
  wk += -79872; wh += -79872; asm volatile("" : "+v"(wk), "+v"(wh));
  DMA1(vD[0], sDq, ldsD, 79872, "sc1");
  DMA1(vD[1], sDq, ldsD, 88064, "sc1"); sDq += WQ_S; OPQ(sDq);
  DMA1(vD[2], sDm, ldsD, 96256, "sc1"); sDm += dm; OPQ(sDm);
  DMA1(vD[3], sDx, ldsD, 104448, "sc1");
  DMA1(vD[4], sDx, ldsE, 79872, "sc1"); sDx += 128; OPQ(sDx);
  SB();
  MV16(av[1], wf[1][3], xf[1][0]); SB();
  xf[0][0] = *(LDS_AS const bf16x8*)(xr0 + 0);
  SB();
  MV16(av[2], wf[1][4], xf[1][0]); SB();
  wf[0][2] = *(LDS_AS const bf16x8*)(wh + 2048);
  SB();
  MV16(ak[1], wf[1][1], xf[1][1]); SB();
  wf[0][3] = *(LDS_AS const bf16x8*)(wh + 8192);
  SB();
  MV16(aq[1], wf[1][0], xf[1][1]); SB();
  wf[0][4] = *(LDS_AS const bf16x8*)(wh + 14336);
  SB();
  MV16(ak[2], wf[1][1], xf[1][2]); SB();
  wf[0][1] = *(LDS_AS const bf16x8*)(wk + 1024);
  SB();
  MV16(aq[2], wf[1][0], xf[1][2]); SB();
  xf[0][1] = *(LDS_AS const bf16x8*)(xr0 + 10240);
  SB();
  MV16(ak[3], wf[1][1], xf[1][3]); SB();
  wf[0][0] = *(LDS_AS const bf16x8*)(wr0 + 0);
  SB();
  MV16(aq[3], wf[1][0], xf[1][3]); SB();
  xf[0][2] = *(LDS_AS const bf16x8*)(xr0 + 12288);
  SB();
  MV16(ak[4], wf[1][1], xf[1][4]); SB();
  xf[0][3] = *(LDS_AS const bf16x8*)(xr0 + 14336);
  SB();
  MV16(aq[4], wf[1][0], xf[1][4]); SB();
  xf[0][4] = *(LDS_AS const bf16x8*)(xr0 + 16384);
  SB();
  SB();
  MV16(av[0], wf[0][2], xf[0][0]); SB();
  xr1 += -79872; PINV(xr1);
  SB();
  MV16(av[1], wf[0][3], xf[0][0]); SB();
  xf[1][0] = *(LDS_AS const bf16x8*)(xr1 + 0);
  SB();
  MV16(av[2], wf[0][4], xf[0][0]); SB();
  wf[1][2] = *(LDS_AS const bf16x8*)(wh + 5120);
  SB();
  MV16(ak[1], wf[0][1], xf[0][1]); SB();
  wf[1][3] = *(LDS_AS const bf16x8*)(wh + 11264);
  SB();
  MV16(aq[1], wf[0][0], xf[0][1]); SB();
  wf[1][4] = *(LDS_AS const bf16x8*)(wh + 17408);
  SB();
  MV16(ak[2], wf[0][1], xf[0][2]); SB();
  wf[1][1] = *(LDS_AS const bf16x8*)(wk + 1536);
  SB();
  MV16(aq[2], wf[0][0], xf[0][2]); SB();
  xf[1][1] = *(LDS_AS const bf16x8*)(xr1 + 10240);
  SB();
  MV16(ak[3], wf[0][1], xf[0][3]); SB();
  wf[1][0] = *(LDS_AS const bf16x8*)(wr0 + 512);
  SB();
  MV16(aq[3], wf[0][0], xf[0][3]); SB();
  xf[1][2] = *(LDS_AS const bf16x8*)(xr1 + 12288);
  SB();
  MV16(ak[4], wf[0][1], xf[0][4]); SB();
  xf[1][3] = *(LDS_AS const bf16x8*)(xr1 + 14336);
  SB();
  MV16(aq[4], wf[0][0], xf[0][4]); SB();
  xf[1][4] = *(LDS_AS const bf16x8*)(xr1 + 16384);
  SB();
  SB();
  MV16(av[0], wf[1][2], xf[1][0]); SB();
  WAITV(5);
  BAR();
  xr0 += 39936; wr0 += 39936; asm volatile("" : "+v"(xr0), "+v"(wr0));
  wk += 39936; wh += 39936; asm volatile("" : "+v"(wk), "+v"(wh));
  STAMP(61);
  DMA1(vD[0], sDq, ldsD, 0, "sc1");
  DMA1(vD[1], sDq, ldsD, 8192, "sc1"); sDq += WQ_S; OPQ(sDq);
  DMA1(vD[2], sDm, ldsD, 16384, "sc1"); sDm += dm; OPQ(sDm);
  DMA1(vD[3], sDx, ldsD, 24576, "sc1");
  DMA1(vD[4], sDx, ldsE, 0, "sc1"); sDx += 128; OPQ(sDx);
  SB();
  MV16(av[1], wf[1][3], xf[1][0]); SB();
  xf[0][0] = *(LDS_AS const bf16x8*)(xr0 + 0);
  SB();
  MV16(av[2], wf[1][4], xf[1][0]); SB();
  wf[0][2] = *(LDS_AS const bf16x8*)(wh + 2048);
  SB();
  MV16(ak[1], wf[1][1], xf[1][1]); SB();
  wf[0][3] = *(LDS_AS const bf16x8*)(wh + 8192);
  SB();
  MV16(aq[1], wf[1][0], xf[1][1]); SB();
  wf[0][4] = *(LDS_AS const bf16x8*)(wh + 14336);
  SB();
  MV16(ak[2], wf[1][1], xf[1][2]); SB();
  wf[0][1] = *(LDS_AS const bf16x8*)(wk + 1024);
  SB();
  MV16(aq[2], wf[1][0], xf[1][2]); SB();
  xf[0][1] = *(LDS_AS const bf16x8*)(xr0 + 10240);
  SB();
  MV16(ak[3], wf[1][1], xf[1][3]); SB();
  wf[0][0] = *(LDS_AS const bf16x8*)(wr0 + 0);
  SB();
  MV16(aq[3], wf[1][0], xf[1][3]); SB();
  xf[0][2] = *(LDS_AS const bf16x8*)(xr0 + 12288);
  SB();
  MV16(ak[4], wf[1][1], xf[1][4]); SB();
  xf[0][3] = *(LDS_AS const bf16x8*)(xr0 + 14336);
  SB();
  MV16(aq[4], wf[1][0], xf[1][4]); SB();
  xf[0][4] = *(LDS_AS const bf16x8*)(xr0 + 16384);
  SB();
  SB();
  MV16(av[0], wf[0][2], xf[0][0]); SB();
  xr1 += 39936; PINV(xr1);
  SB();
  MV16(av[1], wf[0][3], xf[0][0]); SB();
  xf[1][0] = *(LDS_AS const bf16x8*)(xr1 + 0);
  SB();
  MV16(av[2], wf[0][4], xf[0][0]); SB();
  wf[1][2] = *(LDS_AS const bf16x8*)(wh + 5120);
  SB();
  MV16(ak[1], wf[0][1], xf[0][1]); SB();
  wf[1][3] = *(LDS_AS const bf16x8*)(wh + 11264);
  SB();
  MV16(aq[1], wf[0][0], xf[0][1]); SB();
  wf[1][4] = *(LDS_AS const bf16x8*)(wh + 17408);
  SB();
  MV16(ak[2], wf[0][1], xf[0][2]); SB();
  wf[1][1] = *(LDS_AS const bf16x8*)(wk + 1536);
  SB();
  MV16(aq[2], wf[0][0], xf[0][2]); SB();
  xf[1][1] = *(LDS_AS const bf16x8*)(xr1 + 10240);
  SB();
  MV16(ak[3], wf[0][1], xf[0][3]); SB();
  wf[1][0] = *(LDS_AS const bf16x8*)(wr0 + 512);
  SB();
  MV16(aq[3], wf[0][0], xf[0][3]); SB();
  xf[1][2] = *(LDS_AS const bf16x8*)(xr1 + 12288);
  SB();
  MV16(ak[4], wf[0][1], xf[0][4]); SB();
  xf[1][3] = *(LDS_AS const bf16x8*)(xr1 + 14336);
  SB();
  MV16(aq[4], wf[0][0], xf[0][4]); SB();
  xf[1][4] = *(LDS_AS const bf16x8*)(xr1 + 16384);
  SB();
  SB();
  MV16(av[0], wf[1][2], xf[1][0]); SB();
  WAITV(5);
  BAR();
  xr0 += 39936; wr0 += 39936; asm volatile("" : "+v"(xr0), "+v"(wr0));
  wk += 39936; wh += 39936; asm volatile("" : "+v"(wk), "+v"(wh));
  DMA2(vWa, vWb, sW, ldsU, 0, ""); sW += dW; OPQ(sW);
  DMA2(vWa, vWb, sW, ldsU, 2048, ""); sW = wrap == 5 ? bW : sW + dW; OPQ(sW);
  SB();
  MV16(av[1], wf[1][3], xf[1][0]); SB();
  xf[0][0] = *(LDS_AS const bf16x8*)(xr0 + 0);
  SB();
  MV16(av[2], wf[1][4], xf[1][0]); SB();
  wf[0][2] = *(LDS_AS const bf16x8*)(wh + 2048);
  SB();
  MV16(ak[1], wf[1][1], xf[1][1]); SB();
  wf[0][3] = *(LDS_AS const bf16x8*)(wh + 8192);
  SB();
  MV16(aq[1], wf[1][0], xf[1][1]); SB();
  wf[0][4] = *(LDS_AS const bf16x8*)(wh + 14336);
  SB();
  MV16(ak[2], wf[1][1], xf[1][2]); SB();
  wf[0][1] = *(LDS_AS const bf16x8*)(wk + 1024);
  SB();
  MV16(aq[2], wf[1][0], xf[1][2]); SB();
  xf[0][1] = *(LDS_AS const bf16x8*)(xr0 + 10240);
  SB();
  MV16(ak[3], wf[1][1], xf[1][3]); SB();
  wf[0][0] = *(LDS_AS const bf16x8*)(wr0 + 0);
  SB();
  MV16(aq[3], wf[1][0], xf[1][3]); SB();
  xf[0][2] = *(LDS_AS const bf16x8*)(xr0 + 12288);
  SB();
  MV16(ak[4], wf[1][1], xf[1][4]); SB();
  xf[0][3] = *(LDS_AS const bf16x8*)(xr0 + 14336);
  SB();
  MV16(aq[4], wf[1][0], xf[1][4]); SB();
  xf[0][4] = *(LDS_AS const bf16x8*)(xr0 + 16384);
  SB();
  SB();
  MV16(av[0], wf[0][2], xf[0][0]); SB();
  xr1 += 39936; PINV(xr1);
  SB();
  MV16(av[1], wf[0][3], xf[0][0]); SB();
  xf[1][0] = *(LDS_AS const bf16x8*)(xr1 + 0);
  SB();
  MV16(av[2], wf[0][4], xf[0][0]); SB();
  wf[1][2] = *(LDS_AS const bf16x8*)(wh + 5120);
  SB();
  MV16(ak[1], wf[0][1], xf[0][1]); SB();
  wf[1][3] = *(LDS_AS const bf16x8*)(wh + 11264);
  SB();
  MV16(aq[1], wf[0][0], xf[0][1]); SB();
  wf[1][4] = *(LDS_AS const bf16x8*)(wh + 17408);
  SB();
  MV16(ak[2], wf[0][1], xf[0][2]); SB();
  wf[1][1] = *(LDS_AS const bf16x8*)(wk + 1536);
  SB();
  MV16(aq[2], wf[0][0], xf[0][2]); SB();
  xf[1][1] = *(LDS_AS const bf16x8*)(xr1 + 10240);
  SB();
  MV16(ak[3], wf[0][1], xf[0][3]); SB();
  wf[1][0] = *(LDS_AS const bf16x8*)(wr0 + 512);
  SB();
  MV16(aq[3], wf[0][0], xf[0][3]); SB();
  xf[1][2] = *(LDS_AS const bf16x8*)(xr1 + 12288);
  SB();
  MV16(ak[4], wf[0][1], xf[0][4]); SB();
  xf[1][3] = *(LDS_AS const bf16x8*)(xr1 + 14336);
  SB();
  MV16(aq[4], wf[0][0], xf[0][4]); SB();
  xf[1][4] = *(LDS_AS const bf16x8*)(xr1 + 16384);
  SB();
  SB();
  MV16(av[0], wf[1][2], xf[1][0]); SB();
  WAITV(4);
  BAR();
  xr0 += -79872; wr0 += -79872; asm volatile("" : "+v"(xr0), "+v"(wr0));
  wk += -79872; wh += -79872; asm volatile("" : "+v"(wk), "+v"(wh));
  STAMP(62);
  WAITV(0);
  fw[5][0] = tr8u(rU0 + 0, 512);
  fw[5][1] = tr8u(rU1 + 0, 512);
  fw[5][2] = tr8u(rU2 + 0, 512);
  fw[5][3] = tr8u(rU3 + 0, 512);
  WAITL(0);
  DMA2(vWa, vWb, sW, ldsU, 0, ""); sW += dW; OPQ(sW);
  DMA2(vWa, vWb, sW, ldsU, 2048, "");
  SB();
  MV16(av[1], wf[1][3], xf[1][0]); SB();
  xf[0][0] = *(LDS_AS const bf16x8*)(xr0 + 0);
  SB();
  MV16(av[2], wf[1][4], xf[1][0]); SB();
  wf[0][2] = *(LDS_AS const bf16x8*)(wh + 2048);
  wf[0][3] = *(LDS_AS const bf16x8*)(wh + 8192);
  SB();
  MV16(ak[1], wf[1][1], xf[1][1]); SB();
  wf[0][4] = *(LDS_AS const bf16x8*)(wh + 14336);
  SB();
  MV16(aq[1], wf[1][0], xf[1][1]); SB();
  wf[0][1] = *(LDS_AS const bf16x8*)(wk + 1024);
  SB();
  MV16(ak[2], wf[1][1], xf[1][2]); SB();
  xf[0][1] = *(LDS_AS const bf16x8*)(xr0 + 10240);
  wf[0][0] = *(LDS_AS const bf16x8*)(wr0 + 0);
  SB();
  MV16(aq[2], wf[1][0], xf[1][2]); SB();
  xf[0][2] = *(LDS_AS const bf16x8*)(xr0 + 12288);
  SB();
  MV16(ak[3], wf[1][1], xf[1][3]); SB();
  xf[0][3] = *(LDS_AS const bf16x8*)(xr0 + 14336);
  SB();
  MV16(aq[3], wf[1][0], xf[1][3]); SB();
  xf[0][4] = *(LDS_AS const bf16x8*)(xr0 + 16384);
  unsigned f0; { const int l_ = lane_now(); vFU = (l_ < 14 ? l_ : 13) * FU; PINV(vFU); }
  LD4(f0, vFU, sFU, 0, "sc1");
  SB();
  MV16(ak[4], wf[1][1], xf[1][4]); SB();
  unsigned tl; { const int l_ = lane_now(); tl = lds0 + TB + l_ * 16; const u32x2 t_ = *(LDS_AS const u32x2*)(lds0 + TB + 6144 + cb * 512 + l_ * 8); sk0 = t_[0]; sv = t_[1];
    vS = *(LDS_AS const u32x2*)(lds0 + TB + 4096 + cb * 512 + l_ * 8); PINV(tl); }
  SB();
  MV16(aq[4], wf[1][0], xf[1][4]); SB();
  { const int l_ = lane_now(); const unsigned a_ = lds0 + TB + 6144 + l_ * 8; sv = *(LDS_AS const unsigned*)(lds0 + TB + 7684 + (l_ & 31) * 8);
    hb[0] = *(LDS_AS const unsigned*)(a_ + 4); hb[1] = *(LDS_AS const unsigned*)(a_ + 516); hb[2] = *(LDS_AS const unsigned*)(a_ + 1028); }
  SB();
  xr1 += -79872; PINV(xr1);
  MV16(av[0], wf[0][2], xf[0][0]); SB();
  xf[1][0] = *(LDS_AS const bf16x8*)(xr1 + 0);
  wf[1][2] = *(LDS_AS const bf16x8*)(wh + 5120);
  wf[1][3] = *(LDS_AS const bf16x8*)(wh + 11264);
  SB();
  MV16(av[1], wf[0][3], xf[0][0]); SB();
  wf[1][4] = *(LDS_AS const bf16x8*)(wh + 17408);
  wf[1][1] = *(LDS_AS const bf16x8*)(wk + 1536);
  SB();
  MV16(av[2], wf[0][4], xf[0][0]); SB();
  xf[1][1] = *(LDS_AS const bf16x8*)(xr1 + 10240);
  wf[1][0] = *(LDS_AS const bf16x8*)(wr0 + 512);
  SB();
  MV16(ak[1], wf[0][1], xf[0][1]); SB();
  xf[1][2] = *(LDS_AS const bf16x8*)(xr1 + 12288);
  xf[1][3] = *(LDS_AS const bf16x8*)(xr1 + 14336);
  SB();
  MV16(aq[1], wf[0][0], xf[0][1]); SB();
  xf[1][4] = *(LDS_AS const bf16x8*)(xr1 + 16384);
  sk1 = sk0 ^ 32; asm volatile("" : "+v"(sk0), "+v"(sk1), "+v"(sv));
  const bool lo_ = lane_now() < 32;
  e0 = lo_ ? sk0 + SK : sv; e1 = lo_ ? sk1 + SK + 512 : sv + 256; e2 = lo_ ? 1024 : 512; asm volatile("" : "+v"(e0), "+v"(e1), "+v"(e2));
  SB();
  MV16(av[0], wf[1][2], xf[1][0]); SB();
  SB();
  MV16(av[1], wf[1][3], xf[1][0]); SB();
  SB();
  MV16(av[2], wf[1][4], xf[1][0]); SB();
  SB();
  MV16(ak[2], wf[0][1], xf[0][2]); SB();
  SB();
  MV16(aq[2], wf[0][0], xf[0][2]); SB();
  SB();
  MV16(ak[1], wf[1][1], xf[1][1]); SB();
  SB();
  MV16(aq[1], wf[1][0], xf[1][1]); SB();
  SB();
  MV16(ak[3], wf[0][1], xf[0][3]); SB();
  SB();
  MV16(aq[3], wf[0][0], xf[0][3]); SB();
  SB();
  MV16(ak[2], wf[1][1], xf[1][2]); SB();
  t3 = *(LDS_AS const u32x4*)(tl + 3072); t2 = *(LDS_AS const u32x4*)(tl + 2048);
  SB();
  MV16(aq[2], wf[1][0], xf[1][2]); SB();
  *(LDS_AS u32x2*)(hb[0]) = (u32x2){pack2(av[0][0], av[0][1]), pack2(av[0][2], av[0][3])};
  SB();
  MV16(ak[4], wf[0][1], xf[0][4]); SB();
  *(LDS_AS u32x2*)(hb[1]) = (u32x2){pack2(av[1][0], av[1][1]), pack2(av[1][2], av[1][3])};
  SB();
  MV16(aq[4], wf[0][0], xf[0][4]); SB();
  *(LDS_AS u32x2*)(hb[2]) = (u32x2){pack2(av[2][0], av[2][1]), pack2(av[2][2], av[2][3])};
  SB();
  MV16(ak[3], wf[1][1], xf[1][3]); SB();
  WAITV1(0, f0);
  if (__ballot(f0 != nonce) != 0ull) poll1<2>(sFU, vFU, nonce);
  STAMP(44);
  { const int l_ = lane_now(); vFU = (14 + (l_ < 28 ? l_ : 27)) * FU; mq_ = l_ >= 48 ? 0u : ~0u; asm volatile("" : "+v"(vFU), "+v"(mq_)); }
  SB();
  MV16(aq[3], wf[1][0], xf[1][3]); SB();
  m1 = *(LDS_AS const u32x4*)(tl + 1024); cm = *(LDS_AS const f32x4*)(tl + cmo);
  SB();
  MV16(ak[4], wf[1][1], xf[1][4]); SB();
  rk = (t3[2] ^ kx) + r0 * 64; rv = t3[3] + r0 * 32; asm volatile("" : "+v"(rk), "+v"(rv));
  *(LDS_AS u32x2*)(e1 + 2 * e2) = (u32x2){pack2(ak[1][0], ak[1][1]), pack2(ak[1][2], ak[1][3])};
  SB();
  MV16(aq[4], wf[1][0], xf[1][4]); SB();
  *(LDS_AS u32x2*)(sk0 + SQ + 2560) = (u32x2){pack2(aq[1][0], aq[1][1]), pack2(aq[1][2], aq[1][3])};
  SB();
  STAMP(36);
  *(LDS_AS u32x2*)(e0 + 3 * e2) = (u32x2){pack2(ak[2][0], ak[2][1]), pack2(ak[2][2], ak[2][3])};
  *(LDS_AS u32x2*)(sk1 + SQ + 3072) = (u32x2){pack2(aq[2][0], aq[2][1]), pack2(aq[2][2], aq[2][3])};
  SB();
  *(LDS_AS u32x2*)(e1 + 3 * e2) = (u32x2){pack2(ak[3][0], ak[3][1]), pack2(ak[3][2], ak[3][3])};
  *(LDS_AS u32x2*)(sk0 + SQ + 3584) = (u32x2){pack2(aq[3][0], aq[3][1]), pack2(aq[3][2], aq[3][3])};
  SB();
  *(LDS_AS u32x2*)(e0 + 4 * e2) = (u32x2){pack2(ak[4][0], ak[4][1]), pack2(ak[4][2], ak[4][3])};
  *(LDS_AS u32x2*)(sk1 + SQ + 4096) = (u32x2){pack2(aq[4][0], aq[4][1]), pack2(aq[4][2], aq[4][3])};
  SB();

  }
  bf16x8 p[8]; unsigned fu;
  const unsigned vP0 = t2[0], vP1 = t2[1], vP2 = t2[2], vP3 = t2[3], vS0 = vS[0], vS1 = vS[1], mp[2] = {t3[0], t3[1]};
  PRIO(3);
  BAR();
  STAMP(37);
  u32x4 qf[2]; bf16x8 kf[2][2], vf[2][2];
  qf[0] = *(LDS_AS const u32x4*)(rk + SQ + 512); kf[0][0] = *(LDS_AS const bf16x8*)(rk + SK); kf[0][1] = *(LDS_AS const bf16x8*)(rk + SK + 1024);
  qf[1] = *(LDS_AS const u32x4*)(rk + SQ + 512 + SKH); kf[1][0] = *(LDS_AS const bf16x8*)(rk + SK + SKH); kf[1][1] = *(LDS_AS const bf16x8*)(rk + SK + SKH + 1024);
  SB();
  vf[0][0] = tr8u(rv, 512); vf[1][0] = tr8u(rv + 2 * SVB, 512); vf[0][1] = tr8u(rv + SVB, 512); vf[1][1] = tr8u(rv + 3 * SVB, 512);
  u32x4 t7; float bias[4];
  { const int l_ = lane_now(); t7 = *(LDS_AS const u32x4*)(lds0 + TB + 9216 + l_ * 16); bias[0] = bias[1] = bias[2] = bias[3] = *(LDS_AS const float*)(bio + (l_ >> 3) * 4); }
  SB();
  SB();
  LD16(p[0], vP0, sP, 0, "sc1");
  SB();
  f32x4 sc[2][2];
  const f32x4 z4 = {0.f, 0.f, 0.f, 0.f};
  qf[0][2] &= mq_; qf[0][3] &= mq_; qf[1][2] &= mq_; qf[1][3] &= mq_;
  sc[0][0] = MFMA16(kf[0][0], __builtin_bit_cast(bf16x8, qf[0]), z4); sc[0][1] = MFMA16(kf[0][1], __builtin_bit_cast(bf16x8, qf[0]), z4);
  sc[1][0] = MFMA16(kf[1][0], __builtin_bit_cast(bf16x8, qf[1]), z4); sc[1][1] = MFMA16(kf[1][1], __builtin_bit_cast(bf16x8, qf[1]), z4);
  SB();
  LD16(p[1], vP1, sP, 0, "sc1");
  LD16(p[2], vP2, sP, 0, "sc1");
  SB();
  f32x2_t lg[2][2];
  const f32x2_t scale = {0.18898223650461362f, 0.18898223650461362f};
#pragma unroll
  for (int h = 0; h < 2; ++h) {
    lg[h][0] = (f32x2_t){SEL(h, 0), SEL(h, 1)}; lg[h][1] = (f32x2_t){SEL(h, 2), SEL(h, 3)};
    lg[h][0] = rbf2(__builtin_elementwise_fma(rbf2(lg[h][0]), scale, (f32x2_t){cm[0], cm[1]}));
    lg[h][1] = rbf2(__builtin_elementwise_fma(rbf2(lg[h][1]), scale, (f32x2_t){cm[2], cm[3]}));
  } asm volatile("" : "+v"(lg[0][0]), "+v"(lg[0][1]), "+v"(lg[1][0]), "+v"(lg[1][1]));
  SB();
  LD16(p[3], vP3, sP, 0, "sc1"); sP += 5120; OPQ(sP);
  LD16(p[4], vP0, sP, 0, "sc1");
  SB();
  f32x2_t mx = xbcast2(xmax2(fmx(fmx(lg[0][0][0], lg[0][0][1]), fmx(lg[0][1][0], lg[0][1][1])), fmx(fmx(lg[1][0][0], lg[1][0][1]), fmx(lg[1][1][0], lg[1][1][1])))); PINV(mx);
  SB();
  LD16(p[5], vP1, sP, 0, "sc1");
  LD16(p[6], vP2, sP, 0, "sc1");
  LD4(fu, vFU, sFU, 0, "sc1");
  SB();
  #pragma unroll
  for (int h = 0; h < 2; ++h)
#pragma unroll
    for (int j = 0; j < 2; ++j) {
      const f32x2_t z = (lg[h][j] - (f32x2_t){mx[h], mx[h]}) * (f32x2_t){1.442695041f, 1.442695041f};
      lg[h][j] = (f32x2_t){__builtin_amdgcn_exp2f(z[0]), __builtin_amdgcn_exp2f(z[1])};
    } asm volatile("" : "+v"(lg[0][0]), "+v"(lg[0][1]), "+v"(lg[1][0]), "+v"(lg[1][1]));
  SB();
  SB();
  const f32x2_t e0 = lg[0][0] + lg[0][1], e1 = lg[1][0] + lg[1][1];
  f32x2_t rs = xbcast2(__builtin_amdgcn_rcpf(xsum2(e0[0] + e0[1], e1[0] + e1[1]))); PINV(rs);
  SB();
  SB();
  u32x4 pf[2];
#pragma unroll
  for (int h = 0; h < 2; ++h) {
    const f32x2_t pa = lg[h][0] * (f32x2_t){rs[h], rs[h]}, pb = lg[h][1] * (f32x2_t){rs[h], rs[h]};
    const unsigned wa = pack2(pa[0], pa[1]), wb = pack2(pb[0], pb[1]);
    pf[h] = (u32x4){wa & ~mp[0], wb & ~mp[1], wa & mp[0], wb & mp[1]};
  }
  const f32x4 oa0 = MFMA16(vf[0][0], __builtin_bit_cast(bf16x8, pf[0]), z4), ob0 = MFMA16(vf[1][0], __builtin_bit_cast(bf16x8, pf[1]), z4);
  const f32x4 oa1 = MFMA16(vf[0][1], __builtin_bit_cast(bf16x8, pf[0]), z4), ob1 = MFMA16(vf[1][1], __builtin_bit_cast(bf16x8, pf[1]), z4);
  SB();
  SB();
  ST16(vS0, sO, 0, ((u32x4){pack2(oa0[0], oa0[1]), pack2(oa0[2], oa0[3]), pack2(ob0[0], ob0[1]), pack2(ob0[2], ob0[3])}));
  ST16L48(vS1, sO, 0, ((u32x4){pack2(oa1[0], oa1[1]), pack2(oa1[2], oa1[3]), pack2(ob1[0], ob1[1]), pack2(ob1[2], ob1[3])}));
  STAMP(38);
  WAITV1(2, fu);
  if (__ballot(fu != nonce) != 0ull) { WAITV(0); { const unsigned l_ = lane_now() * 16; ST16X(15, l_, sFO, nonce); } poll1<2>(sFU, vFU, nonce); }
  STAMP(51);
  WAITV(10);
  fw[6][0] = tr8u(rU0 + 0, 512);
  fw[6][1] = tr8u(rU1 + 0, 512);
  fw[6][2] = tr8u(rU2 + 0, 512);
  fw[6][3] = tr8u(rU3 + 0, 512);
  WAITV(0);
  { const unsigned l_ = lane_now() * 16; ST16X(15, l_, sFO, nonce); }
  STAMP(39);
  unsigned wa0 = ldsP + t7[0], wa1 = wa0 ^ 64, ra = rao + t7[1], rah = ra + 65536, fs, vFS;
  const unsigned vOut = t7[2], vSt = t7[3];
  asm volatile("" : "+v"(wa0), "+v"(wa1), "+v"(ra), "+v"(rah));
  f32x4 acc[8], t4[8]; float gy[4][4], zz[4];
  u32x4 xv[2], sv4[2]; f32x2_t xf2[8];
  SB();
  WAITVB(10); SB();
  STAMP(45);
  PRIO(2); SB();
  MV16Z(acc[0], p[0], fw[0][0]); SB();
  LD16(p[7], vP3, sP, 0, "sc1"); sP += 5120; OPQ(sP);
  SB();
  MV16Z(acc[1], p[0], fw[0][1]); SB();
  MV16Z(acc[2], p[0], fw[0][2]); SB();
  MV16Z(acc[3], p[0], fw[0][3]); SB();
  SB();
  WAITVB(10); SB();
  MV16(acc[0], p[1], fw[1][0]); SB();
  LD16(p[0], vP0, sP, 0, "sc1");
  SB();
  MV16(acc[1], p[1], fw[1][1]); SB();
  MV16(acc[2], p[1], fw[1][2]); SB();
  MV16(acc[3], p[1], fw[1][3]); SB();
  SB();
  WAITVB(10); SB();
  MV16(acc[0], p[2], fw[2][0]); SB();
  LD16(p[1], vP1, sP, 0, "sc1");
  SB();
  MV16(acc[1], p[2], fw[2][1]); SB();
  MV16(acc[2], p[2], fw[2][2]); SB();
  MV16(acc[3], p[2], fw[2][3]); SB();
  SB();
  WAITVB(10); SB();
  MV16(acc[0], p[3], fw[3][0]); SB();
  LD16(p[2], vP2, sP, 0, "sc1");
  SB();
  MV16(acc[1], p[3], fw[3][1]); SB();
  MV16(acc[2], p[3], fw[3][2]); SB();
  MV16(acc[3], p[3], fw[3][3]); SB();
  SB();
  WAITVB(10); SB();
  MV16(acc[0], p[4], fw[4][0]); SB();
  LD16(p[3], vP3, sP, 0, "sc1"); sP += 5120; OPQ(sP);
  SB();
  MV16(acc[1], p[4], fw[4][1]); SB();
  MV16(acc[2], p[4], fw[4][2]); SB();
  MV16(acc[3], p[4], fw[4][3]); SB();
  SB();
  WAITVB(10); SB();
  MV16(acc[0], p[5], fw[5][0]); SB();
  LD16(p[4], vP0, sP, 0, "sc1");
  SB();
  MV16(acc[1], p[5], fw[5][1]); SB();
  MV16(acc[2], p[5], fw[5][2]); SB();
  MV16(acc[3], p[5], fw[5][3]); SB();
  SB();
  WAITVB(10); SB();
  MV16(acc[0], p[6], fw[6][0]); SB();
  LD16(p[5], vP1, sP, 0, "sc1");
  SB();
  MV16(acc[1], p[6], fw[6][1]); SB();
  BAR0();
  SB();
  MV16(acc[2], p[6], fw[6][2]); SB();
  MV16(acc[3], p[6], fw[6][3]); SB();
  SB();
  WAITVB(6); SB();
  PRIO(1); SB();
  MV16Z(acc[4], p[7], fw[0][0]); SB();
  LD16(p[6], vP2, sP, 0, "sc1");
  SB();
  MV16Z(acc[5], p[7], fw[0][1]); SB();
  MV16Z(acc[6], p[7], fw[0][2]); SB();
  MV16Z(acc[7], p[7], fw[0][3]); SB();
  SB();
  WAITVB(6); SB();
  MV16(acc[4], p[0], fw[1][0]); SB();
  LD16(p[7], vP3, sP, 0, "sc1"); sP += 5120; OPQ(sP);
  SB();
  MV16(acc[5], p[0], fw[1][1]); SB();
  *(LDS_AS f32x4*)(wa0 + 0) = acc[0];
  SB();
  MV16(acc[6], p[0], fw[1][2]); SB();
  *(LDS_AS f32x4*)(wa0 + 2048) = acc[1];
  SB();
  MV16(acc[7], p[0], fw[1][3]); SB();
  SB();
  WAITVB(6); SB();
  PRIO(0); SB();
  MV16(acc[4], p[1], fw[2][0]); SB();
  LD16(p[0], vP0, sP, 0, "sc1");
  SB();
  MV16(acc[5], p[1], fw[2][1]); SB();
  *(LDS_AS f32x4*)(wa0 + 4096) = acc[2];
  SB();
  MV16(acc[6], p[1], fw[2][2]); SB();
  *(LDS_AS f32x4*)(wa0 + 6144) = acc[3];
  SB();
  MV16(acc[7], p[1], fw[2][3]); SB();
  SB();
  WAITVB(6); SB();
  MV16(acc[4], p[2], fw[3][0]); SB();
  LD16(p[1], vP1, sP, 0, "sc1");
  SB();
  MV16(acc[5], p[2], fw[3][1]); SB();
  { const int l_ = lane_now(); vFU = (42 + (l_ < 28 ? l_ : 27)) * FU; PINV(vFU); }
  LD4(fu, vFU, sFU, 0, "sc1");
  SB();
  MV16(acc[6], p[2], fw[3][2]); SB();
  BAR();
  STAMP(46);
  SB();
  MV16(acc[7], p[2], fw[3][3]); SB();
  SB();
  WAITVB(7); SB();
  PRIO(3); SB();
  MV16(acc[4], p[3], fw[4][0]); SB();
  LD16(p[2], vP2, sP, 0, "sc1");
  t4[0] = *(LDS_AS const f32x4*)(ra + 0);
  SB();
  MV16(acc[5], p[3], fw[4][1]); SB();
  t4[1] = *(LDS_AS const f32x4*)(ra + 16384);
  SB();
  MV16(acc[6], p[3], fw[4][2]); SB();
  t4[2] = *(LDS_AS const f32x4*)(ra + 32768);
  SB();
  MV16(acc[7], p[3], fw[4][3]); SB();
  t4[3] = *(LDS_AS const f32x4*)(ra + 49152);
  SB();
  SB();
  WAITVB(7); SB();
  MV16(acc[4], p[4], fw[5][0]); SB();
  LD16(p[3], vP3, sP, 0, "sc1"); sP += 5120; OPQ(sP);
  t4[4] = *(LDS_AS const f32x4*)(rah + 0);
  SB();
  MV16(acc[5], p[4], fw[5][1]); SB();
  t4[5] = *(LDS_AS const f32x4*)(rah + 16384);
  SB();
  MV16(acc[6], p[4], fw[5][2]); SB();
  t4[6] = *(LDS_AS const f32x4*)(rah + 32768);
  SB();
  MV16(acc[7], p[4], fw[5][3]); SB();
  t4[7] = *(LDS_AS const f32x4*)(rah + 49152);
  SB();
  SB();
  WAITVB(7); SB();
  MV16(acc[4], p[5], fw[6][0]); SB();
  LD16(p[4], vP0, sP, 0, "sc1");
  SB();
  MV16(acc[5], p[5], fw[6][1]); SB();
  OP4_3("v_add_f32", gy[3], t4[0], bias);
  SB();
  MV16(acc[6], p[5], fw[6][2]); SB();
  MV16(acc[7], p[5], fw[6][3]); SB();
  SB();
  WAITVB(7); SB();
  MV16Z(acc[0], p[6], fw[0][0]); SB();
  WAITV1(3, fu);
  if (__ballot(fu != nonce) != 0ull) poll1<2>(sFU, vFU, nonce);
  STAMP(53);
  LD16(p[5], vP1, sP, 0, "sc1");
  SB();
  MV16Z(acc[1], p[6], fw[0][1]); SB();
  OP4_3("v_add_f32", zz, t4[1], t4[2]);
  SB();
  MV16Z(acc[2], p[6], fw[0][2]); SB();
  MV16Z(acc[3], p[6], fw[0][3]); SB();
  SB();
  WAITVB(7); SB();
  MV16(acc[0], p[7], fw[1][0]); SB();
  LD16(p[6], vP2, sP, 0, "sc1");
  SB();
  MV16(acc[1], p[7], fw[1][1]); SB();
  *(LDS_AS f32x4*)(wa0 + 8192) = acc[4];
  SB();
  MV16(acc[2], p[7], fw[1][2]); SB();
  ACC4("v_add_f32", gy[3], t4[3]);
  SB();
  MV16(acc[3], p[7], fw[1][3]); SB();
  SB();
  WAITVB(7); SB();
  PRIO(2); SB();
  MV16(acc[0], p[0], fw[2][0]); SB();
  LD16(p[7], vP3, sP, 0, "sc1"); sP += 5120; OPQ(sP);
  SB();
  MV16(acc[1], p[0], fw[2][1]); SB();
  *(LDS_AS f32x4*)(wa0 + 10240) = acc[5];
  SB();
  MV16(acc[2], p[0], fw[2][2]); SB();
  ACC4("v_add_f32", zz, t4[4]);
  SB();
  MV16(acc[3], p[0], fw[2][3]); SB();
  SB();
  WAITVB(7); SB();
  MV16(acc[0], p[1], fw[3][0]); SB();
  LD16(p[0], vP0, sP, 0, "sc1");
  SB();
  MV16(acc[1], p[1], fw[3][1]); SB();
  *(LDS_AS f32x4*)(wa0 + 12288) = acc[6];
  SB();
  MV16(acc[2], p[1], fw[3][2]); SB();
  ACC4("v_add_f32", gy[3], t4[5]);
  SB();
  MV16(acc[3], p[1], fw[3][3]); SB();
  SB();
  WAITVB(6); SB();
  MV16(acc[0], p[2], fw[4][0]); SB();
  LD16(p[1], vP1, sP, 0, "sc1");
  SB();
  MV16(acc[1], p[2], fw[4][1]); SB();
  *(LDS_AS f32x4*)(wa0 + 14336) = acc[7];
  SB();
  MV16(acc[2], p[2], fw[4][2]); SB();
  ACC4("v_add_f32", zz, t4[6]);
  SB();
  MV16(acc[3], p[2], fw[4][3]); SB();
  SB();
  WAITVB(6); SB();
  MV16(acc[0], p[3], fw[5][0]); SB();
  LD16(p[2], vP2, sP, 0, "sc1");
  SB();
  MV16(acc[1], p[3], fw[5][1]); SB();
  ACC4("v_add_f32", gy[3], t4[7]);
  SB();
  MV16(acc[2], p[3], fw[5][2]); SB();
  MV16(acc[3], p[3], fw[5][3]); SB();
  SB();
  WAITVB(6); SB();
  MV16(acc[0], p[4], fw[6][0]); SB();
  LD16(p[3], vP3, sP, 0, "sc1"); sP += 5120; OPQ(sP);
  SB();
  MV16(acc[1], p[4], fw[6][1]); SB();
  ACC4("v_add_f32", gy[3], zz);
  SB();
  MV16(acc[2], p[4], fw[6][2]); SB();
  MV16(acc[3], p[4], fw[6][3]); SB();
  SB();
  WAITVB(6); SB();
  PRIO(1); SB();
  MV16Z(acc[4], p[5], fw[0][0]); SB();
  LD16(p[4], vP0, sP, 0, "sc1");
  SB();
  MV16Z(acc[5], p[5], fw[0][1]); SB();
  SCL4(zz, gy[3]);
  SB();
  MV16Z(acc[6], p[5], fw[0][2]); SB();
  MV16Z(acc[7], p[5], fw[0][3]); SB();
  SB();
  WAITVB(6); SB();
  MV16(acc[4], p[6], fw[1][0]); SB();
  LD16(p[5], vP1, sP, 0, "sc1");
  SB();
  MV16(acc[5], p[6], fw[1][1]); SB();
  *(LDS_AS f32x4*)(wa1 + 8192) = acc[0];
  SB();
  MV16(acc[6], p[6], fw[1][2]); SB();
  *(LDS_AS f32x4*)(wa1 + 10240) = acc[1];
  SB();
  MV16(acc[7], p[6], fw[1][3]); SB();
  UN4("v_exp_f32", "", "", zz);
  SB();
  SB();
  WAITVB(6); SB();
  PRIO(0); SB();
  MV16(acc[4], p[7], fw[2][0]); SB();
  LD16(p[6], vP2, sP, 0, "sc1");
  SB();
  MV16(acc[5], p[7], fw[2][1]); SB();
  *(LDS_AS f32x4*)(wa1 + 12288) = acc[2];
  SB();
  MV16(acc[6], p[7], fw[2][2]); SB();
  *(LDS_AS f32x4*)(wa1 + 14336) = acc[3];
  SB();
  MV16(acc[7], p[7], fw[2][3]); SB();
  UN4("v_add_f32", "1.0, ", "", zz);
  SB();
  SB();
  WAITVB(6); SB();
  MV16(acc[4], p[0], fw[3][0]); SB();
  LD16(p[7], vP3, sP, 0, "sc1"); sP += 5120; OPQ(sP);
  SB();
  MV16(acc[5], p[0], fw[3][1]); SB();
  { const int l_ = lane_now(); vFU = (70 + (l_ < 28 ? l_ : 27)) * FU; PINV(vFU); }
  LD4(fu, vFU, sFU, 0, "sc1");
  SB();
  MV16(acc[6], p[0], fw[3][2]); SB();
  UN4("v_rcp_f32", "", "", zz);
  SB();
  MV16(acc[7], p[0], fw[3][3]); SB();
  BAR();
  STAMP(47);
  SB();
  SB();
  WAITVB(7); SB();
  PRIO(3); SB();
  MV16(acc[4], p[1], fw[4][0]); SB();
  LD16(p[0], vP0, sP, 0, "sc1");
  ACC4("v_mul_f32", gy[3], zz);
  SB();
  MV16(acc[5], p[1], fw[4][1]); SB();
  t4[0] = *(LDS_AS const f32x4*)(ra + 8192);
  SB();
  MV16(acc[6], p[1], fw[4][2]); SB();
  t4[1] = *(LDS_AS const f32x4*)(ra + 24576);
  t4[2] = *(LDS_AS const f32x4*)(ra + 40960);
  SB();
  MV16(acc[7], p[1], fw[4][3]); SB();
  t4[3] = *(LDS_AS const f32x4*)(ra + 57344);
  SB();
  SB();
  WAITVB(7); SB();
  MV16(acc[4], p[2], fw[5][0]); SB();
  LD16(p[1], vP1, sP, 0, "sc1");
  t4[4] = *(LDS_AS const f32x4*)(rah + 8192);
  SB();
  MV16(acc[5], p[2], fw[5][1]); SB();
  t4[5] = *(LDS_AS const f32x4*)(rah + 24576);
  SB();
  MV16(acc[6], p[2], fw[5][2]); SB();
  t4[6] = *(LDS_AS const f32x4*)(rah + 40960);
  SB();
  MV16(acc[7], p[2], fw[5][3]); SB();
  t4[7] = *(LDS_AS const f32x4*)(rah + 57344);
  SB();
  SB();
  WAITVB(7); SB();
  MV16(acc[4], p[3], fw[6][0]); SB();
  LD16(p[2], vP2, sP, 0, "sc1");
  SB();
  MV16(acc[5], p[3], fw[6][1]); SB();
  OP4_3("v_add_f32", gy[0], t4[0], bias);
  SB();
  MV16(acc[6], p[3], fw[6][2]); SB();
  MV16(acc[7], p[3], fw[6][3]); SB();
  SB();
  WAITVB(7); SB();
  MV16Z(acc[0], p[4], fw[0][0]); SB();
  WAITV1(3, fu);
  if (__ballot(fu != nonce) != 0ull) poll1<2>(sFU, vFU, nonce);
  STAMP(43);
  LD16(p[3], vP3, sP, 0, "sc1"); sP += 5120; OPQ(sP);
  SB();
  MV16Z(acc[1], p[4], fw[0][1]); SB();
  OP4_3("v_add_f32", zz, t4[1], t4[2]);
  SB();
  MV16Z(acc[2], p[4], fw[0][2]); SB();
  MV16Z(acc[3], p[4], fw[0][3]); SB();
  SB();
  WAITVB(7); SB();
  MV16(acc[0], p[5], fw[1][0]); SB();
  LD16(p[4], vP0, sP, 0, "sc1");
  SB();
  MV16(acc[1], p[5], fw[1][1]); SB();
  *(LDS_AS f32x4*)(wa0 + 0) = acc[4];
  SB();
  MV16(acc[2], p[5], fw[1][2]); SB();
  ACC4("v_add_f32", gy[0], t4[3]);
  SB();
  MV16(acc[3], p[5], fw[1][3]); SB();
  SB();
  WAITVB(7); SB();
  PRIO(2); SB();
  MV16(acc[0], p[6], fw[2][0]); SB();
  LD16(p[5], vP1, sP, 0, "sc1");
  SB();
  MV16(acc[1], p[6], fw[2][1]); SB();
  *(LDS_AS f32x4*)(wa0 + 2048) = acc[5];
  SB();
  MV16(acc[2], p[6], fw[2][2]); SB();
  ACC4("v_add_f32", zz, t4[4]);
  SB();
  MV16(acc[3], p[6], fw[2][3]); SB();
  SB();
  WAITVB(7); SB();
  MV16(acc[0], p[7], fw[3][0]); SB();
  LD16(p[6], vP2, sP, 0, "sc1");
  SB();
  MV16(acc[1], p[7], fw[3][1]); SB();
  *(LDS_AS f32x4*)(wa0 + 4096) = acc[6];
  SB();
  MV16(acc[2], p[7], fw[3][2]); SB();
  ACC4("v_add_f32", gy[0], t4[5]);
  SB();
  MV16(acc[3], p[7], fw[3][3]); SB();
  SB();
  WAITVB(6); SB();
  MV16(acc[0], p[0], fw[4][0]); SB();
  LD16(p[7], vP3, sP, 0, "sc1"); sP += 5120; OPQ(sP);
  SB();
  MV16(acc[1], p[0], fw[4][1]); SB();
  *(LDS_AS f32x4*)(wa0 + 6144) = acc[7];
  SB();
  MV16(acc[2], p[0], fw[4][2]); SB();
  ACC4("v_add_f32", zz, t4[6]);
  SB();
  MV16(acc[3], p[0], fw[4][3]); SB();
  SB();
  WAITVB(6); SB();
  MV16(acc[0], p[1], fw[5][0]); SB();
  LD16(p[0], vP0, sP, 0, "sc1");
  SB();
  MV16(acc[1], p[1], fw[5][1]); SB();
  ACC4("v_add_f32", gy[0], t4[7]);
  SB();
  MV16(acc[2], p[1], fw[5][2]); SB();
  MV16(acc[3], p[1], fw[5][3]); SB();
  SB();
  WAITVB(6); SB();
  MV16(acc[0], p[2], fw[6][0]); SB();
  LD16(p[1], vP1, sP, 0, "sc1");
  SB();
  MV16(acc[1], p[2], fw[6][1]); SB();
  ACC4("v_add_f32", gy[0], zz);
  SB();
  MV16(acc[2], p[2], fw[6][2]); SB();
  MV16(acc[3], p[2], fw[6][3]); SB();
  SB();
  WAITVB(6); SB();
  PRIO(1); SB();
  MV16Z(acc[4], p[3], fw[0][0]); SB();
  LD16(p[2], vP2, sP, 0, "sc1");
  SB();
  MV16Z(acc[5], p[3], fw[0][1]); SB();
  SCL4(zz, gy[0]);
  SB();
  MV16Z(acc[6], p[3], fw[0][2]); SB();
  MV16Z(acc[7], p[3], fw[0][3]); SB();
  SB();
  WAITVB(6); SB();
  MV16(acc[4], p[4], fw[1][0]); SB();
  LD16(p[3], vP3, sP, 0, "sc1"); sP += 5120; OPQ(sP);
  *(LDS_AS f32x4*)(wa1 + 0) = acc[0];
  SB();
  MV16(acc[5], p[4], fw[1][1]); SB();
  *(LDS_AS f32x4*)(wa1 + 2048) = acc[1];
  SB();
  MV16(acc[6], p[4], fw[1][2]); SB();
  unsigned c6; { const int l_ = lane_now(); c6 = (l_ & 6) == 6 ? 32 : 0; vFS = (l_ < 7 ? l_ : 6) * FS; asm volatile("" : "+v"(c6), "+v"(vFS)); }
  LD16(xv[0], vOut, sXo, 0, "");
  LD16(xv[1], vOut - c6, sXo, 128, "");
  SB();
  MV16(acc[7], p[4], fw[1][3]); SB();
  UN4("v_exp_f32", "", "", zz);
  SB();
  SB();
  WAITVB(8); SB();
  PRIO(0); SB();
  MV16(acc[4], p[5], fw[2][0]); SB();
  LD16(p[4], vP0, sP, 0, "sc1");
  SB();
  MV16(acc[5], p[5], fw[2][1]); SB();
  *(LDS_AS f32x4*)(wa1 + 4096) = acc[2];
  SB();
  MV16(acc[6], p[5], fw[2][2]); SB();
  *(LDS_AS f32x4*)(wa1 + 6144) = acc[3];
  SB();
  MV16(acc[7], p[5], fw[2][3]); SB();
  UN4("v_add_f32", "1.0, ", "", zz);
  SB();
  SB();
  WAITVB(8); SB();
  MV16(acc[4], p[6], fw[3][0]); SB();
  LD16(p[5], vP1, sP, 0, "sc1");
  SB();
  MV16(acc[5], p[6], fw[3][1]); SB();
  UN4("v_rcp_f32", "", "", zz);
  SB();
  MV16(acc[6], p[6], fw[3][2]); SB();
  BAR();
  STAMP(48);
  SB();
  MV16(acc[7], p[6], fw[3][3]); SB();
  SB();
  WAITVB(8); SB();
  PRIO(3); SB();
  MV16(acc[4], p[7], fw[4][0]); SB();
  LD16(p[6], vP2, sP, 0, "sc1");
  ACC4("v_mul_f32", gy[0], zz);
  SB();
  MV16(acc[5], p[7], fw[4][1]); SB();
  t4[0] = *(LDS_AS const f32x4*)(ra + 0);
  SB();
  MV16(acc[6], p[7], fw[4][2]); SB();
  t4[1] = *(LDS_AS const f32x4*)(ra + 16384);
  t4[2] = *(LDS_AS const f32x4*)(ra + 32768);
  SB();
  MV16(acc[7], p[7], fw[4][3]); SB();
  t4[3] = *(LDS_AS const f32x4*)(ra + 49152);
  SB();
  SB();
  WAITVB(8); SB();
  PRIO(2); SB();
  MV16(acc[4], p[0], fw[5][0]); SB();
  LD16(p[7], vP3, sP, 0, "sc1"); sP += 5120; OPQ(sP);
  t4[4] = *(LDS_AS const f32x4*)(rah + 0);
  SB();
  MV16(acc[5], p[0], fw[5][1]); SB();
  t4[5] = *(LDS_AS const f32x4*)(rah + 16384);
  SB();
  MV16(acc[6], p[0], fw[5][2]); SB();
  t4[6] = *(LDS_AS const f32x4*)(rah + 32768);
  SB();
  MV16(acc[7], p[0], fw[5][3]); SB();
  t4[7] = *(LDS_AS const f32x4*)(rah + 49152);
  SB();
  SB();
  WAITVB(8); SB();
  MV16(acc[4], p[1], fw[6][0]); SB();
  LD16(p[0], vP0, sP, 0, "sc1");
  SB();
  MV16(acc[5], p[1], fw[6][1]); SB();
  LD4(fs, vFS, sFS, 0, "sc1");
  SB();
  MV16(acc[6], p[1], fw[6][2]); SB();
  OP4_3("v_add_f32", gy[1], t4[0], bias);
  SB();
  MV16(acc[7], p[1], fw[6][3]); SB();
  SB();
  WAITVB(9); SB();
  MV16Z(acc[0], p[2], fw[0][0]); SB();
  OP4_3("v_add_f32", zz, t4[1], t4[2]);
  SB();
  MV16Z(acc[1], p[2], fw[0][1]); SB();
  ACC4("v_add_f32", gy[1], t4[3]);
  SB();
  MV16Z(acc[2], p[2], fw[0][2]); SB();
  MV16Z(acc[3], p[2], fw[0][3]); SB();
  SB();
  WAITVB(8); SB();
  MV16(acc[0], p[3], fw[1][0]); SB();
  *(LDS_AS f32x4*)(wa0 + 8192) = acc[4];
  SB();
  MV16(acc[1], p[3], fw[1][1]); SB();
  ACC4("v_add_f32", zz, t4[4]);
  SB();
  MV16(acc[2], p[3], fw[1][2]); SB();
  ACC4("v_add_f32", gy[1], t4[5]);
  SB();
  MV16(acc[3], p[3], fw[1][3]); SB();
  SB();
  WAITVB(5); SB();
  MV16(acc[0], p[4], fw[2][0]); SB();
  *(LDS_AS f32x4*)(wa0 + 10240) = acc[5];
  SB();
  MV16(acc[1], p[4], fw[2][1]); SB();
  ACC4("v_add_f32", zz, t4[6]);
  SB();
  MV16(acc[2], p[4], fw[2][2]); SB();
  ACC4("v_add_f32", gy[1], t4[7]);
  SB();
  MV16(acc[3], p[4], fw[2][3]); SB();
  SB();
  WAITVB(4); SB();
  PRIO(1); SB();
  MV16(acc[0], p[5], fw[3][0]); SB();
  *(LDS_AS f32x4*)(wa0 + 12288) = acc[6];
  SB();
  MV16(acc[1], p[5], fw[3][1]); SB();
  ACC4("v_add_f32", gy[1], zz);
  SB();
  MV16(acc[2], p[5], fw[3][2]); SB();
  MV16(acc[3], p[5], fw[3][3]); SB();
  SB();
  WAITVB(3); SB();
  MV16(acc[0], p[6], fw[4][0]); SB();
  *(LDS_AS f32x4*)(wa0 + 14336) = acc[7];
  SB();
  MV16(acc[1], p[6], fw[4][1]); SB();
  SCL4(zz, gy[1]);
  SB();
  MV16(acc[2], p[6], fw[4][2]); SB();
  UN4("v_exp_f32", "", "", zz);
  SB();
  MV16(acc[3], p[6], fw[4][3]); SB();
  SB();
  WAITVB(2); SB();
  PRIO(0); SB();
  MV16(acc[0], p[7], fw[5][0]); SB();
  WAITV1(7, xv[0]);
  xf2[0] = (f32x2_t){lo2f(xv[0][0]), hi2f(xv[0][0])}; PINV(xf2[0]);
  xf2[1] = (f32x2_t){lo2f(xv[0][1]), hi2f(xv[0][1])}; PINV(xf2[1]);
  xf2[2] = (f32x2_t){lo2f(xv[0][2]), hi2f(xv[0][2])}; PINV(xf2[2]);
  xf2[3] = (f32x2_t){lo2f(xv[0][3]), hi2f(xv[0][3])}; PINV(xf2[3]);
  SB();
  MV16(acc[1], p[7], fw[5][1]); SB();
  WAITV1(0, fs);
  if (__ballot(fs != nonce) != 0ull) poll1<4>(sFS, vFS, nonce);
  STAMP(40);
  LD16(sv4[0], vSt, sS, 0, "sc1");
  { const char* s_ = sS + 12288; LD16(sv4[1], vSt - c6 * 96, s_, 0, "sc1"); }
  SB();
  MV16(acc[2], p[7], fw[5][2]); SB();
  UN4("v_add_f32", "1.0, ", "", zz);
  SB();
  MV16(acc[3], p[7], fw[5][3]); SB();
  UN4("v_rcp_f32", "", "", zz);
  SB();
  SB();
  WAITVB(3); SB();
  MV16(acc[0], p[0], fw[6][0]); SB();
  WAITV1(8, xv[1]);
  xf2[4] = (f32x2_t){lo2f(xv[1][0]), hi2f(xv[1][0])}; PINV(xf2[4]);
  xf2[5] = (f32x2_t){lo2f(xv[1][1]), hi2f(xv[1][1])}; PINV(xf2[5]);
  xf2[6] = (f32x2_t){lo2f(xv[1][2]), hi2f(xv[1][2])}; PINV(xf2[6]);
  xf2[7] = (f32x2_t){lo2f(xv[1][3]), hi2f(xv[1][3])}; PINV(xf2[7]);
  SB();
  MV16(acc[1], p[0], fw[6][1]); SB();
  ACC4("v_mul_f32", gy[1], zz);
  SB();
  MV16(acc[2], p[0], fw[6][2]); SB();
  xchg4(gy[0], gy[1]);
  SB();
  MV16(acc[3], p[0], fw[6][3]); SB();
  NOP(4); SB();
  *(LDS_AS f32x4*)(wa1 + 8192) = acc[0];
  SB();
  *(LDS_AS f32x4*)(wa1 + 10240) = acc[1];
  SB();
  *(LDS_AS f32x4*)(wa1 + 12288) = acc[2];
  SB();
  *(LDS_AS f32x4*)(wa1 + 14336) = acc[3];
  SB();
  BAR();
  STAMP(49);
  t4[0] = *(LDS_AS const f32x4*)(ra + 8192);
  t4[1] = *(LDS_AS const f32x4*)(ra + 24576);
  t4[2] = *(LDS_AS const f32x4*)(ra + 40960);
  t4[3] = *(LDS_AS const f32x4*)(ra + 57344);
  t4[4] = *(LDS_AS const f32x4*)(rah + 8192);
  t4[5] = *(LDS_AS const f32x4*)(rah + 24576);
  t4[6] = *(LDS_AS const f32x4*)(rah + 40960);
  t4[7] = *(LDS_AS const f32x4*)(rah + 57344);
  SB();
  WAITV1(1, sv4[0]);
  ST16(vOut, sOut, 0, ((u32x4){fin2(sv4[0][0], gy[0][0], gy[0][1], xf2[0]), fin2(sv4[0][1], gy[0][2], gy[0][3], xf2[1]), fin2(sv4[0][2], gy[1][0], gy[1][1], xf2[2]), fin2(sv4[0][3], gy[1][2], gy[1][3], xf2[3])}));
  SB();
  { f32x4 y_ = ((t4[0] + (f32x4){bias[0], bias[1], bias[2], bias[3]}) + (t4[1] + t4[2])) + ((t4[3] + t4[4]) + ((t4[5] + t4[6]) + t4[7]));
    f32x4 z_ = y_ * -1.442695041f;
    z_ = (f32x4){__builtin_amdgcn_exp2f(z_[0]), __builtin_amdgcn_exp2f(z_[1]), __builtin_amdgcn_exp2f(z_[2]), __builtin_amdgcn_exp2f(z_[3])} + 1.f;
    z_ = (f32x4){__builtin_amdgcn_rcpf(z_[0]), __builtin_amdgcn_rcpf(z_[1]), __builtin_amdgcn_rcpf(z_[2]), __builtin_amdgcn_rcpf(z_[3])};
    y_ = y_ * z_;
    gy[2][0] = y_[0]; gy[2][1] = y_[1]; gy[2][2] = y_[2]; gy[2][3] = y_[3]; }
  xchg4(gy[2], gy[3]);
  STAMP(41);
  WAITV1(1, sv4[1]);
  { const char* o_ = sOut + 128; ST16E(0x3f3f3f3f, vOut, o_, ((u32x4){fin2(sv4[1][0], gy[2][0], gy[2][1], xf2[4]), fin2(sv4[1][1], gy[2][2], gy[2][3], xf2[5]), fin2(sv4[1][2], gy[3][0], gy[3][1], xf2[6]), fin2(sv4[1][3], gy[3][2], gy[3][3], xf2[7])})); }
  STAMP(42);
  STAMP(50);
  STAMP_DUMP();

  } else {
  if (__builtin_expect(jb < 28, 1)) {
  DMA2(vWa, vWb, sW, ldsP, 6144, ""); sW = wrap == 1 ? bW : sW + dW; OPQ(sW);
  bf16x8 fw[8][4], p[9]; f32x4 acc[8], t4[8]; float mm[4], zz[4], bias[4], c23 = 2.3022082f;
  unsigned rW0 = ldsP + (8 * g4 + q4) * 128 + (((q4 >> 1) | (2 * (g4 & 1))) * 32) + pq * 8, rW1 = rW0 ^ 32, rW2 = rW0 ^ 64, rW3 = rW0 ^ 96;
  asm volatile("" : "+v"(rW0), "+v"(rW1), "+v"(rW2), "+v"(rW3));
  unsigned vP0 = frag(0, lane), vP1 = frag(1, lane), vP2 = frag(2, lane), vP3 = frag(3, lane);
  const char* sP = A.ws + WS_HT + (size_t)(dg * 8 + w) * HT_W;
  asm volatile("" : "+s"(sP)); asm volatile("" : "+v"(vP0), "+v"(vP1), "+v"(vP2), "+v"(vP3));
  WAITV(5);
  fw[0][0] = tr8u(rW0 + 0, 512);
  fw[0][1] = tr8u(rW1 + 0, 512);
  fw[0][2] = tr8u(rW2 + 0, 512);
  fw[0][3] = tr8u(rW3 + 0, 512);
  DMA2(vWa, vWb, sW, ldsP, 8192, ""); sW += dW; OPQ(sW);
  DMA2(vWa, vWb, sW, ldsP, 10240, ""); sW = wrap == 2 ? bW : sW + dW; OPQ(sW);
  WAITV(4);
  fw[1][0] = tr8u(rW0 + 4096, 512);
  fw[1][1] = tr8u(rW1 + 4096, 512);
  fw[1][2] = tr8u(rW2 + 4096, 512);
  fw[1][3] = tr8u(rW3 + 4096, 512);
  DMA2(vWa, vWb, sW, ldsP, 12288, ""); sW += dW; OPQ(sW);
  DMA2(vWa, vWb, sW, ldsP, 14336, ""); sW = wrap == 3 ? bW : sW + dW; OPQ(sW);
  WAITV(4);
  fw[2][0] = tr8u(rW0 + 8192, 512);
  fw[2][1] = tr8u(rW1 + 8192, 512);
  fw[2][2] = tr8u(rW2 + 8192, 512);
  fw[2][3] = tr8u(rW3 + 8192, 512);
  WAITL(0);
  DMA2(vWa, vWb, sW, ldsP, 0, ""); sW += dW; OPQ(sW);
  DMA2(vWa, vWb, sW, ldsP, 2048, ""); sW = wrap == 4 ? bW : sW + dW; OPQ(sW);
  WAITV(4);
  fw[3][0] = tr8u(rW0 + 12288, 512);
  fw[3][1] = tr8u(rW1 + 12288, 512);
  fw[3][2] = tr8u(rW2 + 12288, 512);
  fw[3][3] = tr8u(rW3 + 12288, 512);
  STAMP(1);
  WAITL(0);
  DMA2(vWa, vWb, sW, ldsP, 4096, ""); sW += dW; OPQ(sW);
  DMA2(vWa, vWb, sW, ldsP, 6144, ""); sW = wrap == 5 ? bW : sW + dW; OPQ(sW);
  bias[0] = lo2f(bv[0]); bias[1] = hi2f(bv[0]); bias[2] = lo2f(bv[1]); bias[3] = hi2f(bv[1]);
  unsigned wa0 = ldsP + n16 * 256 + ((g4 ^ n16) << 4), wa1 = wa0 ^ 64, wa2 = wa0 ^ 128, wa3 = wa0 ^ 192;
  unsigned ra = lds0 + (16 * (w >> 2) + n16) * 256 + (((4 * (w & 3) + g4) ^ n16) << 4), rah = ra + 65536, uL = 32 * (w & 1) + 16 * (g4 >> 1) + n16, vU;
  const int kap = 2 * ft + ((w >> 1) & 1), wv = (kap * 37) >> 8, us = kap - 7 * wv, up_ = us - KROT2 * dg;
  int up = up_ < 0 ? up_ + 7 : up_, mU = ((w - 4) >> 31) & 0xffff;
  const char* sU = A.ws + WS_UT + (size_t)(dg * 8 + wv) * UT_W;
  const char* sFU = A.ws + WS_FLAGU + ((dg * 8 + wv) * 128 + (w >> 2) * 14 + us * 2 + (w & 1)) * FU;
  asm volatile("" : "+s"(sU), "+s"(sFU), "+s"(up), "+s"(mU)); asm volatile("" : "+v"(wa0), "+v"(wa1), "+v"(wa2), "+v"(wa3), "+v"(ra), "+v"(rah), "+v"(uL), "+v"(bias[0]), "+v"(bias[1]), "+v"(bias[2]), "+v"(bias[3]), "+v"(c23));
  WAITV(4);
  fw[4][0] = tr8u(rW0 + 0, 512);
  fw[4][1] = tr8u(rW1 + 0, 512);
  fw[4][2] = tr8u(rW2 + 0, 512);
  fw[4][3] = tr8u(rW3 + 0, 512);
  WAITL(0);
  DMA2(vWa, vWb, sW, ldsP, 8192, ""); sW += dW; OPQ(sW);
  LD4(fh, vFH, sFH, 0, "sc1");
  DMA2(vWa, vWb, sW, ldsP, 10240, ""); sW = wrap == 6 ? bW : sW + dW; OPQ(sW);
  WAITV(5);
  fw[5][0] = tr8u(rW0 + 4096, 512);
  fw[5][1] = tr8u(rW1 + 4096, 512);
  fw[5][2] = tr8u(rW2 + 4096, 512);
  fw[5][3] = tr8u(rW3 + 4096, 512);
  WAITL(0);
  DMA2(vWa, vWb, sW, ldsP, 12288, ""); sW += dW; OPQ(sW);
  DMA2(vWa, vWb, sW, ldsP, 14336, "");
  WAITV(4);
  fw[6][0] = tr8u(rW0 + 8192, 512);
  fw[6][1] = tr8u(rW1 + 8192, 512);
  fw[6][2] = tr8u(rW2 + 8192, 512);
  fw[6][3] = tr8u(rW3 + 8192, 512);
  WAITV1(6, fh);
  if (__ballot(fh != nonce) != 0ull) poll1<16>(sFH, vFH, nonce);
  STAMP(3);
  LD16(p[0], vP0, sP, 0, "sc1");
  LD16(p[1], vP1, sP, 0, "sc1");
  LD16(p[2], vP2, sP, 0, "sc1");
  LD16(p[3], vP3, sP, 0, "sc1"); sP += 5120; OPQ(sP);
  WAITV(4);
  fw[7][0] = tr8u(rW0 + 12288, 512);
  fw[7][1] = tr8u(rW1 + 12288, 512);
  fw[7][2] = tr8u(rW2 + 12288, 512);
  fw[7][3] = tr8u(rW3 + 12288, 512);
  STAMP(2);
  LD16(p[4], vP0, sP, 0, "sc1");
  LD16(p[5], vP1, sP, 0, "sc1");
  LD16(p[6], vP2, sP, 0, "sc1");
  LD16(p[7], vP3, sP, 0, "sc1"); sP += 5120; OPQ(sP);
  SB();
  WAITVB(7); SB();
  STAMP(4);
  PRIO(3); SB();
  MV16Z(acc[0], fw[0][0], p[0]); SB();
  LD16(p[8], vP0, sP, 0, "sc1");
  SB();
  MV16Z(acc[1], fw[0][1], p[0]); SB();
  MV16Z(acc[2], fw[0][2], p[0]); SB();
  MV16Z(acc[3], fw[0][3], p[0]); SB();
  SB();
  WAITVB(7); SB();
  MV16(acc[0], fw[1][0], p[1]); SB();
  LD16(p[0], vP1, sP, 0, "sc1");
  SB();
  MV16(acc[1], fw[1][1], p[1]); SB();
  MV16(acc[2], fw[1][2], p[1]); SB();
  MV16(acc[3], fw[1][3], p[1]); SB();
  SB();
  WAITVB(7); SB();
  PRIO(2); SB();
  MV16(acc[0], fw[2][0], p[2]); SB();
  LD16(p[1], vP2, sP, 0, "sc1");
  SB();
  MV16(acc[1], fw[2][1], p[2]); SB();
  MV16(acc[2], fw[2][2], p[2]); SB();
  MV16(acc[3], fw[2][3], p[2]); SB();
  SB();
  WAITVB(7); SB();
  MV16(acc[0], fw[3][0], p[3]); SB();
  LD16(p[2], vP3, sP, 0, "sc1"); sP += 5120; OPQ(sP);
  SB();
  MV16(acc[1], fw[3][1], p[3]); SB();
  MV16(acc[2], fw[3][2], p[3]); SB();
  MV16(acc[3], fw[3][3], p[3]); SB();
  SB();
  WAITVB(7); SB();
  MV16(acc[0], fw[4][0], p[4]); SB();
  LD16(p[3], vP0, sP, 0, "sc1");
  SB();
  MV16(acc[1], fw[4][1], p[4]); SB();
  MV16(acc[2], fw[4][2], p[4]); SB();
  MV16(acc[3], fw[4][3], p[4]); SB();
  SB();
  WAITVB(7); SB();
  MV16(acc[0], fw[5][0], p[5]); SB();
  LD16(p[4], vP1, sP, 0, "sc1");
  SB();
  MV16(acc[1], fw[5][1], p[5]); SB();
  MV16(acc[2], fw[5][2], p[5]); SB();
  MV16(acc[3], fw[5][3], p[5]); SB();
  SB();
  WAITVB(7); SB();
  MV16(acc[0], fw[6][0], p[6]); SB();
  LD16(p[5], vP2, sP, 0, "sc1");
  SB();
  MV16(acc[1], fw[6][1], p[6]); SB();
  MV16(acc[2], fw[6][2], p[6]); SB();
  MV16(acc[3], fw[6][3], p[6]); SB();
  SB();
  WAITVB(7); SB();
  PRIO(1); SB();
  MV16(acc[0], fw[7][0], p[7]); SB();
  LD16(p[6], vP3, sP, 0, "sc1"); sP += 5120; OPQ(sP);
  SB();
  MV16(acc[1], fw[7][1], p[7]); SB();
  MV16(acc[2], fw[7][2], p[7]); SB();
  MV16(acc[3], fw[7][3], p[7]); SB();
  SB();
  WAITVB(7); SB();
  MV16Z(acc[4], fw[0][0], p[8]); SB();
  LD16(p[7], vP0, sP, 0, "sc1");
  SB();
  MV16Z(acc[5], fw[0][1], p[8]); SB();
  MV16Z(acc[6], fw[0][2], p[8]); SB();
  MV16Z(acc[7], fw[0][3], p[8]); SB();
  SB();
  WAITVB(7); SB();
  PRIO(0); SB();
  MV16(acc[4], fw[1][0], p[0]); SB();
  LD16(p[8], vP1, sP, 0, "sc1");
  SB();
  MV16(acc[5], fw[1][1], p[0]); SB();
  *(LDS_AS f32x4*)(wa0 + 0) = acc[0];
  SB();
  MV16(acc[6], fw[1][2], p[0]); SB();
  *(LDS_AS f32x4*)(wa1 + 0) = acc[1];
  SB();
  MV16(acc[7], fw[1][3], p[0]); SB();
  SB();
  WAITVB(7); SB();
  MV16(acc[4], fw[2][0], p[1]); SB();
  LD16(p[0], vP2, sP, 0, "sc1");
  SB();
  MV16(acc[5], fw[2][1], p[1]); SB();
  *(LDS_AS f32x4*)(wa2 + 0) = acc[2];
  SB();
  MV16(acc[6], fw[2][2], p[1]); SB();
  *(LDS_AS f32x4*)(wa3 + 0) = acc[3];
  SB();
  MV16(acc[7], fw[2][3], p[1]); SB();
  BAR();
  STAMP(5);
  SB();
  SB();
  WAITVB(7); SB();
  PRIO(3); SB();
  MV16(acc[4], fw[3][0], p[2]); SB();
  LD16(p[1], vP3, sP, 0, "sc1"); sP += 5120; OPQ(sP);
  t4[0] = *(LDS_AS const f32x4*)(ra + 0);
  SB();
  MV16(acc[5], fw[3][1], p[2]); SB();
  t4[1] = *(LDS_AS const f32x4*)(ra + 16384);
  SB();
  MV16(acc[6], fw[3][2], p[2]); SB();
  t4[2] = *(LDS_AS const f32x4*)(ra + 32768);
  SB();
  MV16(acc[7], fw[3][3], p[2]); SB();
  t4[3] = *(LDS_AS const f32x4*)(ra + 49152);
  SB();
  SB();
  WAITVB(7); SB();
  MV16(acc[4], fw[4][0], p[3]); SB();
  LD16(p[2], vP0, sP, 0, "sc1");
  t4[4] = *(LDS_AS const f32x4*)(rah + 0);
  SB();
  MV16(acc[5], fw[4][1], p[3]); SB();
  t4[5] = *(LDS_AS const f32x4*)(rah + 16384);
  SB();
  MV16(acc[6], fw[4][2], p[3]); SB();
  t4[6] = *(LDS_AS const f32x4*)(rah + 32768);
  t4[7] = *(LDS_AS const f32x4*)(rah + 49152);
  SB();
  MV16(acc[7], fw[4][3], p[3]); SB();
  { const int ix = up; vU = (ix >> 2) * 5120 + frag(ix & 3, uL); PINV(vU); }
  SB();
  SB();
  WAITVB(7); SB();
  MV16(acc[4], fw[5][0], p[4]); SB();
  LD16(p[3], vP1, sP, 0, "sc1");
  OP4_3("v_add_f32", mm, t4[0], bias);
  SB();
  MV16(acc[5], fw[5][1], p[4]); SB();
  OP4_3("v_add_f32", zz, t4[1], t4[2]);
  SB();
  MV16(acc[6], fw[5][2], p[4]); SB();
  ACC4("v_add_f32", mm, t4[3]);
  SB();
  MV16(acc[7], fw[5][3], p[4]); SB();
  ACC4("v_add_f32", zz, t4[4]);
  SB();
  SB();
  WAITVB(7); SB();
  MV16(acc[4], fw[6][0], p[5]); SB();
  LD16(p[4], vP2, sP, 0, "sc1");
  ACC4("v_add_f32", mm, t4[5]);
  SB();
  MV16(acc[5], fw[6][1], p[5]); SB();
  ACC4("v_add_f32", zz, t4[6]);
  SB();
  MV16(acc[6], fw[6][2], p[5]); SB();
  ACC4("v_add_f32", mm, t4[7]);
  SB();
  MV16(acc[7], fw[6][3], p[5]); SB();
  ACC4("v_add_f32", mm, zz);
  SB();
  SB();
  WAITVB(7); SB();
  MV16(acc[4], fw[7][0], p[6]); SB();
  LD16(p[5], vP3, sP, 0, "sc1"); sP += 5120; OPQ(sP);
  OP4_3("v_mul_f32", zz, mm, mm);
  SB();
  MV16(acc[5], fw[7][1], p[6]); SB();
  POLY4(zz, c23);
  SB();
  MV16(acc[6], fw[7][2], p[6]); SB();
  ACC4("v_mul_f32", zz, mm);
  SB();
  MV16(acc[7], fw[7][3], p[6]); SB();
  UN4("v_exp_f32", "", "", zz);
  SB();
  SB();
  WAITVB(7); SB();
  MV16Z(acc[0], fw[0][0], p[7]); SB();
  LD16(p[6], vP0, sP, 0, "sc1");
  UN4("v_add_f32", "1.0, ", "", zz);
  SB();
  MV16Z(acc[1], fw[0][1], p[7]); SB();
  UN4("v_rcp_f32", "", "", zz);
  SB();
  MV16Z(acc[2], fw[0][2], p[7]); SB();
  NFMA4(mm, zz);
  SB();
  MV16Z(acc[3], fw[0][3], p[7]); SB();
  { const unsigned x0 = pack2(mm[0], mm[1]), x1 = pack2(mm[2], mm[3]);
    const auto r0 = __builtin_amdgcn_permlane16_swap(x0, x0, false, false), r1 = __builtin_amdgcn_permlane16_swap(x1, x1, false, false);
    ST16M(mU, vU, sU, ((u32x4){r0[0], r1[0], r0[1], r1[1]})); }
  SB();
  SB();
  WAITVB(7); SB();
  MV16(acc[0], fw[1][0], p[8]); SB();
  LD16(p[7], vP1, sP, 0, "sc1");
  SB();
  MV16(acc[1], fw[1][1], p[8]); SB();
  *(LDS_AS f32x4*)(wa0 + 8192) = acc[4];
  SB();
  MV16(acc[2], fw[1][2], p[8]); SB();
  MV16(acc[3], fw[1][3], p[8]); SB();
  SB();
  WAITVB(7); SB();
  PRIO(2); SB();
  MV16(acc[0], fw[2][0], p[0]); SB();
  LD16(p[8], vP2, sP, 0, "sc1");
  SB();
  MV16(acc[1], fw[2][1], p[0]); SB();
  *(LDS_AS f32x4*)(wa1 + 8192) = acc[5];
  SB();
  MV16(acc[2], fw[2][2], p[0]); SB();
  MV16(acc[3], fw[2][3], p[0]); SB();
  SB();
  WAITVB(7); SB();
  MV16(acc[0], fw[3][0], p[1]); SB();
  LD16(p[0], vP3, sP, 0, "sc1"); sP += 5120; OPQ(sP);
  SB();
  MV16(acc[1], fw[3][1], p[1]); SB();
  *(LDS_AS f32x4*)(wa2 + 8192) = acc[6];
  SB();
  MV16(acc[2], fw[3][2], p[1]); SB();
  MV16(acc[3], fw[3][3], p[1]); SB();
  SB();
  WAITVB(7); SB();
  MV16(acc[0], fw[4][0], p[2]); SB();
  LD16(p[1], vP0, sP, 0, "sc1");
  SB();
  MV16(acc[1], fw[4][1], p[2]); SB();
  *(LDS_AS f32x4*)(wa3 + 8192) = acc[7];
  SB();
  MV16(acc[2], fw[4][2], p[2]); SB();
  MV16(acc[3], fw[4][3], p[2]); SB();
  SB();
  WAITVB(7); SB();
  MV16(acc[0], fw[5][0], p[3]); SB();
  LD16(p[2], vP1, sP, 0, "sc1");
  SB();
  MV16(acc[1], fw[5][1], p[3]); SB();
  MV16(acc[2], fw[5][2], p[3]); SB();
  MV16(acc[3], fw[5][3], p[3]); SB();
  SB();
  WAITVB(7); SB();
  MV16(acc[0], fw[6][0], p[4]); SB();
  LD16(p[3], vP2, sP, 0, "sc1");
  SB();
  MV16(acc[1], fw[6][1], p[4]); SB();
  MV16(acc[2], fw[6][2], p[4]); SB();
  MV16(acc[3], fw[6][3], p[4]); SB();
  SB();
  WAITVB(7); SB();
  PRIO(1); SB();
  MV16(acc[0], fw[7][0], p[5]); SB();
  LD16(p[4], vP3, sP, 0, "sc1"); sP += 5120; OPQ(sP);
  SB();
  MV16(acc[1], fw[7][1], p[5]); SB();
  MV16(acc[2], fw[7][2], p[5]); SB();
  MV16(acc[3], fw[7][3], p[5]); SB();
  SB();
  WAITVB(7); SB();
  MV16Z(acc[4], fw[0][0], p[6]); SB();
  LD16(p[5], vP0, sP, 0, "sc1");
  SB();
  MV16Z(acc[5], fw[0][1], p[6]); SB();
  MV16Z(acc[6], fw[0][2], p[6]); SB();
  MV16Z(acc[7], fw[0][3], p[6]); SB();
  SB();
  WAITVB(7); SB();
  PRIO(0); SB();
  MV16(acc[4], fw[1][0], p[7]); SB();
  LD16(p[6], vP1, sP, 0, "sc1");
  SB();
  MV16(acc[5], fw[1][1], p[7]); SB();
  *(LDS_AS f32x4*)(wa0 + 12288) = acc[0];
  SB();
  MV16(acc[6], fw[1][2], p[7]); SB();
  *(LDS_AS f32x4*)(wa1 + 12288) = acc[1];
  SB();
  MV16(acc[7], fw[1][3], p[7]); SB();
  if (w < 4) { const unsigned l_ = lane_now() * 16; ST16X(1, l_, sFU, nonce); }
  SB();
  SB();
  WAITVB(7); SB();
  MV16(acc[4], fw[2][0], p[8]); SB();
  LD16(p[7], vP2, sP, 0, "sc1");
  SB();
  MV16(acc[5], fw[2][1], p[8]); SB();
  *(LDS_AS f32x4*)(wa2 + 12288) = acc[2];
  SB();
  MV16(acc[6], fw[2][2], p[8]); SB();
  *(LDS_AS f32x4*)(wa3 + 12288) = acc[3];
  SB();
  MV16(acc[7], fw[2][3], p[8]); SB();
  BAR();
  STAMP(6);
  SB();
  SB();
  WAITVB(7); SB();
  PRIO(3); SB();
  MV16(acc[4], fw[3][0], p[0]); SB();
  LD16(p[8], vP3, sP, 0, "sc1"); sP += 5120; OPQ(sP);
  t4[0] = *(LDS_AS const f32x4*)(ra + 8192);
  SB();
  MV16(acc[5], fw[3][1], p[0]); SB();
  t4[1] = *(LDS_AS const f32x4*)(ra + 24576);
  SB();
  MV16(acc[6], fw[3][2], p[0]); SB();
  t4[2] = *(LDS_AS const f32x4*)(ra + 40960);
  SB();
  MV16(acc[7], fw[3][3], p[0]); SB();
  t4[3] = *(LDS_AS const f32x4*)(ra + 57344);
  SB();
  SB();
  WAITVB(7); SB();
  MV16(acc[4], fw[4][0], p[1]); SB();
  LD16(p[0], vP0, sP, 0, "sc1");
  t4[4] = *(LDS_AS const f32x4*)(rah + 8192);
  SB();
  MV16(acc[5], fw[4][1], p[1]); SB();
  t4[5] = *(LDS_AS const f32x4*)(rah + 24576);
  SB();
  MV16(acc[6], fw[4][2], p[1]); SB();
  t4[6] = *(LDS_AS const f32x4*)(rah + 40960);
  t4[7] = *(LDS_AS const f32x4*)(rah + 57344);
  SB();
  MV16(acc[7], fw[4][3], p[1]); SB();
  { const int ix = 7 + up + 7 * (w >> 2); vU = (ix >> 2) * 5120 + frag(ix & 3, uL); PINV(vU); }
  SB();
  SB();
  WAITVB(7); SB();
  MV16(acc[4], fw[5][0], p[2]); SB();
  LD16(p[1], vP1, sP, 0, "sc1");
  OP4_3("v_add_f32", mm, t4[0], bias);
  SB();
  MV16(acc[5], fw[5][1], p[2]); SB();
  OP4_3("v_add_f32", zz, t4[1], t4[2]);
  SB();
  MV16(acc[6], fw[5][2], p[2]); SB();
  ACC4("v_add_f32", mm, t4[3]);
  SB();
  MV16(acc[7], fw[5][3], p[2]); SB();
  ACC4("v_add_f32", zz, t4[4]);
  SB();
  SB();
  WAITVB(7); SB();
  MV16(acc[4], fw[6][0], p[3]); SB();
  LD16(p[2], vP2, sP, 0, "sc1");
  ACC4("v_add_f32", mm, t4[5]);
  SB();
  MV16(acc[5], fw[6][1], p[3]); SB();
  ACC4("v_add_f32", zz, t4[6]);
  SB();
  MV16(acc[6], fw[6][2], p[3]); SB();
  ACC4("v_add_f32", mm, t4[7]);
  SB();
  MV16(acc[7], fw[6][3], p[3]); SB();
  ACC4("v_add_f32", mm, zz);
  SB();
  SB();
  WAITVB(7); SB();
  MV16(acc[4], fw[7][0], p[4]); SB();
  LD16(p[3], vP3, sP, 0, "sc1"); sP += 5120; OPQ(sP);
  OP4_3("v_mul_f32", zz, mm, mm);
  SB();
  MV16(acc[5], fw[7][1], p[4]); SB();
  POLY4(zz, c23);
  SB();
  MV16(acc[6], fw[7][2], p[4]); SB();
  ACC4("v_mul_f32", zz, mm);
  SB();
  MV16(acc[7], fw[7][3], p[4]); SB();
  UN4("v_exp_f32", "", "", zz);
  SB();
  SB();
  WAITVB(7); SB();
  MV16Z(acc[0], fw[0][0], p[5]); SB();
  LD16(p[4], vP0, sP, 0, "sc1");
  UN4("v_add_f32", "1.0, ", "", zz);
  SB();
  MV16Z(acc[1], fw[0][1], p[5]); SB();
  UN4("v_rcp_f32", "", "", zz);
  SB();
  MV16Z(acc[2], fw[0][2], p[5]); SB();
  NFMA4(mm, zz);
  SB();
  MV16Z(acc[3], fw[0][3], p[5]); SB();
  { const unsigned x0 = pack2(mm[0], mm[1]), x1 = pack2(mm[2], mm[3]);
    const auto r0 = __builtin_amdgcn_permlane16_swap(x0, x0, false, false), r1 = __builtin_amdgcn_permlane16_swap(x1, x1, false, false);
    ST16E(0xffff, vU, sU, ((u32x4){r0[0], r1[0], r0[1], r1[1]})); }
  SB();
  SB();
  WAITVB(8); SB();
  MV16(acc[0], fw[1][0], p[6]); SB();
  LD16(p[5], vP1, sP, 0, "sc1");
  SB();
  MV16(acc[1], fw[1][1], p[6]); SB();
  *(LDS_AS f32x4*)(wa0 + 0) = acc[4];
  SB();
  MV16(acc[2], fw[1][2], p[6]); SB();
  MV16(acc[3], fw[1][3], p[6]); SB();
  SB();
  WAITVB(8); SB();
  PRIO(2); SB();
  MV16(acc[0], fw[2][0], p[7]); SB();
  LD16(p[6], vP2, sP, 0, "sc1");
  SB();
  MV16(acc[1], fw[2][1], p[7]); SB();
  *(LDS_AS f32x4*)(wa1 + 0) = acc[5];
  SB();
  MV16(acc[2], fw[2][2], p[7]); SB();
  MV16(acc[3], fw[2][3], p[7]); SB();
  SB();
  WAITVB(8); SB();
  MV16(acc[0], fw[3][0], p[8]); SB();
  LD16(p[7], vP3, sP, 0, "sc1"); sP += 5120; OPQ(sP);
  SB();
  MV16(acc[1], fw[3][1], p[8]); SB();
  *(LDS_AS f32x4*)(wa2 + 0) = acc[6];
  SB();
  MV16(acc[2], fw[3][2], p[8]); SB();
  MV16(acc[3], fw[3][3], p[8]); SB();
  SB();
  WAITVB(8); SB();
  MV16(acc[0], fw[4][0], p[0]); SB();
  LD16(p[8], vP0, sP, 0, "sc1");
  SB();
  MV16(acc[1], fw[4][1], p[0]); SB();
  *(LDS_AS f32x4*)(wa3 + 0) = acc[7];
  SB();
  MV16(acc[2], fw[4][2], p[0]); SB();
  MV16(acc[3], fw[4][3], p[0]); SB();
  SB();
  WAITVB(8); SB();
  MV16(acc[0], fw[5][0], p[1]); SB();
  LD16(p[0], vP1, sP, 0, "sc1");
  SB();
  MV16(acc[1], fw[5][1], p[1]); SB();
  MV16(acc[2], fw[5][2], p[1]); SB();
  MV16(acc[3], fw[5][3], p[1]); SB();
  SB();
  WAITVB(8); SB();
  MV16(acc[0], fw[6][0], p[2]); SB();
  LD16(p[1], vP2, sP, 0, "sc1");
  SB();
  MV16(acc[1], fw[6][1], p[2]); SB();
  MV16(acc[2], fw[6][2], p[2]); SB();
  MV16(acc[3], fw[6][3], p[2]); SB();
  SB();
  WAITVB(8); SB();
  PRIO(1); SB();
  MV16(acc[0], fw[7][0], p[3]); SB();
  LD16(p[2], vP3, sP, 0, "sc1"); sP += 5120; OPQ(sP);
  SB();
  MV16(acc[1], fw[7][1], p[3]); SB();
  MV16(acc[2], fw[7][2], p[3]); SB();
  MV16(acc[3], fw[7][3], p[3]); SB();
  SB();
  WAITVB(8); SB();
  MV16Z(acc[4], fw[0][0], p[4]); SB();
  LD16(p[3], vP0, sP, 0, "sc1");
  SB();
  MV16Z(acc[5], fw[0][1], p[4]); SB();
  MV16Z(acc[6], fw[0][2], p[4]); SB();
  MV16Z(acc[7], fw[0][3], p[4]); SB();
  SB();
  WAITVB(7); SB();
  PRIO(0); SB();
  MV16(acc[4], fw[1][0], p[5]); SB();
  LD16(p[4], vP1, sP, 0, "sc1");
  SB();
  MV16(acc[5], fw[1][1], p[5]); SB();
  *(LDS_AS f32x4*)(wa0 + 4096) = acc[0];
  SB();
  MV16(acc[6], fw[1][2], p[5]); SB();
  *(LDS_AS f32x4*)(wa1 + 4096) = acc[1];
  SB();
  MV16(acc[7], fw[1][3], p[5]); SB();
  { const char* f_ = sFU + 14 * FU; const unsigned l_ = lane_now() * 16; ST16X(1, l_, f_, nonce); }
  SB();
  SB();
  WAITVB(8); SB();
  MV16(acc[4], fw[2][0], p[6]); SB();
  LD16(p[5], vP2, sP, 0, "sc1");
  SB();
  MV16(acc[5], fw[2][1], p[6]); SB();
  *(LDS_AS f32x4*)(wa2 + 4096) = acc[2];
  SB();
  MV16(acc[6], fw[2][2], p[6]); SB();
  *(LDS_AS f32x4*)(wa3 + 4096) = acc[3];
  SB();
  MV16(acc[7], fw[2][3], p[6]); SB();
  BAR();
  STAMP(7);
  SB();
  SB();
  WAITVB(8); SB();
  PRIO(3); SB();
  MV16(acc[4], fw[3][0], p[7]); SB();
  LD16(p[6], vP3, sP, 0, "sc1"); sP += 5120; OPQ(sP);
  t4[0] = *(LDS_AS const f32x4*)(ra + 0);
  SB();
  MV16(acc[5], fw[3][1], p[7]); SB();
  t4[1] = *(LDS_AS const f32x4*)(ra + 16384);
  SB();
  MV16(acc[6], fw[3][2], p[7]); SB();
  t4[2] = *(LDS_AS const f32x4*)(ra + 32768);
  SB();
  MV16(acc[7], fw[3][3], p[7]); SB();
  t4[3] = *(LDS_AS const f32x4*)(ra + 49152);
  SB();
  SB();
  WAITVB(8); SB();
  MV16(acc[4], fw[4][0], p[8]); SB();
  LD16(p[7], vP0, sP, 0, "sc1");
  t4[4] = *(LDS_AS const f32x4*)(rah + 0);
  SB();
  MV16(acc[5], fw[4][1], p[8]); SB();
  t4[5] = *(LDS_AS const f32x4*)(rah + 16384);
  SB();
  MV16(acc[6], fw[4][2], p[8]); SB();
  t4[6] = *(LDS_AS const f32x4*)(rah + 32768);
  t4[7] = *(LDS_AS const f32x4*)(rah + 49152);
  SB();
  MV16(acc[7], fw[4][3], p[8]); SB();
  { const int ix = 21 + up + 7 * (w >> 2); vU = (ix >> 2) * 5120 + frag(ix & 3, uL); PINV(vU); }
  SB();
  SB();
  WAITVB(8); SB();
  MV16(acc[4], fw[5][0], p[0]); SB();
  LD16(p[8], vP1, sP, 0, "sc1");
  OP4_3("v_add_f32", mm, t4[0], bias);
  SB();
  MV16(acc[5], fw[5][1], p[0]); SB();
  OP4_3("v_add_f32", zz, t4[1], t4[2]);
  SB();
  MV16(acc[6], fw[5][2], p[0]); SB();
  ACC4("v_add_f32", mm, t4[3]);
  SB();
  MV16(acc[7], fw[5][3], p[0]); SB();
  ACC4("v_add_f32", zz, t4[4]);
  SB();
  SB();
  WAITVB(8); SB();
  MV16(acc[4], fw[6][0], p[1]); SB();
  LD16(p[0], vP2, sP, 0, "sc1");
  ACC4("v_add_f32", mm, t4[5]);
  SB();
  MV16(acc[5], fw[6][1], p[1]); SB();
  ACC4("v_add_f32", zz, t4[6]);
  SB();
  MV16(acc[6], fw[6][2], p[1]); SB();
  ACC4("v_add_f32", mm, t4[7]);
  SB();
  MV16(acc[7], fw[6][3], p[1]); SB();
  ACC4("v_add_f32", mm, zz);
  SB();
  SB();
  WAITVB(8); SB();
  PRIO(2); SB();
  MV16(acc[4], fw[7][0], p[2]); SB();
  LD16(p[1], vP3, sP, 0, "sc1");
  OP4_3("v_mul_f32", zz, mm, mm);
  SB();
  MV16(acc[5], fw[7][1], p[2]); SB();
  POLY4(zz, c23);
  SB();
  MV16(acc[6], fw[7][2], p[2]); SB();
  ACC4("v_mul_f32", zz, mm);
  SB();
  MV16(acc[7], fw[7][3], p[2]); SB();
  UN4("v_exp_f32", "", "", zz);
  SB();
  SB();
  WAITVB(8); SB();
  MV16Z(acc[0], fw[0][0], p[3]); SB();
  bf16x8 fo[14];
  const char* sWo = A.ws + WS_WO + (size_t)(dg * 7 + (w < 7 ? w : 6)) * WO_B; unsigned long long mW = w < 7 ? ~0ull : 1ull; OPQ(sWo); OPQ(mW);
  UN4("v_add_f32", "1.0, ", "", zz);
  SB();
  MV16Z(acc[1], fw[0][1], p[3]); SB();
  UN4("v_rcp_f32", "", "", zz);
  SB();
  MV16Z(acc[2], fw[0][2], p[3]); SB();
  NFMA4(mm, zz);
  SB();
  MV16Z(acc[3], fw[0][3], p[3]); SB();
  { const unsigned x0 = pack2(mm[0], mm[1]), x1 = pack2(mm[2], mm[3]);
    const auto r0 = __builtin_amdgcn_permlane16_swap(x0, x0, false, false), r1 = __builtin_amdgcn_permlane16_swap(x1, x1, false, false);
    ST16E(0xffff, vU, sU, ((u32x4){r0[0], r1[0], r0[1], r1[1]})); }
  SB();
  SB();
  WAITVB(8); SB();
  MV16(acc[0], fw[1][0], p[4]); SB();
  *(LDS_AS f32x4*)(wa0 + 8192) = acc[4];
  SB();
  MV16(acc[1], fw[1][1], p[4]); SB();
  MV16(acc[2], fw[1][2], p[4]); SB();
  MV16(acc[3], fw[1][3], p[4]); SB();
  SB();
  WAITVB(6); SB();
  MV16(acc[0], fw[2][0], p[5]); SB();
  *(LDS_AS f32x4*)(wa1 + 8192) = acc[5];
  SB();
  MV16(acc[1], fw[2][1], p[5]); SB();
  MV16(acc[2], fw[2][2], p[5]); SB();
  MV16(acc[3], fw[2][3], p[5]); SB();
  SB();
  WAITVB(5); SB();
  MV16(acc[0], fw[3][0], p[6]); SB();
  *(LDS_AS f32x4*)(wa2 + 8192) = acc[6];
  SB();
  MV16(acc[1], fw[3][1], p[6]); SB();
  MV16(acc[2], fw[3][2], p[6]); SB();
  MV16(acc[3], fw[3][3], p[6]); SB();
  SB();
  WAITVB(4); SB();
  PRIO(1); SB();
  MV16(acc[0], fw[4][0], p[7]); SB();
  *(LDS_AS f32x4*)(wa3 + 8192) = acc[7];
  SB();
  MV16(acc[1], fw[4][1], p[7]); SB();
  MV16(acc[2], fw[4][2], p[7]); SB();
  MV16(acc[3], fw[4][3], p[7]); SB();
  SB();
  WAITVB(3); SB();
  MV16(acc[0], fw[5][0], p[8]); SB();
  LD16M(fo[0], vP0, sWo, mW);
  LD16M(fo[1], vP1, sWo, mW);
  SB();
  MV16(acc[1], fw[5][1], p[8]); SB();
  MV16(acc[2], fw[5][2], p[8]); SB();
  MV16(acc[3], fw[5][3], p[8]); SB();
  SB();
  WAITVB(4); SB();
  PRIO(0); SB();
  MV16(acc[0], fw[6][0], p[0]); SB();
  LD16M(fo[2], vP2, sWo, mW);
  LD16M(fo[3], vP3, sWo, mW); sWo += 5120; OPQ(sWo);
  SB();
  MV16(acc[1], fw[6][1], p[0]); SB();
  MV16(acc[2], fw[6][2], p[0]); SB();
  MV16(acc[3], fw[6][3], p[0]); SB();
  SB();
  WAITVB(5); SB();
  MV16(acc[0], fw[7][0], p[1]); SB();
  LD16M(fo[4], vP0, sWo, mW);
  LD16M(fo[5], vP1, sWo, mW);
  SB();
  MV16(acc[1], fw[7][1], p[1]); SB();
  MV16(acc[2], fw[7][2], p[1]); SB();
  MV16(acc[3], fw[7][3], p[1]); SB();
  NOP(4); SB();
  *(LDS_AS f32x4*)(wa0 + 12288) = acc[0];
  SB();
  *(LDS_AS f32x4*)(wa1 + 12288) = acc[1];
  SB();
  *(LDS_AS f32x4*)(wa2 + 12288) = acc[2];
  SB();
  *(LDS_AS f32x4*)(wa3 + 12288) = acc[3];
  SB();
  BAR();
  STAMP(8);
  t4[0] = *(LDS_AS const f32x4*)(ra + 8192);
  t4[1] = *(LDS_AS const f32x4*)(ra + 24576);
  t4[2] = *(LDS_AS const f32x4*)(ra + 40960);
  t4[3] = *(LDS_AS const f32x4*)(ra + 57344);
  t4[4] = *(LDS_AS const f32x4*)(rah + 8192);
  t4[5] = *(LDS_AS const f32x4*)(rah + 24576);
  t4[6] = *(LDS_AS const f32x4*)(rah + 40960);
  t4[7] = *(LDS_AS const f32x4*)(rah + 57344);
  { const int ix = 35 + up + 7 * (w >> 2); vU = (ix >> 2) * 5120 + frag(ix & 3, uL); PINV(vU); }
  WAITV(6);
  { const char* f_ = sFU + 42 * FU; const unsigned l_ = lane_now() * 16; ST16X(1, l_, f_, nonce); }
  { f32x4 y_ = ((t4[0] + (f32x4){bias[0], bias[1], bias[2], bias[3]}) + (t4[1] + t4[2])) + ((t4[3] + t4[4]) + ((t4[5] + t4[6]) + t4[7]));
    f32x4 z_ = y_ * (y_ * y_ * 0.10294324f + 2.3022082f);
    z_ = (f32x4){__builtin_amdgcn_exp2f(z_[0]), __builtin_amdgcn_exp2f(z_[1]), __builtin_amdgcn_exp2f(z_[2]), __builtin_amdgcn_exp2f(z_[3])} + 1.f;
    z_ = (f32x4){__builtin_amdgcn_rcpf(z_[0]), __builtin_amdgcn_rcpf(z_[1]), __builtin_amdgcn_rcpf(z_[2]), __builtin_amdgcn_rcpf(z_[3])};
    y_ = y_ - y_ * z_;
    mm[0] = y_[0]; mm[1] = y_[1]; mm[2] = y_[2]; mm[3] = y_[3]; }
  { const unsigned x0 = pack2(mm[0], mm[1]), x1 = pack2(mm[2], mm[3]);
    const auto r0 = __builtin_amdgcn_permlane16_swap(x0, x0, false, false), r1 = __builtin_amdgcn_permlane16_swap(x1, x1, false, false);
    ST16E(0xffff, vU, sU, ((u32x4){r0[0], r1[0], r0[1], r1[1]})); }
  STAMP(9);
  LD16M(fo[6], vP2, sWo, mW);
  LD16M(fo[7], vP3, sWo, mW); sWo += 5120; OPQ(sWo);
  WAITV(2);
  { const char* f_ = sFU + 70 * FU; const unsigned l_ = lane_now() * 16; ST16X(1, l_, f_, nonce); }
  LD16M(fo[8], vP0, sWo, mW);
  LD16M(fo[9], vP1, sWo, mW);
  LD16M(fo[10], vP2, sWo, mW);
  LD16M(fo[11], vP3, sWo, mW); sWo += 5120; OPQ(sWo);
  LD16M(fo[12], vP0, sWo, mW);
  LD16M(fo[13], vP1, sWo, mW);
  { int w_ = w; OPQ(w_); { const int w = w_ & 7, lane = lane_now(); const int n16 = lane & 15, g4 = lane >> 4, q4 = n16 >> 2, pq = lane & 3, r8 = lane >> 3; const unsigned vL16 = lane * 16;
  unsigned vO[7];
#pragma unroll
  for (int i = 0; i < 7; ++i) { vO[i] = line7(64 * i + 8 * w + r8) + (lane & 7) * 16; PINV(vO[i]); }
  const char* sO = A.ws + WS_OT + (size_t)st * OT_T;
  unsigned vFO = (((lane >> 2) & 7) * 8 + (lane & 3)) * FO, ro = lds0 + vL16, roh = ro + 65536;
  unsigned vS0 = pair3(q4) + (2 * (n16 & 3) + (g4 & 1)) * 32 + (g4 >> 1) * 16, vS1 = vS0 - pair3(q4) + pair3(4 + q4);
  const char* sFO = A.ws + WS_FLAGO + ((st >> 1) * 64 + (st & 1) * 4) * FO;
  const char* sS = A.ws + WS_ST + (size_t)(st * 4 + dg) * ST_T + w * 3072;
  const char* sFS = A.ws + WS_FLAGS + ((st * 4 + dg) * 8 + w) * FS;
  asm volatile("" : "+s"(sO), "+s"(sFO), "+s"(sS), "+s"(sFS)); asm volatile("" : "+v"(vFO), "+v"(ro), "+v"(roh), "+v"(vS0), "+v"(vS1));
  STAMP(10);
  STAMP(11);
  if (w == 7) poll1<3>(sFO, vFO, nonce);
  BAR();
  STAMP(12);
  bf16x8 of[3][4]; f32x4 as[4];
  DMA1(vO[0], sO, ldsP, 0, "sc1");
  DMA1(vO[1], sO, ldsP, 1024, "sc1");
  DMA1(vO[2], sO, ldsP, 2048, "sc1");
  DMA1(vO[3], sO, ldsP, 3072, "sc1");
  DMA1(vO[4], sO, ldsP, 4096, "sc1");
  DMA1(vO[5], sO, ldsP, 5120, "sc1");
  DMA1(vO[6], sO, ldsP, 6144, "sc1");
  WAITV(4);
  BAR0();
  if (w == 7) {
  WAITV(0);
  BAR0();
  } else {
  of[0][0] = *(LDS_AS const bf16x8*)(ro + 0);
  of[0][1] = *(LDS_AS const bf16x8*)(ro + 16384);
  of[0][2] = *(LDS_AS const bf16x8*)(ro + 32768);
  of[0][3] = *(LDS_AS const bf16x8*)(ro + 49152);
  STAMP(13);
  of[1][0] = *(LDS_AS const bf16x8*)(roh + 0);
  of[1][1] = *(LDS_AS const bf16x8*)(roh + 16384);
  of[1][2] = *(LDS_AS const bf16x8*)(roh + 32768);
  of[1][3] = *(LDS_AS const bf16x8*)(roh + 49152);
  SB();
  MV16Z(as[0], fo[0], of[0][0]); SB();
  of[2][0] = *(LDS_AS const bf16x8*)(ro + 1024);
  SB();
  MV16Z(as[1], fo[0], of[0][1]); SB();
  of[2][1] = *(LDS_AS const bf16x8*)(ro + 17408);
  SB();
  MV16Z(as[2], fo[0], of[0][2]); SB();
  of[2][2] = *(LDS_AS const bf16x8*)(ro + 33792);
  SB();
  MV16Z(as[3], fo[0], of[0][3]); SB();
  of[2][3] = *(LDS_AS const bf16x8*)(ro + 50176);
  SB();
  SB();
  MV16(as[0], fo[1], of[1][0]); SB();
  of[0][0] = *(LDS_AS const bf16x8*)(roh + 1024);
  SB();
  MV16(as[1], fo[1], of[1][1]); SB();
  of[0][1] = *(LDS_AS const bf16x8*)(roh + 17408);
  SB();
  MV16(as[2], fo[1], of[1][2]); SB();
  of[0][2] = *(LDS_AS const bf16x8*)(roh + 33792);
  SB();
  MV16(as[3], fo[1], of[1][3]); SB();
  of[0][3] = *(LDS_AS const bf16x8*)(roh + 50176);
  SB();
  SB();
  MV16(as[0], fo[2], of[2][0]); SB();
  of[1][0] = *(LDS_AS const bf16x8*)(ro + 2048);
  SB();
  MV16(as[1], fo[2], of[2][1]); SB();
  of[1][1] = *(LDS_AS const bf16x8*)(ro + 18432);
  SB();
  MV16(as[2], fo[2], of[2][2]); SB();
  of[1][2] = *(LDS_AS const bf16x8*)(ro + 34816);
  SB();
  MV16(as[3], fo[2], of[2][3]); SB();
  of[1][3] = *(LDS_AS const bf16x8*)(ro + 51200);
  SB();
  SB();
  MV16(as[0], fo[3], of[0][0]); SB();
  of[2][0] = *(LDS_AS const bf16x8*)(roh + 2048);
  SB();
  MV16(as[1], fo[3], of[0][1]); SB();
  of[2][1] = *(LDS_AS const bf16x8*)(roh + 18432);
  SB();
  MV16(as[2], fo[3], of[0][2]); SB();
  of[2][2] = *(LDS_AS const bf16x8*)(roh + 34816);
  SB();
  MV16(as[3], fo[3], of[0][3]); SB();
  of[2][3] = *(LDS_AS const bf16x8*)(roh + 51200);
  SB();
  SB();
  MV16(as[0], fo[4], of[1][0]); SB();
  WAITV(0);
  BAR0();
  of[0][0] = *(LDS_AS const bf16x8*)(ro + 3072);
  SB();
  MV16(as[1], fo[4], of[1][1]); SB();
  of[0][1] = *(LDS_AS const bf16x8*)(ro + 19456);
  SB();
  MV16(as[2], fo[4], of[1][2]); SB();
  of[0][2] = *(LDS_AS const bf16x8*)(ro + 35840);
  SB();
  MV16(as[3], fo[4], of[1][3]); SB();
  of[0][3] = *(LDS_AS const bf16x8*)(ro + 52224);
  SB();
  SB();
  MV16(as[0], fo[5], of[2][0]); SB();
  of[1][0] = *(LDS_AS const bf16x8*)(roh + 3072);
  SB();
  MV16(as[1], fo[5], of[2][1]); SB();
  of[1][1] = *(LDS_AS const bf16x8*)(roh + 19456);
  SB();
  MV16(as[2], fo[5], of[2][2]); SB();
  of[1][2] = *(LDS_AS const bf16x8*)(roh + 35840);
  SB();
  MV16(as[3], fo[5], of[2][3]); SB();
  of[1][3] = *(LDS_AS const bf16x8*)(roh + 52224);
  SB();
  SB();
  MV16(as[0], fo[6], of[0][0]); SB();
  of[2][0] = *(LDS_AS const bf16x8*)(ro + 4096);
  SB();
  MV16(as[1], fo[6], of[0][1]); SB();
  of[2][1] = *(LDS_AS const bf16x8*)(ro + 20480);
  SB();
  MV16(as[2], fo[6], of[0][2]); SB();
  of[2][2] = *(LDS_AS const bf16x8*)(ro + 36864);
  SB();
  MV16(as[3], fo[6], of[0][3]); SB();
  of[2][3] = *(LDS_AS const bf16x8*)(ro + 53248);
  SB();
  SB();
  MV16(as[0], fo[7], of[1][0]); SB();
  of[0][0] = *(LDS_AS const bf16x8*)(roh + 4096);
  SB();
  MV16(as[1], fo[7], of[1][1]); SB();
  of[0][1] = *(LDS_AS const bf16x8*)(roh + 20480);
  SB();
  MV16(as[2], fo[7], of[1][2]); SB();
  of[0][2] = *(LDS_AS const bf16x8*)(roh + 36864);
  SB();
  MV16(as[3], fo[7], of[1][3]); SB();
  of[0][3] = *(LDS_AS const bf16x8*)(roh + 53248);
  SB();
  SB();
  MV16(as[0], fo[8], of[2][0]); SB();
  of[1][0] = *(LDS_AS const bf16x8*)(ro + 5120);
  SB();
  MV16(as[1], fo[8], of[2][1]); SB();
  of[1][1] = *(LDS_AS const bf16x8*)(ro + 21504);
  SB();
  MV16(as[2], fo[8], of[2][2]); SB();
  of[1][2] = *(LDS_AS const bf16x8*)(ro + 37888);
  SB();
  MV16(as[3], fo[8], of[2][3]); SB();
  of[1][3] = *(LDS_AS const bf16x8*)(ro + 54272);
  SB();
  SB();
  MV16(as[0], fo[9], of[0][0]); SB();
  of[2][0] = *(LDS_AS const bf16x8*)(roh + 5120);
  SB();
  MV16(as[1], fo[9], of[0][1]); SB();
  of[2][1] = *(LDS_AS const bf16x8*)(roh + 21504);
  SB();
  MV16(as[2], fo[9], of[0][2]); SB();
  of[2][2] = *(LDS_AS const bf16x8*)(roh + 37888);
  SB();
  MV16(as[3], fo[9], of[0][3]); SB();
  of[2][3] = *(LDS_AS const bf16x8*)(roh + 54272);
  SB();
  SB();
  MV16(as[0], fo[10], of[1][0]); SB();
  of[0][0] = *(LDS_AS const bf16x8*)(ro + 6144);
  SB();
  MV16(as[1], fo[10], of[1][1]); SB();
  of[0][1] = *(LDS_AS const bf16x8*)(ro + 22528);
  SB();
  MV16(as[2], fo[10], of[1][2]); SB();
  of[0][2] = *(LDS_AS const bf16x8*)(ro + 38912);
  SB();
  MV16(as[3], fo[10], of[1][3]); SB();
  of[0][3] = *(LDS_AS const bf16x8*)(ro + 55296);
  SB();
  SB();
  MV16(as[0], fo[11], of[2][0]); SB();
  of[1][0] = *(LDS_AS const bf16x8*)(roh + 6144);
  SB();
  MV16(as[1], fo[11], of[2][1]); SB();
  of[1][1] = *(LDS_AS const bf16x8*)(roh + 22528);
  SB();
  MV16(as[2], fo[11], of[2][2]); SB();
  of[1][2] = *(LDS_AS const bf16x8*)(roh + 38912);
  SB();
  MV16(as[3], fo[11], of[2][3]); SB();
  of[1][3] = *(LDS_AS const bf16x8*)(roh + 55296);
  SB();
  SB();
  MV16(as[0], fo[12], of[0][0]); SB();
  MV16(as[1], fo[12], of[0][1]); SB();
  MV16(as[2], fo[12], of[0][2]); SB();
  MV16(as[3], fo[12], of[0][3]); SB();
  SB();
  MV16(as[0], fo[13], of[1][0]); SB();
  MV16(as[1], fo[13], of[1][1]); SB();
  MV16(as[2], fo[13], of[1][2]); SB();
  MV16(as[3], fo[13], of[1][3]); SB();
  NOP(5); SB();
  { const auto r0 = __builtin_amdgcn_permlane16_swap(pack2(as[0][0], as[0][1]), pack2(as[1][0], as[1][1]), false, false);
    const auto r1 = __builtin_amdgcn_permlane16_swap(pack2(as[0][2], as[0][3]), pack2(as[1][2], as[1][3]), false, false);
    ST16(vS0, sS, 0, ((u32x4){r0[0], r1[0], r0[1], r1[1]})); } SB();
  { const auto r0 = __builtin_amdgcn_permlane16_swap(pack2(as[2][0], as[2][1]), pack2(as[3][0], as[3][1]), false, false);
    const auto r1 = __builtin_amdgcn_permlane16_swap(pack2(as[2][2], as[2][3]), pack2(as[3][2], as[3][3]), false, false);
    ST16(vS1, sS, 0, ((u32x4){r0[0], r1[0], r0[1], r1[1]})); } SB();
  STAMP(14);
  WAITV(0);
  ST16X(15, vL16, sFS, nonce);
  STAMP(15);
  }
  STAMP_DUMP();
  }}
  } else {
  WAITV(0);
  __builtin_amdgcn_s_sleep(40);
  poll1<16>(sFH, vFH, nonce);
  STAMP(3);
  __builtin_amdgcn_s_sleep(64);
  BAR();
  const unsigned vP0 = frag(0, lane), vP1 = frag(1, lane), vP2 = frag(2, lane), vP3 = frag(3, lane);
  bf16x8 fo[14];
  const char* sWo = A.ws + WS_WO + (size_t)(dg * 7 + (w < 7 ? w : 6)) * WO_B; unsigned long long mW = w < 7 ? ~0ull : 1ull; OPQ(sWo); OPQ(mW);
  LD16M(fo[0], vP0, sWo, mW);
  LD16M(fo[1], vP1, sWo, mW);
  LD16M(fo[2], vP2, sWo, mW);
  LD16M(fo[3], vP3, sWo, mW); sWo += 5120; OPQ(sWo);
  LD16M(fo[4], vP0, sWo, mW);
  LD16M(fo[5], vP1, sWo, mW);
  LD16M(fo[6], vP2, sWo, mW);
  LD16M(fo[7], vP3, sWo, mW); sWo += 5120; OPQ(sWo);
  LD16M(fo[8], vP0, sWo, mW);
  LD16M(fo[9], vP1, sWo, mW);
  LD16M(fo[10], vP2, sWo, mW);
  LD16M(fo[11], vP3, sWo, mW); sWo += 5120; OPQ(sWo);
  LD16M(fo[12], vP0, sWo, mW);
  LD16M(fo[13], vP1, sWo, mW);
  { int w_ = w; OPQ(w_); { const int w = w_ & 7, lane = lane_now(); const int n16 = lane & 15, g4 = lane >> 4, q4 = n16 >> 2, pq = lane & 3, r8 = lane >> 3; const unsigned vL16 = lane * 16;
  unsigned vO[7];
#pragma unroll
  for (int i = 0; i < 7; ++i) { vO[i] = line7(64 * i + 8 * w + r8) + (lane & 7) * 16; PINV(vO[i]); }
  const char* sO = A.ws + WS_OT + (size_t)st * OT_T;
  unsigned vFO = (((lane >> 2) & 7) * 8 + (lane & 3)) * FO, ro = lds0 + vL16, roh = ro + 65536;
  unsigned vS0 = pair3(q4) + (2 * (n16 & 3) + (g4 & 1)) * 32 + (g4 >> 1) * 16, vS1 = vS0 - pair3(q4) + pair3(4 + q4);
  const char* sFO = A.ws + WS_FLAGO + ((st >> 1) * 64 + (st & 1) * 4) * FO;
  const char* sS = A.ws + WS_ST + (size_t)(st * 4 + dg) * ST_T + w * 3072;
  const char* sFS = A.ws + WS_FLAGS + ((st * 4 + dg) * 8 + w) * FS;
  asm volatile("" : "+s"(sO), "+s"(sFO), "+s"(sS), "+s"(sFS)); asm volatile("" : "+v"(vFO), "+v"(ro), "+v"(roh), "+v"(vS0), "+v"(vS1));
  STAMP(11);
  if (w == 7) poll1<3>(sFO, vFO, nonce);
  BAR();
  STAMP(12);
  bf16x8 of[3][4]; f32x4 as[4];
  DMA1(vO[0], sO, ldsP, 0, "sc1");
  DMA1(vO[1], sO, ldsP, 1024, "sc1");
  DMA1(vO[2], sO, ldsP, 2048, "sc1");
  DMA1(vO[3], sO, ldsP, 3072, "sc1");
  DMA1(vO[4], sO, ldsP, 4096, "sc1");
  DMA1(vO[5], sO, ldsP, 5120, "sc1");
  DMA1(vO[6], sO, ldsP, 6144, "sc1");
  WAITV(4);
  BAR0();
  if (w == 7) {
  WAITV(0);
  BAR0();
  } else {
  of[0][0] = *(LDS_AS const bf16x8*)(ro + 0);
  of[0][1] = *(LDS_AS const bf16x8*)(ro + 16384);
  of[0][2] = *(LDS_AS const bf16x8*)(ro + 32768);
  of[0][3] = *(LDS_AS const bf16x8*)(ro + 49152);
  STAMP(13);
  of[1][0] = *(LDS_AS const bf16x8*)(roh + 0);
  of[1][1] = *(LDS_AS const bf16x8*)(roh + 16384);
  of[1][2] = *(LDS_AS const bf16x8*)(roh + 32768);
  of[1][3] = *(LDS_AS const bf16x8*)(roh + 49152);
  SB();
  MV16Z(as[0], fo[0], of[0][0]); SB();
  of[2][0] = *(LDS_AS const bf16x8*)(ro + 1024);
  SB();
  MV16Z(as[1], fo[0], of[0][1]); SB();
  of[2][1] = *(LDS_AS const bf16x8*)(ro + 17408);
  SB();
  MV16Z(as[2], fo[0], of[0][2]); SB();
  of[2][2] = *(LDS_AS const bf16x8*)(ro + 33792);
  SB();
  MV16Z(as[3], fo[0], of[0][3]); SB();
  of[2][3] = *(LDS_AS const bf16x8*)(ro + 50176);
  SB();
  SB();
  MV16(as[0], fo[1], of[1][0]); SB();
  of[0][0] = *(LDS_AS const bf16x8*)(roh + 1024);
  SB();
  MV16(as[1], fo[1], of[1][1]); SB();
  of[0][1] = *(LDS_AS const bf16x8*)(roh + 17408);
  SB();
  MV16(as[2], fo[1], of[1][2]); SB();
  of[0][2] = *(LDS_AS const bf16x8*)(roh + 33792);
  SB();
  MV16(as[3], fo[1], of[1][3]); SB();
  of[0][3] = *(LDS_AS const bf16x8*)(roh + 50176);
  SB();
  SB();
  MV16(as[0], fo[2], of[2][0]); SB();
  of[1][0] = *(LDS_AS const bf16x8*)(ro + 2048);
  SB();
  MV16(as[1], fo[2], of[2][1]); SB();
  of[1][1] = *(LDS_AS const bf16x8*)(ro + 18432);
  SB();
  MV16(as[2], fo[2], of[2][2]); SB();
  of[1][2] = *(LDS_AS const bf16x8*)(ro + 34816);
  SB();
  MV16(as[3], fo[2], of[2][3]); SB();
  of[1][3] = *(LDS_AS const bf16x8*)(ro + 51200);
  SB();
  SB();
  MV16(as[0], fo[3], of[0][0]); SB();
  of[2][0] = *(LDS_AS const bf16x8*)(roh + 2048);
  SB();
  MV16(as[1], fo[3], of[0][1]); SB();
  of[2][1] = *(LDS_AS const bf16x8*)(roh + 18432);
  SB();
  MV16(as[2], fo[3], of[0][2]); SB();
  of[2][2] = *(LDS_AS const bf16x8*)(roh + 34816);
  SB();
  MV16(as[3], fo[3], of[0][3]); SB();
  of[2][3] = *(LDS_AS const bf16x8*)(roh + 51200);
  SB();
  SB();
  MV16(as[0], fo[4], of[1][0]); SB();
  WAITV(0);
  BAR0();
  of[0][0] = *(LDS_AS const bf16x8*)(ro + 3072);
  SB();
  MV16(as[1], fo[4], of[1][1]); SB();
  of[0][1] = *(LDS_AS const bf16x8*)(ro + 19456);
  SB();
  MV16(as[2], fo[4], of[1][2]); SB();
  of[0][2] = *(LDS_AS const bf16x8*)(ro + 35840);
  SB();
  MV16(as[3], fo[4], of[1][3]); SB();
  of[0][3] = *(LDS_AS const bf16x8*)(ro + 52224);
  SB();
  SB();
  MV16(as[0], fo[5], of[2][0]); SB();
  of[1][0] = *(LDS_AS const bf16x8*)(roh + 3072);
  SB();
  MV16(as[1], fo[5], of[2][1]); SB();
  of[1][1] = *(LDS_AS const bf16x8*)(roh + 19456);
  SB();
  MV16(as[2], fo[5], of[2][2]); SB();
  of[1][2] = *(LDS_AS const bf16x8*)(roh + 35840);
  SB();
  MV16(as[3], fo[5], of[2][3]); SB();
  of[1][3] = *(LDS_AS const bf16x8*)(roh + 52224);
  SB();
  SB();
  MV16(as[0], fo[6], of[0][0]); SB();
  of[2][0] = *(LDS_AS const bf16x8*)(ro + 4096);
  SB();
  MV16(as[1], fo[6], of[0][1]); SB();
  of[2][1] = *(LDS_AS const bf16x8*)(ro + 20480);
  SB();
  MV16(as[2], fo[6], of[0][2]); SB();
  of[2][2] = *(LDS_AS const bf16x8*)(ro + 36864);
  SB();
  MV16(as[3], fo[6], of[0][3]); SB();
  of[2][3] = *(LDS_AS const bf16x8*)(ro + 53248);
  SB();
  SB();
  MV16(as[0], fo[7], of[1][0]); SB();
  of[0][0] = *(LDS_AS const bf16x8*)(roh + 4096);
  SB();
  MV16(as[1], fo[7], of[1][1]); SB();
  of[0][1] = *(LDS_AS const bf16x8*)(roh + 20480);
  SB();
  MV16(as[2], fo[7], of[1][2]); SB();
  of[0][2] = *(LDS_AS const bf16x8*)(roh + 36864);
  SB();
  MV16(as[3], fo[7], of[1][3]); SB();
  of[0][3] = *(LDS_AS const bf16x8*)(roh + 53248);
  SB();
  SB();
  MV16(as[0], fo[8], of[2][0]); SB();
  of[1][0] = *(LDS_AS const bf16x8*)(ro + 5120);
  SB();
  MV16(as[1], fo[8], of[2][1]); SB();
  of[1][1] = *(LDS_AS const bf16x8*)(ro + 21504);
  SB();
  MV16(as[2], fo[8], of[2][2]); SB();
  of[1][2] = *(LDS_AS const bf16x8*)(ro + 37888);
  SB();
  MV16(as[3], fo[8], of[2][3]); SB();
  of[1][3] = *(LDS_AS const bf16x8*)(ro + 54272);
  SB();
  SB();
  MV16(as[0], fo[9], of[0][0]); SB();
  of[2][0] = *(LDS_AS const bf16x8*)(roh + 5120);
  SB();
  MV16(as[1], fo[9], of[0][1]); SB();
  of[2][1] = *(LDS_AS const bf16x8*)(roh + 21504);
  SB();
  MV16(as[2], fo[9], of[0][2]); SB();
  of[2][2] = *(LDS_AS const bf16x8*)(roh + 37888);
  SB();
  MV16(as[3], fo[9], of[0][3]); SB();
  of[2][3] = *(LDS_AS const bf16x8*)(roh + 54272);
  SB();
  SB();
  MV16(as[0], fo[10], of[1][0]); SB();
  of[0][0] = *(LDS_AS const bf16x8*)(ro + 6144);
  SB();
  MV16(as[1], fo[10], of[1][1]); SB();
  of[0][1] = *(LDS_AS const bf16x8*)(ro + 22528);
  SB();
  MV16(as[2], fo[10], of[1][2]); SB();
  of[0][2] = *(LDS_AS const bf16x8*)(ro + 38912);
  SB();
  MV16(as[3], fo[10], of[1][3]); SB();
  of[0][3] = *(LDS_AS const bf16x8*)(ro + 55296);
  SB();
  SB();
  MV16(as[0], fo[11], of[2][0]); SB();
  of[1][0] = *(LDS_AS const bf16x8*)(roh + 6144);
  SB();
  MV16(as[1], fo[11], of[2][1]); SB();
  of[1][1] = *(LDS_AS const bf16x8*)(roh + 22528);
  SB();
  MV16(as[2], fo[11], of[2][2]); SB();
  of[1][2] = *(LDS_AS const bf16x8*)(roh + 38912);
  SB();
  MV16(as[3], fo[11], of[2][3]); SB();
  of[1][3] = *(LDS_AS const bf16x8*)(roh + 55296);
  SB();
  SB();
  MV16(as[0], fo[12], of[0][0]); SB();
  MV16(as[1], fo[12], of[0][1]); SB();
  MV16(as[2], fo[12], of[0][2]); SB();
  MV16(as[3], fo[12], of[0][3]); SB();
  SB();
  MV16(as[0], fo[13], of[1][0]); SB();
  MV16(as[1], fo[13], of[1][1]); SB();
  MV16(as[2], fo[13], of[1][2]); SB();
  MV16(as[3], fo[13], of[1][3]); SB();
  NOP(5); SB();
  { const auto r0 = __builtin_amdgcn_permlane16_swap(pack2(as[0][0], as[0][1]), pack2(as[1][0], as[1][1]), false, false);
    const auto r1 = __builtin_amdgcn_permlane16_swap(pack2(as[0][2], as[0][3]), pack2(as[1][2], as[1][3]), false, false);
    ST16(vS0, sS, 0, ((u32x4){r0[0], r1[0], r0[1], r1[1]})); } SB();
  { const auto r0 = __builtin_amdgcn_permlane16_swap(pack2(as[2][0], as[2][1]), pack2(as[3][0], as[3][1]), false, false);
    const auto r1 = __builtin_amdgcn_permlane16_swap(pack2(as[2][2], as[2][3]), pack2(as[3][2], as[3][3]), false, false);
    ST16(vS1, sS, 0, ((u32x4){r0[0], r1[0], r0[1], r1[1]})); } SB();
  STAMP(14);
  WAITV(0);
  ST16X(15, vL16, sFS, nonce);
  STAMP(15);
  }
  if (q == 0 && jb == 31 && w == 7) ST4X(0u, A.ws0, nonce);
  STAMP_DUMP();
  asm volatile(".Lcpf_e:");
  }}
  }
  }
}

extern "C" void kernel_launch(void* const* d_in, const int* in_sizes, int n_in,
                              void* d_out, int out_size, void* d_ws, size_t ws_size,
                              hipStream_t stream) {
  fused_kernel<<<256, 512, 0, stream>>>((const char*)d_in[0], (const char*)d_in[1], (const char*)d_in[2], (const char*)d_in[4],
                                        (const char*)d_in[5], (const char*)d_in[6], (const char*)d_in[7], (char*)d_ws, (char*)d_out);
}
```

```cpp
#include <hip/hip_runtime.h>
#include <hip/hip_bf16.h>
#include <stdint.h>

typedef short bf16x8 __attribute__((ext_vector_type(8)));
typedef short s16x4 __attribute__((ext_vector_type(4)));
typedef float f32x4 __attribute__((ext_vector_type(4)));
typedef float f32x16 __attribute__((ext_vector_type(16)));
typedef unsigned u32x4 __attribute__((ext_vector_type(4)));
typedef unsigned u32x2 __attribute__((ext_vector_type(2)));
typedef __bf16 bf16x2_t __attribute__((ext_vector_type(2)));
typedef float f32x2_t __attribute__((ext_vector_type(2)));
#define LDS_AS __attribute__((address_space(3)))
#define DEV __device__ __forceinline__

#define STAMP(i)
#define STAMP_DUMP()

#define SB() __builtin_amdgcn_sched_barrier(0)
#define WAITV(n) asm volatile("s_waitcnt vmcnt(" #n ")" ::: "memory")
#define WAITV1(n, a) asm volatile("s_waitcnt vmcnt(" #n ")" : "+v"(a) :: "memory")
#define WAITV2(n, a, b) asm volatile("s_waitcnt vmcnt(" #n ")" : "+v"(a), "+v"(b) :: "memory")
#define WAITV3(n, a, b, c) asm volatile("s_waitcnt vmcnt(" #n ")" : "+v"(a), "+v"(b), "+v"(c) :: "memory")
#define WAITV4(n, a, b, c, d) asm volatile("s_waitcnt vmcnt(" #n ")" : "+v"(a), "+v"(b), "+v"(c), "+v"(d) :: "memory")
#define WAITL(n) do { asm volatile("" ::: "memory"); __builtin_amdgcn_s_waitcnt(0xc07f | ((n) << 8)); asm volatile("" ::: "memory"); } while (0)
#define BAR() do { WAITL(0); __builtin_amdgcn_s_barrier(); asm volatile("" ::: "memory"); } while (0)
#define BAR0() do { asm volatile("s_barrier" ::: "memory"); } while (0)
#define OPQ(p) asm volatile("" : "+s"(p))
#define PINV(p) asm volatile("" : "+v"(p))
#define PINF(p) asm("" : "+v"(p))
#define PRIO(n) __builtin_amdgcn_s_setprio(n)
#define LD16(dst, voff, sb, imm, bits) asm volatile("global_load_dwordx4 %0, %1, %2 offset:" #imm " " bits : "=&v"(dst) : "v"(voff), "s"(sb) : "memory")
#define LD16M(dst, voff, sb, m) asm volatile("s_mov_b64 exec, %3\n\tglobal_load_dwordx4 %0, %1, %2 sc1\n\ts_mov_b64 exec, -1" : "=&v"(dst) : "v"(voff), "s"(sb), "s"(m) : "memory")
#define LD8(dst, voff, sb, imm, bits) asm volatile("global_load_dwordx2 %0, %1, %2 offset:" #imm " " bits : "=&v"(dst) : "v"(voff), "s"(sb) : "memory")
#define LD4(dst, voff, sb, imm, bits) asm volatile("global_load_dword %0, %1, %2 offset:" #imm " " bits : "=&v"(dst) : "v"(voff), "s"(sb) : "memory")
#define LD2(dst, voff, sb, imm, bits) asm volatile("global_load_ushort %0, %1, %2 offset:" #imm " " bits : "=&v"(dst) : "v"(voff), "s"(sb) : "memory")
#define DMA1(v0, sb, lds, limm, bits) \
  asm volatile("s_add_u32 m0, %2, %3\n\ts_nop 0\n\tglobal_load_lds_dwordx4 %0, %1 " bits :: "v"(v0), "s"(sb), "s"(lds), "n"(limm) : "memory", "scc")
#define DMA2(v0, v1, sb, lds, limm, bits) \
  asm volatile("s_add_u32 m0, %3, %4\n\ts_nop 0\n\tglobal_load_lds_dwordx4 %0, %2 " bits "\n\tglobal_load_lds_dwordx4 %1, %2 offset:1024 " bits \
               :: "v"(v0), "v"(v1), "s"(sb), "s"(lds), "n"(limm) : "memory", "scc")
#define TOUCH(v0, sb, lds) asm volatile("s_mov_b32 m0, %2\n\ts_nop 0\n\tglobal_load_lds_dword %0, %1 sc1" :: "v"(v0), "s"(sb), "s"(lds) : "memory")
#define ST16(voff, sb, imm, v) do { const u32x4 v_ = (v); asm volatile("global_store_dwordx4 %0, %1, %2 offset:" #imm " sc1\n\ts_nop 1" :: "v"(voff), "v"(v_), "s"(sb) : "memory"); } while (0)
#define ST16M(mask, voff, sb, v) do { const u32x4 v_ = (v); \
  asm volatile("s_mov_b32 exec_lo, %3\n\ts_mov_b32 exec_hi, %3\n\tglobal_store_dwordx4 %0, %1, %2 sc1\n\ts_mov_b64 exec, -1\n\ts_nop 0" :: "v"(voff), "v"(v_), "s"(sb), "s"(mask) : "memory"); } while (0)
#define ST16E(mask, voff, sb, v) do { const u32x4 v_ = (v); \
  asm volatile("s_mov_b32 exec_lo, " #mask "\n\ts_mov_b32 exec_hi, " #mask "\n\tglobal_store_dwordx4 %0, %1, %2 sc1\n\ts_mov_b64 exec, -1\n\ts_nop 0" :: "v"(voff), "v"(v_), "s"(sb) : "memory"); } while (0)
#define ST16L56(voff, sb, v) do { const u32x4 v_ = (v); \
  asm volatile("s_mov_b32 exec_hi, 0xffffff\n\tglobal_store_dwordx4 %0, %1, %2 sc1\n\ts_mov_b32 exec_hi, -1\n\ts_nop 0" :: "v"(voff), "v"(v_), "s"(sb) : "memory"); } while (0)
#define ST16L48(voff, sb, imm, v) do { const u32x4 v_ = (v); \
  asm volatile("s_mov_b32 exec_hi, 0xffff\n\tglobal_store_dwordx4 %0, %1, %2 offset:" #imm " sc1\n\ts_mov_b32 exec_hi, -1\n\ts_nop 0" :: "v"(voff), "v"(v_), "s"(sb) : "memory"); } while (0)
#define ST4(voff, sb, v) do { const unsigned v_ = (v); asm volatile("global_store_dword %0, %1, %2 sc1" :: "v"(voff), "v"(v_), "s"(sb) : "memory"); } while (0)
#define ST4M(mask, voff, sb) asm volatile("s_mov_b32 exec_lo, %2\n\ts_mov_b32 exec_hi, %2\n\tglobal_store_dword %0, %0, %1 sc1\n\ts_mov_b64 exec, -1" :: "v"(voff), "s"(sb), "s"(mask) : "memory")
#define ST4X(voff, sb, v) do { const unsigned v_ = (v); asm volatile("s_mov_b64 exec, 1\n\tglobal_store_dword %0, %1, %2 sc1\n\ts_mov_b64 exec, -1" :: "v"(voff), "v"(v_), "s"(sb) : "memory"); } while (0)
#define ST16X(mask, voff, sb, v) do { const u32x4 v_ = {(v), (v), (v), (v)}; asm volatile("s_mov_b64 exec, " #mask "\n\tglobal_store_dwordx4 %0, %1, %2 sc1\n\ts_mov_b64 exec, -1\n\ts_nop 0" :: "v"(voff), "v"(v_), "s"(sb) : "memory"); } while (0)

DEV float lo2f(unsigned v) { return __uint_as_float(v << 16); }
DEV float hi2f(unsigned v) { return __uint_as_float(v & 0xffff0000u); }
DEV unsigned pack2(float a, float b) { f32x2_t v = {a, b}; return __builtin_bit_cast(unsigned, __builtin_convertvector(v, bf16x2_t)); }
DEV float rbf(float a) { return __uint_as_float(pack2(0.f, a)); }
DEV f32x2_t rbf2(f32x2_t a) { return (f32x2_t){rbf(a[0]), rbf(a[1])}; }
DEV float dot2(unsigned a, unsigned b, float c) {
  return __builtin_amdgcn_fdot2_f32_bf16(__builtin_bit_cast(bf16x2_t, a), __builtin_bit_cast(bf16x2_t, b), c, false);
}
DEV bf16x8 cat8(s16x4 a, s16x4 b) { return __builtin_shufflevector(a, b, 0, 1, 2, 3, 4, 5, 6, 7); }
DEV bf16x8 tr8u(unsigned p, int second) {
  return cat8(__builtin_amdgcn_ds_read_tr16_b64_v4i16((LDS_AS s16x4*)p), __builtin_amdgcn_ds_read_tr16_b64_v4i16((LDS_AS s16x4*)(p + second)));
}
template <int CTRL> DEV float dpp_add(float v) {
  return v + __builtin_bit_cast(float, __builtin_amdgcn_update_dpp(0, __builtin_bit_cast(int, v), CTRL, 0xf, 0xf, true));
}
DEV float red32(float v) {
  v = dpp_add<0x140>(dpp_add<0x141>(dpp_add<0x4E>(dpp_add<0xB1>(v))));
  const auto r = __builtin_amdgcn_permlane16_swap(__float_as_uint(v), __float_as_uint(v), false, false);
  return __uint_as_float(r[0]) + __uint_as_float(r[1]);
}
DEV float fmx(float a, float b) { return __builtin_elementwise_maximum(a, b); }
DEV float xmax2(float a, float b) {
  const auto r = __builtin_amdgcn_permlane16_swap(__float_as_uint(a), __float_as_uint(b), false, false);
  const float v = fmx(__uint_as_float(r[0]), __uint_as_float(r[1]));
  const auto s = __builtin_amdgcn_permlane32_swap(__float_as_uint(v), __float_as_uint(v), false, false);
  return fmx(__uint_as_float(s[0]), __uint_as_float(s[1]));
}
DEV float xsum2(float a, float b) {
  const auto r = __builtin_amdgcn_permlane16_swap(__float_as_uint(a), __float_as_uint(b), false, false);
  const float v = __uint_as_float(r[0]) + __uint_as_float(r[1]);
  const auto s = __builtin_amdgcn_permlane32_swap(__float_as_uint(v), __float_as_uint(v), false, false);
  return __uint_as_float(s[0]) + __uint_as_float(s[1]);
}
DEV f32x2_t xbcast2(float v) {
  const auto r = __builtin_amdgcn_permlane16_swap(__float_as_uint(v), __float_as_uint(v), false, false);
  return (f32x2_t){__uint_as_float(r[0]), __uint_as_float(r[1])};
}
#define MFMA32(a, b, c) __builtin_amdgcn_mfma_f32_32x32x16_bf16(a, b, c, 0, 0, 0)
#define MFMA16(a, b, c) __builtin_amdgcn_mfma_f32_16x16x32_bf16(a, b, c, 0, 0, 0)
#define MV16(d, a, b) asm volatile("v_mfma_f32_16x16x32_bf16 %0, %1, %2, %0" : "+v"(d) : "v"(a), "v"(b))
#define MV16Z(d, a, b) asm volatile("v_mfma_f32_16x16x32_bf16 %0, %1, %2, 0" : "=&v"(d) : "v"(a), "v"(b))
#define MV32(d, a, b) asm volatile("v_mfma_f32_32x32x16_bf16 %0, %1, %2, %0" : "+v"(d) : "v"(a), "v"(b))
#define MV32Z(d, a, b) asm volatile("v_mfma_f32_32x32x16_bf16 %0, %1, %2, 0" : "=&v"(d) : "v"(a), "v"(b))
#define NOP(n) asm volatile("s_nop " #n)
DEV int lane_now() { int l; asm volatile("v_mbcnt_lo_u32_b32 %0, -1, 0\n\tv_mbcnt_hi_u32_b32 %0, -1, %0" : "=v"(l)); __builtin_assume(l >= 0 && l < 64); return l; }
#define SEL(h, i) __uint_as_float((m1[i] & __float_as_uint(sc[h][1][i])) | (~m1[i] & __float_as_uint(sc[h][0][i])))
#define Q4(a, i) (f32x4){a[4 * (i)], a[4 * (i) + 1], a[4 * (i) + 2], a[4 * (i) + 3]}

constexpr int kSpinCap = 20000;
template <int SLEEP> DEV void poll1(const char* sb, unsigned voff, unsigned nonce) {
#pragma unroll 1
  for (int spin = 0; spin < kSpinCap; ++spin) {
    __builtin_amdgcn_s_sleep(SLEEP);
    unsigned v;
    asm volatile("global_load_dword %0, %1, %2 sc1\n\ts_waitcnt vmcnt(0)" : "=&v"(v) : "v"(voff), "s"(sb) : "memory");
    if (__ballot(v != nonce) == 0ull) return;
  }
}
DEV unsigned bad4(const u32x4& v, unsigned n) { return (v[0] ^ n) | (v[1] ^ n) | (v[2] ^ n) | (v[3] ^ n); }
template <int SLEEP> DEV void poll4(const char* sb, unsigned voff, unsigned nonce) {
#pragma unroll 1
  for (int spin = 0; spin < kSpinCap; ++spin) {
    __builtin_amdgcn_s_sleep(SLEEP);
    u32x4 a;
    asm volatile("global_load_dwordx4 %0, %1, %2 sc1\n\ts_waitcnt vmcnt(0)" : "=&v"(a) : "v"(voff), "s"(sb) : "memory");
    if (__ballot(bad4(a, nonce) != 0u) == 0ull) return;
  }
}
#define LDS_ARRIVE(old, addr) do { unsigned v_; \
  asm volatile("s_mov_b64 exec, 1\n\tds_add_rtn_u32 %0, %2, %3\n\ts_mov_b64 exec, -1\n\ts_waitcnt lgkmcnt(0)\n\tv_readfirstlane_b32 %1, %0" \
               : "=&v"(v_), "=s"(old) : "v"(addr), "v"(1u) : "memory"); old = __builtin_amdgcn_readfirstlane(old); } while (0)
DEV unsigned frag(int h, int L) {
  return 512 + 1152 * h + L * 16 + (L >= 56 - 8 * h ? 128 : 0);
}

DEV unsigned line7(unsigned l) { return (l + ((l + 4) * 9363 >> 16)) * 128; }

DEV int pair3(int p) { const int g = (p * 11) >> 5, r = p - 3 * g; return g * 1024 + (r ? 256 + 256 * r : 0); }

DEV float ln_inv(float var) {
  const float sd = rbf(__builtin_amdgcn_sqrtf(rbf(rbf(fmaxf(var, 0.f)) + 1e-5f)));
  return __builtin_amdgcn_rcpf(sd);
}
DEV float red64(float v) {
  v = red32(v);
  const auto r = __builtin_amdgcn_permlane32_swap(__float_as_uint(v), __float_as_uint(v), false, false);
  return __uint_as_float(r[0]) + __uint_as_float(r[1]);
}
DEV void ln_1(u32x4& p, f32x2_t (&d)[4], bool act) {
  const float s = dot2(p[0], 0x3f803f80u, dot2(p[1], 0x3f803f80u, 0.f)) + dot2(p[2], 0x3f803f80u, dot2(p[3], 0x3f803f80u, 0.f));
  const float mean = red64(act ? s : 0.f) * (1.0f / 448.0f), mb = rbf(mean), dm = mean - mb;
  f32x2_t q = {0.f, 0.f};
#pragma unroll
  for (int i = 0; i < 4; ++i) { d[i] = (f32x2_t){lo2f(p[i]), hi2f(p[i])} - (f32x2_t){mb, mb}; q = d[i] * d[i] + q; }
  const float inv = ln_inv(red64(act ? q[0] + q[1] : 0.f) * (1.0f / 448.0f) - dm * dm);
#pragma unroll
  for (int i = 0; i < 4; ++i) { d[i] = rbf2(d[i]) * (f32x2_t){inv, inv}; p[i] = pack2(d[i][0], d[i][1]); }
}
template <bool EXACT> DEV void ln_2(u32x4& p, const f32x2_t (&d)[4], bool act) {
  const float s = dot2(p[0], 0x3f803f80u, dot2(p[1], 0x3f803f80u, 0.f)) + dot2(p[2], 0x3f803f80u, dot2(p[3], 0x3f803f80u, 0.f));
  const float q = dot2(p[0], p[0], dot2(p[1], p[1], 0.f)) + dot2(p[2], p[2], dot2(p[3], p[3], 0.f));
  const float mean = red64(act ? s : 0.f) * (1.0f / 448.0f), msq = red64(act ? q : 0.f) * (1.0f / 448.0f);
  const float inv = ln_inv(msq - mean * mean), mb = rbf(mean), c = -mb * inv;
#pragma unroll
  for (int i = 0; i < 4; ++i) {
    const f32x2_t r = EXACT ? rbf2((f32x2_t){lo2f(p[i]), hi2f(p[i])} - (f32x2_t){mb, mb}) * (f32x2_t){inv, inv}
                            : __builtin_elementwise_fma(d[i], (f32x2_t){inv, inv}, (f32x2_t){c, c});
    p[i] = pack2(r[0], r[1]);
  }
}
#define OP4_3(op, d, a, b) asm volatile(op " %0, %4, %8\n\t" op " %1, %5, %9\n\t" op " %2, %6, %10\n\t" op " %3, %7, %11" \
  : "=&v"(d[0]), "=&v"(d[1]), "=&v"(d[2]), "=v"(d[3]) : "v"(a[0]), "v"(a[1]), "v"(a[2]), "v"(a[3]), "v"(b[0]), "v"(b[1]), "v"(b[2]), "v"(b[3]))
#define ACC4(op, d, a) asm volatile(op " %0, %4, %0\n\t" op " %1, %5, %1\n\t" op " %2, %6, %2\n\t" op " %3, %7, %3" \
  : "+v"(d[0]), "+v"(d[1]), "+v"(d[2]), "+v"(d[3]) : "v"(a[0]), "v"(a[1]), "v"(a[2]), "v"(a[3]))
#define UN4(op, pre, post, d) asm volatile(op " %0, " pre "%0" post "\n\t" op " %1, " pre "%1" post "\n\t" op " %2, " pre "%2" post "\n\t" op " %3, " pre "%3" post \
  : "+v"(d[0]), "+v"(d[1]), "+v"(d[2]), "+v"(d[3]))
#define POLY4(d, c) asm volatile("v_fmamk_f32 %0, %0, 0x3dd2d3e8, %4\n\tv_fmamk_f32 %1, %1, 0x3dd2d3e8, %4\n\tv_fmamk_f32 %2, %2, 0x3dd2d3e8, %4\n\tv_fmamk_f32 %3, %3, 0x3dd2d3e8, %4" \
  : "+v"(d[0]), "+v"(d[1]), "+v"(d[2]), "+v"(d[3]) : "v"(c))
#define FMK4(d, k, c) asm volatile("v_fmamk_f32 %0, %0, " k ", %4\n\tv_fmamk_f32 %1, %1, " k ", %5\n\tv_fmamk_f32 %2, %2, " k ", %6\n\tv_fmamk_f32 %3, %3, " k ", %7" \
  : "+v"(d[0]), "+v"(d[1]), "+v"(d[2]), "+v"(d[3]) : "v"(c[0]), "v"(c[1]), "v"(c[2]), "v"(c[3]))
#define NFMA4(d, a) asm volatile("v_fma_f32 %0, -%0, %4, %0\n\tv_fma_f32 %1, -%1, %5, %1\n\tv_fma_f32 %2, -%2, %6, %2\n\tv_fma_f32 %3, -%3, %7, %3" \
  : "+v"(d[0]), "+v"(d[1]), "+v"(d[2]), "+v"(d[3]) : "v"(a[0]), "v"(a[1]), "v"(a[2]), "v"(a[3]))
#define SCL4(d, a) asm volatile("v_mul_f32 %0, 0xbfb8aa3b, %4\n\tv_mul_f32 %1, 0xbfb8aa3b, %5\n\tv_mul_f32 %2, 0xbfb8aa3b, %6\n\tv_mul_f32 %3, 0xbfb8aa3b, %7" \
  : "=&v"(d[0]), "=&v"(d[1]), "=&v"(d[2]), "=v"(d[3]) : "v"(a[0]), "v"(a[1]), "v"(a[2]), "v"(a[3]))
#define WAITVB(n) __builtin_amdgcn_s_waitcnt(0x0f70 | ((n) & 15) | (((n) >> 4) << 14))
DEV void xchg4(float (&a)[4], float (&b)[4]) {
#pragma unroll
  for (int i = 0; i < 4; ++i) {
    const int x = __float_as_int(a[i]), y = __float_as_int(b[i]);
    a[i] = __builtin_bit_cast(float, __builtin_amdgcn_update_dpp(x, y, 0x114, 0xf, 0xa, false));
    b[i] = __builtin_bit_cast(float, __builtin_amdgcn_update_dpp(y, x, 0x104, 0xf, 0x5, false));
  }
}
DEV unsigned fin2(unsigned s, float g0, float g1, f32x2_t x) {
  const f32x2_t r = __builtin_elementwise_fma((f32x2_t){lo2f(s), hi2f(s)}, (f32x2_t){g0, g1}, x);
  return pack2(r[0], r[1]);
}

constexpr int FA = 16, FU = 16, FO = 64, FS = 64;
constexpr size_t WS_FLAGA = 0;
constexpr size_t WS_FLAGU = WS_FLAGA + 256 * 128;
constexpr size_t WS_FLAGO = WS_FLAGU + 32 * 128 * 128;
constexpr size_t WS_FLAGS = WS_FLAGO + 1024 * 128;
constexpr size_t HT_W = 14 * 5120, UT_W = 13 * 5120, WQ_S = 24576, OT_T = 65536, WO_B = 4 * 5120, ST_T = 24576;
constexpr size_t WS_HT = WS_FLAGS + 1024 * 128, WS_XN = WS_HT + 32 * HT_W, WS_WQ = WS_XN + 2049 * 1024 + 3072, WS_UT = WS_WQ + 56 * WQ_S;
constexpr size_t WS_OT = WS_UT + 32 * UT_W, WS_WO = WS_OT + 32 * OT_T, WS_ST = WS_WO + 28 * WO_B;
static_assert(WS_HT % 1024 == 0 && WS_XN % 1024 == 0 && WS_WQ % 1024 == 0 && WS_UT % 1024 == 0 && WS_OT % 1024 == 0 && WS_WO % 1024 == 0 && WS_ST % 1024 == 0, "alignment");
constexpr int KROT = 2, KROT2 = 2;
constexpr int PW = 16384;
constexpr int TH = 8 * PW, TP = 960, TQ = TH + 8 * TP, TQP = 1088;
constexpr int L_CNT = TH + 896, L_DUMP = TQ + 8 * TQP;
constexpr int SG = 39936, SGX = 21504;
constexpr int TB = 3 * SG;
constexpr int WU = 8 * PW;
constexpr int SVB = 4608, SKH = 9216, SK = 4 * SVB, SQ = SK + 2 * SKH - 512;
constexpr int LDS_BYTES = 163840;

struct Args { const char *x, *wqkv, *wo, *w1, *b1, *w2, *b2; char *ws0, *ws, *out; };

__global__ __launch_bounds__(512, 2) void fused_kernel(const char* __restrict__ x, const char* __restrict__ wqkv, const char* __restrict__ wo,
                                                       const char* __restrict__ w1, const char* __restrict__ b1,
                                                       const char* __restrict__ w2, const char* __restrict__ b2,
                                                       char* __restrict__ ws, char* __restrict__ out) {
  __shared__ __attribute__((aligned(256))) char smem[LDS_BYTES];
  const int w = __builtin_amdgcn_readfirstlane(threadIdx.x >> 6);
  const int b = blockIdx.x, jb = b >> 3, q = (b >> 1) & 3, side = b & 1;
  STAMP(0);
  const unsigned lds0 = (unsigned)(size_t)(LDS_AS char*)smem;
  { const char* pc_; unsigned vo_ = (threadIdx.x * 32 + ((b >> 3) & 31)) * 128;
    asm volatile("s_getpc_b64 %0\n.Lcpf_b:\n\ts_mov_b32 m0, %2\n\tv_min_u32 %1, .Lcpf_e-.Lcpf_b-4, %1\n\tglobal_load_lds_dword %1, %0"
                 : "=&s"(pc_), "+v"(vo_) : "s"(lds0 + L_DUMP) : "memory"); }
  Args A = {x, wqkv, wo, w1, b1, w2, b2, ws, ws, out};
  const int lane = threadIdx.x & 63, ci = (b & 7) * 32 + (b >> 3);
  const unsigned vQ = (lane < 56 ? lane : 55) * 16;
  unsigned oX = (8 * ci + w) * 896;
  asm volatile("" : "+s"(oX) : "v"(vQ));
  asm volatile("" :: "s"(A.x), "s"(A.wqkv), "s"(A.wo), "s"(A.w1), "s"(A.b1), "s"(A.w2), "s"(A.b2), "s"(A.out), "s"(A.ws0));
  u32x4 xr;
  const char* sX = A.x + oX;
  LD16(xr, vQ, sX, 0, "");
  SB();
  PRIO(3);
  const int n16 = lane & 15, g4 = lane >> 4, q4 = n16 >> 2, pq = lane & 3, r8 = lane >> 3; const unsigned vL16 = lane * 16;
  const int dg = jb & 3, pk = jb < 4 ? 0 : (2 * q + side) * 28 + jb - 4, qp = (pk * 293) >> 14, kg = pk - 56 * qp;
  const unsigned ldsP = lds0 + w * PW;
  { const int oh = (kg * 37) >> 8, oj = kg - 7 * oh;
    int oq_ = (8 * kg + w) * 2688 + qp * 896, oo_ = (56 * oh + 4 * oj + (w & 3) + 28 * (w >> 2)) * 896; OPQ(oq_); OPQ(oo_);
    const char* sQ = (qp < 3 ? A.wqkv : A.wo) + (qp < 3 ? oq_ : oo_);
    DMA1(vQ, sQ, lds0 + w * TQP, TQ, ""); }
  unsigned nonce; asm volatile("s_load_dword %0, %1, 0x0" : "=&s"(nonce) : "s"(A.ws0) : "memory");
  const int t7 = (jb < 28 ? jb >> 2 : 1) + ((3 - (int)((size_t)A.w1 >> 7) - 7 * q) & 3), ft = 7 * q + (t7 >= 7 ? t7 - 7 : t7);
  const int swp = 0 * (((side ? 35 - jb : jb + 4) >> 5) & 1), is2 = side | swp;
  const int st = 8 * ((q + (side ? 0 : 0) + swp) & 3) + ((((jb >> 2) ^ (7 * swp)) + 3 - (int)((size_t)A.w2 >> 7)) & 7);
  const int rot = side ? KROT2 * dg : KROT * dg + ((jb + 4) >> 5), ld = 3584 + 512 * side;
  int wrap = 7 - side - rot, dW = 16 * ld;
  unsigned vWa = r8 * ld + ((lane & 7) ^ (r8 & 2)) * 16, vWb = (vWa ^ 64) + (8 * ld - 1024);
  vWa &= (side & swp) - 1; vWb &= (side & swp) - 1;
  const char* bW = (side ? A.w2 : A.w1) + ((side ? st : ft) * 128u + w * (256u * 3584u));
  const char* sW = bW + (unsigned)(rot * 2 * dW);
  asm volatile("" : "+s"(wrap), "+s"(dW), "+s"(bW), "+s"(sW) : "v"(vWa), "v"(vWb));
  const unsigned ldsW = lds0 + (w << (14 - side * 1));
  OPQ(A.ws); A.ws = (char*)(((size_t)A.ws + 8191) & ~(size_t)4095);
  *(LDS_AS unsigned*)(lds0 + L_CNT) = 0u;
  unsigned vH, tH, vq, tq, hw = lds0 + TH + w * TP + vQ;
  const int ks = kg >> 2;
  { const int task = w < 7 ? w : 6, bk = 4 * task + g4, d4 = (bk * 37) >> 8, b7 = bk - 7 * d4;
    const int pos = ((ci >> 2) - KROT * d4) & 7, ix = (b7 < 6 ? 8 * b7 + 8 : 0) + pos;
    vH = d4 * (8 * (unsigned)HT_W) + (ix >> 2) * 5120 + frag(ix & 3, 16 * (ci & 3) + n16);
    tH = lds0 + TH + q4 * TP + bk * 32 + pq * 8;
    const int ti = w ? w - 1 : 0, c0 = 64 * ti + lane, hp0 = (c0 * 1171) >> 16, cc = c0 - 56 * hp0, kk = (kg >> 2) & 1, g_ = kg & 3;
    const unsigned off = (cc >> 4) * 6144 + qp * 1024 + (cc < 48 ? kk * 3072 + g_ * 256 : kk * 512 + g_ * 128) + (cc & 15) * 16;
    unsigned a_ = hp0 * (7 * (unsigned)WQ_S) + line7(off >> 7) + (off & 127), b_ = (4 * ti + g4) * (unsigned)WO_B + frag(ks & 3, 16 * g_ + n16);
    PINV(a_); PINV(b_);
    vq = qp < 3 ? a_ : b_;
    tq = lds0 + TQ + q4 * TQP + ti * 128 + g4 * 32 + pq * 8; }
  unsigned wq_ = WS_WQ + (kg >> 3) * WQ_S, wo_ = WS_WO + (ks >> 2) * 5120; OPQ(wq_); OPQ(wo_);
  const char* sN = A.ws + WS_XN + 512 + (size_t)(8 * ci + w) * 1024;
  const char* sH = A.ws + WS_HT + (size_t)(ci >> 5) * HT_W;
  const char* sWq = A.ws + (qp < 3 ? wq_ : wo_);
  const char* sFA = A.ws + WS_FLAGA + ci * FA;
  int j_ = 3 - jb, n_ = -w; OPQ(j_); OPQ(n_);
  int mH = (w - 7) >> 31, mQ = (j_ & n_) >> 31;
  asm volatile("" : "+s"(mH), "+s"(mQ), "+s"(sN), "+s"(sH), "+s"(sWq), "+s"(sFA)); asm volatile("" : "+v"(vH), "+v"(tH), "+v"(vq), "+v"(tq), "+v"(hw));
  u32x2 bv; { const char* sB = is2 ? A.b2 + st * 128 : A.b1 + ft * 128; const unsigned vB = is2 ? n16 * 8 : ((w & 3) * 16 + 4 * g4) * 2;
    LD8(bv, vB, sB, 0, ""); }
  PRIO(0);
  DMA2(vWa, vWb, sW, ldsW, 0, ""); sW += dW; OPQ(sW);
  DMA2(vWa, vWb, sW, ldsW, 2048, ""); sW = wrap == 0 ? bW : sW + dW; OPQ(sW);
  PRIO(3);
  WAITV1(6, xr);
  STAMP(24);
  f32x2_t dd[4];
  ln_1(xr, dd, lane < 56);
  ST16L56(vQ, sN, xr);
  ln_2<true>(xr, dd, lane < 56);
  *(LDS_AS u32x4*)hw = xr;
  WAITV(6);
  asm volatile("s_waitcnt lgkmcnt(0)" : "+s"(nonce) :: "memory"); nonce += 1u;
  BAR();
  STAMP(25);
  { const bf16x8 h0 = tr8u(tH, 4 * TP), q0 = tr8u(tq, 4 * TQP);
    ST16M(mH, vH, sH, __builtin_bit_cast(u32x4, h0));
    ST16M(mQ, vq, sWq, __builtin_bit_cast(u32x4, q0)); }
  SB();
  PRIO(0);
  DMA2(vWa, vWb, sW, ldsW, 4096, ""); sW += dW; OPQ(sW);
  PRIO(3);
  STAMP(26);
  unsigned fh; const char* sFH = A.ws + WS_FLAGA + w * (32 * FA); unsigned vFH = (lane & 31) * FA;
  asm volatile("" : "+s"(sFH)); asm volatile("" : "+v"(vFH));
  WAITV1(2, bv);
  BAR0(); if (w == 0) ST16X(1, vL16, sFA, nonce);
  PRIO(0);
  STAMP(27);

  if (__builtin_expect(side, 1)) {
  const unsigned ldsU = lds0 + WU + w * 4096, ldsL = lds0 + 2 * SG + w * 4096; bf16x8 fw[7][4];
  DMA2(vWa, vWb, sW, ldsW, 6144, ""); sW = wrap == 1 ? bW : sW + dW; OPQ(sW);
  unsigned rW0 = ldsW + (8 * g4 + q4) * 128 + (((q4 >> 1) | (2 * (g4 & 1))) * 32) + pq * 8, rW1 = rW0 ^ 32, rW2 = rW0 ^ 64, rW3 = rW0 ^ 96;
  asm volatile("" : "+v"(rW0), "+v"(rW1), "+v"(rW2), "+v"(rW3));
  unsigned rL0 = ldsL + (8 * g4 + q4) * 128 + (((q4 >> 1) | (2 * (g4 & 1))) * 32) + pq * 8, rL1 = rL0 ^ 32, rL2 = rL0 ^ 64, rL3 = rL0 ^ 96;
  asm volatile("" : "+v"(rL0), "+v"(rL1), "+v"(rL2), "+v"(rL3));
  unsigned rU0 = ldsU + (8 * g4 + q4) * 128 + (((q4 >> 1) | (2 * (g4 & 1))) * 32) + pq * 8, rU1 = rU0 ^ 32, rU2 = rU0 ^ 64, rU3 = rU0 ^ 96;
  asm volatile("" : "+v"(rU0), "+v"(rU1), "+v"(rU2), "+v"(rU3));
  const int tt = 4 * q + (jb >> 3), hp = jb & 7, t0 = 128 * tt, cb = w & 3;
  unsigned vD[5];
#pragma unroll
  for (int m = 0; m < 5; ++m) {
    const int i = m == 4 && w == 7 ? 31 : w + 8 * m, row = 8 * (i - 21) + r8, tok = t0 - 16 + row;
    unsigned a_ = line7(8 * i + r8) + (lane & 7) * 16, b_ = (tok < 0 ? 0 : tok) * 1024 + ((lane & 7) ^ ((row >> 1) & 7)) * 16;
    vD[m] = m < 2 ? a_ : m > 2 ? b_ : i < 21 ? a_ : b_;
    PINV(vD[m]);
  }
  const char* sDq = A.ws + WS_WQ + (size_t)hp * (7 * WQ_S); const char* sDx = A.ws + WS_XN + 512; const char* sDm = w < 5 ? sDq : sDx;
  int dm = w < 5 ? (int)WQ_S : 128;
  unsigned ldsD = lds0 + w * 1024, ldsE = w == 7 ? lds0 + 31 * 1024 : ldsD + 32768;
  asm volatile("" : "+s"(sDq), "+s"(sDx), "+s"(sDm), "+s"(dm), "+s"(ldsD), "+s"(ldsE));
  WAITV(5);
  fw[0][0] = tr8u(rW0 + 0, 512);
  fw[0][1] = tr8u(rW1 + 0, 512);
  fw[0][2] = tr8u(rW2 + 0, 512);
  fw[0][3] = tr8u(rW3 + 0, 512);
  WAITL(0);
  DMA2(vWa, vWb, sW, ldsW, 0, ""); sW += dW; OPQ(sW);
  DMA2(vWa, vWb, sW, ldsW, 2048, ""); sW = wrap == 2 ? bW : sW + dW; OPQ(sW);
  u32x4 tb0; u32x2 tb4; unsigned tba = lds0 + TB + (w < 4 ? w : w + 4) * 1024 + vL16, tbb = lds0 + TB + 4096 + w * 512 + lane * 8;
  { const int dk = 4 * g4 - n16;
    u32x4 m_;
#pragma unroll
    for (int i = 0; i < 4; ++i) m_[i] = (unsigned)((dk + i + 7) >> 31);
    tb0 = m_;
    if ((w & 3) == 0) {
#pragma unroll
      for (int i = 0; i < 4; ++i) tb0[i] = ~m_[i] & ((unsigned)(dk + i - 1) < 8u ? (w && lane < 32 ? 0xce6e0000u : 0u) : 0xff800000u);
    } else if (w == 2) {
      tb0 = (u32x4){frag(0, lane), frag(1, lane), frag(2, lane), frag(3, lane)};
    } else if (w == 3) {
      tb0 = (u32x4){(m_[0] & 0xffffu) | (m_[1] & 0xffff0000u), (m_[2] & 0xffffu) | (m_[3] & 0xffff0000u),
                    lds0 + SG + n16 * 64 + ((g4 ^ q4) << 4), lds0 + SG + (4 * g4 + q4) * 32 + pq * 8};
    } else if (w == 5) {
      tb0 = (u32x4){n16 * 128u + ((g4 ^ (n16 & 7)) << 4), r8 * 128u + (((2 * pq + ((lane >> 2) & 1)) ^ r8) << 4), r8 * 896u + (lane & 7) * 16,
                    ((lane & 7) >> 1) * 3072u + r8 * 32 + (lane & 1) * 16};
    }
    if (w < 4) {
      const int ga = 7 * hp + g4;
      const unsigned l0 = ((ga >> 2) * 4 + cb) * 8 + (ga & 3) * 2 + (n16 >> 3);
      tb4 = (u32x2){line7(l0) + (n16 & 7) * 16, line7(l0 + 32) + (n16 & 7) * 16};
    } else {
      const int c = 16 * cb + 4 * g4;
      const bool ok = c < 56;
      const int hd = ok ? (c >= 28) : (g4 & 1), d = ok ? c - 28 * hd : 28, row = (n16 >> 1) + 72 * (n16 & 1);
      tb4 = (u32x2){lds0 + SG + hd * SKH + row * 64 + (((d >> 3) ^ (n16 >> 3) ^ (2 * (n16 & 1))) << 4) + ((d >> 2) & 1) * 8,
                    lds0 + SG + (2 * hd + (d >> 4)) * SVB + row * 32 + (d & 15) * 2};
    } }
  unsigned xr0 = lds0 + SGX + n16 * 128 + ((g4 ^ (n16 >> 1)) << 4), xr1 = xr0 ^ 64;
  unsigned wr0 = lds0 + cb * 6144 + (cb < 3 ? vL16 : g4 * 128 + (n16 & 7) * 16);
  asm volatile("" : "+v"(tb0), "+v"(tb4), "+v"(tba), "+v"(tbb), "+v"(xr0), "+v"(xr1), "+v"(wr0));
  unsigned oP_ = WS_UT + (dg * 8 + w) * UT_W, oW_ = WS_WO + (dg * 7 + (w < 7 ? w : 6)) * WO_B; OPQ(oP_); OPQ(oW_);
  const char* sP = A.ws + (swp & 1 ? oW_ : oP_);
  const char* sFU = A.ws + WS_FLAGU + (dg * 8 + w) * (128 * FU);
  const char* sO = A.ws + WS_OT + (size_t)(2 * tt + (w >> 2)) * OT_T;
  const char* sFO = A.ws + WS_FLAGO + ((tt * 8 + hp) * 8 + w) * FO;
  const char* sS = A.ws + WS_ST + (size_t)(st * 4 + dg) * ST_T + pair3(w);
  const char* sFS = A.ws + WS_FLAGS + (st * 4 + dg) * (8 * FS);
  const char* sXo = A.x + ((64 * st + 8 * w) * 896 + dg * 224); const char* sOut = A.out + ((64 * st + 8 * w) * 896 + dg * 224);
  int kx = 32 * (w & 1), r0 = 72 * (w & 1) + 16 * (w >> 1), cmo = t0 + (w >> 1) == 0 ? 8192 : 0, rao = lds0 + w * 1024, bio = lds0 + TB + 10496 + 32 * w;
  asm volatile("" : "+s"(sP), "+s"(sFU), "+s"(sO), "+s"(sFO), "+s"(sS), "+s"(sFS), "+s"(sXo), "+s"(sOut), "+s"(kx), "+s"(r0), "+s"(cmo), "+s"(rao), "+s"(bio));
  u32x4 t2, t3, m1; f32x4 cm; u32x2 vS; unsigned rk, rv, mq_, vFU;
  *(LDS_AS u32x2*)(tbb) = tb4;
  if (w < 6) *(LDS_AS u32x4*)(tba) = tb0; else *(LDS_AS u32x4*)(lds0 + TB + 10496 + n16 * 16) = (u32x4){bv[0] << 16, bv[0] & 0xffff0000u, bv[1] << 16, bv[1] & 0xffff0000u};
  SB();
  WAITV(4);
  fw[1][0] = tr8u(rW0 + 4096, 512);
  fw[1][1] = tr8u(rW1 + 4096, 512);
  fw[1][2] = tr8u(rW2 + 4096, 512);
  fw[1][3] = tr8u(rW3 + 4096, 512);
  WAITL(0);
  DMA2(vWa, vWb, sW, ldsW, 4096, ""); sW += dW; OPQ(sW);
  DMA2(vWa, vWb, sW, ldsW, 6144, ""); sW = wrap == 3 ? bW : sW + dW; OPQ(sW);
  WAITV(4);
  fw[2][0] = tr8u(rW0 + 0, 512);
  fw[2][1] = tr8u(rW1 + 0, 512);
  fw[2][2] = tr8u(rW2 + 0, 512);
  fw[2][3] = tr8u(rW3 + 0, 512);
  STAMP(33);
  LD4(fh, vFH, sFH, 0, "sc1");
  DMA2(vWa, vWb, sW, ldsL, 0, ""); sW += dW; OPQ(sW);
  DMA2(vWa, vWb, sW, ldsL, 2048, ""); sW = wrap == 4 ? bW : sW + dW; OPQ(sW);
  WAITV(5);
  fw[3][0] = tr8u(rW0 + 4096, 512);
  fw[3][1] = tr8u(rW1 + 4096, 512);
  fw[3][2] = tr8u(rW2 + 4096, 512);
  fw[3][3] = tr8u(rW3 + 4096, 512);
  DMA2(vWa, vWb, sW, ldsU, 0, ""); sW += dW; OPQ(sW);
  DMA2(vWa, vWb, sW, ldsU, 2048, ""); sW = wrap == 5 ? bW : sW + dW; OPQ(sW);
  WAITV1(8, fh);
  if (__ballot(fh != nonce) != 0ull) poll1<4>(sFH, vFH, nonce);
  BAR();
  STAMP(34);
  DMA1(vD[0], sDq, ldsD, 0, "sc1");
  DMA1(vD[1], sDq, ldsD, 8192, "sc1"); sDq += WQ_S; OPQ(sDq);
  DMA1(vD[2], sDm, ldsD, 16384, "sc1"); sDm += dm; OPQ(sDm);
  DMA1(vD[3], sDx, ldsD, 24576, "sc1");
  DMA1(vD[4], sDx, ldsE, 0, "sc1"); sDx += 128; OPQ(sDx);
  BAR0();
  DMA1(vD[0], sDq, ldsD, 39936, "sc1");
  DMA1(vD[1], sDq, ldsD, 48128, "sc1"); sDq += WQ_S; OPQ(sDq);
  DMA1(vD[2], sDm, ldsD, 56320, "sc1"); sDm += dm; OPQ(sDm);
  DMA1(vD[3], sDx, ldsD, 64512, "sc1");
  DMA1(vD[4], sDx, ldsE, 39936, "sc1"); sDx += 128; OPQ(sDx);
  WAITV(14);
  fw[4][0] = tr8u(rL0 + 0, 512);
  fw[4][1] = tr8u(rL1 + 0, 512);
  fw[4][2] = tr8u(rL2 + 0, 512);
  fw[4][3] = tr8u(rL3 + 0, 512);
  WAITV(10);
  fw[5][0] = tr8u(rU0 + 0, 512);
  fw[5][1] = tr8u(rU1 + 0, 512);
  fw[5][2] = tr8u(rU2 + 0, 512);
  fw[5][3] = tr8u(rU3 + 0, 512);
  if (w < 3) {
  f32x4 aq[5], ak[5], av[5];
  bf16x8 xf[2][5], wf[2][5];
  unsigned sk0, sk1, sv;
  WAITV(5);
  BAR();
  STAMP(35);
  wf[0][1] = *(LDS_AS const bf16x8*)(wr0 + 1024);
  xf[0][1] = *(LDS_AS const bf16x8*)(xr0 + 2048);
  wf[0][2] = *(LDS_AS const bf16x8*)(wr0 + 2048);
  wf[0][0] = *(LDS_AS const bf16x8*)(wr0 + 0);
  xf[0][2] = *(LDS_AS const bf16x8*)(xr0 + 4096);
  xf[0][3] = *(LDS_AS const bf16x8*)(xr0 + 6144);
  xf[0][4] = *(LDS_AS const bf16x8*)(xr0 + 8192);
  SB();
  MV16Z(ak[1], wf[0][1], xf[0][1]); SB();
  wf[1][1] = *(LDS_AS const bf16x8*)(wr0 + 4096);
  SB();
  MV16Z(av[1], wf[0][2], xf[0][1]); SB();
  xf[1][1] = *(LDS_AS const bf16x8*)(xr1 + 2048);
  SB();
  MV16Z(aq[1], wf[0][0], xf[0][1]); SB();
  wf[1][2] = *(LDS_AS const bf16x8*)(wr0 + 5120);
  SB();
  MV16Z(ak[2], wf[0][1], xf[0][2]); SB();
  wf[1][0] = *(LDS_AS const bf16x8*)(wr0 + 3072);
  SB();
  MV16Z(av[2], wf[0][2], xf[0][2]); SB();
  xf[1][2] = *(LDS_AS const bf16x8*)(xr1 + 4096);
  SB();
  MV16Z(aq[2], wf[0][0], xf[0][2]); SB();
  xf[1][3] = *(LDS_AS const bf16x8*)(xr1 + 6144);
  SB();
  MV16Z(ak[3], wf[0][1], xf[0][3]); SB();
  xf[1][4] = *(LDS_AS const bf16x8*)(xr1 + 8192);
  SB();
  MV16Z(av[3], wf[0][2], xf[0][3]); SB();
  DMA1(vD[0], sDq, ldsD, 79872, "sc1");
  SB();
  MV16Z(aq[3], wf[0][0], xf[0][3]); SB();
  DMA1(vD[1], sDq, ldsD, 88064, "sc1"); sDq += WQ_S; OPQ(sDq);
  SB();
  MV16Z(ak[4], wf[0][1], xf[0][4]); SB();
  DMA1(vD[2], sDm, ldsD, 96256, "sc1"); sDm += dm; OPQ(sDm);
  SB();
  MV16Z(av[4], wf[0][2], xf[0][4]); SB();
  DMA1(vD[3], sDx, ldsD, 104448, "sc1");
  SB();
  MV16Z(aq[4], wf[0][0], xf[0][4]); SB();
  DMA1(vD[4], sDx, ldsE, 79872, "sc1"); sDx += 128; OPQ(sDx);
  SB();
  SB();
  MV16(ak[1], wf[1][1], xf[1][1]); SB();
  SB();
  MV16(av[1], wf[1][2], xf[1][1]); SB();
  WAITV(5);
  BAR();
  xr0 += 39936; wr0 += 39936; asm volatile("" : "+v"(xr0), "+v"(wr0));
  SB();
  MV16(aq[1], wf[1][0], xf[1][1]); SB();
  wf[0][1] = *(LDS_AS const bf16x8*)(wr0 + 1024);
  SB();
  MV16(ak[2], wf[1][1], xf[1][2]); SB();
  xf[0][1] = *(LDS_AS const bf16x8*)(xr0 + 2048);
  SB();
  MV16(av[2], wf[1][2], xf[1][2]); SB();
  wf[0][2] = *(LDS_AS const bf16x8*)(wr0 + 2048);
  SB();
  MV16(aq[2], wf[1][0], xf[1][2]); SB();
  wf[0][0] = *(LDS_AS const bf16x8*)(wr0 + 0);
  SB();
  MV16(ak[3], wf[1][1], xf[1][3]); SB();
  xf[0][2] = *(LDS_AS const bf16x8*)(xr0 + 4096);
  SB();
  MV16(av[3], wf[1][2], xf[1][3]); SB();
  xf[0][3] = *(LDS_AS const bf16x8*)(xr0 + 6144);
  SB();
  MV16(aq[3], wf[1][0], xf[1][3]); SB();
  xf[0][4] = *(LDS_AS const bf16x8*)(xr0 + 8192);
  SB();
  MV16(ak[4], wf[1][1], xf[1][4]); SB();
  MV16(av[4], wf[1][2], xf[1][4]); SB();
  MV16(aq[4], wf[1][0], xf[1][4]); SB();
  SB();
  MV16(ak[1], wf[0][1], xf[0][1]); SB();
  xr1 += 39936; PINV(xr1);
  wf[1][1] = *(LDS_AS const bf16x8*)(wr0 + 4096);
  SB();
  MV16(av[1], wf[0][2], xf[0][1]); SB();
  xf[1][1] = *(LDS_AS const bf16x8*)(xr1 + 2048);
  SB();
  MV16(aq[1], wf[0][0], xf[0][1]); SB();
  wf[1][2] = *(LDS_AS const bf16x8*)(wr0 + 5120);
  SB();
  MV16(ak[2], wf[0][1], xf[0][2]); SB();
  wf[1][0] = *(LDS_AS const bf16x8*)(wr0 + 3072);
  SB();
  MV16(av[2], wf[0][2], xf[0][2]); SB();
  xf[1][2] = *(LDS_AS const bf16x8*)(xr1 + 4096);
  SB();
  MV16(aq[2], wf[0][0], xf[0][2]); SB();
  xf[1][3] = *(LDS_AS const bf16x8*)(xr1 + 6144);
  SB();
  MV16(ak[3], wf[0][1], xf[0][3]); SB();
  xf[1][4] = *(LDS_AS const bf16x8*)(xr1 + 8192);
  SB();
  MV16(av[3], wf[0][2], xf[0][3]); SB();
  DMA1(vD[0], sDq, ldsD, 0, "sc1");
  SB();
  MV16(aq[3], wf[0][0], xf[0][3]); SB();
  DMA1(vD[1], sDq, ldsD, 8192, "sc1"); sDq += WQ_S; OPQ(sDq);
  SB();
  MV16(ak[4], wf[0][1], xf[0][4]); SB();
  DMA1(vD[2], sDm, ldsD, 16384, "sc1"); sDm += dm; OPQ(sDm);
  SB();
  MV16(av[4], wf[0][2], xf[0][4]); SB();
  DMA1(vD[3], sDx, ldsD, 24576, "sc1");
  SB();
  MV16(aq[4], wf[0][0], xf[0][4]); SB();
  DMA1(vD[4], sDx, ldsE, 0, "sc1"); sDx += 128; OPQ(sDx);
  SB();
  SB();
  MV16(ak[1], wf[1][1], xf[1][1]); SB();
  SB();
  MV16(av[1], wf[1][2], xf[1][1]); SB();
  WAITV(5);
  BAR();
  xr0 += 39936; wr0 += 39936; asm volatile("" : "+v"(xr0), "+v"(wr0));
  STAMP(60);
  SB();
  MV16(aq[1], wf[1][0], xf[1][1]); SB();
  wf[0][1] = *(LDS_AS const bf16x8*)(wr0 + 1024);
  SB();
  MV16(ak[2], wf[1][1], xf[1][2]); SB();
  xf[0][1] = *(LDS_AS const bf16x8*)(xr0 + 2048);
  SB();
  MV16(av[2], wf[1][2], xf[1][2]); SB();
  wf[0][2] = *(LDS_AS const bf16x8*)(wr0 + 2048);
  SB();
  MV16(aq[2], wf[1][0], xf[1][2]); SB();
  wf[0][0] = *(LDS_AS const bf16x8*)(wr0 + 0);
  SB();
  MV16(ak[3], wf[1][1], xf[1][3]); SB();
  xf[0][2] = *(LDS_AS const bf16x8*)(xr0 + 4096);
  SB();
  MV16(av[3], wf[1][2], xf[1][3]); SB();
  xf[0][3] = *(LDS_AS const bf16x8*)(xr0 + 6144);
  SB();
  MV16(aq[3], wf[1][0], xf[1][3]); SB();
  xf[0][4] = *(LDS_AS const bf16x8*)(xr0 + 8192);
  SB();
  MV16(ak[4], wf[1][1], xf[1][4]); SB();
  MV16(av[4], wf[1][2], xf[1][4]); SB();
  MV16(aq[4], wf[1][0], xf[1][4]); SB();
  SB();
  MV16(ak[1], wf[0][1], xf[0][1]); SB();
  xr1 += 39936; PINV(xr1);
  wf[1][1] = *(LDS_AS const bf16x8*)(wr0 + 4096);
  SB();
  MV16(av[1], wf[0][2], xf[0][1]); SB();
  xf[1][1] = *(LDS_AS const bf16x8*)(xr1 + 2048);
  SB();
  MV16(aq[1], wf[0][0], xf[0][1]); SB();
  wf[1][2] = *(LDS_AS const bf16x8*)(wr0 + 5120);
  SB();
  MV16(ak[2], wf[0][1], xf[0][2]); SB();
  wf[1][0] = *(LDS_AS const bf16x8*)(wr0 + 3072);
  SB();
  MV16(av[2], wf[0][2], xf[0][2]); SB();
  xf[1][2] = *(LDS_AS const bf16x8*)(xr1 + 4096);
  SB();
  MV16(aq[2], wf[0][0], xf[0][2]); SB();
  xf[1][3] = *(LDS_AS const bf16x8*)(xr1 + 6144);
  SB();
  MV16(ak[3], wf[0][1], xf[0][3]); SB();
  xf[1][4] = *(LDS_AS const bf16x8*)(xr1 + 8192);
  SB();
  MV16(av[3], wf[0][2], xf[0][3]); SB();
  DMA1(vD[0], sDq, ldsD, 39936, "sc1");
  SB();
  MV16(aq[3], wf[0][0], xf[0][3]); SB();
  DMA1(vD[1], sDq, ldsD, 48128, "sc1"); sDq += WQ_S; OPQ(sDq);
  SB();
  MV16(ak[4], wf[0][1], xf[0][4]); SB();
  DMA1(vD[2], sDm, ldsD, 56320, "sc1"); sDm += dm; OPQ(sDm);
  SB();
  MV16(av[4], wf[0][2], xf[0][4]); SB();
  DMA1(vD[3], sDx, ldsD, 64512, "sc1");
  SB();
  MV16(aq[4], wf[0][0], xf[0][4]); SB();
  DMA1(vD[4], sDx, ldsE, 39936, "sc1"); sDx += 128; OPQ(sDx);
  SB();
  SB();
  MV16(ak[1], wf[1][1], xf[1][1]); SB();
  SB();
  MV16(av[1], wf[1][2], xf[1][1]); SB();
  WAITV(5);
  BAR();
  xr0 += -79872; wr0 += -79872; asm volatile("" : "+v"(xr0), "+v"(wr0));
  SB();
  MV16(aq[1], wf[1][0], xf[1][1]); SB();
  wf[0][1] = *(LDS_AS const bf16x8*)(wr0 + 1024);
  SB();
  MV16(ak[2], wf[1][1], xf[1][2]); SB();
  xf[0][1] = *(LDS_AS const bf16x8*)(xr0 + 2048);
  SB();
  MV16(av[2], wf[1][2], xf[1][2]); SB();
  wf[0][2] = *(LDS_AS const bf16x8*)(wr0 + 2048);
  SB();
  MV16(aq[2], wf[1][0], xf[1][2]); SB();
  wf[0][0] = *(LDS_AS const bf16x8*)(wr0 + 0);
  SB();
  MV16(ak[3], wf[1][1], xf[1][3]); SB();
  xf[0][2] = *(LDS_AS const bf16x8*)(xr0 + 4096);
  SB();
  MV16(av[3], wf[1][2], xf[1][3]); SB();
  xf[0][3] = *(LDS_AS const bf16x8*)(xr0 + 6144);
  SB();
  MV16(aq[3], wf[1][0], xf[1][3]); SB();
  xf[0][4] = *(LDS_AS const bf16x8*)(xr0 + 8192);
  SB();
  MV16(ak[4], wf[1][1], xf[1][4]); SB();
  MV16(av[4], wf[1][2], xf[1][4]); SB();
  MV16(aq[4], wf[1][0], xf[1][4]); SB();
  SB();
  MV16(ak[1], wf[0][1], xf[0][1]); SB();
  xr1 += -79872; PINV(xr1);
  wf[1][1] = *(LDS_AS const bf16x8*)(wr0 + 4096);
  SB();
  MV16(av[1], wf[0][2], xf[0][1]); SB();
  xf[1][1] = *(LDS_AS const bf16x8*)(xr1 + 2048);
  SB();
  MV16(aq[1], wf[0][0], xf[0][1]); SB();
  wf[1][2] = *(LDS_AS const bf16x8*)(wr0 + 5120);
  SB();
  MV16(ak[2], wf[0][1], xf[0][2]); SB();
  wf[1][0] = *(LDS_AS const bf16x8*)(wr0 + 3072);
  SB();
  MV16(av[2], wf[0][2], xf[0][2]); SB();
  xf[1][2] = *(LDS_AS const bf16x8*)(xr1 + 4096);
  SB();
  MV16(aq[2], wf[0][0], xf[0][2]); SB();
  xf[1][3] = *(LDS_AS const bf16x8*)(xr1 + 6144);
  SB();
  MV16(ak[3], wf[0][1], xf[0][3]); SB();
  xf[1][4] = *(LDS_AS const bf16x8*)(xr1 + 8192);
  SB();
  MV16(av[3], wf[0][2], xf[0][3]); SB();
  DMA1(vD[0], sDq, ldsD, 79872, "sc1");
  SB();
  MV16(aq[3], wf[0][0], xf[0][3]); SB();
  DMA1(vD[1], sDq, ldsD, 88064, "sc1"); sDq += WQ_S; OPQ(sDq);
  SB();
  MV16(ak[4], wf[0][1], xf[0][4]); SB();
  DMA1(vD[2], sDm, ldsD, 96256, "sc1"); sDm += dm; OPQ(sDm);
  SB();
  MV16(av[4], wf[0][2], xf[0][4]); SB();
  DMA1(vD[3], sDx, ldsD, 104448, "sc1");
  SB();
  MV16(aq[4], wf[0][0], xf[0][4]); SB();
  DMA1(vD[4], sDx, ldsE, 79872, "sc1"); sDx += 128; OPQ(sDx);
  SB();
  SB();
  MV16(ak[1], wf[1][1], xf[1][1]); SB();
  SB();
  MV16(av[1], wf[1][2], xf[1][1]); SB();
  WAITV(5);
  BAR();
  xr0 += 39936; wr0 += 39936; asm volatile("" : "+v"(xr0), "+v"(wr0));
  STAMP(61);
  SB();
  MV16(aq[1], wf[1][0], xf[1][1]); SB();
  wf[0][1] = *(LDS_AS const bf16x8*)(wr0 + 1024);
  SB();
  MV16(ak[2], wf[1][1], xf[1][2]); SB();
  xf[0][1] = *(LDS_AS const bf16x8*)(xr0 + 2048);
  SB();
  MV16(av[2], wf[1][2], xf[1][2]); SB();
  wf[0][2] = *(LDS_AS const bf16x8*)(wr0 + 2048);
  SB();
  MV16(aq[2], wf[1][0], xf[1][2]); SB();
  wf[0][0] = *(LDS_AS const bf16x8*)(wr0 + 0);
  SB();
  MV16(ak[3], wf[1][1], xf[1][3]); SB();
  xf[0][2] = *(LDS_AS const bf16x8*)(xr0 + 4096);
  SB();
  MV16(av[3], wf[1][2], xf[1][3]); SB();
  xf[0][3] = *(LDS_AS const bf16x8*)(xr0 + 6144);
  SB();
  MV16(aq[3], wf[1][0], xf[1][3]); SB();
  xf[0][4] = *(LDS_AS const bf16x8*)(xr0 + 8192);
  SB();
  MV16(ak[4], wf[1][1], xf[1][4]); SB();
  MV16(av[4], wf[1][2], xf[1][4]); SB();
  MV16(aq[4], wf[1][0], xf[1][4]); SB();
  SB();
  MV16(ak[1], wf[0][1], xf[0][1]); SB();
  xr1 += 39936; PINV(xr1);
  wf[1][1] = *(LDS_AS const bf16x8*)(wr0 + 4096);
  SB();
  MV16(av[1], wf[0][2], xf[0][1]); SB();
  xf[1][1] = *(LDS_AS const bf16x8*)(xr1 + 2048);
  SB();
  MV16(aq[1], wf[0][0], xf[0][1]); SB();
  wf[1][2] = *(LDS_AS const bf16x8*)(wr0 + 5120);
  SB();
  MV16(ak[2], wf[0][1], xf[0][2]); SB();
  wf[1][0] = *(LDS_AS const bf16x8*)(wr0 + 3072);
  SB();
  MV16(av[2], wf[0][2], xf[0][2]); SB();
  xf[1][2] = *(LDS_AS const bf16x8*)(xr1 + 4096);
  SB();
  MV16(aq[2], wf[0][0], xf[0][2]); SB();
  xf[1][3] = *(LDS_AS const bf16x8*)(xr1 + 6144);
  SB();
  MV16(ak[3], wf[0][1], xf[0][3]); SB();
  xf[1][4] = *(LDS_AS const bf16x8*)(xr1 + 8192);
  SB();
  MV16(av[3], wf[0][2], xf[0][3]); SB();
  DMA1(vD[0], sDq, ldsD, 0, "sc1");
  SB();
  MV16(aq[3], wf[0][0], xf[0][3]); SB();
  DMA1(vD[1], sDq, ldsD, 8192, "sc1"); sDq += WQ_S; OPQ(sDq);
  SB();
  MV16(ak[4], wf[0][1], xf[0][4]); SB();
  DMA1(vD[2], sDm, ldsD, 16384, "sc1"); sDm += dm; OPQ(sDm);
  SB();
  MV16(av[4], wf[0][2], xf[0][4]); SB();
  DMA1(vD[3], sDx, ldsD, 24576, "sc1");
  SB();
  MV16(aq[4], wf[0][0], xf[0][4]); SB();
  DMA1(vD[4], sDx, ldsE, 0, "sc1"); sDx += 128; OPQ(sDx);
  SB();
  SB();
  MV16(ak[1], wf[1][1], xf[1][1]); SB();
  SB();
  MV16(av[1], wf[1][2], xf[1][1]); SB();
  WAITV(5);
  BAR();
  xr0 += 39936; wr0 += 39936; asm volatile("" : "+v"(xr0), "+v"(wr0));
  SB();
  MV16(aq[1], wf[1][0], xf[1][1]); SB();
  wf[0][1] = *(LDS_AS const bf16x8*)(wr0 + 1024);
  SB();
  MV16(ak[2], wf[1][1], xf[1][2]); SB();
  xf[0][1] = *(LDS_AS const bf16x8*)(xr0 + 2048);
  SB();
  MV16(av[2], wf[1][2], xf[1][2]); SB();
  wf[0][2] = *(LDS_AS const bf16x8*)(wr0 + 2048);
  SB();
  MV16(aq[2], wf[1][0], xf[1][2]); SB();
  wf[0][0] = *(LDS_AS const bf16x8*)(wr0 + 0);
  SB();
  MV16(ak[3], wf[1][1], xf[1][3]); SB();
  xf[0][2] = *(LDS_AS const bf16x8*)(xr0 + 4096);
  SB();
  MV16(av[3], wf[1][2], xf[1][3]); SB();
  xf[0][3] = *(LDS_AS const bf16x8*)(xr0 + 6144);
  SB();
  MV16(aq[3], wf[1][0], xf[1][3]); SB();
  xf[0][4] = *(LDS_AS const bf16x8*)(xr0 + 8192);
  SB();
  MV16(ak[4], wf[1][1], xf[1][4]); SB();
  MV16(av[4], wf[1][2], xf[1][4]); SB();
  MV16(aq[4], wf[1][0], xf[1][4]); SB();
  SB();
  MV16(ak[1], wf[0][1], xf[0][1]); SB();
  xr1 += 39936; PINV(xr1);
  SB();
  MV16(av[1], wf[0][2], xf[0][1]); SB();
  wf[1][1] = *(LDS_AS const bf16x8*)(wr0 + 4096);
  SB();
  MV16(aq[1], wf[0][0], xf[0][1]); SB();
  xf[1][1] = *(LDS_AS const bf16x8*)(xr1 + 2048);
  SB();
  MV16(ak[2], wf[0][1], xf[0][2]); SB();
  wf[1][2] = *(LDS_AS const bf16x8*)(wr0 + 5120);
  SB();
  MV16(av[2], wf[0][2], xf[0][2]); SB();
  wf[1][0] = *(LDS_AS const bf16x8*)(wr0 + 3072);
  SB();
  MV16(aq[2], wf[0][0], xf[0][2]); SB();
  xf[1][2] = *(LDS_AS const bf16x8*)(xr1 + 4096);
  SB();
  MV16(ak[3], wf[0][1], xf[0][3]); SB();
  xf[1][3] = *(LDS_AS const bf16x8*)(xr1 + 6144);
  SB();
  MV16(av[3], wf[0][2], xf[0][3]); SB();
  xf[1][4] = *(LDS_AS const bf16x8*)(xr1 + 8192);
  SB();
  MV16(aq[3], wf[0][0], xf[0][3]); SB();
  DMA2(vWa, vWb, sW, ldsU, 0, ""); sW += dW; OPQ(sW);
  SB();
  MV16(ak[4], wf[0][1], xf[0][4]); SB();
  DMA2(vWa, vWb, sW, ldsU, 2048, "");
  SB();
  MV16(av[4], wf[0][2], xf[0][4]); SB();
  MV16(aq[4], wf[0][0], xf[0][4]); SB();
  SB();
  MV16(ak[1], wf[1][1], xf[1][1]); SB();
  SB();
  MV16(av[1], wf[1][2], xf[1][1]); SB();
  WAITV(4);
  BAR();
  xr0 += -79872; wr0 += -79872; asm volatile("" : "+v"(xr0), "+v"(wr0));
  STAMP(62);
  SB();
  MV16(aq[1], wf[1][0], xf[1][1]); SB();
  wf[0][1] = *(LDS_AS const bf16x8*)(wr0 + 1024);
  SB();
  MV16(ak[2], wf[1][1], xf[1][2]); SB();
  xf[0][1] = *(LDS_AS const bf16x8*)(xr0 + 2048);
  SB();
  MV16(av[2], wf[1][2], xf[1][2]); SB();
  wf[0][2] = *(LDS_AS const bf16x8*)(wr0 + 2048);
  SB();
  MV16(aq[2], wf[1][0], xf[1][2]); SB();
  wf[0][0] = *(LDS_AS const bf16x8*)(wr0 + 0);
  SB();
  MV16(ak[3], wf[1][1], xf[1][3]); SB();
  xf[0][2] = *(LDS_AS const bf16x8*)(xr0 + 4096);
  SB();
  MV16(av[3], wf[1][2], xf[1][3]); SB();
  xf[0][3] = *(LDS_AS const bf16x8*)(xr0 + 6144);
  SB();
  MV16(aq[3], wf[1][0], xf[1][3]); SB();
  xf[0][4] = *(LDS_AS const bf16x8*)(xr0 + 8192);
  SB();
  MV16(ak[4], wf[1][1], xf[1][4]); SB();
  unsigned f0; { const int l_ = lane_now(); vFU = (l_ < 14 ? l_ : 13) * FU; PINV(vFU); }
  LD4(f0, vFU, sFU, 0, "sc1");
  SB();
  MV16(av[4], wf[1][2], xf[1][4]); SB();
  unsigned tl; { const int l_ = lane_now(); tl = lds0 + TB + l_ * 16; const u32x2 t_ = *(LDS_AS const u32x2*)(lds0 + TB + 6144 + cb * 512 + l_ * 8); sk0 = t_[0]; sv = t_[1];
    vS = *(LDS_AS const u32x2*)(lds0 + TB + 4096 + cb * 512 + l_ * 8); PINV(tl); }
  SB();
  MV16(aq[4], wf[1][0], xf[1][4]); SB();
  xr1 += -79872; PINV(xr1);
  MV16(ak[1], wf[0][1], xf[0][1]); SB();
  wf[1][1] = *(LDS_AS const bf16x8*)(wr0 + 4096);
  xf[1][1] = *(LDS_AS const bf16x8*)(xr1 + 2048);
  SB();
  MV16(av[1], wf[0][2], xf[0][1]); SB();
  wf[1][2] = *(LDS_AS const bf16x8*)(wr0 + 5120);
  SB();
  MV16(aq[1], wf[0][0], xf[0][1]); SB();
  wf[1][0] = *(LDS_AS const bf16x8*)(wr0 + 3072);
  SB();
  MV16(ak[2], wf[0][1], xf[0][2]); SB();
  xf[1][2] = *(LDS_AS const bf16x8*)(xr1 + 4096);
  xf[1][3] = *(LDS_AS const bf16x8*)(xr1 + 6144);
  SB();
  MV16(av[2], wf[0][2], xf[0][2]); SB();
  xf[1][4] = *(LDS_AS const bf16x8*)(xr1 + 8192);
  SB();
  MV16(aq[2], wf[0][0], xf[0][2]); SB();
  sk1 = sk0 ^ 32; asm volatile("" : "+v"(sk0), "+v"(sk1), "+v"(sv));
  SB();
  MV16(ak[1], wf[1][1], xf[1][1]); SB();
  SB();
  MV16(av[1], wf[1][2], xf[1][1]); SB();
  SB();
  MV16(aq[1], wf[1][0], xf[1][1]); SB();
  SB();
  MV16(ak[3], wf[0][1], xf[0][3]); SB();
  SB();
  MV16(av[3], wf[0][2], xf[0][3]); SB();
  SB();
  MV16(aq[3], wf[0][0], xf[0][3]); SB();
  SB();
  MV16(ak[2], wf[1][1], xf[1][2]); SB();
  t3 = *(LDS_AS const u32x4*)(tl + 3072); t2 = *(LDS_AS const u32x4*)(tl + 2048);
  SB();
  MV16(av[2], wf[1][2], xf[1][2]); SB();
  SB();
  MV16(aq[2], wf[1][0], xf[1][2]); SB();
  SB();
  MV16(ak[4], wf[0][1], xf[0][4]); SB();
  *(LDS_AS u32x2*)(sk1 + SK + 512) = (u32x2){pack2(ak[1][0], ak[1][1]), pack2(ak[1][2], ak[1][3])};
  SB();
  MV16(av[4], wf[0][2], xf[0][4]); SB();
  *(LDS_AS u32x2*)(sv + 256) = (u32x2){pack2(av[1][0], av[1][1]), pack2(av[1][2], av[1][3])};
  SB();
  MV16(aq[4], wf[0][0], xf[0][4]); SB();
  *(LDS_AS u32x2*)(sk0 + SQ + 512) = (u32x2){pack2(aq[1][0], aq[1][1]), pack2(aq[1][2], aq[1][3])};
  SB();
  MV16(ak[3], wf[1][1], xf[1][3]); SB();
  m1 = *(LDS_AS const u32x4*)(tl + 1024); cm = *(LDS_AS const f32x4*)(tl + cmo);
  SB();
  MV16(av[3], wf[1][2], xf[1][3]); SB();
  SB();
  MV16(aq[3], wf[1][0], xf[1][3]); SB();
  SB();
  MV16(ak[4], wf[1][1], xf[1][4]); SB();
  WAITV1(0, f0);
  if (__ballot(f0 != nonce) != 0ull) poll1<2>(sFU, vFU, nonce);
  STAMP(44);
  mq_ = lane_now() >= 48 ? 0u : ~0u; PINV(mq_); sFU += 14 * FU; OPQ(sFU);
  rk = (t3[2] ^ kx) + r0 * 64; rv = t3[3] + r0 * 32; asm volatile("" : "+v"(rk), "+v"(rv));
  SB();
  MV16(av[4], wf[1][2], xf[1][4]); SB();
  *(LDS_AS u32x2*)(sk0 + SK + 1024) = (u32x2){pack2(ak[2][0], ak[2][1]), pack2(ak[2][2], ak[2][3])};
  *(LDS_AS u32x2*)(sv + 512) = (u32x2){pack2(av[2][0], av[2][1]), pack2(av[2][2], av[2][3])};
  SB();
  MV16(aq[4], wf[1][0], xf[1][4]); SB();
  *(LDS_AS u32x2*)(sk1 + SQ + 1024) = (u32x2){pack2(aq[2][0], aq[2][1]), pack2(aq[2][2], aq[2][3])};
  SB();
  STAMP(36);
  *(LDS_AS u32x2*)(sk1 + SK + 1536) = (u32x2){pack2(ak[3][0], ak[3][1]), pack2(ak[3][2], ak[3][3])};
  *(LDS_AS u32x2*)(sv + 768) = (u32x2){pack2(av[3][0], av[3][1]), pack2(av[3][2], av[3][3])};
  *(LDS_AS u32x2*)(sk0 + SQ + 1536) = (u32x2){pack2(aq[3][0], aq[3][1]), pack2(aq[3][2], aq[3][3])};
  SB();
  *(LDS_AS u32x2*)(sk0 + SK + 2048) = (u32x2){pack2(ak[4][0], ak[4][1]), pack2(ak[4][2], ak[4][3])};
  *(LDS_AS u32x2*)(sv + 1024) = (u32x2){pack2(av[4][0], av[4][1]), pack2(av[4][2], av[4][3])};
  *(LDS_AS u32x2*)(sk1 + SQ + 2048) = (u32x2){pack2(aq[4][0], aq[4][1]), pack2(aq[4][2], aq[4][3])};
  SB();

  } else if (w == 3) {
  f32x4 aq[5], ak[5], av[5];
  bf16x8 xf[2][5], wf[2][5];
  unsigned sk0, sk1, sv;
  unsigned wk, wh, e0, e1, e2, hb[3]; { const int l_ = lane_now(); wk = wr0 + (l_ & 8) * 128; wh = lds0 + l_ * 16; asm volatile("" : "+v"(wk), "+v"(wh)); }
  const unsigned kz = t0 == 0 ? 0u : ~0u;
  WAITV(5);
  BAR();
  STAMP(35);
  xf[0][0] = *(LDS_AS const bf16x8*)(xr0 + 0);
  wf[0][2] = *(LDS_AS const bf16x8*)(wh + 1024);
  wf[0][3] = *(LDS_AS const bf16x8*)(wh + 7168);
  wf[0][4] = *(LDS_AS const bf16x8*)(wh + 13312);
  wf[0][1] = *(LDS_AS const bf16x8*)(wk + 1024);
  xf[0][1] = *(LDS_AS const bf16x8*)(xr0 + 2048);
  wf[0][0] = *(LDS_AS const bf16x8*)(wr0 + 0);
  xf[0][2] = *(LDS_AS const bf16x8*)(xr0 + 4096);
  xf[0][3] = *(LDS_AS const bf16x8*)(xr0 + 6144);
  xf[0][4] = *(LDS_AS const bf16x8*)(xr0 + 8192);
  SB();
  MV16Z(av[0], wf[0][2], xf[0][0]); SB();
  xf[1][0] = *(LDS_AS const bf16x8*)(xr1 + 0);
  wf[1][2] = *(LDS_AS const bf16x8*)(wh + 4096);
  SB();
  MV16Z(av[1], wf[0][3], xf[0][0]); SB();
  wf[1][3] = *(LDS_AS const bf16x8*)(wh + 10240);
  SB();
  MV16Z(av[2], wf[0][4], xf[0][0]); SB();
  wf[1][4] = *(LDS_AS const bf16x8*)(wh + 16384);
  SB();
  MV16Z(ak[0], wf[0][1], xf[0][0]); SB();
  wf[1][1] = *(LDS_AS const bf16x8*)(wk + 1536);
  SB();
  MV16Z(ak[1], wf[0][1], xf[0][1]); SB();
  xf[1][1] = *(LDS_AS const bf16x8*)(xr1 + 2048);
  wf[1][0] = *(LDS_AS const bf16x8*)(wr0 + 512);
  SB();
  MV16Z(aq[1], wf[0][0], xf[0][1]); SB();
  xf[1][2] = *(LDS_AS const bf16x8*)(xr1 + 4096);
  SB();
  MV16Z(ak[2], wf[0][1], xf[0][2]); SB();
  xf[1][3] = *(LDS_AS const bf16x8*)(xr1 + 6144);
  SB();
  MV16Z(aq[2], wf[0][0], xf[0][2]); SB();
  xf[1][4] = *(LDS_AS const bf16x8*)(xr1 + 8192);
  SB();
  MV16Z(ak[3], wf[0][1], xf[0][3]); SB();
  DMA1(vD[0], sDq, ldsD, 79872, "sc1");
  DMA1(vD[1], sDq, ldsD, 88064, "sc1"); sDq += WQ_S; OPQ(sDq);
  SB();
  MV16Z(aq[3], wf[0][0], xf[0][3]); SB();
  DMA1(vD[2], sDm, ldsD, 96256, "sc1"); sDm += dm; OPQ(sDm);
  SB();
  MV16Z(ak[4], wf[0][1], xf[0][4]); SB();
  DMA1(vD[3], sDx, ldsD, 104448, "sc1");
  SB();
  MV16Z(aq[4], wf[0][0], xf[0][4]); SB();
  DMA1(vD[4], sDx, ldsE, 79872, "sc1"); sDx += 128; OPQ(sDx);
  SB();
  SB();
  MV16(av[0], wf[1][2], xf[1][0]); SB();
  SB();
  MV16(av[1], wf[1][3], xf[1][0]); SB();
  WAITV(5);
  BAR();
  xr0 += 39936; wr0 += 39936; asm volatile("" : "+v"(xr0), "+v"(wr0));
  wk += 39936; wh += 39936; asm volatile("" : "+v"(wk), "+v"(wh));
  SB();
  MV16(av[2], wf[1][4], xf[1][0]); SB();
  xf[0][0] = *(LDS_AS const bf16x8*)(xr0 + 0);
  SB();
  MV16(ak[0], wf[1][1], xf[1][0]); SB();
  wf[0][2] = *(LDS_AS const bf16x8*)(wh + 1024);
  SB();
  MV16(ak[1], wf[1][1], xf[1][1]); SB();
  wf[0][3] = *(LDS_AS const bf16x8*)(wh + 7168);
  SB();
  MV16(aq[1], wf[1][0], xf[1][1]); SB();
  wf[0][4] = *(LDS_AS const bf16x8*)(wh + 13312);
  SB();
  MV16(ak[2], wf[1][1], xf[1][2]); SB();
  wf[0][1] = *(LDS_AS const bf16x8*)(wk + 1024);
  SB();
  MV16(aq[2], wf[1][0], xf[1][2]); SB();
  xf[0][1] = *(LDS_AS const bf16x8*)(xr0 + 2048);
  SB();
  MV16(ak[3], wf[1][1], xf[1][3]); SB();
  wf[0][0] = *(LDS_AS const bf16x8*)(wr0 + 0);
  SB();
  MV16(aq[3], wf[1][0], xf[1][3]); SB();
  xf[0][2] = *(LDS_AS const bf16x8*)(xr0 + 4096);
  SB();
  MV16(ak[4], wf[1][1], xf[1][4]); SB();
  xf[0][3] = *(LDS_AS const bf16x8*)(xr0 + 6144);
  SB();
  MV16(aq[4], wf[1][0], xf[1][4]); SB();
  xf[0][4] = *(LDS_AS const bf16x8*)(xr0 + 8192);
  SB();
  SB();
  MV16(av[0], wf[0][2], xf[0][0]); SB();
  xr1 += 39936; PINV(xr1);
  xf[1][0] = *(LDS_AS const bf16x8*)(xr1 + 0);
  SB();
  MV16(av[1], wf[0][3], xf[0][0]); SB();
  wf[1][2] = *(LDS_AS const bf16x8*)(wh + 4096);
  SB();
  MV16(av[2], wf[0][4], xf[0][0]); SB();
  wf[1][3] = *(LDS_AS const bf16x8*)(wh + 10240);
  SB();
  MV16(ak[0], wf[0][1], xf[0][0]); SB();
  wf[1][4] = *(LDS_AS const bf16x8*)(wh + 16384);
  wf[1][1] = *(LDS_AS const bf16x8*)(wk + 1536);
  SB();
  MV16(ak[1], wf[0][1], xf[0][1]); SB();
  xf[1][1] = *(LDS_AS const bf16x8*)(xr1 + 2048);
  SB();
  MV16(aq[1], wf[0][0], xf[0][1]); SB();
  wf[1][0] = *(LDS_AS const bf16x8*)(wr0 + 512);
  SB();
  MV16(ak[2], wf[0][1], xf[0][2]); SB();
  xf[1][2] = *(LDS_AS const bf16x8*)(xr1 + 4096);
  xf[1][3] = *(LDS_AS const bf16x8*)(xr1 + 6144);
  SB();
  MV16(aq[2], wf[0][0], xf[0][2]); SB();
  xf[1][4] = *(LDS_AS const bf16x8*)(xr1 + 8192);
  SB();
  MV16(ak[3], wf[0][1], xf[0][3]); SB();
  DMA1(vD[0], sDq, ldsD, 0, "sc1");
  SB();
  MV16(aq[3], wf[0][0], xf[0][3]); SB();
  DMA1(vD[1], sDq, ldsD, 8192, "sc1"); sDq += WQ_S; OPQ(sDq);
  DMA1(vD[2], sDm, ldsD, 16384, "sc1"); sDm += dm; OPQ(sDm);
  SB();
  MV16(ak[4], wf[0][1], xf[0][4]); SB();
  DMA1(vD[3], sDx, ldsD, 24576, "sc1");
  SB();
  MV16(aq[4], wf[0][0], xf[0][4]); SB();
  DMA1(vD[4], sDx, ldsE, 0, "sc1"); sDx += 128; OPQ(sDx);
  SB();
  SB();
  MV16(av[0], wf[1][2], xf[1][0]); SB();
  SB();
  MV16(av[1], wf[1][3], xf[1][0]); SB();
  WAITV(5);
  BAR();
  xr0 += 39936; wr0 += 39936; asm volatile("" : "+v"(xr0), "+v"(wr0));
  wk += 39936; wh += 39936; asm volatile("" : "+v"(wk), "+v"(wh));
  STAMP(60);
  SB();
  MV16(av[2], wf[1][4], xf[1][0]); SB();
  xf[0][0] = *(LDS_AS const bf16x8*)(xr0 + 0);
  SB();
  MV16(ak[0], wf[1][1], xf[1][0]); SB();
  wf[0][2] = *(LDS_AS const bf16x8*)(wh + 1024);
  SB();
  MV16(ak[1], wf[1][1], xf[1][1]); SB();
  wf[0][3] = *(LDS_AS const bf16x8*)(wh + 7168);
  SB();
  MV16(aq[1], wf[1][0], xf[1][1]); SB();
  wf[0][4] = *(LDS_AS const bf16x8*)(wh + 13312);
  SB();
  MV16(ak[2], wf[1][1], xf[1][2]); SB();
  wf[0][1] = *(LDS_AS const bf16x8*)(wk + 1024);
  SB();
  MV16(aq[2], wf[1][0], xf[1][2]); SB();
  xf[0][1] = *(LDS_AS const bf16x8*)(xr0 + 2048);
  SB();
  MV16(ak[3], wf[1][1], xf[1][3]); SB();
  wf[0][0] = *(LDS_AS const bf16x8*)(wr0 + 0);
  SB();
  MV16(aq[3], wf[1][0], xf[1][3]); SB();
  xf[0][2] = *(LDS_AS const bf16x8*)(xr0 + 4096);
  SB();
  MV16(ak[4], wf[1][1], xf[1][4]); SB();
  xf[0][3] = *(LDS_AS const bf16x8*)(xr0 + 6144);
  SB();
  MV16(aq[4], wf[1][0], xf[1][4]); SB();
  xf[0][4] = *(LDS_AS const bf16x8*)(xr0 + 8192);
  SB();
  SB();
  MV16(av[0], wf[0][2], xf[0][0]); SB();
  xr1 += 39936; PINV(xr1);
  xf[1][0] = *(LDS_AS const bf16x8*)(xr1 + 0);
  SB();
  MV16(av[1], wf[0][3], xf[0][0]); SB();
  wf[1][2] = *(LDS_AS const bf16x8*)(wh + 4096);
  SB();
  MV16(av[2], wf[0][4], xf[0][0]); SB();
  wf[1][3] = *(LDS_AS const bf16x8*)(wh + 10240);
  SB();
  MV16(ak[0], wf[0][1], xf[0][0]); SB();
  wf[1][4] = *(LDS_AS const bf16x8*)(wh + 16384);
  wf[1][1] = *(LDS_AS const bf16x8*)(wk + 1536);
  SB();
  MV16(ak[1], wf[0][1], xf[0][1]); SB();
  xf[1][1] = *(LDS_AS const bf16x8*)(xr1 + 2048);
  SB();
  MV16(aq[1], wf[0][0], xf[0][1]); SB();
  wf[1][0] = *(LDS_AS const bf16x8*)(wr0 + 512);
  SB();
  MV16(ak[2], wf[0][1], xf[0][2]); SB();
  xf[1][2] = *(LDS_AS const bf16x8*)(xr1 + 4096);
  xf[1][3] = *(LDS_AS const bf16x8*)(xr1 + 6144);
  SB();
  MV16(aq[2], wf[0][0], xf[0][2]); SB();
  xf[1][4] = *(LDS_AS const bf16x8*)(xr1 + 8192);
  SB();
  MV16(ak[3], wf[0][1], xf[0][3]); SB();
  DMA1(vD[0], sDq, ldsD, 39936, "sc1");
  SB();
  MV16(aq[3], wf[0][0], xf[0][3]); SB();
  DMA1(vD[1], sDq, ldsD, 48128, "sc1"); sDq += WQ_S; OPQ(sDq);
  DMA1(vD[2], sDm, ldsD, 56320, "sc1"); sDm += dm; OPQ(sDm);
  SB();
  MV16(ak[4], wf[0][1], xf[0][4]); SB();
  DMA1(vD[3], sDx, ldsD, 64512, "sc1");
  SB();
  MV16(aq[4], wf[0][0], xf[0][4]); SB();
  DMA1(vD[4], sDx, ldsE, 39936, "sc1"); sDx += 128; OPQ(sDx);
  SB();
  SB();
  MV16(av[0], wf[1][2], xf[1][0]); SB();
  SB();
  MV16(av[1], wf[1][3], xf[1][0]); SB();
  WAITV(5);
  BAR();
  xr0 += -79872; wr0 += -79872; asm volatile("" : "+v"(xr0), "+v"(wr0));
  wk += -79872; wh += -79872; asm volatile("" : "+v"(wk), "+v"(wh));
  SB();
  MV16(av[2], wf[1][4], xf[1][0]); SB();
  xf[0][0] = *(LDS_AS const bf16x8*)(xr0 + 0);
  SB();
  MV16(ak[0], wf[1][1], xf[1][0]); SB();
  wf[0][2] = *(LDS_AS const bf16x8*)(wh + 1024);
  SB();
  MV16(ak[1], wf[1][1], xf[1][1]); SB();
  wf[0][3] = *(LDS_AS const bf16x8*)(wh + 7168);
  SB();
  MV16(aq[1], wf[1][0], xf[1][1]); SB();
  wf[0][4] = *(LDS_AS const bf16x8*)(wh + 13312);
  SB();
  MV16(ak[2], wf[1][1], xf[1][2]); SB();
  wf[0][1] = *(LDS_AS const bf16x8*)(wk + 1024);
  SB();
  MV16(aq[2], wf[1][0], xf[1][2]); SB();
  xf[0][1] = *(LDS_AS const bf16x8*)(xr0 + 2048);
  SB();
  MV16(ak[3], wf[1][1], xf[1][3]); SB();
  wf[0][0] = *(LDS_AS const bf16x8*)(wr0 + 0);
  SB();
  MV16(aq[3], wf[1][0], xf[1][3]); SB();
  xf[0][2] = *(LDS_AS const bf16x8*)(xr0 + 4096);
  SB();
  MV16(ak[4], wf[1][1], xf[1][4]); SB();
  xf[0][3] = *(LDS_AS const bf16x8*)(xr0 + 6144);
  SB();
  MV16(aq[4], wf[1][0], xf[1][4]); SB();
  xf[0][4] = *(LDS_AS const bf16x8*)(xr0 + 8192);
  SB();
  SB();
  MV16(av[0], wf[0][2], xf[0][0]); SB();
  xr1 += -79872; PINV(xr1);
  xf[1][0] = *(LDS_AS const bf16x8*)(xr1 + 0);
  SB();
  MV16(av[1], wf[0][3], xf[0][0]); SB();
  wf[1][2] = *(LDS_AS const bf16x8*)(wh + 4096);
  SB();
  MV16(av[2], wf[0][4], xf[0][0]); SB();
  wf[1][3] = *(LDS_AS const bf16x8*)(wh + 10240);
  SB();
  MV16(ak[0], wf[0][1], xf[0][0]); SB();
  wf[1][4] = *(LDS_AS const bf16x8*)(wh + 16384);
  wf[1][1] = *(LDS_AS const bf16x8*)(wk + 1536);
  SB();
  MV16(ak[1], wf[0][1], xf[0][1]); SB();
  xf[1][1] = *(LDS_AS const bf16x8*)(xr1 + 2048);
  SB();
  MV16(aq[1], wf[0][0], xf[0][1]); SB();
  wf[1][0] = *(LDS_AS const bf16x8*)(wr0 + 512);
  SB();
  MV16(ak[2], wf[0][1], xf[0][2]); SB();
  xf[1][2] = *(LDS_AS const bf16x8*)(xr1 + 4096);
  xf[1][3] = *(LDS_AS const bf16x8*)(xr1 + 6144);
  SB();
  MV16(aq[2], wf[0][0], xf[0][2]); SB();
  xf[1][4] = *(LDS_AS const bf16x8*)(xr1 + 8192);
  SB();
  MV16(ak[3], wf[0][1], xf[0][3]); SB();
  DMA1(vD[0], sDq, ldsD, 79872, "sc1");
  SB();
  MV16(aq[3], wf[0][0], xf[0][3]); SB();
  DMA1(vD[1], sDq, ldsD, 88064, "sc1"); sDq += WQ_S; OPQ(sDq);
  DMA1(vD[2], sDm, ldsD, 96256, "sc1"); sDm += dm; OPQ(sDm);
  SB();
  MV16(ak[4], wf[0][1], xf[0][4]); SB();
  DMA1(vD[3], sDx, ldsD, 104448, "sc1");
  SB();
  MV16(aq[4], wf[0][0], xf[0][4]); SB();
  DMA1(vD[4], sDx, ldsE, 79872, "sc1"); sDx += 128; OPQ(sDx);
  SB();
  SB();
  MV16(av[0], wf[1][2], xf[1][0]); SB();
  SB();
  MV16(av[1], wf[1][3], xf[1][0]); SB();
  WAITV(5);
  BAR();
  xr0 += 39936; wr0 += 39936; asm volatile("" : "+v"(xr0), "+v"(wr0));
  wk += 39936; wh += 39936; asm volatile("" : "+v"(wk), "+v"(wh));
  STAMP(61);
  SB();
  MV16(av[2], wf[1][4], xf[1][0]); SB();
  xf[0][0] = *(LDS_AS const bf16x8*)(xr0 + 0);
  SB();
  MV16(ak[0], wf[1][1], xf[1][0]); SB();
  wf[0][2] = *(LDS_AS const bf16x8*)(wh + 1024);
  SB();
  MV16(ak[1], wf[1][1], xf[1][1]); SB();
  wf[0][3] = *(LDS_AS const bf16x8*)(wh + 7168);
  SB();
  MV16(aq[1], wf[1][0], xf[1][1]); SB();
  wf[0][4] = *(LDS_AS const bf16x8*)(wh + 13312);
  SB();
  MV16(ak[2], wf[1][1], xf[1][2]); SB();
  wf[0][1] = *(LDS_AS const bf16x8*)(wk + 1024);
  SB();
  MV16(aq[2], wf[1][0], xf[1][2]); SB();
  xf[0][1] = *(LDS_AS const bf16x8*)(xr0 + 2048);
  SB();
  MV16(ak[3], wf[1][1], xf[1][3]); SB();
  wf[0][0] = *(LDS_AS const bf16x8*)(wr0 + 0);
  SB();
  MV16(aq[3], wf[1][0], xf[1][3]); SB();
  xf[0][2] = *(LDS_AS const bf16x8*)(xr0 + 4096);
  SB();
  MV16(ak[4], wf[1][1], xf[1][4]); SB();
  xf[0][3] = *(LDS_AS const bf16x8*)(xr0 + 6144);
  SB();
  MV16(aq[4], wf[1][0], xf[1][4]); SB();
  xf[0][4] = *(LDS_AS const bf16x8*)(xr0 + 8192);
  SB();
  SB();
  MV16(av[0], wf[0][2], xf[0][0]); SB();
  xr1 += 39936; PINV(xr1);
  xf[1][0] = *(LDS_AS const bf16x8*)(xr1 + 0);
  SB();
  MV16(av[1], wf[0][3], xf[0][0]); SB();
  wf[1][2] = *(LDS_AS const bf16x8*)(wh + 4096);
  SB();
  MV16(av[2], wf[0][4], xf[0][0]); SB();
  wf[1][3] = *(LDS_AS const bf16x8*)(wh + 10240);
  SB();
  MV16(ak[0], wf[0][1], xf[0][0]); SB();
  wf[1][4] = *(LDS_AS const bf16x8*)(wh + 16384);
  wf[1][1] = *(LDS_AS const bf16x8*)(wk + 1536);
  SB();
  MV16(ak[1], wf[0][1], xf[0][1]); SB();
  xf[1][1] = *(LDS_AS const bf16x8*)(xr1 + 2048);
  SB();
  MV16(aq[1], wf[0][0], xf[0][1]); SB();
  wf[1][0] = *(LDS_AS const bf16x8*)(wr0 + 512);
  SB();
  MV16(ak[2], wf[0][1], xf[0][2]); SB();
  xf[1][2] = *(LDS_AS const bf16x8*)(xr1 + 4096);
  xf[1][3] = *(LDS_AS const bf16x8*)(xr1 + 6144);
  SB();
  MV16(aq[2], wf[0][0], xf[0][2]); SB();
  xf[1][4] = *(LDS_AS const bf16x8*)(xr1 + 8192);
  SB();
  MV16(ak[3], wf[0][1], xf[0][3]); SB();
  DMA1(vD[0], sDq, ldsD, 0, "sc1");
  SB();
  MV16(aq[3], wf[0][0], xf[0][3]); SB();
  DMA1(vD[1], sDq, ldsD, 8192, "sc1"); sDq += WQ_S; OPQ(sDq);
  DMA1(vD[2], sDm, ldsD, 16384, "sc1"); sDm += dm; OPQ(sDm);
  SB();
  MV16(ak[4], wf[0][1], xf[0][4]); SB();
  DMA1(vD[3], sDx, ldsD, 24576, "sc1");
  SB();
  MV16(aq[4], wf[0][0], xf[0][4]); SB();
  DMA1(vD[4], sDx, ldsE, 0, "sc1"); sDx += 128; OPQ(sDx);
  SB();
  SB();
  MV16(av[0], wf[1][2], xf[1][0]); SB();
  SB();
  MV16(av[1], wf[1][3], xf[1][0]); SB();
  WAITV(5);
  BAR();
  xr0 += 39936; wr0 += 39936; asm volatile("" : "+v"(xr0), "+v"(wr0));
  wk += 39936; wh += 39936; asm volatile("" : "+v"(wk), "+v"(wh));
  SB();
  MV16(av[2], wf[1][4], xf[1][0]); SB();
  xf[0][0] = *(LDS_AS const bf16x8*)(xr0 + 0);
  SB();
  MV16(ak[0], wf[1][1], xf[1][0]); SB();
  wf[0][2] = *(LDS_AS const bf16x8*)(wh + 1024);
  SB();
  MV16(ak[1], wf[1][1], xf[1][1]); SB();
  wf[0][3] = *(LDS_AS const bf16x8*)(wh + 7168);
  SB();
  MV16(aq[1], wf[1][0], xf[1][1]); SB();
  wf[0][4] = *(LDS_AS const bf16x8*)(wh + 13312);
  SB();
  MV16(ak[2], wf[1][1], xf[1][2]); SB();
  wf[0][1] = *(LDS_AS const bf16x8*)(wk + 1024);
  SB();
  MV16(aq[2], wf[1][0], xf[1][2]); SB();
  xf[0][1] = *(LDS_AS const bf16x8*)(xr0 + 2048);
  SB();
  MV16(ak[3], wf[1][1], xf[1][3]); SB();
  wf[0][0] = *(LDS_AS const bf16x8*)(wr0 + 0);
  SB();
  MV16(aq[3], wf[1][0], xf[1][3]); SB();
  xf[0][2] = *(LDS_AS const bf16x8*)(xr0 + 4096);
  SB();
  MV16(ak[4], wf[1][1], xf[1][4]); SB();
  xf[0][3] = *(LDS_AS const bf16x8*)(xr0 + 6144);
  SB();
  MV16(aq[4], wf[1][0], xf[1][4]); SB();
  xf[0][4] = *(LDS_AS const bf16x8*)(xr0 + 8192);
  SB();
  SB();
  MV16(av[0], wf[0][2], xf[0][0]); SB();
  xr1 += 39936; PINV(xr1);
  xf[1][0] = *(LDS_AS const bf16x8*)(xr1 + 0);
  SB();
  MV16(av[1], wf[0][3], xf[0][0]); SB();
  wf[1][2] = *(LDS_AS const bf16x8*)(wh + 4096);
  SB();
  MV16(av[2], wf[0][4], xf[0][0]); SB();
  wf[1][3] = *(LDS_AS const bf16x8*)(wh + 10240);
  SB();
  MV16(ak[0], wf[0][1], xf[0][0]); SB();
  wf[1][4] = *(LDS_AS const bf16x8*)(wh + 16384);
  SB();
  MV16(ak[1], wf[0][1], xf[0][1]); SB();
  wf[1][1] = *(LDS_AS const bf16x8*)(wk + 1536);
  SB();
  MV16(aq[1], wf[0][0], xf[0][1]); SB();
  xf[1][1] = *(LDS_AS const bf16x8*)(xr1 + 2048);
  SB();
  MV16(ak[2], wf[0][1], xf[0][2]); SB();
  wf[1][0] = *(LDS_AS const bf16x8*)(wr0 + 512);
  SB();
  MV16(aq[2], wf[0][0], xf[0][2]); SB();
  xf[1][2] = *(LDS_AS const bf16x8*)(xr1 + 4096);
  SB();
  MV16(ak[3], wf[0][1], xf[0][3]); SB();
  xf[1][3] = *(LDS_AS const bf16x8*)(xr1 + 6144);
  SB();
  MV16(aq[3], wf[0][0], xf[0][3]); SB();
  xf[1][4] = *(LDS_AS const bf16x8*)(xr1 + 8192);
  SB();
  MV16(ak[4], wf[0][1], xf[0][4]); SB();
  DMA2(vWa, vWb, sW, ldsU, 0, ""); sW += dW; OPQ(sW);
  SB();
  MV16(aq[4], wf[0][0], xf[0][4]); SB();
  DMA2(vWa, vWb, sW, ldsU, 2048, "");
  SB();
  SB();
  MV16(av[0], wf[1][2], xf[1][0]); SB();
  WAITV(4);
  BAR();
  xr0 += -79872; wr0 += -79872; asm volatile("" : "+v"(xr0), "+v"(wr0));
  wk += -79872; wh += -79872; asm volatile("" : "+v"(wk), "+v"(wh));
  STAMP(62);
  SB();
  MV16(av[1], wf[1][3], xf[1][0]); SB();
  xf[0][0] = *(LDS_AS const bf16x8*)(xr0 + 0);
  SB();
  MV16(av[2], wf[1][4], xf[1][0]); SB();
  wf[0][2] = *(LDS_AS const bf16x8*)(wh + 1024);
  SB();
  MV16(ak[0], wf[1][1], xf[1][0]); SB();
  wf[0][3] = *(LDS_AS const bf16x8*)(wh + 7168);
  SB();
  MV16(ak[1], wf[1][1], xf[1][1]); SB();
  wf[0][4] = *(LDS_AS const bf16x8*)(wh + 13312);
  wf[0][1] = *(LDS_AS const bf16x8*)(wk + 1024);
  SB();
  MV16(aq[1], wf[1][0], xf[1][1]); SB();
  xf[0][1] = *(LDS_AS const bf16x8*)(xr0 + 2048);
  SB();
  MV16(ak[2], wf[1][1], xf[1][2]); SB();
  wf[0][0] = *(LDS_AS const bf16x8*)(wr0 + 0);
  SB();
  MV16(aq[2], wf[1][0], xf[1][2]); SB();
  xf[0][2] = *(LDS_AS const bf16x8*)(xr0 + 4096);
  SB();
  MV16(ak[3], wf[1][1], xf[1][3]); SB();
  xf[0][3] = *(LDS_AS const bf16x8*)(xr0 + 6144);
  xf[0][4] = *(LDS_AS const bf16x8*)(xr0 + 8192);
  SB();
  MV16(aq[3], wf[1][0], xf[1][3]); SB();
  unsigned f0; { const int l_ = lane_now(); vFU = (l_ < 14 ? l_ : 13) * FU; PINV(vFU); }
  LD4(f0, vFU, sFU, 0, "sc1");
  SB();
  MV16(ak[4], wf[1][1], xf[1][4]); SB();
  unsigned tl; { const int l_ = lane_now(); tl = lds0 + TB + l_ * 16; const u32x2 t_ = *(LDS_AS const u32x2*)(lds0 + TB + 6144 + cb * 512 + l_ * 8); sk0 = t_[0]; sv = t_[1];
    vS = *(LDS_AS const u32x2*)(lds0 + TB + 4096 + cb * 512 + l_ * 8); PINV(tl); }
  SB();
  MV16(aq[4], wf[1][0], xf[1][4]); SB();
  { const int l_ = lane_now(); const unsigned a_ = lds0 + TB + 6144 + l_ * 8; sv = *(LDS_AS const unsigned*)(lds0 + TB + 7684 + (l_ & 31) * 8);
    hb[0] = *(LDS_AS const unsigned*)(a_ + 0); hb[1] = *(LDS_AS const unsigned*)(a_ + 512); hb[2] = *(LDS_AS const unsigned*)(a_ + 1024); }
  SB();
  xr1 += -79872; PINV(xr1);
  MV16(av[0], wf[0][2], xf[0][0]); SB();
  xf[1][0] = *(LDS_AS const bf16x8*)(xr1 + 0);
  wf[1][2] = *(LDS_AS const bf16x8*)(wh + 4096);
  SB();
  MV16(av[1], wf[0][3], xf[0][0]); SB();
  wf[1][3] = *(LDS_AS const bf16x8*)(wh + 10240);
  wf[1][4] = *(LDS_AS const bf16x8*)(wh + 16384);
  SB();
  MV16(av[2], wf[0][4], xf[0][0]); SB();
  wf[1][1] = *(LDS_AS const bf16x8*)(wk + 1536);
  xf[1][1] = *(LDS_AS const bf16x8*)(xr1 + 2048);
  SB();
  MV16(ak[0], wf[0][1], xf[0][0]); SB();
  wf[1][0] = *(LDS_AS const bf16x8*)(wr0 + 512);
  xf[1][2] = *(LDS_AS const bf16x8*)(xr1 + 4096);
  SB();
  MV16(ak[1], wf[0][1], xf[0][1]); SB();
  xf[1][3] = *(LDS_AS const bf16x8*)(xr1 + 6144);
  xf[1][4] = *(LDS_AS const bf16x8*)(xr1 + 8192);
  SB();
  MV16(aq[1], wf[0][0], xf[0][1]); SB();
  sk1 = sk0 ^ 32; asm volatile("" : "+v"(sk0), "+v"(sk1), "+v"(sv));
  const bool lo_ = lane_now() < 32;
  e0 = lo_ ? sk0 + SK : sv; e1 = lo_ ? sk1 + SK + 512 : sv + 256; e2 = lo_ ? 1024 : 512; asm volatile("" : "+v"(e0), "+v"(e1), "+v"(e2));
  const unsigned kl = lo_ ? kz : ~0u;
  SB();
  MV16(av[0], wf[1][2], xf[1][0]); SB();
  SB();
  MV16(av[1], wf[1][3], xf[1][0]); SB();
  SB();
  MV16(av[2], wf[1][4], xf[1][0]); SB();
  SB();
  MV16(ak[0], wf[1][1], xf[1][0]); SB();
  SB();
  MV16(ak[2], wf[0][1], xf[0][2]); SB();
  SB();
  MV16(aq[2], wf[0][0], xf[0][2]); SB();
  SB();
  MV16(ak[1], wf[1][1], xf[1][1]); SB();
  SB();
  MV16(aq[1], wf[1][0], xf[1][1]); SB();
  SB();
  MV16(ak[3], wf[0][1], xf[0][3]); SB();
  SB();
  MV16(aq[3], wf[0][0], xf[0][3]); SB();
  SB();
  MV16(ak[2], wf[1][1], xf[1][2]); SB();
  t3 = *(LDS_AS const u32x4*)(tl + 3072); t2 = *(LDS_AS const u32x4*)(tl + 2048);
  *(LDS_AS u32x2*)(hb[0] + SK) = (u32x2){pack2(av[0][0], av[0][1]) & kz, pack2(av[0][2], av[0][3]) & kz};
  SB();
  MV16(aq[2], wf[1][0], xf[1][2]); SB();
  *(LDS_AS u32x2*)(hb[1] + SK) = (u32x2){pack2(av[1][0], av[1][1]) & kz, pack2(av[1][2], av[1][3]) & kz};
  SB();
  MV16(ak[4], wf[0][1], xf[0][4]); SB();
  *(LDS_AS u32x2*)(hb[2] + SK) = (u32x2){pack2(av[2][0], av[2][1]) & kz, pack2(av[2][2], av[2][3]) & kz};
  SB();
  MV16(aq[4], wf[0][0], xf[0][4]); SB();
  *(LDS_AS u32x2*)(e0) = (u32x2){pack2(ak[0][0], ak[0][1]) & kl, pack2(ak[0][2], ak[0][3]) & kl};
  SB();
  MV16(ak[3], wf[1][1], xf[1][3]); SB();
  WAITV1(0, f0);
  if (__ballot(f0 != nonce) != 0ull) poll1<2>(sFU, vFU, nonce);
  STAMP(44);
  mq_ = lane_now() >= 48 ? 0u : ~0u; PINV(mq_); sFU += 14 * FU; OPQ(sFU);
  SB();
  MV16(aq[3], wf[1][0], xf[1][3]); SB();
  m1 = *(LDS_AS const u32x4*)(tl + 1024); cm = *(LDS_AS const f32x4*)(tl + cmo);
  SB();
  MV16(ak[4], wf[1][1], xf[1][4]); SB();
  rk = (t3[2] ^ kx) + r0 * 64; rv = t3[3] + r0 * 32; asm volatile("" : "+v"(rk), "+v"(rv));
  *(LDS_AS u32x2*)(e1 + 0 * e2) = (u32x2){pack2(ak[1][0], ak[1][1]), pack2(ak[1][2], ak[1][3])};
  SB();
  MV16(aq[4], wf[1][0], xf[1][4]); SB();
  *(LDS_AS u32x2*)(sk0 + SQ + 512) = (u32x2){pack2(aq[1][0], aq[1][1]), pack2(aq[1][2], aq[1][3])};
  SB();
  STAMP(36);
  *(LDS_AS u32x2*)(e0 + 1 * e2) = (u32x2){pack2(ak[2][0], ak[2][1]), pack2(ak[2][2], ak[2][3])};
  *(LDS_AS u32x2*)(sk1 + SQ + 1024) = (u32x2){pack2(aq[2][0], aq[2][1]), pack2(aq[2][2], aq[2][3])};
  SB();
  *(LDS_AS u32x2*)(e1 + 1 * e2) = (u32x2){pack2(ak[3][0], ak[3][1]), pack2(ak[3][2], ak[3][3])};
  *(LDS_AS u32x2*)(sk0 + SQ + 1536) = (u32x2){pack2(aq[3][0], aq[3][1]), pack2(aq[3][2], aq[3][3])};
  SB();
  *(LDS_AS u32x2*)(e0 + 2 * e2) = (u32x2){pack2(ak[4][0], ak[4][1]), pack2(ak[4][2], ak[4][3])};
  *(LDS_AS u32x2*)(sk1 + SQ + 2048) = (u32x2){pack2(aq[4][0], aq[4][1]), pack2(aq[4][2], aq[4][3])};
  SB();

  } else if (w < 7) {
  f32x4 aq[5], ak[5], av[5];
  bf16x8 xf[2][5], wf[2][5];
  unsigned sk0, sk1, sv;
  WAITV(5);
  BAR();
  STAMP(35);
  wf[0][1] = *(LDS_AS const bf16x8*)(wr0 + 1024);
  xf[0][1] = *(LDS_AS const bf16x8*)(xr0 + 10240);
  wf[0][2] = *(LDS_AS const bf16x8*)(wr0 + 2048);
  wf[0][0] = *(LDS_AS const bf16x8*)(wr0 + 0);
  xf[0][2] = *(LDS_AS const bf16x8*)(xr0 + 12288);
  xf[0][3] = *(LDS_AS const bf16x8*)(xr0 + 14336);
  xf[0][4] = *(LDS_AS const bf16x8*)(xr0 + 16384);
  SB();
  MV16Z(ak[1], wf[0][1], xf[0][1]); SB();
  wf[1][1] = *(LDS_AS const bf16x8*)(wr0 + 4096);
  SB();
  MV16Z(av[1], wf[0][2], xf[0][1]); SB();
  xf[1][1] = *(LDS_AS const bf16x8*)(xr1 + 10240);
  SB();
  MV16Z(aq[1], wf[0][0], xf[0][1]); SB();
  wf[1][2] = *(LDS_AS const bf16x8*)(wr0 + 5120);
  SB();
  MV16Z(ak[2], wf[0][1], xf[0][2]); SB();
  wf[1][0] = *(LDS_AS const bf16x8*)(wr0 + 3072);
  SB();
  MV16Z(av[2], wf[0][2], xf[0][2]); SB();
  xf[1][2] = *(LDS_AS const bf16x8*)(xr1 + 12288);
  SB();
  MV16Z(aq[2], wf[0][0], xf[0][2]); SB();
  xf[1][3] = *(LDS_AS const bf16x8*)(xr1 + 14336);
  SB();
  MV16Z(ak[3], wf[0][1], xf[0][3]); SB();
  xf[1][4] = *(LDS_AS const bf16x8*)(xr1 + 16384);
  SB();
  MV16Z(av[3], wf[0][2], xf[0][3]); SB();
  DMA1(vD[0], sDq, ldsD, 79872, "sc1");
  SB();
  MV16Z(aq[3], wf[0][0], xf[0][3]); SB();
  DMA1(vD[1], sDq, ldsD, 88064, "sc1"); sDq += WQ_S; OPQ(sDq);
  SB();
  MV16Z(ak[4], wf[0][1], xf[0][4]); SB();
  DMA1(vD[2], sDm, ldsD, 96256, "sc1"); sDm += dm; OPQ(sDm);
  SB();
  MV16Z(av[4], wf[0][2], xf[0][4]); SB();
  DMA1(vD[3], sDx, ldsD, 104448, "sc1");
  SB();
  MV16Z(aq[4], wf[0][0], xf[0][4]); SB();
  DMA1(vD[4], sDx, ldsE, 79872, "sc1"); sDx += 128; OPQ(sDx);
  SB();
  SB();
  MV16(ak[1], wf[1][1], xf[1][1]); SB();
  SB();
  MV16(av[1], wf[1][2], xf[1][1]); SB();
  WAITV(5);
  BAR();
  xr0 += 39936; wr0 += 39936; asm volatile("" : "+v"(xr0), "+v"(wr0));
  DMA1(vD[0], sDq, ldsD, 0, "sc1");
  DMA1(vD[1], sDq, ldsD, 8192, "sc1"); sDq += WQ_S; OPQ(sDq);
  DMA1(vD[2], sDm, ldsD, 16384, "sc1"); sDm += dm; OPQ(sDm);
  DMA1(vD[3], sDx, ldsD, 24576, "sc1");
  DMA1(vD[4], sDx, ldsE, 0, "sc1"); sDx += 128; OPQ(sDx);
  SB();
  MV16(aq[1], wf[1][0], xf[1][1]); SB();
  wf[0][1] = *(LDS_AS const bf16x8*)(wr0 + 1024);
  SB();
  MV16(ak[2], wf[1][1], xf[1][2]); SB();
  xf[0][1] = *(LDS_AS const bf16x8*)(xr0 + 10240);
  SB();
  MV16(av[2], wf[1][2], xf[1][2]); SB();
  wf[0][2] = *(LDS_AS const bf16x8*)(wr0 + 2048);
  SB();
  MV16(aq[2], wf[1][0], xf[1][2]); SB();
  wf[0][0] = *(LDS_AS const bf16x8*)(wr0 + 0);
  SB();
  MV16(ak[3], wf[1][1], xf[1][3]); SB();
  xf[0][2] = *(LDS_AS const bf16x8*)(xr0 + 12288);
  SB();
  MV16(av[3], wf[1][2], xf[1][3]); SB();
  xf[0][3] = *(LDS_AS const bf16x8*)(xr0 + 14336);
  SB();
  MV16(aq[3], wf[1][0], xf[1][3]); SB();
  xf[0][4] = *(LDS_AS const bf16x8*)(xr0 + 16384);
  SB();
  MV16(ak[4], wf[1][1], xf[1][4]); SB();
  MV16(av[4], wf[1][2], xf[1][4]); SB();
  MV16(aq[4], wf[1][0], xf[1][4]); SB();
  SB();
  MV16(ak[1], wf[0][1], xf[0][1]); SB();
  xr1 += 39936; PINV(xr1);
  SB();
  MV16(av[1], wf[0][2], xf[0][1]); SB();
  wf[1][1] = *(LDS_AS const bf16x8*)(wr0 + 4096);
  SB();
  MV16(aq[1], wf[0][0], xf[0][1]); SB();
  xf[1][1] = *(LDS_AS const bf16x8*)(xr1 + 10240);
  SB();
  MV16(ak[2], wf[0][1], xf[0][2]); SB();
  wf[1][2] = *(LDS_AS const bf16x8*)(wr0 + 5120);
  SB();
  MV16(av[2], wf[0][2], xf[0][2]); SB();
  wf[1][0] = *(LDS_AS const bf16x8*)(wr0 + 3072);
  SB();
  MV16(aq[2], wf[0][0], xf[0][2]); SB();
  xf[1][2] = *(LDS_AS const bf16x8*)(xr1 + 12288);
  SB();
  MV16(ak[3], wf[0][1], xf[0][3]); SB();
  xf[1][3] = *(LDS_AS const bf16x8*)(xr1 + 14336);
  SB();
  MV16(av[3], wf[0][2], xf[0][3]); SB();
  xf[1][4] = *(LDS_AS const bf16x8*)(xr1 + 16384);
  SB();
  MV16(aq[3], wf[0][0], xf[0][3]); SB();
  MV16(ak[4], wf[0][1], xf[0][4]); SB();
  MV16(av[4], wf[0][2], xf[0][4]); SB();
  MV16(aq[4], wf[0][0], xf[0][4]); SB();
  SB();
  MV16(ak[1], wf[1][1], xf[1][1]); SB();
  SB();
  MV16(av[1], wf[1][2], xf[1][1]); SB();
  WAITV(5);
  BAR();
  xr0 += 39936; wr0 += 39936; asm volatile("" : "+v"(xr0), "+v"(wr0));
  STAMP(60);
  DMA1(vD[0], sDq, ldsD, 39936, "sc1");
  DMA1(vD[1], sDq, ldsD, 48128, "sc1"); sDq += WQ_S; OPQ(sDq);
  DMA1(vD[2], sDm, ldsD, 56320, "sc1"); sDm += dm; OPQ(sDm);
  DMA1(vD[3], sDx, ldsD, 64512, "sc1");
  DMA1(vD[4], sDx, ldsE, 39936, "sc1"); sDx += 128; OPQ(sDx);
  SB();
  MV16(aq[1], wf[1][0], xf[1][1]); SB();
  wf[0][1] = *(LDS_AS const bf16x8*)(wr0 + 1024);
  SB();
  MV16(ak[2], wf[1][1], xf[1][2]); SB();
  xf[0][1] = *(LDS_AS const bf16x8*)(xr0 + 10240);
  SB();
  MV16(av[2], wf[1][2], xf[1][2]); SB();
  wf[0][2] = *(LDS_AS const bf16x8*)(wr0 + 2048);
  SB();
  MV16(aq[2], wf[1][0], xf[1][2]); SB();
  wf[0][0] = *(LDS_AS const bf16x8*)(wr0 + 0);
  SB();
  MV16(ak[3], wf[1][1], xf[1][3]); SB();
  xf[0][2] = *(LDS_AS const bf16x8*)(xr0 + 12288);
  SB();
  MV16(av[3], wf[1][2], xf[1][3]); SB();
  xf[0][3] = *(LDS_AS const bf16x8*)(xr0 + 14336);
  SB();
  MV16(aq[3], wf[1][0], xf[1][3]); SB();
  xf[0][4] = *(LDS_AS const bf16x8*)(xr0 + 16384);
  SB();
  MV16(ak[4], wf[1][1], xf[1][4]); SB();
  MV16(av[4], wf[1][2], xf[1][4]); SB();
  MV16(aq[4], wf[1][0], xf[1][4]); SB();
  SB();
  MV16(ak[1], wf[0][1], xf[0][1]); SB();
  xr1 += 39936; PINV(xr1);
  SB();
  MV16(av[1], wf[0][2], xf[0][1]); SB();
  wf[1][1] = *(LDS_AS const bf16x8*)(wr0 + 4096);
  SB();
  MV16(aq[1], wf[0][0], xf[0][1]); SB();
  xf[1][1] = *(LDS_AS const bf16x8*)(xr1 + 10240);
  SB();
  MV16(ak[2], wf[0][1], xf[0][2]); SB();
  wf[1][2] = *(LDS_AS const bf16x8*)(wr0 + 5120);
  SB();
  MV16(av[2], wf[0][2], xf[0][2]); SB();
  wf[1][0] = *(LDS_AS const bf16x8*)(wr0 + 3072);
  SB();
  MV16(aq[2], wf[0][0], xf[0][2]); SB();
  xf[1][2] = *(LDS_AS const bf16x8*)(xr1 + 12288);
  SB();
  MV16(ak[3], wf[0][1], xf[0][3]); SB();
  xf[1][3] = *(LDS_AS const bf16x8*)(xr1 + 14336);
  SB();
  MV16(av[3], wf[0][2], xf[0][3]); SB();
  xf[1][4] = *(LDS_AS const bf16x8*)(xr1 + 16384);
  SB();
  MV16(aq[3], wf[0][0], xf[0][3]); SB();
  MV16(ak[4], wf[0][1], xf[0][4]); SB();
  MV16(av[4], wf[0][2], xf[0][4]); SB();
  MV16(aq[4], wf[0][0], xf[0][4]); SB();
  SB();
  MV16(ak[1], wf[1][1], xf[1][1]); SB();
  SB();
  MV16(av[1], wf[1][2], xf[1][1]); SB();
  WAITV(5);
  BAR();
  xr0 += -79872; wr0 += -79872; asm volatile("" : "+v"(xr0), "+v"(wr0));
  DMA1(vD[0], sDq, ldsD, 79872, "sc1");
  DMA1(vD[1], sDq, ldsD, 88064, "sc1"); sDq += WQ_S; OPQ(sDq);
  DMA1(vD[2], sDm, ldsD, 96256, "sc1"); sDm += dm; OPQ(sDm);
  DMA1(vD[3], sDx, ldsD, 104448, "sc1");
  DMA1(vD[4], sDx, ldsE, 79872, "sc1"); sDx += 128; OPQ(sDx);
  SB();
  MV16(aq[1], wf[1][0], xf[1][1]); SB();
  wf[0][1] = *(LDS_AS const bf16x8*)(wr0 + 1024);
  SB();
  MV16(ak[2], wf[1][1], xf[1][2]); SB();
  xf[0][1] = *(LDS_AS const bf16x8*)(xr0 + 10240);
  SB();
  MV16(av[2], wf[1][2], xf[1][2]); SB();
  wf[0][2] = *(LDS_AS const bf16x8*)(wr0 + 2048);
  SB();
  MV16(aq[2], wf[1][0], xf[1][2]); SB();
  wf[0][0] = *(LDS_AS const bf16x8*)(wr0 + 0);
  SB();
  MV16(ak[3], wf[1][1], xf[1][3]); SB();
  xf[0][2] = *(LDS_AS const bf16x8*)(xr0 + 12288);
  SB();
  MV16(av[3], wf[1][2], xf[1][3]); SB();
  xf[0][3] = *(LDS_AS const bf16x8*)(xr0 + 14336);
  SB();
  MV16(aq[3], wf[1][0], xf[1][3]); SB();
  xf[0][4] = *(LDS_AS const bf16x8*)(xr0 + 16384);
  SB();
  MV16(ak[4], wf[1][1], xf[1][4]); SB();
  MV16(av[4], wf[1][2], xf[1][4]); SB();
  MV16(aq[4], wf[1][0], xf[1][4]); SB();
  SB();
  MV16(ak[1], wf[0][1], xf[0][1]); SB();
  xr1 += -79872; PINV(xr1);
  SB();
  MV16(av[1], wf[0][2], xf[0][1]); SB();
  wf[1][1] = *(LDS_AS const bf16x8*)(wr0 + 4096);
  SB();
  MV16(aq[1], wf[0][0], xf[0][1]); SB();
  xf[1][1] = *(LDS_AS const bf16x8*)(xr1 + 10240);
  SB();
  MV16(ak[2], wf[0][1], xf[0][2]); SB();
  wf[1][2] = *(LDS_AS const bf16x8*)(wr0 + 5120);
  SB();
  MV16(av[2], wf[0][2], xf[0][2]); SB();
  wf[1][0] = *(LDS_AS const bf16x8*)(wr0 + 3072);
  SB();
  MV16(aq[2], wf[0][0], xf[0][2]); SB();
  xf[1][2] = *(LDS_AS const bf16x8*)(xr1 + 12288);
  SB();
  MV16(ak[3], wf[0][1], xf[0][3]); SB();
  xf[1][3] = *(LDS_AS const bf16x8*)(xr1 + 14336);
  SB();
  MV16(av[3], wf[0][2], xf[0][3]); SB();
  xf[1][4] = *(LDS_AS const bf16x8*)(xr1 + 16384);
  SB();
  MV16(aq[3], wf[0][0], xf[0][3]); SB();
  MV16(ak[4], wf[0][1], xf[0][4]); SB();
  MV16(av[4], wf[0][2], xf[0][4]); SB();
  MV16(aq[4], wf[0][0], xf[0][4]); SB();
  SB();
  MV16(ak[1], wf[1][1], xf[1][1]); SB();
  SB();
  MV16(av[1], wf[1][2], xf[1][1]); SB();
  WAITV(5);
  BAR();
  xr0 += 39936; wr0 += 39936; asm volatile("" : "+v"(xr0), "+v"(wr0));
  STAMP(61);
  DMA1(vD[0], sDq, ldsD, 0, "sc1");
  DMA1(vD[1], sDq, ldsD, 8192, "sc1"); sDq += WQ_S; OPQ(sDq);
  DMA1(vD[2], sDm, ldsD, 16384, "sc1"); sDm += dm; OPQ(sDm);
  DMA1(vD[3], sDx, ldsD, 24576, "sc1");
  DMA1(vD[4], sDx, ldsE, 0, "sc1"); sDx += 128; OPQ(sDx);
  SB();
  MV16(aq[1], wf[1][0], xf[1][1]); SB();
  wf[0][1] = *(LDS_AS const bf16x8*)(wr0 + 1024);
  SB();
  MV16(ak[2], wf[1][1], xf[1][2]); SB();
  xf[0][1] = *(LDS_AS const bf16x8*)(xr0 + 10240);
  SB();
  MV16(av[2], wf[1][2], xf[1][2]); SB();
  wf[0][2] = *(LDS_AS const bf16x8*)(wr0 + 2048);
  SB();
  MV16(aq[2], wf[1][0], xf[1][2]); SB();
  wf[0][0] = *(LDS_AS const bf16x8*)(wr0 + 0);
  SB();
  MV16(ak[3], wf[1][1], xf[1][3]); SB();
  xf[0][2] = *(LDS_AS const bf16x8*)(xr0 + 12288);
  SB();
  MV16(av[3], wf[1][2], xf[1][3]); SB();
  xf[0][3] = *(LDS_AS const bf16x8*)(xr0 + 14336);
  SB();
  MV16(aq[3], wf[1][0], xf[1][3]); SB();
  xf[0][4] = *(LDS_AS const bf16x8*)(xr0 + 16384);
  SB();
  MV16(ak[4], wf[1][1], xf[1][4]); SB();
  MV16(av[4], wf[1][2], xf[1][4]); SB();
  MV16(aq[4], wf[1][0], xf[1][4]); SB();
  SB();
  MV16(ak[1], wf[0][1], xf[0][1]); SB();
  xr1 += 39936; PINV(xr1);
  SB();
  MV16(av[1], wf[0][2], xf[0][1]); SB();
  wf[1][1] = *(LDS_AS const bf16x8*)(wr0 + 4096);
  SB();
  MV16(aq[1], wf[0][0], xf[0][1]); SB();
  xf[1][1] = *(LDS_AS const bf16x8*)(xr1 + 10240);
  SB();
  MV16(ak[2], wf[0][1], xf[0][2]); SB();
  wf[1][2] = *(LDS_AS const bf16x8*)(wr0 + 5120);
  SB();
  MV16(av[2], wf[0][2], xf[0][2]); SB();
  wf[1][0] = *(LDS_AS const bf16x8*)(wr0 + 3072);
  SB();
  MV16(aq[2], wf[0][0], xf[0][2]); SB();
  xf[1][2] = *(LDS_AS const bf16x8*)(xr1 + 12288);
  SB();
  MV16(ak[3], wf[0][1], xf[0][3]); SB();
  xf[1][3] = *(LDS_AS const bf16x8*)(xr1 + 14336);
  SB();
  MV16(av[3], wf[0][2], xf[0][3]); SB();
  xf[1][4] = *(LDS_AS const bf16x8*)(xr1 + 16384);
  SB();
  MV16(aq[3], wf[0][0], xf[0][3]); SB();
  MV16(ak[4], wf[0][1], xf[0][4]); SB();
  MV16(av[4], wf[0][2], xf[0][4]); SB();
  MV16(aq[4], wf[0][0], xf[0][4]); SB();
  SB();
  MV16(ak[1], wf[1][1], xf[1][1]); SB();
  SB();
  MV16(av[1], wf[1][2], xf[1][1]); SB();
  WAITV(5);
  BAR();
  xr0 += 39936; wr0 += 39936; asm volatile("" : "+v"(xr0), "+v"(wr0));
  DMA2(vWa, vWb, sW, ldsU, 0, ""); sW += dW; OPQ(sW);
  DMA2(vWa, vWb, sW, ldsU, 2048, "");
  SB();
  MV16(aq[1], wf[1][0], xf[1][1]); SB();
  wf[0][1] = *(LDS_AS const bf16x8*)(wr0 + 1024);
  SB();
  MV16(ak[2], wf[1][1], xf[1][2]); SB();
  xf[0][1] = *(LDS_AS const bf16x8*)(xr0 + 10240);
  SB();
  MV16(av[2], wf[1][2], xf[1][2]); SB();
  wf[0][2] = *(LDS_AS const bf16x8*)(wr0 + 2048);
  SB();
  MV16(aq[2], wf[1][0], xf[1][2]); SB();
  wf[0][0] = *(LDS_AS const bf16x8*)(wr0 + 0);
  SB();
  MV16(ak[3], wf[1][1], xf[1][3]); SB();
  xf[0][2] = *(LDS_AS const bf16x8*)(xr0 + 12288);
  SB();
  MV16(av[3], wf[1][2], xf[1][3]); SB();
  xf[0][3] = *(LDS_AS const bf16x8*)(xr0 + 14336);
  SB();
  MV16(aq[3], wf[1][0], xf[1][3]); SB();
  xf[0][4] = *(LDS_AS const bf16x8*)(xr0 + 16384);
  SB();
  MV16(ak[4], wf[1][1], xf[1][4]); SB();
  MV16(av[4], wf[1][2], xf[1][4]); SB();
  MV16(aq[4], wf[1][0], xf[1][4]); SB();
  SB();
  MV16(ak[1], wf[0][1], xf[0][1]); SB();
  xr1 += 39936; PINV(xr1);
  SB();
  MV16(av[1], wf[0][2], xf[0][1]); SB();
  wf[1][1] = *(LDS_AS const bf16x8*)(wr0 + 4096);
  SB();
  MV16(aq[1], wf[0][0], xf[0][1]); SB();
  xf[1][1] = *(LDS_AS const bf16x8*)(xr1 + 10240);
  SB();
  MV16(ak[2], wf[0][1], xf[0][2]); SB();
  wf[1][2] = *(LDS_AS const bf16x8*)(wr0 + 5120);
  SB();
  MV16(av[2], wf[0][2], xf[0][2]); SB();
  wf[1][0] = *(LDS_AS const bf16x8*)(wr0 + 3072);
  SB();
  MV16(aq[2], wf[0][0], xf[0][2]); SB();
  xf[1][2] = *(LDS_AS const bf16x8*)(xr1 + 12288);
  SB();
  MV16(ak[3], wf[0][1], xf[0][3]); SB();
  xf[1][3] = *(LDS_AS const bf16x8*)(xr1 + 14336);
  SB();
  MV16(av[3], wf[0][2], xf[0][3]); SB();
  xf[1][4] = *(LDS_AS const bf16x8*)(xr1 + 16384);
  SB();
  MV16(aq[3], wf[0][0], xf[0][3]); SB();
  MV16(ak[4], wf[0][1], xf[0][4]); SB();
  MV16(av[4], wf[0][2], xf[0][4]); SB();
  MV16(aq[4], wf[0][0], xf[0][4]); SB();
  SB();
  MV16(ak[1], wf[1][1], xf[1][1]); SB();
  SB();
  MV16(av[1], wf[1][2], xf[1][1]); SB();
  WAITV(4);
  BAR();
  xr0 += -79872; wr0 += -79872; asm volatile("" : "+v"(xr0), "+v"(wr0));
  STAMP(62);
  SB();
  MV16(aq[1], wf[1][0], xf[1][1]); SB();
  wf[0][1] = *(LDS_AS const bf16x8*)(wr0 + 1024);
  SB();
  MV16(ak[2], wf[1][1], xf[1][2]); SB();
  xf[0][1] = *(LDS_AS const bf16x8*)(xr0 + 10240);
  SB();
  MV16(av[2], wf[1][2], xf[1][2]); SB();
  wf[0][2] = *(LDS_AS const bf16x8*)(wr0 + 2048);
  SB();
  MV16(aq[2], wf[1][0], xf[1][2]); SB();
  wf[0][0] = *(LDS_AS const bf16x8*)(wr0 + 0);
  SB();
  MV16(ak[3], wf[1][1], xf[1][3]); SB();
  xf[0][2] = *(LDS_AS const bf16x8*)(xr0 + 12288);
  SB();
  MV16(av[3], wf[1][2], xf[1][3]); SB();
  xf[0][3] = *(LDS_AS const bf16x8*)(xr0 + 14336);
  SB();
  MV16(aq[3], wf[1][0], xf[1][3]); SB();
  xf[0][4] = *(LDS_AS const bf16x8*)(xr0 + 16384);
  SB();
  MV16(ak[4], wf[1][1], xf[1][4]); SB();
  unsigned f0; { const int l_ = lane_now(); vFU = (l_ < 14 ? l_ : 13) * FU; PINV(vFU); }
  LD4(f0, vFU, sFU, 0, "sc1");
  SB();
  MV16(av[4], wf[1][2], xf[1][4]); SB();
  unsigned tl; { const int l_ = lane_now(); tl = lds0 + TB + l_ * 16; const u32x2 t_ = *(LDS_AS const u32x2*)(lds0 + TB + 6144 + cb * 512 + l_ * 8); sk0 = t_[0]; sv = t_[1];
    vS = *(LDS_AS const u32x2*)(lds0 + TB + 4096 + cb * 512 + l_ * 8); PINV(tl); }
  SB();
  MV16(aq[4], wf[1][0], xf[1][4]); SB();
  xr1 += -79872; PINV(xr1);
  MV16(ak[1], wf[0][1], xf[0][1]); SB();
  wf[1][1] = *(LDS_AS const bf16x8*)(wr0 + 4096);
  xf[1][1] = *(LDS_AS const bf16x8*)(xr1 + 10240);
  SB();
  MV16(av[1], wf[0][2], xf[0][1]); SB();
  wf[1][2] = *(LDS_AS const bf16x8*)(wr0 + 5120);
  SB();
  MV16(aq[1], wf[0][0], xf[0][1]); SB();
  wf[1][0] = *(LDS_AS const bf16x8*)(wr0 + 3072);
  SB();
  MV16(ak[2], wf[0][1], xf[0][2]); SB();
  xf[1][2] = *(LDS_AS const bf16x8*)(xr1 + 12288);
  xf[1][3] = *(LDS_AS const bf16x8*)(xr1 + 14336);
  SB();
  MV16(av[2], wf[0][2], xf[0][2]); SB();
  xf[1][4] = *(LDS_AS const bf16x8*)(xr1 + 16384);
  SB();
  MV16(aq[2], wf[0][0], xf[0][2]); SB();
  sk1 = sk0 ^ 32; asm volatile("" : "+v"(sk0), "+v"(sk1), "+v"(sv));
  SB();
  MV16(ak[1], wf[1][1], xf[1][1]); SB();
  SB();
  MV16(av[1], wf[1][2], xf[1][1]); SB();
  SB();
  MV16(aq[1], wf[1][0], xf[1][1]); SB();
  SB();
  MV16(ak[3], wf[0][1], xf[0][3]); SB();
  SB();
  MV16(av[3], wf[0][2], xf[0][3]); SB();
  SB();
  MV16(aq[3], wf[0][0], xf[0][3]); SB();
  SB();
  MV16(ak[2], wf[1][1], xf[1][2]); SB();
  t3 = *(LDS_AS const u32x4*)(tl + 3072); t2 = *(LDS_AS const u32x4*)(tl + 2048);
  SB();
  MV16(av[2], wf[1][2], xf[1][2]); SB();
  SB();
  MV16(aq[2], wf[1][0], xf[1][2]); SB();
  SB();
  MV16(ak[4], wf[0][1], xf[0][4]); SB();
  *(LDS_AS u32x2*)(sk1 + SK + 2560) = (u32x2){pack2(ak[1][0], ak[1][1]), pack2(ak[1][2], ak[1][3])};
  SB();
  MV16(av[4], wf[0][2], xf[0][4]); SB();
  *(LDS_AS u32x2*)(sv + 1280) = (u32x2){pack2(av[1][0], av[1][1]), pack2(av[1][2], av[1][3])};
  SB();
  MV16(aq[4], wf[0][0], xf[0][4]); SB();
  *(LDS_AS u32x2*)(sk0 + SQ + 2560) = (u32x2){pack2(aq[1][0], aq[1][1]), pack2(aq[1][2], aq[1][3])};
  SB();
  MV16(ak[3], wf[1][1], xf[1][3]); SB();
  m1 = *(LDS_AS const u32x4*)(tl + 1024); cm = *(LDS_AS const f32x4*)(tl + cmo);
  SB();
  MV16(av[3], wf[1][2], xf[1][3]); SB();
  SB();
  MV16(aq[3], wf[1][0], xf[1][3]); SB();
  SB();
  MV16(ak[4], wf[1][1], xf[1][4]); SB();
  WAITV1(0, f0);
  if (__ballot(f0 != nonce) != 0ull) poll1<2>(sFU, vFU, nonce);
  STAMP(44);
  mq_ = lane_now() >= 48 ? 0u : ~0u; PINV(mq_); sFU += 14 * FU; OPQ(sFU);
  rk = (t3[2] ^ kx) + r0 * 64; rv = t3[3] + r0 * 32; asm volatile("" : "+v"(rk), "+v"(rv));
  SB();
  MV16(av[4], wf[1][2], xf[1][4]); SB();
  *(LDS_AS u32x2*)(sk0 + SK + 3072) = (u32x2){pack2(ak[2][0], ak[2][1]), pack2(ak[2][2], ak[2][3])};
  *(LDS_AS u32x2*)(sv + 1536) = (u32x2){pack2(av[2][0], av[2][1]), pack2(av[2][2], av[2][3])};
  SB();
  MV16(aq[4], wf[1][0], xf[1][4]); SB();
  *(LDS_AS u32x2*)(sk1 + SQ + 3072) = (u32x2){pack2(aq[2][0], aq[2][1]), pack2(aq[2][2], aq[2][3])};
  SB();
  STAMP(36);
  *(LDS_AS u32x2*)(sk1 + SK + 3584) = (u32x2){pack2(ak[3][0], ak[3][1]), pack2(ak[3][2], ak[3][3])};
  *(LDS_AS u32x2*)(sv + 1792) = (u32x2){pack2(av[3][0], av[3][1]), pack2(av[3][2], av[3][3])};
  *(LDS_AS u32x2*)(sk0 + SQ + 3584) = (u32x2){pack2(aq[3][0], aq[3][1]), pack2(aq[3][2], aq[3][3])};
  SB();
  *(LDS_AS u32x2*)(sk0 + SK + 4096) = (u32x2){pack2(ak[4][0], ak[4][1]), pack2(ak[4][2], ak[4][3])};
  *(LDS_AS u32x2*)(sv + 2048) = (u32x2){pack2(av[4][0], av[4][1]), pack2(av[4][2], av[4][3])};
  *(LDS_AS u32x2*)(sk1 + SQ + 4096) = (u32x2){pack2(aq[4][0], aq[4][1]), pack2(aq[4][2], aq[4][3])};
  SB();

  } else {
  f32x4 aq[5], ak[5], av[5];
  bf16x8 xf[2][5], wf[2][5];
  unsigned sk0, sk1, sv;
  unsigned wk, wh, e0, e1, e2, hb[3]; { const int l_ = lane_now(); wk = wr0 + (l_ & 8) * 128; wh = lds0 + l_ * 16; asm volatile("" : "+v"(wk), "+v"(wh)); }
  WAITV(5);
  BAR();
  STAMP(35);
  xf[0][0] = *(LDS_AS const bf16x8*)(xr0 + 0);
  wf[0][2] = *(LDS_AS const bf16x8*)(wh + 2048);
  wf[0][3] = *(LDS_AS const bf16x8*)(wh + 8192);
  wf[0][4] = *(LDS_AS const bf16x8*)(wh + 14336);
  wf[0][1] = *(LDS_AS const bf16x8*)(wk + 1024);
  xf[0][1] = *(LDS_AS const bf16x8*)(xr0 + 10240);
  wf[0][0] = *(LDS_AS const bf16x8*)(wr0 + 0);
  xf[0][2] = *(LDS_AS const bf16x8*)(xr0 + 12288);
  xf[0][3] = *(LDS_AS const bf16x8*)(xr0 + 14336);
  xf[0][4] = *(LDS_AS const bf16x8*)(xr0 + 16384);
  SB();
  MV16Z(av[0], wf[0][2], xf[0][0]); SB();
  xf[1][0] = *(LDS_AS const bf16x8*)(xr1 + 0);
  wf[1][2] = *(LDS_AS const bf16x8*)(wh + 5120);
  SB();
  MV16Z(av[1], wf[0][3], xf[0][0]); SB();
  wf[1][3] = *(LDS_AS const bf16x8*)(wh + 11264);
  SB();
  MV16Z(av[2], wf[0][4], xf[0][0]); SB();
  wf[1][4] = *(LDS_AS const bf16x8*)(wh + 17408);
  wf[1][1] = *(LDS_AS const bf16x8*)(wk + 1536);
  SB();
  MV16Z(ak[1], wf[0][1], xf[0][1]); SB();
  xf[1][1] = *(LDS_AS const bf16x8*)(xr1 + 10240);
  SB();
  MV16Z(aq[1], wf[0][0], xf[0][1]); SB();
  wf[1][0] = *(LDS_AS const bf16x8*)(wr0 + 512);
  SB();
  MV16Z(ak[2], wf[0][1], xf[0][2]); SB();
  xf[1][2] = *(LDS_AS const bf16x8*)(xr1 + 12288);
  xf[1][3] = *(LDS_AS const bf16x8*)(xr1 + 14336);
  SB();
  MV16Z(aq[2], wf[0][0], xf[0][2]); SB();
  xf[1][4] = *(LDS_AS const bf16x8*)(xr1 + 16384);
  SB();
  MV16Z(ak[3], wf[0][1], xf[0][3]); SB();
  DMA1(vD[0], sDq, ldsD, 79872, "sc1");
  SB();
  MV16Z(aq[3], wf[0][0], xf[0][3]); SB();
  DMA1(vD[1], sDq, ldsD, 88064, "sc1"); sDq += WQ_S; OPQ(sDq);
  DMA1(vD[2], sDm, ldsD, 96256, "sc1"); sDm += dm; OPQ(sDm);
  SB();
  MV16Z(ak[4], wf[0][1], xf[0][4]); SB();
  DMA1(vD[3], sDx, ldsD, 104448, "sc1");
  SB();
  MV16Z(aq[4], wf[0][0], xf[0][4]); SB();
  DMA1(vD[4], sDx, ldsE, 79872, "sc1"); sDx += 128; OPQ(sDx);
  SB();
  SB();
  MV16(av[0], wf[1][2], xf[1][0]); SB();
  WAITV(5);
  BAR();
  xr0 += 39936; wr0 += 39936; asm volatile("" : "+v"(xr0), "+v"(wr0));
  wk += 39936; wh += 39936; asm volatile("" : "+v"(wk), "+v"(wh));
  DMA1(vD[0], sDq, ldsD, 0, "sc1");
  DMA1(vD[1], sDq, ldsD, 8192, "sc1"); sDq += WQ_S; OPQ(sDq);
  DMA1(vD[2], sDm, ldsD, 16384, "sc1"); sDm += dm; OPQ(sDm);
  DMA1(vD[3], sDx, ldsD, 24576, "sc1");
  DMA1(vD[4], sDx, ldsE, 0, "sc1"); sDx += 128; OPQ(sDx);
  SB();
  MV16(av[1], wf[1][3], xf[1][0]); SB();
  xf[0][0] = *(LDS_AS const bf16x8*)(xr0 + 0);
  SB();
  MV16(av[2], wf[1][4], xf[1][0]); SB();
  wf[0][2] = *(LDS_AS const bf16x8*)(wh + 2048);
  SB();
  MV16(ak[1], wf[1][1], xf[1][1]); SB();
  wf[0][3] = *(LDS_AS const bf16x8*)(wh + 8192);
  SB();
  MV16(aq[1], wf[1][0], xf[1][1]); SB();
  wf[0][4] = *(LDS_AS const bf16x8*)(wh + 14336);
  SB();
  MV16(ak[2], wf[1][1], xf[1][2]); SB();
  wf[0][1] = *(LDS_AS const bf16x8*)(wk + 1024);
  SB();
  MV16(aq[2], wf[1][0], xf[1][2]); SB();
  xf[0][1] = *(LDS_AS const bf16x8*)(xr0 + 10240);
  SB();
  MV16(ak[3], wf[1][1], xf[1][3]); SB();
  wf[0][0] = *(LDS_AS const bf16x8*)(wr0 + 0);
  SB();
  MV16(aq[3], wf[1][0], xf[1][3]); SB();
  xf[0][2] = *(LDS_AS const bf16x8*)(xr0 + 12288);
  SB();
  MV16(ak[4], wf[1][1], xf[1][4]); SB();
  xf[0][3] = *(LDS_AS const bf16x8*)(xr0 + 14336);
  SB();
  MV16(aq[4], wf[1][0], xf[1][4]); SB();
  xf[0][4] = *(LDS_AS const bf16x8*)(xr0 + 16384);
  SB();
  SB();
  MV16(av[0], wf[0][2], xf[0][0]); SB();
  xr1 += 39936; PINV(xr1);
  SB();
  MV16(av[1], wf[0][3], xf[0][0]); SB();
  xf[1][0] = *(LDS_AS const bf16x8*)(xr1 + 0);
  SB();
  MV16(av[2], wf[0][4], xf[0][0]); SB();
  wf[1][2] = *(LDS_AS const bf16x8*)(wh + 5120);
  SB();
  MV16(ak[1], wf[0][1], xf[0][1]); SB();
  wf[1][3] = *(LDS_AS const bf16x8*)(wh + 11264);
  SB();
  MV16(aq[1], wf[0][0], xf[0][1]); SB();
  wf[1][4] = *(LDS_AS const bf16x8*)(wh + 17408);
  SB();
  MV16(ak[2], wf[0][1], xf[0][2]); SB();
  wf[1][1] = *(LDS_AS const bf16x8*)(wk + 1536);
  SB();
  MV16(aq[2], wf[0][0], xf[0][2]); SB();
  xf[1][1] = *(LDS_AS const bf16x8*)(xr1 + 10240);
  SB();
  MV16(ak[3], wf[0][1], xf[0][3]); SB();
  wf[1][0] = *(LDS_AS const bf16x8*)(wr0 + 512);
  SB();
  MV16(aq[3], wf[0][0], xf[0][3]); SB();
  xf[1][2] = *(LDS_AS const bf16x8*)(xr1 + 12288);
  SB();
  MV16(ak[4], wf[0][1], xf[0][4]); SB();
  xf[1][3] = *(LDS_AS const bf16x8*)(xr1 + 14336);
  SB();
  MV16(aq[4], wf[0][0], xf[0][4]); SB();
  xf[1][4] = *(LDS_AS const bf16x8*)(xr1 + 16384);
  SB();
  SB();
  MV16(av[0], wf[1][2], xf[1][0]); SB();
  WAITV(5);
  BAR();
  xr0 += 39936; wr0 += 39936; asm volatile("" : "+v"(xr0), "+v"(wr0));
  wk += 39936; wh += 39936; asm volatile("" : "+v"(wk), "+v"(wh));
  STAMP(60);
  DMA1(vD[0], sDq, ldsD, 39936, "sc1");
  DMA1(vD[1], sDq, ldsD, 48128, "sc1"); sDq += WQ_S; OPQ(sDq);
  DMA1(vD[2], sDm, ldsD, 56320, "sc1"); sDm += dm; OPQ(sDm);
  DMA1(vD[3], sDx, ldsD, 64512, "sc1");
  DMA1(vD[4], sDx, ldsE, 39936, "sc1"); sDx += 128; OPQ(sDx);
  SB();
  MV16(av[1], wf[1][3], xf[1][0]); SB();
  xf[0][0] = *(LDS_AS const bf16x8*)(xr0 + 0);
  SB();
  MV16(av[2], wf[1][4], xf[1][0]); SB();
  wf[0][2] = *(LDS_AS const bf16x8*)(wh + 2048);
  SB();
  MV16(ak[1], wf[1][1], xf[1][1]); SB();
  wf[0][3] = *(LDS_AS const bf16x8*)(wh + 8192);
  SB();
  MV16(aq[1], wf[1][0], xf[1][1]); SB();
  wf[0][4] = *(LDS_AS const bf16x8*)(wh + 14336);
  SB();
  MV16(ak[2], wf[1][1], xf[1][2]); SB();
  wf[0][1] = *(LDS_AS const bf16x8*)(wk + 1024);
  SB();
  MV16(aq[2], wf[1][0], xf[1][2]); SB();
  xf[0][1] = *(LDS_AS const bf16x8*)(xr0 + 10240);
  SB();
  MV16(ak[3], wf[1][1], xf[1][3]); SB();
  wf[0][0] = *(LDS_AS const bf16x8*)(wr0 + 0);
  SB();
  MV16(aq[3], wf[1][0], xf[1][3]); SB();
  xf[0][2] = *(LDS_AS const bf16x8*)(xr0 + 12288);
  SB();
  MV16(ak[4], wf[1][1], xf[1][4]); SB();
  xf[0][3] = *(LDS_AS const bf16x8*)(xr0 + 14336);
  SB();
  MV16(aq[4], wf[1][0], xf[1][4]); SB();
  xf[0][4] = *(LDS_AS const bf16x8*)(xr0 + 16384);
  SB();
  SB();
  MV16(av[0], wf[0][2], xf[0][0]); SB();
  xr1 += 39936; PINV(xr1);
  SB();
  MV16(av[1], wf[0][3], xf[0][0]); SB();
  xf[1][0] = *(LDS_AS const bf16x8*)(xr1 + 0);
  SB();
  MV16(av[2], wf[0][4], xf[0][0]); SB();
  wf[1][2] = *(LDS_AS const bf16x8*)(wh + 5120);
  SB();
  MV16(ak[1], wf[0][1], xf[0][1]); SB();
  wf[1][3] = *(LDS_AS const bf16x8*)(wh + 11264);
  SB();
  MV16(aq[1], wf[0][0], xf[0][1]); SB();
  wf[1][4] = *(LDS_AS const bf16x8*)(wh + 17408);
  SB();
  MV16(ak[2], wf[0][1], xf[0][2]); SB();
  wf[1][1] = *(LDS_AS const bf16x8*)(wk + 1536);
  SB();
  MV16(aq[2], wf[0][0], xf[0][2]); SB();
  xf[1][1] = *(LDS_AS const bf16x8*)(xr1 + 10240);
  SB();
  MV16(ak[3], wf[0][1], xf[0][3]); SB();
  wf[1][0] = *(LDS_AS const bf16x8*)(wr0 + 512);
  SB();
  MV16(aq[3], wf[0][0], xf[0][3]); SB();
  xf[1][2] = *(LDS_AS const bf16x8*)(xr1 + 12288);
  SB();
  MV16(ak[4], wf[0][1], xf[0][4]); SB();
  xf[1][3] = *(LDS_AS const bf16x8*)(xr1 + 14336);
  SB();
  MV16(aq[4], wf[0][0], xf[0][4]); SB();
  xf[1][4] = *(LDS_AS const bf16x8*)(xr1 + 16384);
  SB();
  SB();
  MV16(av[0], wf[1][2], xf[1][0]); SB();
  WAITV(5);
  BAR();
  xr0 += -79872; wr0 += -79872; asm volatile("" : "+v"(xr0), "+v"(wr0));
  wk += -79872; wh += -79872; asm volatile("" : "+v"(wk), "+v"(wh));
  DMA1(vD[0], sDq, ldsD, 79872, "sc1");
  DMA1(vD[1], sDq, ldsD, 88064, "sc1"); sDq += WQ_S; OPQ(sDq);
  DMA1(vD[2], sDm, ldsD, 96256, "sc1"); sDm += dm; OPQ(sDm);
  DMA1(vD[3], sDx, ldsD, 104448, "sc1");
  DMA1(vD[4], sDx, ldsE, 79872, "sc1"); sDx += 128; OPQ(sDx);
  SB();
  MV16(av[1], wf[1][3], xf[1][0]); SB();
  xf[0][0] = *(LDS_AS const bf16x8*)(xr0 + 0);
  SB();
  MV16(av[2], wf[1][4], xf[1][0]); SB();
  wf[0][2] = *(LDS_AS const bf16x8*)(wh + 2048);
  SB();
  MV16(ak[1], wf[1][1], xf[1][1]); SB();
  wf[0][3] = *(LDS_AS const bf16x8*)(wh + 8192);
  SB();
  MV16(aq[1], wf[1][0], xf[1][1]); SB();
  wf[0][4] = *(LDS_AS const bf16x8*)(wh + 14336);
  SB();
  MV16(ak[2], wf[1][1], xf[1][2]); SB();
  wf[0][1] = *(LDS_AS const bf16x8*)(wk + 1024);
  SB();
  MV16(aq[2], wf[1][0], xf[1][2]); SB();
  xf[0][1] = *(LDS_AS const bf16x8*)(xr0 + 10240);
  SB();
  MV16(ak[3], wf[1][1], xf[1][3]); SB();
  wf[0][0] = *(LDS_AS const bf16x8*)(wr0 + 0);
  SB();
  MV16(aq[3], wf[1][0], xf[1][3]); SB();
  xf[0][2] = *(LDS_AS const bf16x8*)(xr0 + 12288);
  SB();
  MV16(ak[4], wf[1][1], xf[1][4]); SB();
  xf[0][3] = *(LDS_AS const bf16x8*)(xr0 + 14336);
  SB();
  MV16(aq[4], wf[1][0], xf[1][4]); SB();
  xf[0][4] = *(LDS_AS const bf16x8*)(xr0 + 16384);
  SB();
  SB();
  MV16(av[0], wf[0][2], xf[0][0]); SB();
  xr1 += -79872; PINV(xr1);
  SB();
  MV16(av[1], wf[0][3], xf[0][0]); SB();
  xf[1][0] = *(LDS_AS const bf16x8*)(xr1 + 0);
  SB();
  MV16(av[2], wf[0][4], xf[0][0]); SB();
  wf[1][2] = *(LDS_AS const bf16x8*)(wh + 5120);
  SB();
  MV16(ak[1], wf[0][1], xf[0][1]); SB();
  wf[1][3] = *(LDS_AS const bf16x8*)(wh + 11264);
  SB();
  MV16(aq[1], wf[0][0], xf[0][1]); SB();
  wf[1][4] = *(LDS_AS const bf16x8*)(wh + 17408);
  SB();
  MV16(ak[2], wf[0][1], xf[0][2]); SB();
  wf[1][1] = *(LDS_AS const bf16x8*)(wk + 1536);
  SB();
  MV16(aq[2], wf[0][0], xf[0][2]); SB();
  xf[1][1] = *(LDS_AS const bf16x8*)(xr1 + 10240);
  SB();
  MV16(ak[3], wf[0][1], xf[0][3]); SB();
  wf[1][0] = *(LDS_AS const bf16x8*)(wr0 + 512);
  SB();
  MV16(aq[3], wf[0][0], xf[0][3]); SB();
  xf[1][2] = *(LDS_AS const bf16x8*)(xr1 + 12288);
  SB();
  MV16(ak[4], wf[0][1], xf[0][4]); SB();
  xf[1][3] = *(LDS_AS const bf16x8*)(xr1 + 14336);
  SB();
  MV16(aq[4], wf[0][0], xf[0][4]); SB();
  xf[1][4] = *(LDS_AS const bf16x8*)(xr1 + 16384);
  SB();
  SB();
  MV16(av[0], wf[1][2], xf[1][0]); SB();
  WAITV(5);
  BAR();
  xr0 += 39936; wr0 += 39936; asm volatile("" : "+v"(xr0), "+v"(wr0));
  wk += 39936; wh += 39936; asm volatile("" : "+v"(wk), "+v"(wh));
  STAMP(61);
  DMA1(vD[0], sDq, ldsD, 0, "sc1");
  DMA1(vD[1], sDq, ldsD, 8192, "sc1"); sDq += WQ_S; OPQ(sDq);
  DMA1(vD[2], sDm, ldsD, 16384, "sc1"); sDm += dm; OPQ(sDm);
  DMA1(vD[3], sDx, ldsD, 24576, "sc1");
  DMA1(vD[4], sDx, ldsE, 0, "sc1"); sDx += 128; OPQ(sDx);
  SB();
  MV16(av[1], wf[1][3], xf[1][0]); SB();
  xf[0][0] = *(LDS_AS const bf16x8*)(xr0 + 0);
  SB();
  MV16(av[2], wf[1][4], xf[1][0]); SB();
  wf[0][2] = *(LDS_AS const bf16x8*)(wh + 2048);
  SB();
  MV16(ak[1], wf[1][1], xf[1][1]); SB();
  wf[0][3] = *(LDS_AS const bf16x8*)(wh + 8192);
  SB();
  MV16(aq[1], wf[1][0], xf[1][1]); SB();
  wf[0][4] = *(LDS_AS const bf16x8*)(wh + 14336);
  SB();
  MV16(ak[2], wf[1][1], xf[1][2]); SB();
  wf[0][1] = *(LDS_AS const bf16x8*)(wk + 1024);
  SB();
  MV16(aq[2], wf[1][0], xf[1][2]); SB();
  xf[0][1] = *(LDS_AS const bf16x8*)(xr0 + 10240);
  SB();
  MV16(ak[3], wf[1][1], xf[1][3]); SB();
  wf[0][0] = *(LDS_AS const bf16x8*)(wr0 + 0);
  SB();
  MV16(aq[3], wf[1][0], xf[1][3]); SB();
  xf[0][2] = *(LDS_AS const bf16x8*)(xr0 + 12288);
  SB();
  MV16(ak[4], wf[1][1], xf[1][4]); SB();
  xf[0][3] = *(LDS_AS const bf16x8*)(xr0 + 14336);
  SB();
  MV16(aq[4], wf[1][0], xf[1][4]); SB();
  xf[0][4] = *(LDS_AS const bf16x8*)(xr0 + 16384);
  SB();
  SB();
  MV16(av[0], wf[0][2], xf[0][0]); SB();
  xr1 += 39936; PINV(xr1);
  SB();
  MV16(av[1], wf[0][3], xf[0][0]); SB();
  xf[1][0] = *(LDS_AS const bf16x8*)(xr1 + 0);
  SB();
  MV16(av[2], wf[0][4], xf[0][0]); SB();
  wf[1][2] = *(LDS_AS const bf16x8*)(wh + 5120);
  SB();
  MV16(ak[1], wf[0][1], xf[0][1]); SB();
  wf[1][3] = *(LDS_AS const bf16x8*)(wh + 11264);
  SB();
  MV16(aq[1], wf[0][0], xf[0][1]); SB();
  wf[1][4] = *(LDS_AS const bf16x8*)(wh + 17408);
  SB();
  MV16(ak[2], wf[0][1], xf[0][2]); SB();
  wf[1][1] = *(LDS_AS const bf16x8*)(wk + 1536);
  SB();
  MV16(aq[2], wf[0][0], xf[0][2]); SB();
  xf[1][1] = *(LDS_AS const bf16x8*)(xr1 + 10240);
  SB();
  MV16(ak[3], wf[0][1], xf[0][3]); SB();
  wf[1][0] = *(LDS_AS const bf16x8*)(wr0 + 512);
  SB();
  MV16(aq[3], wf[0][0], xf[0][3]); SB();
  xf[1][2] = *(LDS_AS const bf16x8*)(xr1 + 12288);
  SB();
  MV16(ak[4], wf[0][1], xf[0][4]); SB();
  xf[1][3] = *(LDS_AS const bf16x8*)(xr1 + 14336);
  SB();
  MV16(aq[4], wf[0][0], xf[0][4]); SB();
  xf[1][4] = *(LDS_AS const bf16x8*)(xr1 + 16384);
  SB();
  SB();
  MV16(av[0], wf[1][2], xf[1][0]); SB();
  WAITV(5);
  BAR();
  xr0 += 39936; wr0 += 39936; asm volatile("" : "+v"(xr0), "+v"(wr0));
  wk += 39936; wh += 39936; asm volatile("" : "+v"(wk), "+v"(wh));
  DMA2(vWa, vWb, sW, ldsU, 0, ""); sW += dW; OPQ(sW);
  DMA2(vWa, vWb, sW, ldsU, 2048, "");
  SB();
  MV16(av[1], wf[1][3], xf[1][0]); SB();
  xf[0][0] = *(LDS_AS const bf16x8*)(xr0 + 0);
  SB();
  MV16(av[2], wf[1][4], xf[1][0]); SB();
  wf[0][2] = *(LDS_AS const bf16x8*)(wh + 2048);
  SB();
  MV16(ak[1], wf[1][1], xf[1][1]); SB();
  wf[0][3] = *(LDS_AS const bf16x8*)(wh + 8192);
  SB();
  MV16(aq[1], wf[1][0], xf[1][1]); SB();
  wf[0][4] = *(LDS_AS const bf16x8*)(wh + 14336);
  SB();
  MV16(ak[2], wf[1][1], xf[1][2]); SB();
  wf[0][1] = *(LDS_AS const bf16x8*)(wk + 1024);
  SB();
  MV16(aq[2], wf[1][0], xf[1][2]); SB();
  xf[0][1] = *(LDS_AS const bf16x8*)(xr0 + 10240);
  SB();
  MV16(ak[3], wf[1][1], xf[1][3]); SB();
  wf[0][0] = *(LDS_AS const bf16x8*)(wr0 + 0);
  SB();
  MV16(aq[3], wf[1][0], xf[1][3]); SB();
  xf[0][2] = *(LDS_AS const bf16x8*)(xr0 + 12288);
  SB();
  MV16(ak[4], wf[1][1], xf[1][4]); SB();
  xf[0][3] = *(LDS_AS const bf16x8*)(xr0 + 14336);
  SB();
  MV16(aq[4], wf[1][0], xf[1][4]); SB();
  xf[0][4] = *(LDS_AS const bf16x8*)(xr0 + 16384);
  SB();
  SB();
  MV16(av[0], wf[0][2], xf[0][0]); SB();
  xr1 += 39936; PINV(xr1);
  SB();
  MV16(av[1], wf[0][3], xf[0][0]); SB();
  xf[1][0] = *(LDS_AS const bf16x8*)(xr1 + 0);
  SB();
  MV16(av[2], wf[0][4], xf[0][0]); SB();
  wf[1][2] = *(LDS_AS const bf16x8*)(wh + 5120);
  SB();
  MV16(ak[1], wf[0][1], xf[0][1]); SB();
  wf[1][3] = *(LDS_AS const bf16x8*)(wh + 11264);
  SB();
  MV16(aq[1], wf[0][0], xf[0][1]); SB();
  wf[1][4] = *(LDS_AS const bf16x8*)(wh + 17408);
  SB();
  MV16(ak[2], wf[0][1], xf[0][2]); SB();
  wf[1][1] = *(LDS_AS const bf16x8*)(wk + 1536);
  SB();
  MV16(aq[2], wf[0][0], xf[0][2]); SB();
  xf[1][1] = *(LDS_AS const bf16x8*)(xr1 + 10240);
  SB();
  MV16(ak[3], wf[0][1], xf[0][3]); SB();
  wf[1][0] = *(LDS_AS const bf16x8*)(wr0 + 512);
  SB();
  MV16(aq[3], wf[0][0], xf[0][3]); SB();
  xf[1][2] = *(LDS_AS const bf16x8*)(xr1 + 12288);
  SB();
  MV16(ak[4], wf[0][1], xf[0][4]); SB();
  xf[1][3] = *(LDS_AS const bf16x8*)(xr1 + 14336);
  SB();
  MV16(aq[4], wf[0][0], xf[0][4]); SB();
  xf[1][4] = *(LDS_AS const bf16x8*)(xr1 + 16384);
  SB();
  SB();
  MV16(av[0], wf[1][2], xf[1][0]); SB();
  WAITV(4);
  BAR();
  xr0 += -79872; wr0 += -79872; asm volatile("" : "+v"(xr0), "+v"(wr0));
  wk += -79872; wh += -79872; asm volatile("" : "+v"(wk), "+v"(wh));
  STAMP(62);
  SB();
  MV16(av[1], wf[1][3], xf[1][0]); SB();
  xf[0][0] = *(LDS_AS const bf16x8*)(xr0 + 0);
  SB();
  MV16(av[2], wf[1][4], xf[1][0]); SB();
  wf[0][2] = *(LDS_AS const bf16x8*)(wh + 2048);
  wf[0][3] = *(LDS_AS const bf16x8*)(wh + 8192);
  SB();
  MV16(ak[1], wf[1][1], xf[1][1]); SB();
  wf[0][4] = *(LDS_AS const bf16x8*)(wh + 14336);
  SB();
  MV16(aq[1], wf[1][0], xf[1][1]); SB();
  wf[0][1] = *(LDS_AS const bf16x8*)(wk + 1024);
  SB();
  MV16(ak[2], wf[1][1], xf[1][2]); SB();
  xf[0][1] = *(LDS_AS const bf16x8*)(xr0 + 10240);
  wf[0][0] = *(LDS_AS const bf16x8*)(wr0 + 0);
  SB();
  MV16(aq[2], wf[1][0], xf[1][2]); SB();
  xf[0][2] = *(LDS_AS const bf16x8*)(xr0 + 12288);
  SB();
  MV16(ak[3], wf[1][1], xf[1][3]); SB();
  xf[0][3] = *(LDS_AS const bf16x8*)(xr0 + 14336);
  SB();
  MV16(aq[3], wf[1][0], xf[1][3]); SB();
  xf[0][4] = *(LDS_AS const bf16x8*)(xr0 + 16384);
  unsigned f0; { const int l_ = lane_now(); vFU = (l_ < 14 ? l_ : 13) * FU; PINV(vFU); }
  LD4(f0, vFU, sFU, 0, "sc1");
  SB();
  MV16(ak[4], wf[1][1], xf[1][4]); SB();
  unsigned tl; { const int l_ = lane_now(); tl = lds0 + TB + l_ * 16; const u32x2 t_ = *(LDS_AS const u32x2*)(lds0 + TB + 6144 + cb * 512 + l_ * 8); sk0 = t_[0]; sv = t_[1];
    vS = *(LDS_AS const u32x2*)(lds0 + TB + 4096 + cb * 512 + l_ * 8); PINV(tl); }
  SB();
  MV16(aq[4], wf[1][0], xf[1][4]); SB();
  { const int l_ = lane_now(); const unsigned a_ = lds0 + TB + 6144 + l_ * 8; sv = *(LDS_AS const unsigned*)(lds0 + TB + 7684 + (l_ & 31) * 8);
    hb[0] = *(LDS_AS const unsigned*)(a_ + 4); hb[1] = *(LDS_AS const unsigned*)(a_ + 516); hb[2] = *(LDS_AS const unsigned*)(a_ + 1028); }
  SB();
  xr1 += -79872; PINV(xr1);
  MV16(av[0], wf[0][2], xf[0][0]); SB();
  xf[1][0] = *(LDS_AS const bf16x8*)(xr1 + 0);
  wf[1][2] = *(LDS_AS const bf16x8*)(wh + 5120);
  wf[1][3] = *(LDS_AS const bf16x8*)(wh + 11264);
  SB();
  MV16(av[1], wf[0][3], xf[0][0]); SB();
  wf[1][4] = *(LDS_AS const bf16x8*)(wh + 17408);
  wf[1][1] = *(LDS_AS const bf16x8*)(wk + 1536);
  SB();
  MV16(av[2], wf[0][4], xf[0][0]); SB();
  xf[1][1] = *(LDS_AS const bf16x8*)(xr1 + 10240);
  wf[1][0] = *(LDS_AS const bf16x8*)(wr0 + 512);
  SB();
  MV16(ak[1], wf[0][1], xf[0][1]); SB();
  xf[1][2] = *(LDS_AS const bf16x8*)(xr1 + 12288);
  xf[1][3] = *(LDS_AS const bf16x8*)(xr1 + 14336);
  SB();
  MV16(aq[1], wf[0][0], xf[0][1]); SB();
  xf[1][4] = *(LDS_AS const bf16x8*)(xr1 + 16384);
  sk1 = sk0 ^ 32; asm volatile("" : "+v"(sk0), "+v"(sk1), "+v"(sv));
  const bool lo_ = lane_now() < 32;
  e0 = lo_ ? sk0 + SK : sv; e1 = lo_ ? sk1 + SK + 512 : sv + 256; e2 = lo_ ? 1024 : 512; asm volatile("" : "+v"(e0), "+v"(e1), "+v"(e2));
  SB();
  MV16(av[0], wf[1][2], xf[1][0]); SB();
  SB();
  MV16(av[1], wf[1][3], xf[1][0]); SB();
  SB();
  MV16(av[2], wf[1][4], xf[1][0]); SB();
  SB();
  MV16(ak[2], wf[0][1], xf[0][2]); SB();
  SB();
  MV16(aq[2], wf[0][0], xf[0][2]); SB();
  SB();
  MV16(ak[1], wf[1][1], xf[1][1]); SB();
  SB();
  MV16(aq[1], wf[1][0], xf[1][1]); SB();
  SB();
  MV16(ak[3], wf[0][1], xf[0][3]); SB();
  SB();
  MV16(aq[3], wf[0][0], xf[0][3]); SB();
  SB();
  MV16(ak[2], wf[1][1], xf[1][2]); SB();
  t3 = *(LDS_AS const u32x4*)(tl + 3072); t2 = *(LDS_AS const u32x4*)(tl + 2048);
  SB();
  MV16(aq[2], wf[1][0], xf[1][2]); SB();
  *(LDS_AS u32x2*)(hb[0]) = (u32x2){pack2(av[0][0], av[0][1]), pack2(av[0][2], av[0][3])};
  SB();
  MV16(ak[4], wf[0][1], xf[0][4]); SB();
  *(LDS_AS u32x2*)(hb[1]) = (u32x2){pack2(av[1][0], av[1][1]), pack2(av[1][2], av[1][3])};
  SB();
  MV16(aq[4], wf[0][0], xf[0][4]); SB();
  *(LDS_AS u32x2*)(hb[2]) = (u32x2){pack2(av[2][0], av[2][1]), pack2(av[2][2], av[2][3])};
  SB();
  MV16(ak[3], wf[1][1], xf[1][3]); SB();
  WAITV1(0, f0);
  if (__ballot(f0 != nonce) != 0ull) poll1<2>(sFU, vFU, nonce);
  STAMP(44);
  mq_ = lane_now() >= 48 ? 0u : ~0u; PINV(mq_); sFU += 14 * FU; OPQ(sFU);
  SB();
  MV16(aq[3], wf[1][0], xf[1][3]); SB();
  m1 = *(LDS_AS const u32x4*)(tl + 1024); cm = *(LDS_AS const f32x4*)(tl + cmo);
  SB();
  MV16(ak[4], wf[1][1], xf[1][4]); SB();
  rk = (t3[2] ^ kx) + r0 * 64; rv = t3[3] + r0 * 32; asm volatile("" : "+v"(rk), "+v"(rv));
  *(LDS_AS u32x2*)(e1 + 2 * e2) = (u32x2){pack2(ak[1][0], ak[1][1]), pack2(ak[1][2], ak[1][3])};
  SB();
  MV16(aq[4], wf[1][0], xf[1][4]); SB();
  *(LDS_AS u32x2*)(sk0 + SQ + 2560) = (u32x2){pack2(aq[1][0], aq[1][1]), pack2(aq[1][2], aq[1][3])};
  SB();
  STAMP(36);
  *(LDS_AS u32x2*)(e0 + 3 * e2) = (u32x2){pack2(ak[2][0], ak[2][1]), pack2(ak[2][2], ak[2][3])};
  *(LDS_AS u32x2*)(sk1 + SQ + 3072) = (u32x2){pack2(aq[2][0], aq[2][1]), pack2(aq[2][2], aq[2][3])};
  SB();
  *(LDS_AS u32x2*)(e1 + 3 * e2) = (u32x2){pack2(ak[3][0], ak[3][1]), pack2(ak[3][2], ak[3][3])};
  *(LDS_AS u32x2*)(sk0 + SQ + 3584) = (u32x2){pack2(aq[3][0], aq[3][1]), pack2(aq[3][2], aq[3][3])};
  SB();
  *(LDS_AS u32x2*)(e0 + 4 * e2) = (u32x2){pack2(ak[4][0], ak[4][1]), pack2(ak[4][2], ak[4][3])};
  *(LDS_AS u32x2*)(sk1 + SQ + 4096) = (u32x2){pack2(aq[4][0], aq[4][1]), pack2(aq[4][2], aq[4][3])};
  SB();

  }
  bf16x8 p[8]; unsigned fu;
  const unsigned vP0 = t2[0], vP1 = t2[1], vP2 = t2[2], vP3 = t2[3], vS0 = vS[0], vS1 = vS[1], mp[2] = {t3[0], t3[1]};
  PRIO(3);
  BAR();
  STAMP(37);
  u32x4 qf[2]; bf16x8 kf[2][2], vf[2][2];
  qf[0] = *(LDS_AS const u32x4*)(rk + SQ + 512); kf[0][0] = *(LDS_AS const bf16x8*)(rk + SK); kf[0][1] = *(LDS_AS const bf16x8*)(rk + SK + 1024);
  qf[1] = *(LDS_AS const u32x4*)(rk + SQ + 512 + SKH); kf[1][0] = *(LDS_AS const bf16x8*)(rk + SK + SKH); kf[1][1] = *(LDS_AS const bf16x8*)(rk + SK + SKH + 1024);
  SB();
  vf[0][0] = tr8u(rv, 512); vf[1][0] = tr8u(rv + 2 * SVB, 512); vf[0][1] = tr8u(rv + SVB, 512); vf[1][1] = tr8u(rv + 3 * SVB, 512);
  u32x4 t7; float bias[4];
  { const int l_ = lane_now(); t7 = *(LDS_AS const u32x4*)(lds0 + TB + 9216 + l_ * 16); bias[0] = bias[1] = bias[2] = bias[3] = *(LDS_AS const float*)(bio + (l_ >> 3) * 4); }
  SB();
  SB();
  LD16(p[0], vP0, sP, 0, "sc1");
  SB();
  f32x4 sc[2][2];
  const f32x4 z4 = {0.f, 0.f, 0.f, 0.f};
  qf[0][2] &= mq_; qf[0][3] &= mq_; qf[1][2] &= mq_; qf[1][3] &= mq_;
  sc[0][0] = MFMA16(kf[0][0], __builtin_bit_cast(bf16x8, qf[0]), z4); sc[0][1] = MFMA16(kf[0][1], __builtin_bit_cast(bf16x8, qf[0]), z4);
  sc[1][0] = MFMA16(kf[1][0], __builtin_bit_cast(bf16x8, qf[1]), z4); sc[1][1] = MFMA16(kf[1][1], __builtin_bit_cast(bf16x8, qf[1]), z4);
  SB();
  LD16(p[1], vP1, sP, 0, "sc1");
  LD16(p[2], vP2, sP, 0, "sc1");
  SB();
  float lg[2][4];
#pragma unroll
  for (int i = 0; i < 8; ++i) lg[i >> 2][i & 3] = rbf(SEL(i >> 2, i & 3));
  FMK4(lg[0], "0x3e41848f", cm); FMK4(lg[1], "0x3e41848f", cm);
#pragma unroll
  for (int i = 0; i < 8; ++i) lg[i >> 2][i & 3] = rbf(lg[i >> 2][i & 3]); asm volatile("" : "+v"(lg[0][0]), "+v"(lg[0][1]), "+v"(lg[0][2]), "+v"(lg[0][3]), "+v"(lg[1][0]), "+v"(lg[1][1]), "+v"(lg[1][2]), "+v"(lg[1][3]));
  SB();
  LD16(p[3], vP3, sP, 0, "sc1"); sP += 5120; OPQ(sP);
  LD16(p[4], vP0, sP, 0, "sc1");
  SB();
  f32x2_t mx = xbcast2(xmax2(fmx(fmx(lg[0][0], lg[0][1]), fmx(lg[0][2], lg[0][3])), fmx(fmx(lg[1][0], lg[1][1]), fmx(lg[1][2], lg[1][3])))); PINV(mx);
  SB();
  LD16(p[5], vP1, sP, 0, "sc1");
  LD16(p[6], vP2, sP, 0, "sc1");
  LD4(fu, vFU, sFU, 0, "sc1");
  SB();
  { const float m0[4] = {mx[0], mx[0], mx[0], mx[0]}, m1_[4] = {mx[1], mx[1], mx[1], mx[1]};
    ACC4("v_subrev_f32", lg[0], m0); ACC4("v_subrev_f32", lg[1], m1_); }
  UN4("v_mul_f32", "0x3fb8aa3b, ", "", lg[0]); UN4("v_mul_f32", "0x3fb8aa3b, ", "", lg[1]);
  UN4("v_exp_f32", "", "", lg[0]); UN4("v_exp_f32", "", "", lg[1]);
  SB();
  SB();
  float e0, e1;
  asm volatile("v_add_f32 %0, %2, %3\n\tv_add_f32 %1, %6, %7\n\tv_add_f32 %0, %0, %4\n\tv_add_f32 %1, %1, %8\n\tv_add_f32 %0, %0, %5\n\tv_add_f32 %1, %1, %9\n\ts_nop 1"
               : "=&v"(e0), "=&v"(e1) : "v"(lg[0][0]), "v"(lg[0][1]), "v"(lg[0][2]), "v"(lg[0][3]), "v"(lg[1][0]), "v"(lg[1][1]), "v"(lg[1][2]), "v"(lg[1][3]));
  f32x2_t rs = xbcast2(__builtin_amdgcn_rcpf(xsum2(e0, e1))); PINV(rs);
  SB();
  SB();
  u32x4 pf[2];
  { const float r0_[4] = {rs[0], rs[0], rs[0], rs[0]}, r1_[4] = {rs[1], rs[1], rs[1], rs[1]};
    ACC4("v_mul_f32", lg[0], r0_); ACC4("v_mul_f32", lg[1], r1_); }
#pragma unroll
  for (int h = 0; h < 2; ++h) {
    const unsigned wa = pack2(lg[h][0], lg[h][1]), wb = pack2(lg[h][2], lg[h][3]);
    pf[h] = (u32x4){wa & ~mp[0], wb & ~mp[1], wa & mp[0], wb & mp[1]};
  }
  const f32x4 oa0 = MFMA16(vf[0][0], __builtin_bit_cast(bf16x8, pf[0]), z4), ob0 = MFMA16(vf[1][0], __builtin_bit_cast(bf16x8, pf[1]), z4);
  const f32x4 oa1 = MFMA16(vf[0][1], __builtin_bit_cast(bf16x8, pf[0]), z4), ob1 = MFMA16(vf[1][1], __builtin_bit_cast(bf16x8, pf[1]), z4);
  SB();
  SB();
  const u32x4 od0 = {pack2(oa0[0], oa0[1]), pack2(oa0[2], oa0[3]), pack2(ob0[0], ob0[1]), pack2(ob0[2], ob0[3])};
  ST16(vS0, sO, 0, od0);
  const u32x4 od1 = {pack2(oa1[0], oa1[1]), pack2(oa1[2], oa1[3]), pack2(ob1[0], ob1[1]), pack2(ob1[2], ob1[3])};
  ST16L48(vS1, sO, 0, od1);
  STAMP(38);
  WAITV1(2, fu);
  if (__ballot(fu != nonce) != 0ull) { WAITV(0); { const unsigned l_ = lane_now() * 16; ST16X(15, l_, sFO, nonce); } poll1<2>(sFU, vFU, nonce); }
  STAMP(51);
  WAITV(11);
  fw[6][0] = tr8u(rU0 + 0, 512);
  fw[6][1] = tr8u(rU1 + 0, 512);
  fw[6][2] = tr8u(rU2 + 0, 512);
  fw[6][3] = tr8u(rU3 + 0, 512);
  WAITV(0);
  { const unsigned l_ = lane_now() * 16; ST16X(15, l_, sFO, nonce); }
  STAMP(39);
  unsigned wa0 = ldsP + t7[0], wa1 = wa0 ^ 64, ra = rao + t7[1], rah = ra + 65536, fs, vFS;
  const unsigned vOut = t7[2], vSt = t7[3];
  asm volatile("" : "+v"(wa0), "+v"(wa1), "+v"(ra), "+v"(rah));
  f32x4 acc[8], t4[8]; float gy[4][4], zz[4];
  u32x4 xv[2], sv4[2]; f32x2_t xf2[8];
  SB();
  WAITVB(10); SB();
  STAMP(45);
  PRIO(2); SB();
  MV16Z(acc[0], p[0], fw[0][0]); SB();
  LD16(p[7], vP3, sP, 0, "sc1"); sP += 5120; OPQ(sP);
  SB();
  MV16Z(acc[1], p[0], fw[0][1]); SB();
  MV16Z(acc[2], p[0], fw[0][2]); SB();
  MV16Z(acc[3], p[0], fw[0][3]); SB();
  SB();
  WAITVB(10); SB();
  MV16(acc[0], p[1], fw[1][0]); SB();
  LD16(p[0], vP0, sP, 0, "sc1");
  SB();
  MV16(acc[1], p[1], fw[1][1]); SB();
  LD4(fu, vFU, sFU, 224, "sc1");
  SB();
  MV16(acc[2], p[1], fw[1][2]); SB();
  MV16(acc[3], p[1], fw[1][3]); SB();
  SB();
  WAITVB(11); SB();
  MV16(acc[0], p[2], fw[2][0]); SB();
  LD16(p[1], vP1, sP, 0, "sc1");
  SB();
  MV16(acc[1], p[2], fw[2][1]); SB();
  MV16(acc[2], p[2], fw[2][2]); SB();
  MV16(acc[3], p[2], fw[2][3]); SB();
  SB();
  WAITVB(11); SB();
  MV16(acc[0], p[3], fw[3][0]); SB();
  LD16(p[2], vP2, sP, 0, "sc1");
  SB();
  MV16(acc[1], p[3], fw[3][1]); SB();
  MV16(acc[2], p[3], fw[3][2]); SB();
  MV16(acc[3], p[3], fw[3][3]); SB();
  SB();
  WAITVB(11); SB();
  MV16(acc[0], p[4], fw[4][0]); SB();
  LD16(p[3], vP3, sP, 0, "sc1"); sP += 5120; OPQ(sP);
  SB();
  MV16(acc[1], p[4], fw[4][1]); SB();
  MV16(acc[2], p[4], fw[4][2]); SB();
  MV16(acc[3], p[4], fw[4][3]); SB();
  SB();
  WAITVB(11); SB();
  MV16(acc[0], p[5], fw[5][0]); SB();
  LD16(p[4], vP0, sP, 0, "sc1");
  SB();
  MV16(acc[1], p[5], fw[5][1]); SB();
  MV16(acc[2], p[5], fw[5][2]); SB();
  MV16(acc[3], p[5], fw[5][3]); SB();
  SB();
  WAITVB(11); SB();
  MV16(acc[0], p[6], fw[6][0]); SB();
  LD16(p[5], vP1, sP, 0, "sc1");
  SB();
  MV16(acc[1], p[6], fw[6][1]); SB();
  BAR0();
  SB();
  MV16(acc[2], p[6], fw[6][2]); SB();
  MV16(acc[3], p[6], fw[6][3]); SB();
  SB();
  WAITVB(7); SB();
  PRIO(1); SB();
  MV16Z(acc[4], p[7], fw[0][0]); SB();
  WAITV1(5, fu);
  if (__ballot(fu != nonce) != 0ull) poll1<2>(sFU + 224, vFU, nonce);
  STAMP(53);
  LD16(p[6], vP2, sP, 0, "sc1");
  SB();
  MV16Z(acc[5], p[7], fw[0][1]); SB();
  MV16Z(acc[6], p[7], fw[0][2]); SB();
  MV16Z(acc[7], p[7], fw[0][3]); SB();
  SB();
  WAITVB(7); SB();
  MV16(acc[4], p[0], fw[1][0]); SB();
  LD16(p[7], vP3, sP, 0, "sc1"); sP += 5120; OPQ(sP);
  SB();
  MV16(acc[5], p[0], fw[1][1]); SB();
  *(LDS_AS f32x4*)(wa0 + 0) = acc[0];
  SB();
  MV16(acc[6], p[0], fw[1][2]); SB();
  *(LDS_AS f32x4*)(wa0 + 2048) = acc[1];
  SB();
  MV16(acc[7], p[0], fw[1][3]); SB();
  LD4(fu, vFU, sFU, 448, "sc1");
  SB();
  SB();
  WAITVB(7); SB();
  PRIO(0); SB();
  MV16(acc[4], p[1], fw[2][0]); SB();
  LD16(p[0], vP0, sP, 0, "sc1");
  SB();
  MV16(acc[5], p[1], fw[2][1]); SB();
  *(LDS_AS f32x4*)(wa0 + 4096) = acc[2];
  SB();
  MV16(acc[6], p[1], fw[2][2]); SB();
  *(LDS_AS f32x4*)(wa0 + 6144) = acc[3];
  SB();
  MV16(acc[7], p[1], fw[2][3]); SB();
  SB();
  WAITVB(7); SB();
  MV16(acc[4], p[2], fw[3][0]); SB();
  LD16(p[1], vP1, sP, 0, "sc1");
  SB();
  MV16(acc[5], p[2], fw[3][1]); SB();
  BAR();
  STAMP(46);
  SB();
  MV16(acc[6], p[2], fw[3][2]); SB();
  MV16(acc[7], p[2], fw[3][3]); SB();
  SB();
  WAITVB(7); SB();
  PRIO(3); SB();
  MV16(acc[4], p[3], fw[4][0]); SB();
  LD16(p[2], vP2, sP, 0, "sc1");
  t4[0] = *(LDS_AS const f32x4*)(ra + 0);
  SB();
  MV16(acc[5], p[3], fw[4][1]); SB();
  t4[1] = *(LDS_AS const f32x4*)(ra + 16384);
  SB();
  MV16(acc[6], p[3], fw[4][2]); SB();
  t4[2] = *(LDS_AS const f32x4*)(ra + 32768);
  SB();
  MV16(acc[7], p[3], fw[4][3]); SB();
  t4[3] = *(LDS_AS const f32x4*)(ra + 49152);
  SB();
  SB();
  WAITVB(7); SB();
  MV16(acc[4], p[4], fw[5][0]); SB();
  LD16(p[3], vP3, sP, 0, "sc1"); sP += 5120; OPQ(sP);
  t4[4] = *(LDS_AS const f32x4*)(rah + 0);
  SB();
  MV16(acc[5], p[4], fw[5][1]); SB();
  t4[5] = *(LDS_AS const f32x4*)(rah + 16384);
  SB();
  MV16(acc[6], p[4], fw[5][2]); SB();
  t4[6] = *(LDS_AS const f32x4*)(rah + 32768);
  SB();
  MV16(acc[7], p[4], fw[5][3]); SB();
  t4[7] = *(LDS_AS const f32x4*)(rah + 49152);
  SB();
  SB();
  WAITVB(7); SB();
  MV16(acc[4], p[5], fw[6][0]); SB();
  LD16(p[4], vP0, sP, 0, "sc1");
  SB();
  MV16(acc[5], p[5], fw[6][1]); SB();
  OP4_3("v_add_f32", gy[3], t4[0], bias);
  SB();
  MV16(acc[6], p[5], fw[6][2]); SB();
  MV16(acc[7], p[5], fw[6][3]); SB();
  SB();
  WAITVB(7); SB();
  MV16Z(acc[0], p[6], fw[0][0]); SB();
  WAITV1(5, fu);
  if (__ballot(fu != nonce) != 0ull) poll1<2>(sFU + 448, vFU, nonce);
  STAMP(53);
  LD16(p[5], vP1, sP, 0, "sc1");
  SB();
  MV16Z(acc[1], p[6], fw[0][1]); SB();
  OP4_3("v_add_f32", zz, t4[1], t4[2]);
  SB();
  MV16Z(acc[2], p[6], fw[0][2]); SB();
  MV16Z(acc[3], p[6], fw[0][3]); SB();
  SB();
  WAITVB(7); SB();
  MV16(acc[0], p[7], fw[1][0]); SB();
  LD16(p[6], vP2, sP, 0, "sc1");
  SB();
  MV16(acc[1], p[7], fw[1][1]); SB();
  *(LDS_AS f32x4*)(wa0 + 8192) = acc[4];
  SB();
  MV16(acc[2], p[7], fw[1][2]); SB();
  LD4(fu, vFU, sFU, 672, "sc1");
  SB();
  MV16(acc[3], p[7], fw[1][3]); SB();
  ACC4("v_add_f32", gy[3], t4[3]);
  SB();
  SB();
  WAITVB(7); SB();
  PRIO(2); SB();
  MV16(acc[0], p[0], fw[2][0]); SB();
  LD16(p[7], vP3, sP, 0, "sc1"); sP += 5120; OPQ(sP);
  SB();
  MV16(acc[1], p[0], fw[2][1]); SB();
  *(LDS_AS f32x4*)(wa0 + 10240) = acc[5];
  SB();
  MV16(acc[2], p[0], fw[2][2]); SB();
  ACC4("v_add_f32", zz, t4[4]);
  SB();
  MV16(acc[3], p[0], fw[2][3]); SB();
  SB();
  WAITVB(7); SB();
  MV16(acc[0], p[1], fw[3][0]); SB();
  LD16(p[0], vP0, sP, 0, "sc1");
  SB();
  MV16(acc[1], p[1], fw[3][1]); SB();
  *(LDS_AS f32x4*)(wa0 + 12288) = acc[6];
  SB();
  MV16(acc[2], p[1], fw[3][2]); SB();
  ACC4("v_add_f32", gy[3], t4[5]);
  SB();
  MV16(acc[3], p[1], fw[3][3]); SB();
  SB();
  WAITVB(7); SB();
  MV16(acc[0], p[2], fw[4][0]); SB();
  LD16(p[1], vP1, sP, 0, "sc1");
  SB();
  MV16(acc[1], p[2], fw[4][1]); SB();
  *(LDS_AS f32x4*)(wa0 + 14336) = acc[7];
  SB();
  MV16(acc[2], p[2], fw[4][2]); SB();
  ACC4("v_add_f32", zz, t4[6]);
  SB();
  MV16(acc[3], p[2], fw[4][3]); SB();
  SB();
  WAITVB(7); SB();
  MV16(acc[0], p[3], fw[5][0]); SB();
  LD16(p[2], vP2, sP, 0, "sc1");
  SB();
  MV16(acc[1], p[3], fw[5][1]); SB();
  ACC4("v_add_f32", gy[3], t4[7]);
  SB();
  MV16(acc[2], p[3], fw[5][2]); SB();
  MV16(acc[3], p[3], fw[5][3]); SB();
  SB();
  WAITVB(7); SB();
  MV16(acc[0], p[4], fw[6][0]); SB();
  LD16(p[3], vP3, sP, 0, "sc1"); sP += 5120; OPQ(sP);
  SB();
  MV16(acc[1], p[4], fw[6][1]); SB();
  ACC4("v_add_f32", gy[3], zz);
  SB();
  MV16(acc[2], p[4], fw[6][2]); SB();
  MV16(acc[3], p[4], fw[6][3]); SB();
  SB();
  WAITVB(7); SB();
  PRIO(1); SB();
  MV16Z(acc[4], p[5], fw[0][0]); SB();
  WAITV1(5, fu);
  if (__ballot(fu != nonce) != 0ull) poll1<2>(sFU + 672, vFU, nonce);
  STAMP(53);
  LD16(p[4], vP0, sP, 0, "sc1");
  SB();
  MV16Z(acc[5], p[5], fw[0][1]); SB();
  SCL4(zz, gy[3]);
  SB();
  MV16Z(acc[6], p[5], fw[0][2]); SB();
  MV16Z(acc[7], p[5], fw[0][3]); SB();
  SB();
  WAITVB(7); SB();
  MV16(acc[4], p[6], fw[1][0]); SB();
  LD16(p[5], vP1, sP, 0, "sc1");
  *(LDS_AS f32x4*)(wa1 + 8192) = acc[0];
  SB();
  MV16(acc[5], p[6], fw[1][1]); SB();
  *(LDS_AS f32x4*)(wa1 + 10240) = acc[1];
  SB();
  MV16(acc[6], p[6], fw[1][2]); SB();
  LD4(fu, vFU, sFU, 896, "sc1");
  SB();
  MV16(acc[7], p[6], fw[1][3]); SB();
  UN4("v_exp_f32", "", "", zz);
  SB();
  SB();
  WAITVB(7); SB();
  PRIO(0); SB();
  MV16(acc[4], p[7], fw[2][0]); SB();
  LD16(p[6], vP2, sP, 0, "sc1");
  SB();
  MV16(acc[5], p[7], fw[2][1]); SB();
  *(LDS_AS f32x4*)(wa1 + 12288) = acc[2];
  SB();
  MV16(acc[6], p[7], fw[2][2]); SB();
  *(LDS_AS f32x4*)(wa1 + 14336) = acc[3];
  SB();
  MV16(acc[7], p[7], fw[2][3]); SB();
  UN4("v_add_f32", "1.0, ", "", zz);
  SB();
  SB();
  WAITVB(7); SB();
  MV16(acc[4], p[0], fw[3][0]); SB();
  LD16(p[7], vP3, sP, 0, "sc1"); sP += 5120; OPQ(sP);
  SB();
  MV16(acc[5], p[0], fw[3][1]); SB();
  UN4("v_rcp_f32", "", "", zz);
  SB();
  MV16(acc[6], p[0], fw[3][2]); SB();
  BAR();
  STAMP(47);
  SB();
  MV16(acc[7], p[0], fw[3][3]); SB();
  SB();
  WAITVB(7); SB();
  PRIO(3); SB();
  MV16(acc[4], p[1], fw[4][0]); SB();
  LD16(p[0], vP0, sP, 0, "sc1");
  ACC4("v_mul_f32", gy[3], zz);
  SB();
  MV16(acc[5], p[1], fw[4][1]); SB();
  t4[0] = *(LDS_AS const f32x4*)(ra + 8192);
  SB();
  MV16(acc[6], p[1], fw[4][2]); SB();
  t4[1] = *(LDS_AS const f32x4*)(ra + 24576);
  t4[2] = *(LDS_AS const f32x4*)(ra + 40960);
  SB();
  MV16(acc[7], p[1], fw[4][3]); SB();
  t4[3] = *(LDS_AS const f32x4*)(ra + 57344);
  SB();
  SB();
  WAITVB(7); SB();
  MV16(acc[4], p[2], fw[5][0]); SB();
  LD16(p[1], vP1, sP, 0, "sc1");
  t4[4] = *(LDS_AS const f32x4*)(rah + 8192);
  SB();
  MV16(acc[5], p[2], fw[5][1]); SB();
  t4[5] = *(LDS_AS const f32x4*)(rah + 24576);
  SB();
  MV16(acc[6], p[2], fw[5][2]); SB();
  t4[6] = *(LDS_AS const f32x4*)(rah + 40960);
  SB();
  MV16(acc[7], p[2], fw[5][3]); SB();
  t4[7] = *(LDS_AS const f32x4*)(rah + 57344);
  SB();
  SB();
  WAITVB(7); SB();
  MV16(acc[4], p[3], fw[6][0]); SB();
  LD16(p[2], vP2, sP, 0, "sc1");
  SB();
  MV16(acc[5], p[3], fw[6][1]); SB();
  OP4_3("v_add_f32", gy[0], t4[0], bias);
  SB();
  MV16(acc[6], p[3], fw[6][2]); SB();
  MV16(acc[7], p[3], fw[6][3]); SB();
  SB();
  WAITVB(7); SB();
  MV16Z(acc[0], p[4], fw[0][0]); SB();
  WAITV1(5, fu);
  if (__ballot(fu != nonce) != 0ull) poll1<2>(sFU + 896, vFU, nonce);
  STAMP(53);
  LD16(p[3], vP3, sP, 0, "sc1"); sP += 5120; OPQ(sP);
  SB();
  MV16Z(acc[1], p[4], fw[0][1]); SB();
  OP4_3("v_add_f32", zz, t4[1], t4[2]);
  SB();
  MV16Z(acc[2], p[4], fw[0][2]); SB();
  MV16Z(acc[3], p[4], fw[0][3]); SB();
  SB();
  WAITVB(7); SB();
  MV16(acc[0], p[5], fw[1][0]); SB();
  LD16(p[4], vP0, sP, 0, "sc1");
  SB();
  MV16(acc[1], p[5], fw[1][1]); SB();
  *(LDS_AS f32x4*)(wa0 + 0) = acc[4];
  SB();
  MV16(acc[2], p[5], fw[1][2]); SB();
  LD4(fu, vFU, sFU, 1120, "sc1");
  SB();
  MV16(acc[3], p[5], fw[1][3]); SB();
  ACC4("v_add_f32", gy[0], t4[3]);
  SB();
  SB();
  WAITVB(7); SB();
  PRIO(2); SB();
  MV16(acc[0], p[6], fw[2][0]); SB();
  LD16(p[5], vP1, sP, 0, "sc1");
  SB();
  MV16(acc[1], p[6], fw[2][1]); SB();
  *(LDS_AS f32x4*)(wa0 + 2048) = acc[5];
  SB();
  MV16(acc[2], p[6], fw[2][2]); SB();
  ACC4("v_add_f32", zz, t4[4]);
  SB();
  MV16(acc[3], p[6], fw[2][3]); SB();
  SB();
  WAITVB(7); SB();
  MV16(acc[0], p[7], fw[3][0]); SB();
  LD16(p[6], vP2, sP, 0, "sc1");
  SB();
  MV16(acc[1], p[7], fw[3][1]); SB();
  *(LDS_AS f32x4*)(wa0 + 4096) = acc[6];
  SB();
  MV16(acc[2], p[7], fw[3][2]); SB();
  ACC4("v_add_f32", gy[0], t4[5]);
  SB();
  MV16(acc[3], p[7], fw[3][3]); SB();
  SB();
  WAITVB(7); SB();
  MV16(acc[0], p[0], fw[4][0]); SB();
  LD16(p[7], vP3, sP, 0, "sc1"); sP += 5120; OPQ(sP);
  SB();
  MV16(acc[1], p[0], fw[4][1]); SB();
  *(LDS_AS f32x4*)(wa0 + 6144) = acc[7];
  SB();
  MV16(acc[2], p[0], fw[4][2]); SB();
  ACC4("v_add_f32", zz, t4[6]);
  SB();
  MV16(acc[3], p[0], fw[4][3]); SB();
  SB();
  WAITVB(7); SB();
  MV16(acc[0], p[1], fw[5][0]); SB();
  LD16(p[0], vP0, sP, 0, "sc1");
  SB();
  MV16(acc[1], p[1], fw[5][1]); SB();
  ACC4("v_add_f32", gy[0], t4[7]);
  SB();
  MV16(acc[2], p[1], fw[5][2]); SB();
  MV16(acc[3], p[1], fw[5][3]); SB();
  SB();
  WAITVB(7); SB();
  MV16(acc[0], p[2], fw[6][0]); SB();
  LD16(p[1], vP1, sP, 0, "sc1");
  SB();
  MV16(acc[1], p[2], fw[6][1]); SB();
  ACC4("v_add_f32", gy[0], zz);
  SB();
  MV16(acc[2], p[2], fw[6][2]); SB();
  MV16(acc[3], p[2], fw[6][3]); SB();
  SB();
  WAITVB(7); SB();
  PRIO(1); SB();
  MV16Z(acc[4], p[3], fw[0][0]); SB();
  WAITV1(5, fu);
  if (__ballot(fu != nonce) != 0ull) poll1<2>(sFU + 1120, vFU, nonce);
  STAMP(43);
  LD16(p[2], vP2, sP, 0, "sc1");
  SB();
  MV16Z(acc[5], p[3], fw[0][1]); SB();
  SCL4(zz, gy[0]);
  SB();
  MV16Z(acc[6], p[3], fw[0][2]); SB();
  MV16Z(acc[7], p[3], fw[0][3]); SB();
  SB();
  WAITVB(7); SB();
  MV16(acc[4], p[4], fw[1][0]); SB();
  LD16(p[3], vP3, sP, 0, "sc1"); sP += 5120; OPQ(sP);
  *(LDS_AS f32x4*)(wa1 + 0) = acc[0];
  SB();
  MV16(acc[5], p[4], fw[1][1]); SB();
  *(LDS_AS f32x4*)(wa1 + 2048) = acc[1];
  SB();
  MV16(acc[6], p[4], fw[1][2]); SB();
  unsigned c6; { const int l_ = lane_now(); c6 = (l_ & 6) == 6 ? 32 : 0; vFS = (l_ < 7 ? l_ : 6) * FS; asm volatile("" : "+v"(c6), "+v"(vFS)); }
  LD16(xv[0], vOut, sXo, 0, "");
  LD16(xv[1], vOut - c6, sXo, 128, "");
  SB();
  MV16(acc[7], p[4], fw[1][3]); SB();
  UN4("v_exp_f32", "", "", zz);
  SB();
  SB();
  WAITVB(8); SB();
  PRIO(0); SB();
  MV16(acc[4], p[5], fw[2][0]); SB();
  LD16(p[4], vP0, sP, 0, "sc1");
  SB();
  MV16(acc[5], p[5], fw[2][1]); SB();
  *(LDS_AS f32x4*)(wa1 + 4096) = acc[2];
  SB();
  MV16(acc[6], p[5], fw[2][2]); SB();
  *(LDS_AS f32x4*)(wa1 + 6144) = acc[3];
  SB();
  MV16(acc[7], p[5], fw[2][3]); SB();
  UN4("v_add_f32", "1.0, ", "", zz);
  SB();
  SB();
  WAITVB(8); SB();
  MV16(acc[4], p[6], fw[3][0]); SB();
  LD16(p[5], vP1, sP, 0, "sc1");
  SB();
  MV16(acc[5], p[6], fw[3][1]); SB();
  UN4("v_rcp_f32", "", "", zz);
  SB();
  MV16(acc[6], p[6], fw[3][2]); SB();
  BAR();
  STAMP(48);
  SB();
  MV16(acc[7], p[6], fw[3][3]); SB();
  SB();
  WAITVB(8); SB();
  PRIO(3); SB();
  MV16(acc[4], p[7], fw[4][0]); SB();
  LD16(p[6], vP2, sP, 0, "sc1");
  ACC4("v_mul_f32", gy[0], zz);
  SB();
  MV16(acc[5], p[7], fw[4][1]); SB();
  t4[0] = *(LDS_AS const f32x4*)(ra + 0);
  SB();
  MV16(acc[6], p[7], fw[4][2]); SB();
  t4[1] = *(LDS_AS const f32x4*)(ra + 16384);
  t4[2] = *(LDS_AS const f32x4*)(ra + 32768);
  SB();
  MV16(acc[7], p[7], fw[4][3]); SB();
  t4[3] = *(LDS_AS const f32x4*)(ra + 49152);
  SB();
  SB();
  WAITVB(8); SB();
  PRIO(2); SB();
  MV16(acc[4], p[0], fw[5][0]); SB();
  LD16(p[7], vP3, sP, 0, "sc1"); sP += 5120; OPQ(sP);
  t4[4] = *(LDS_AS const f32x4*)(rah + 0);
  SB();
  MV16(acc[5], p[0], fw[5][1]); SB();
  t4[5] = *(LDS_AS const f32x4*)(rah + 16384);
  SB();
  MV16(acc[6], p[0], fw[5][2]); SB();
  t4[6] = *(LDS_AS const f32x4*)(rah + 32768);
  SB();
  MV16(acc[7], p[0], fw[5][3]); SB();
  t4[7] = *(LDS_AS const f32x4*)(rah + 49152);
  SB();
  SB();
  WAITVB(8); SB();
  MV16(acc[4], p[1], fw[6][0]); SB();
  LD16(p[0], vP0, sP, 0, "sc1");
  SB();
  MV16(acc[5], p[1], fw[6][1]); SB();
  LD4(fs, vFS, sFS, 0, "sc1");
  SB();
  MV16(acc[6], p[1], fw[6][2]); SB();
  OP4_3("v_add_f32", gy[1], t4[0], bias);
  SB();
  MV16(acc[7], p[1], fw[6][3]); SB();
  SB();
  WAITVB(9); SB();
  MV16Z(acc[0], p[2], fw[0][0]); SB();
  OP4_3("v_add_f32", zz, t4[1], t4[2]);
  SB();
  MV16Z(acc[1], p[2], fw[0][1]); SB();
  ACC4("v_add_f32", gy[1], t4[3]);
  SB();
  MV16Z(acc[2], p[2], fw[0][2]); SB();
  MV16Z(acc[3], p[2], fw[0][3]); SB();
  SB();
  WAITVB(8); SB();
  MV16(acc[0], p[3], fw[1][0]); SB();
  *(LDS_AS f32x4*)(wa0 + 8192) = acc[4];
  SB();
  MV16(acc[1], p[3], fw[1][1]); SB();
  ACC4("v_add_f32", zz, t4[4]);
  SB();
  MV16(acc[2], p[3], fw[1][2]); SB();
  ACC4("v_add_f32", gy[1], t4[5]);
  SB();
  MV16(acc[3], p[3], fw[1][3]); SB();
  SB();
  WAITVB(5); SB();
  MV16(acc[0], p[4], fw[2][0]); SB();
  *(LDS_AS f32x4*)(wa0 + 10240) = acc[5];
  SB();
  MV16(acc[1], p[4], fw[2][1]); SB();
  ACC4("v_add_f32", zz, t4[6]);
  SB();
  MV16(acc[2], p[4], fw[2][2]); SB();
  ACC4("v_add_f32", gy[1], t4[7]);
  SB();
  MV16(acc[3], p[4], fw[2][3]); SB();
  SB();
  WAITVB(4); SB();
  PRIO(1); SB();
  MV16(acc[0], p[5], fw[3][0]); SB();
  *(LDS_AS f32x4*)(wa0 + 12288) = acc[6];
  SB();
  MV16(acc[1], p[5], fw[3][1]); SB();
  ACC4("v_add_f32", gy[1], zz);
  SB();
  MV16(acc[2], p[5], fw[3][2]); SB();
  MV16(acc[3], p[5], fw[3][3]); SB();
  SB();
  WAITVB(3); SB();
  MV16(acc[0], p[6], fw[4][0]); SB();
  *(LDS_AS f32x4*)(wa0 + 14336) = acc[7];
  SB();
  MV16(acc[1], p[6], fw[4][1]); SB();
  SCL4(zz, gy[1]);
  SB();
  MV16(acc[2], p[6], fw[4][2]); SB();
  UN4("v_exp_f32", "", "", zz);
  SB();
  MV16(acc[3], p[6], fw[4][3]); SB();
  SB();
  WAITVB(2); SB();
  PRIO(0); SB();
  MV16(acc[0], p[7], fw[5][0]); SB();
  WAITV1(7, xv[0]);
  xf2[0] = (f32x2_t){lo2f(xv[0][0]), hi2f(xv[0][0])}; PINV(xf2[0]);
  xf2[1] = (f32x2_t){lo2f(xv[0][1]), hi2f(xv[0][1])}; PINV(xf2[1]);
  xf2[2] = (f32x2_t){lo2f(xv[0][2]), hi2f(xv[0][2])}; PINV(xf2[2]);
  xf2[3] = (f32x2_t){lo2f(xv[0][3]), hi2f(xv[0][3])}; PINV(xf2[3]);
  SB();
  MV16(acc[1], p[7], fw[5][1]); SB();
  WAITV1(0, fs);
  if (__ballot(fs != nonce) != 0ull) poll1<4>(sFS, vFS, nonce);
  STAMP(40);
  LD16(sv4[0], vSt, sS, 0, "sc1");
  { const char* s_ = sS + 12288; LD16(sv4[1], vSt - c6 * 96, s_, 0, "sc1"); }
  SB();
  MV16(acc[2], p[7], fw[5][2]); SB();
  UN4("v_add_f32", "1.0, ", "", zz);
  SB();
  MV16(acc[3], p[7], fw[5][3]); SB();
  UN4("v_rcp_f32", "", "", zz);
  SB();
  SB();
  WAITVB(3); SB();
  MV16(acc[0], p[0], fw[6][0]); SB();
  WAITV1(8, xv[1]);
  xf2[4] = (f32x2_t){lo2f(xv[1][0]), hi2f(xv[1][0])}; PINV(xf2[4]);
  xf2[5] = (f32x2_t){lo2f(xv[1][1]), hi2f(xv[1][1])}; PINV(xf2[5]);
  xf2[6] = (f32x2_t){lo2f(xv[1][2]), hi2f(xv[1][2])}; PINV(xf2[6]);
  xf2[7] = (f32x2_t){lo2f(xv[1][3]), hi2f(xv[1][3])}; PINV(xf2[7]);
  SB();
  MV16(acc[1], p[0], fw[6][1]); SB();
  ACC4("v_mul_f32", gy[1], zz);
  SB();
  MV16(acc[2], p[0], fw[6][2]); SB();
  xchg4(gy[0], gy[1]);
  SB();
  MV16(acc[3], p[0], fw[6][3]); SB();
  NOP(4); SB();
  *(LDS_AS f32x4*)(wa1 + 8192) = acc[0];
  SB();
  *(LDS_AS f32x4*)(wa1 + 10240) = acc[1];
  SB();
  *(LDS_AS f32x4*)(wa1 + 12288) = acc[2];
  SB();
  *(LDS_AS f32x4*)(wa1 + 14336) = acc[3];
  SB();
  BAR();
  STAMP(49);
  t4[0] = *(LDS_AS const f32x4*)(ra + 8192);
  t4[1] = *(LDS_AS const f32x4*)(ra + 24576);
  t4[2] = *(LDS_AS const f32x4*)(ra + 40960);
  t4[3] = *(LDS_AS const f32x4*)(ra + 57344);
  t4[4] = *(LDS_AS const f32x4*)(rah + 8192);
  t4[5] = *(LDS_AS const f32x4*)(rah + 24576);
  t4[6] = *(LDS_AS const f32x4*)(rah + 40960);
  t4[7] = *(LDS_AS const f32x4*)(rah + 57344);
  SB();
  WAITV1(1, sv4[0]);
  ST16(vOut, sOut, 0, ((u32x4){fin2(sv4[0][0], gy[0][0], gy[0][1], xf2[0]), fin2(sv4[0][1], gy[0][2], gy[0][3], xf2[1]), fin2(sv4[0][2], gy[1][0], gy[1][1], xf2[2]), fin2(sv4[0][3], gy[1][2], gy[1][3], xf2[3])}));
  SB();
  { f32x4 y_ = ((t4[0] + (f32x4){bias[0], bias[1], bias[2], bias[3]}) + (t4[1] + t4[2])) + ((t4[3] + t4[4]) + ((t4[5] + t4[6]) + t4[7]));
    f32x4 z_ = y_ * -1.442695041f;
    z_ = (f32x4){__builtin_amdgcn_exp2f(z_[0]), __builtin_amdgcn_exp2f(z_[1]), __builtin_amdgcn_exp2f(z_[2]), __builtin_amdgcn_exp2f(z_[3])} + 1.f;
    z_ = (f32x4){__builtin_amdgcn_rcpf(z_[0]), __builtin_amdgcn_rcpf(z_[1]), __builtin_amdgcn_rcpf(z_[2]), __builtin_amdgcn_rcpf(z_[3])};
    y_ = y_ * z_;
    gy[2][0] = y_[0]; gy[2][1] = y_[1]; gy[2][2] = y_[2]; gy[2][3] = y_[3]; }
  xchg4(gy[2], gy[3]);
  STAMP(41);
  WAITV1(1, sv4[1]);
  { const char* o_ = sOut + 128; ST16E(0x3f3f3f3f, vOut, o_, ((u32x4){fin2(sv4[1][0], gy[2][0], gy[2][1], xf2[4]), fin2(sv4[1][1], gy[2][2], gy[2][3], xf2[5]), fin2(sv4[1][2], gy[3][0], gy[3][1], xf2[6]), fin2(sv4[1][3], gy[3][2], gy[3][3], xf2[7])})); }
  STAMP(42);
  STAMP(50);
  STAMP_DUMP();

  } else {
  if (__builtin_expect(jb < 28, 1)) {
  if (w < 4) {
  DMA2(vWa, vWb, sW, ldsW, 6144, ""); sW = wrap == 1 ? bW : sW + dW; OPQ(sW);
  { int w_ = w; OPQ(w_); { const int w = w_ & 7, lane = lane_now(); const int n16 = lane & 15, g4 = lane >> 4, q4 = n16 >> 2, pq = lane & 3, r8 = lane >> 3; const unsigned vL16 = lane * 16;
  const unsigned vFH = (lane & 31) * FA;
  bf16x8 fw[8][4], p[9]; f32x4 acc[8], t4[8]; float mm[4], zz[4], bias[4], c23 = 2.3022082f;
  unsigned rW0 = ldsP + (8 * g4 + q4) * 128 + (((q4 >> 1) | (2 * (g4 & 1))) * 32) + pq * 8, rW1 = rW0 ^ 32, rW2 = rW0 ^ 64, rW3 = rW0 ^ 96;
  asm volatile("" : "+v"(rW0), "+v"(rW1), "+v"(rW2), "+v"(rW3));
  unsigned vP0 = frag(0, lane), vP1 = frag(1, lane), vP2 = frag(2, lane), vP3 = frag(3, lane);
  const char* sP = A.ws + WS_HT + (size_t)(dg * 8 + w) * HT_W;
  asm volatile("" : "+s"(sP)); asm volatile("" : "+v"(vP0), "+v"(vP1), "+v"(vP2), "+v"(vP3));
  WAITV(5);
  fw[0][0] = tr8u(rW0 + 0, 512);
  fw[0][1] = tr8u(rW1 + 0, 512);
  fw[0][2] = tr8u(rW2 + 0, 512);
  fw[0][3] = tr8u(rW3 + 0, 512);
  DMA2(vWa, vWb, sW, ldsW, 8192, ""); sW += dW; OPQ(sW);
  DMA2(vWa, vWb, sW, ldsW, 10240, ""); sW = wrap == 2 ? bW : sW + dW; OPQ(sW);
  WAITV(4);
  fw[1][0] = tr8u(rW0 + 4096, 512);
  fw[1][1] = tr8u(rW1 + 4096, 512);
  fw[1][2] = tr8u(rW2 + 4096, 512);
  fw[1][3] = tr8u(rW3 + 4096, 512);
  DMA2(vWa, vWb, sW, ldsW, 12288, ""); sW += dW; OPQ(sW);
  DMA2(vWa, vWb, sW, ldsW, 14336, ""); sW = wrap == 3 ? bW : sW + dW; OPQ(sW);
  WAITV(4);
  fw[2][0] = tr8u(rW0 + 8192, 512);
  fw[2][1] = tr8u(rW1 + 8192, 512);
  fw[2][2] = tr8u(rW2 + 8192, 512);
  fw[2][3] = tr8u(rW3 + 8192, 512);
  WAITL(0);
  DMA2(vWa, vWb, sW, ldsW, 0, ""); sW += dW; OPQ(sW);
  DMA2(vWa, vWb, sW, ldsW, 2048, ""); sW = wrap == 4 ? bW : sW + dW; OPQ(sW);
  WAITV(4);
  fw[3][0] = tr8u(rW0 + 12288, 512);
  fw[3][1] = tr8u(rW1 + 12288, 512);
  fw[3][2] = tr8u(rW2 + 12288, 512);
  fw[3][3] = tr8u(rW3 + 12288, 512);
  STAMP(1);
  WAITL(0);
  DMA2(vWa, vWb, sW, ldsW, 4096, ""); sW += dW; OPQ(sW);
  DMA2(vWa, vWb, sW, ldsW, 6144, ""); sW = wrap == 5 ? bW : sW + dW; OPQ(sW);
  bias[0] = lo2f(bv[0]); bias[1] = hi2f(bv[0]); bias[2] = lo2f(bv[1]); bias[3] = hi2f(bv[1]);
  unsigned wa0 = ldsP + n16 * 256 + ((g4 ^ n16) << 4), wa1 = wa0 ^ 64, wa2 = wa0 ^ 128, wa3 = wa0 ^ 192;
  unsigned ra = lds0 + (16 * 0 + n16) * 256 + (((4 * (w & 3) + g4) ^ n16) << 4), rah = ra + 65536, uL = 32 * (w & 1) + 16 * (g4 >> 1) + n16, vU;
  const int kap = 2 * ft + ((w >> 1) & 1), wv = (kap * 37) >> 8, us = kap - 7 * wv, up_ = us - KROT2 * dg;
  int up = up_ < 0 ? up_ + 7 : up_, mU = ((w - 4) >> 31) & 0xffff;
  const char* sU = A.ws + WS_UT + (size_t)(dg * 8 + wv) * UT_W;
  const char* sFU = A.ws + WS_FLAGU + ((dg * 8 + wv) * 128 + 0 * 14 + us * 2 + (w & 1)) * FU;
  asm volatile("" : "+s"(sU), "+s"(sFU), "+s"(up), "+s"(mU)); asm volatile("" : "+v"(wa0), "+v"(wa1), "+v"(wa2), "+v"(wa3), "+v"(ra), "+v"(rah), "+v"(uL), "+v"(bias[0]), "+v"(bias[1]), "+v"(bias[2]), "+v"(bias[3]), "+v"(c23));
  WAITV(4);
  fw[4][0] = tr8u(rW0 + 0, 512);
  fw[4][1] = tr8u(rW1 + 0, 512);
  fw[4][2] = tr8u(rW2 + 0, 512);
  fw[4][3] = tr8u(rW3 + 0, 512);
  WAITL(0);
  DMA2(vWa, vWb, sW, ldsW, 8192, ""); sW += dW; OPQ(sW);
  LD4(fh, vFH, sFH, 0, "sc1");
  DMA2(vWa, vWb, sW, ldsW, 10240, ""); sW = wrap == 6 ? bW : sW + dW; OPQ(sW);
  WAITV(5);
  fw[5][0] = tr8u(rW0 + 4096, 512);
  fw[5][1] = tr8u(rW1 + 4096, 512);
  fw[5][2] = tr8u(rW2 + 4096, 512);
  fw[5][3] = tr8u(rW3 + 4096, 512);
  WAITL(0);
  DMA2(vWa, vWb, sW, ldsW, 12288, ""); sW += dW; OPQ(sW);
  DMA2(vWa, vWb, sW, ldsW, 14336, "");
  WAITV(4);
  fw[6][0] = tr8u(rW0 + 8192, 512);
  fw[6][1] = tr8u(rW1 + 8192, 512);
  fw[6][2] = tr8u(rW2 + 8192, 512);
  fw[6][3] = tr8u(rW3 + 8192, 512);
  WAITV1(6, fh);
  if (__ballot(fh != nonce) != 0ull) poll1<16>(sFH, vFH, nonce);
  STAMP(3);
  LD16(p[0], vP0, sP, 0, "sc1");
  LD16(p[1], vP1, sP, 0, "sc1");
  LD16(p[2], vP2, sP, 0, "sc1");
  LD16(p[3], vP3, sP, 0, "sc1"); sP += 5120; OPQ(sP);
  WAITV(4);
  fw[7][0] = tr8u(rW0 + 12288, 512);
  fw[7][1] = tr8u(rW1 + 12288, 512);
  fw[7][2] = tr8u(rW2 + 12288, 512);
  fw[7][3] = tr8u(rW3 + 12288, 512);
  STAMP(2);
  LD16(p[4], vP0, sP, 0, "sc1");
  LD16(p[5], vP1, sP, 0, "sc1");
  LD16(p[6], vP2, sP, 0, "sc1");
  LD16(p[7], vP3, sP, 0, "sc1"); sP += 5120; OPQ(sP);
  SB();
  WAITVB(7); SB();
  STAMP(4);
  PRIO(3); SB();
  MV16Z(acc[0], fw[0][0], p[0]); SB();
  LD16(p[8], vP0, sP, 0, "sc1");
  SB();
  MV16Z(acc[1], fw[0][1], p[0]); SB();
  MV16Z(acc[2], fw[0][2], p[0]); SB();
  MV16Z(acc[3], fw[0][3], p[0]); SB();
  SB();
  WAITVB(7); SB();
  MV16(acc[0], fw[1][0], p[1]); SB();
  LD16(p[0], vP1, sP, 0, "sc1");
  SB();
  MV16(acc[1], fw[1][1], p[1]); SB();
  MV16(acc[2], fw[1][2], p[1]); SB();
  MV16(acc[3], fw[1][3], p[1]); SB();
  SB();
  WAITVB(7); SB();
  MV16(acc[0], fw[2][0], p[2]); SB();
  LD16(p[1], vP2, sP, 0, "sc1");
  SB();
  MV16(acc[1], fw[2][1], p[2]); SB();
  MV16(acc[2], fw[2][2], p[2]); SB();
  MV16(acc[3], fw[2][3], p[2]); SB();
  SB();
  WAITVB(7); SB();
  MV16(acc[0], fw[3][0], p[3]); SB();
  LD16(p[2], vP3, sP, 0, "sc1"); sP += 5120; OPQ(sP);
  SB();
  MV16(acc[1], fw[3][1], p[3]); SB();
  MV16(acc[2], fw[3][2], p[3]); SB();
  MV16(acc[3], fw[3][3], p[3]); SB();
  SB();
  WAITVB(7); SB();
  MV16(acc[0], fw[4][0], p[4]); SB();
  LD16(p[3], vP0, sP, 0, "sc1");
  SB();
  MV16(acc[1], fw[4][1], p[4]); SB();
  MV16(acc[2], fw[4][2], p[4]); SB();
  MV16(acc[3], fw[4][3], p[4]); SB();
  SB();
  WAITVB(7); SB();
  MV16(acc[0], fw[5][0], p[5]); SB();
  LD16(p[4], vP1, sP, 0, "sc1");
  SB();
  MV16(acc[1], fw[5][1], p[5]); SB();
  MV16(acc[2], fw[5][2], p[5]); SB();
  MV16(acc[3], fw[5][3], p[5]); SB();
  SB();
  WAITVB(7); SB();
  PRIO(2); SB();
  MV16(acc[0], fw[6][0], p[6]); SB();
  LD16(p[5], vP2, sP, 0, "sc1");
  SB();
  MV16(acc[1], fw[6][1], p[6]); SB();
  MV16(acc[2], fw[6][2], p[6]); SB();
  MV16(acc[3], fw[6][3], p[6]); SB();
  SB();
  WAITVB(7); SB();
  MV16(acc[0], fw[7][0], p[7]); SB();
  LD16(p[6], vP3, sP, 0, "sc1"); sP += 5120; OPQ(sP);
  SB();
  MV16(acc[1], fw[7][1], p[7]); SB();
  MV16(acc[2], fw[7][2], p[7]); SB();
  MV16(acc[3], fw[7][3], p[7]); SB();
  SB();
  WAITVB(7); SB();
  MV16Z(acc[4], fw[0][0], p[8]); SB();
  LD16(p[7], vP0, sP, 0, "sc1");
  SB();
  MV16Z(acc[5], fw[0][1], p[8]); SB();
  MV16Z(acc[6], fw[0][2], p[8]); SB();
  MV16Z(acc[7], fw[0][3], p[8]); SB();
  SB();
  WAITVB(7); SB();
  PRIO(1); SB();
  MV16(acc[4], fw[1][0], p[0]); SB();
  LD16(p[8], vP1, sP, 0, "sc1");
  SB();
  MV16(acc[5], fw[1][1], p[0]); SB();
  *(LDS_AS f32x4*)(wa0 + 0) = acc[0];
  SB();
  MV16(acc[6], fw[1][2], p[0]); SB();
  *(LDS_AS f32x4*)(wa1 + 0) = acc[1];
  SB();
  MV16(acc[7], fw[1][3], p[0]); SB();
  SB();
  WAITVB(7); SB();
  PRIO(0); SB();
  MV16(acc[4], fw[2][0], p[1]); SB();
  LD16(p[0], vP2, sP, 0, "sc1");
  SB();
  MV16(acc[5], fw[2][1], p[1]); SB();
  *(LDS_AS f32x4*)(wa2 + 0) = acc[2];
  SB();
  MV16(acc[6], fw[2][2], p[1]); SB();
  *(LDS_AS f32x4*)(wa3 + 0) = acc[3];
  SB();
  MV16(acc[7], fw[2][3], p[1]); SB();
  BAR();
  STAMP(5);
  SB();
  SB();
  WAITVB(7); SB();
  PRIO(3); SB();
  MV16(acc[4], fw[3][0], p[2]); SB();
  LD16(p[1], vP3, sP, 0, "sc1"); sP += 5120; OPQ(sP);
  t4[0] = *(LDS_AS const f32x4*)(ra + 0);
  SB();
  MV16(acc[5], fw[3][1], p[2]); SB();
  t4[1] = *(LDS_AS const f32x4*)(ra + 16384);
  SB();
  MV16(acc[6], fw[3][2], p[2]); SB();
  t4[2] = *(LDS_AS const f32x4*)(ra + 32768);
  SB();
  MV16(acc[7], fw[3][3], p[2]); SB();
  t4[3] = *(LDS_AS const f32x4*)(ra + 49152);
  SB();
  SB();
  WAITVB(7); SB();
  MV16(acc[4], fw[4][0], p[3]); SB();
  LD16(p[2], vP0, sP, 0, "sc1");
  t4[4] = *(LDS_AS const f32x4*)(rah + 0);
  SB();
  MV16(acc[5], fw[4][1], p[3]); SB();
  t4[5] = *(LDS_AS const f32x4*)(rah + 16384);
  SB();
  MV16(acc[6], fw[4][2], p[3]); SB();
  t4[6] = *(LDS_AS const f32x4*)(rah + 32768);
  t4[7] = *(LDS_AS const f32x4*)(rah + 49152);
  SB();
  MV16(acc[7], fw[4][3], p[3]); SB();
  { const int ix = 0 + up; vU = (ix >> 2) * 5120 + frag(ix & 3, uL); PINV(vU); }
  SB();
  SB();
  WAITVB(7); SB();
  MV16(acc[4], fw[5][0], p[4]); SB();
  LD16(p[3], vP1, sP, 0, "sc1");
  OP4_3("v_add_f32", mm, t4[0], bias);
  SB();
  MV16(acc[5], fw[5][1], p[4]); SB();
  OP4_3("v_add_f32", zz, t4[1], t4[2]);
  SB();
  MV16(acc[6], fw[5][2], p[4]); SB();
  ACC4("v_add_f32", mm, t4[3]);
  SB();
  MV16(acc[7], fw[5][3], p[4]); SB();
  ACC4("v_add_f32", zz, t4[4]);
  SB();
  SB();
  WAITVB(7); SB();
  PRIO(2); SB();
  MV16(acc[4], fw[6][0], p[5]); SB();
  LD16(p[4], vP2, sP, 0, "sc1");
  ACC4("v_add_f32", mm, t4[5]);
  SB();
  MV16(acc[5], fw[6][1], p[5]); SB();
  ACC4("v_add_f32", zz, t4[6]);
  SB();
  MV16(acc[6], fw[6][2], p[5]); SB();
  ACC4("v_add_f32", mm, t4[7]);
  SB();
  MV16(acc[7], fw[6][3], p[5]); SB();
  ACC4("v_add_f32", mm, zz);
  SB();
  SB();
  WAITVB(7); SB();
  MV16(acc[4], fw[7][0], p[6]); SB();
  LD16(p[5], vP3, sP, 0, "sc1"); sP += 5120; OPQ(sP);
  OP4_3("v_mul_f32", zz, mm, mm);
  SB();
  MV16(acc[5], fw[7][1], p[6]); SB();
  POLY4(zz, c23);
  SB();
  MV16(acc[6], fw[7][2], p[6]); SB();
  ACC4("v_mul_f32", zz, mm);
  SB();
  MV16(acc[7], fw[7][3], p[6]); SB();
  UN4("v_exp_f32", "", "", zz);
  SB();
  SB();
  WAITVB(7); SB();
  MV16Z(acc[0], fw[0][0], p[7]); SB();
  LD16(p[6], vP0, sP, 0, "sc1");
  UN4("v_add_f32", "1.0, ", "", zz);
  SB();
  MV16Z(acc[1], fw[0][1], p[7]); SB();
  UN4("v_rcp_f32", "", "", zz);
  SB();
  MV16Z(acc[2], fw[0][2], p[7]); SB();
  NFMA4(mm, zz);
  SB();
  MV16Z(acc[3], fw[0][3], p[7]); SB();
  { const unsigned x0 = pack2(mm[0], mm[1]), x1 = pack2(mm[2], mm[3]);
    const auto r0 = __builtin_amdgcn_permlane16_swap(x0, x0, false, false), r1 = __builtin_amdgcn_permlane16_swap(x1, x1, false, false);
    ST16E(0xffff, vU, sU, ((u32x4){r0[0], r1[0], r0[1], r1[1]})); }
  SB();
  SB();
  WAITVB(8); SB();
  PRIO(1); SB();
  MV16(acc[0], fw[1][0], p[8]); SB();
  LD16(p[7], vP1, sP, 0, "sc1");
  SB();
  MV16(acc[1], fw[1][1], p[8]); SB();
  *(LDS_AS f32x4*)(wa0 + 8192) = acc[4];
  SB();
  MV16(acc[2], fw[1][2], p[8]); SB();
  *(LDS_AS f32x4*)(wa1 + 8192) = acc[5];
  SB();
  MV16(acc[3], fw[1][3], p[8]); SB();
  SB();
  WAITVB(8); SB();
  PRIO(0); SB();
  MV16(acc[0], fw[2][0], p[0]); SB();
  LD16(p[8], vP2, sP, 0, "sc1");
  SB();
  MV16(acc[1], fw[2][1], p[0]); SB();
  *(LDS_AS f32x4*)(wa2 + 8192) = acc[6];
  SB();
  MV16(acc[2], fw[2][2], p[0]); SB();
  *(LDS_AS f32x4*)(wa3 + 8192) = acc[7];
  SB();
  MV16(acc[3], fw[2][3], p[0]); SB();
  BAR();
  SB();
  SB();
  WAITVB(8); SB();
  PRIO(3); SB();
  MV16(acc[0], fw[3][0], p[1]); SB();
  LD16(p[0], vP3, sP, 0, "sc1"); sP += 5120; OPQ(sP);
  SB();
  MV16(acc[1], fw[3][1], p[1]); SB();
  MV16(acc[2], fw[3][2], p[1]); SB();
  MV16(acc[3], fw[3][3], p[1]); SB();
  SB();
  WAITVB(8); SB();
  MV16(acc[0], fw[4][0], p[2]); SB();
  LD16(p[1], vP0, sP, 0, "sc1");
  SB();
  MV16(acc[1], fw[4][1], p[2]); SB();
  MV16(acc[2], fw[4][2], p[2]); SB();
  MV16(acc[3], fw[4][3], p[2]); SB();
  SB();
  WAITVB(8); SB();
  MV16(acc[0], fw[5][0], p[3]); SB();
  LD16(p[2], vP1, sP, 0, "sc1");
  SB();
  MV16(acc[1], fw[5][1], p[3]); SB();
  MV16(acc[2], fw[5][2], p[3]); SB();
  MV16(acc[3], fw[5][3], p[3]); SB();
  SB();
  WAITVB(8); SB();
  PRIO(2); SB();
  MV16(acc[0], fw[6][0], p[4]); SB();
  LD16(p[3], vP2, sP, 0, "sc1");
  SB();
  MV16(acc[1], fw[6][1], p[4]); SB();
  MV16(acc[2], fw[6][2], p[4]); SB();
  MV16(acc[3], fw[6][3], p[4]); SB();
  SB();
  WAITVB(8); SB();
  MV16(acc[0], fw[7][0], p[5]); SB();
  LD16(p[4], vP3, sP, 0, "sc1"); sP += 5120; OPQ(sP);
  SB();
  MV16(acc[1], fw[7][1], p[5]); SB();
  MV16(acc[2], fw[7][2], p[5]); SB();
  MV16(acc[3], fw[7][3], p[5]); SB();
  SB();
  WAITVB(8); SB();
  MV16Z(acc[4], fw[0][0], p[6]); SB();
  LD16(p[5], vP0, sP, 0, "sc1");
  SB();
  MV16Z(acc[5], fw[0][1], p[6]); SB();
  MV16Z(acc[6], fw[0][2], p[6]); SB();
  MV16Z(acc[7], fw[0][3], p[6]); SB();
  SB();
  WAITVB(7); SB();
  PRIO(1); SB();
  MV16(acc[4], fw[1][0], p[7]); SB();
  LD16(p[6], vP1, sP, 0, "sc1");
  SB();
  MV16(acc[5], fw[1][1], p[7]); SB();
  *(LDS_AS f32x4*)(wa0 + 12288) = acc[0];
  SB();
  MV16(acc[6], fw[1][2], p[7]); SB();
  *(LDS_AS f32x4*)(wa1 + 12288) = acc[1];
  SB();
  MV16(acc[7], fw[1][3], p[7]); SB();
  { const char* f_ = sFU + 0 * FU; const unsigned l_ = lane_now() * 16; ST16X(1, l_, f_, nonce); }
  SB();
  SB();
  WAITVB(8); SB();
  PRIO(0); SB();
  MV16(acc[4], fw[2][0], p[8]); SB();
  LD16(p[7], vP2, sP, 0, "sc1");
  SB();
  MV16(acc[5], fw[2][1], p[8]); SB();
  *(LDS_AS f32x4*)(wa2 + 12288) = acc[2];
  SB();
  MV16(acc[6], fw[2][2], p[8]); SB();
  *(LDS_AS f32x4*)(wa3 + 12288) = acc[3];
  SB();
  MV16(acc[7], fw[2][3], p[8]); SB();
  BAR();
  STAMP(6);
  SB();
  SB();
  WAITVB(8); SB();
  PRIO(3); SB();
  MV16(acc[4], fw[3][0], p[0]); SB();
  LD16(p[8], vP3, sP, 0, "sc1"); sP += 5120; OPQ(sP);
  t4[0] = *(LDS_AS const f32x4*)(ra + 12288);
  SB();
  MV16(acc[5], fw[3][1], p[0]); SB();
  t4[1] = *(LDS_AS const f32x4*)(ra + 28672);
  SB();
  MV16(acc[6], fw[3][2], p[0]); SB();
  t4[2] = *(LDS_AS const f32x4*)(ra + 45056);
  SB();
  MV16(acc[7], fw[3][3], p[0]); SB();
  t4[3] = *(LDS_AS const f32x4*)(ra + 61440);
  SB();
  SB();
  WAITVB(8); SB();
  MV16(acc[4], fw[4][0], p[1]); SB();
  LD16(p[0], vP0, sP, 0, "sc1");
  t4[4] = *(LDS_AS const f32x4*)(rah + 12288);
  SB();
  MV16(acc[5], fw[4][1], p[1]); SB();
  t4[5] = *(LDS_AS const f32x4*)(rah + 28672);
  SB();
  MV16(acc[6], fw[4][2], p[1]); SB();
  t4[6] = *(LDS_AS const f32x4*)(rah + 45056);
  t4[7] = *(LDS_AS const f32x4*)(rah + 61440);
  SB();
  MV16(acc[7], fw[4][3], p[1]); SB();
  { const int ix = 14 + up; vU = (ix >> 2) * 5120 + frag(ix & 3, uL); PINV(vU); }
  SB();
  SB();
  WAITVB(8); SB();
  MV16(acc[4], fw[5][0], p[2]); SB();
  LD16(p[1], vP1, sP, 0, "sc1");
  OP4_3("v_add_f32", mm, t4[0], bias);
  SB();
  MV16(acc[5], fw[5][1], p[2]); SB();
  OP4_3("v_add_f32", zz, t4[1], t4[2]);
  SB();
  MV16(acc[6], fw[5][2], p[2]); SB();
  ACC4("v_add_f32", mm, t4[3]);
  SB();
  MV16(acc[7], fw[5][3], p[2]); SB();
  ACC4("v_add_f32", zz, t4[4]);
  SB();
  SB();
  WAITVB(8); SB();
  PRIO(2); SB();
  MV16(acc[4], fw[6][0], p[3]); SB();
  LD16(p[2], vP2, sP, 0, "sc1");
  ACC4("v_add_f32", mm, t4[5]);
  SB();
  MV16(acc[5], fw[6][1], p[3]); SB();
  ACC4("v_add_f32", zz, t4[6]);
  SB();
  MV16(acc[6], fw[6][2], p[3]); SB();
  ACC4("v_add_f32", mm, t4[7]);
  SB();
  MV16(acc[7], fw[6][3], p[3]); SB();
  ACC4("v_add_f32", mm, zz);
  SB();
  SB();
  WAITVB(8); SB();
  MV16(acc[4], fw[7][0], p[4]); SB();
  LD16(p[3], vP3, sP, 0, "sc1"); sP += 5120; OPQ(sP);
  OP4_3("v_mul_f32", zz, mm, mm);
  SB();
  MV16(acc[5], fw[7][1], p[4]); SB();
  POLY4(zz, c23);
  SB();
  MV16(acc[6], fw[7][2], p[4]); SB();
  ACC4("v_mul_f32", zz, mm);
  SB();
  MV16(acc[7], fw[7][3], p[4]); SB();
  UN4("v_exp_f32", "", "", zz);
  SB();
  SB();
  WAITVB(8); SB();
  MV16Z(acc[0], fw[0][0], p[5]); SB();
  LD16(p[4], vP0, sP, 0, "sc1");
  UN4("v_add_f32", "1.0, ", "", zz);
  SB();
  MV16Z(acc[1], fw[0][1], p[5]); SB();
  UN4("v_rcp_f32", "", "", zz);
  SB();
  MV16Z(acc[2], fw[0][2], p[5]); SB();
  NFMA4(mm, zz);
  SB();
  MV16Z(acc[3], fw[0][3], p[5]); SB();
  { const unsigned x0 = pack2(mm[0], mm[1]), x1 = pack2(mm[2], mm[3]);
    const auto r0 = __builtin_amdgcn_permlane16_swap(x0, x0, false, false), r1 = __builtin_amdgcn_permlane16_swap(x1, x1, false, false);
    ST16E(0xffff, vU, sU, ((u32x4){r0[0], r1[0], r0[1], r1[1]})); }
  SB();
  SB();
  WAITVB(9); SB();
  PRIO(1); SB();
  MV16(acc[0], fw[1][0], p[6]); SB();
  LD16(p[5], vP1, sP, 0, "sc1");
  SB();
  MV16(acc[1], fw[1][1], p[6]); SB();
  *(LDS_AS f32x4*)(wa0 + 0) = acc[4];
  SB();
  MV16(acc[2], fw[1][2], p[6]); SB();
  *(LDS_AS f32x4*)(wa1 + 0) = acc[5];
  SB();
  MV16(acc[3], fw[1][3], p[6]); SB();
  SB();
  WAITVB(8); SB();
  PRIO(0); SB();
  MV16(acc[0], fw[2][0], p[7]); SB();
  LD16(p[6], vP2, sP, 0, "sc1");
  SB();
  MV16(acc[1], fw[2][1], p[7]); SB();
  *(LDS_AS f32x4*)(wa2 + 0) = acc[6];
  SB();
  MV16(acc[2], fw[2][2], p[7]); SB();
  *(LDS_AS f32x4*)(wa3 + 0) = acc[7];
  SB();
  MV16(acc[3], fw[2][3], p[7]); SB();
  BAR();
  SB();
  SB();
  WAITVB(8); SB();
  PRIO(3); SB();
  MV16(acc[0], fw[3][0], p[8]); SB();
  LD16(p[7], vP3, sP, 0, "sc1"); sP += 5120; OPQ(sP);
  SB();
  MV16(acc[1], fw[3][1], p[8]); SB();
  MV16(acc[2], fw[3][2], p[8]); SB();
  MV16(acc[3], fw[3][3], p[8]); SB();
  SB();
  WAITVB(8); SB();
  MV16(acc[0], fw[4][0], p[0]); SB();
  LD16(p[8], vP0, sP, 0, "sc1");
  SB();
  MV16(acc[1], fw[4][1], p[0]); SB();
  MV16(acc[2], fw[4][2], p[0]); SB();
  MV16(acc[3], fw[4][3], p[0]); SB();
  SB();
  WAITVB(8); SB();
  MV16(acc[0], fw[5][0], p[1]); SB();
  LD16(p[0], vP1, sP, 0, "sc1");
  SB();
  MV16(acc[1], fw[5][1], p[1]); SB();
  MV16(acc[2], fw[5][2], p[1]); SB();
  MV16(acc[3], fw[5][3], p[1]); SB();
  SB();
  WAITVB(8); SB();
  PRIO(2); SB();
  MV16(acc[0], fw[6][0], p[2]); SB();
  LD16(p[1], vP2, sP, 0, "sc1");
  SB();
  MV16(acc[1], fw[6][1], p[2]); SB();
  MV16(acc[2], fw[6][2], p[2]); SB();
  MV16(acc[3], fw[6][3], p[2]); SB();
  SB();
  WAITVB(8); SB();
  MV16(acc[0], fw[7][0], p[3]); SB();
  LD16(p[2], vP3, sP, 0, "sc1"); sP += 5120; OPQ(sP);
  SB();
  MV16(acc[1], fw[7][1], p[3]); SB();
  MV16(acc[2], fw[7][2], p[3]); SB();
  MV16(acc[3], fw[7][3], p[3]); SB();
  SB();
  WAITVB(8); SB();
  MV16Z(acc[4], fw[0][0], p[4]); SB();
  LD16(p[3], vP0, sP, 0, "sc1");
  SB();
  MV16Z(acc[5], fw[0][1], p[4]); SB();
  MV16Z(acc[6], fw[0][2], p[4]); SB();
  MV16Z(acc[7], fw[0][3], p[4]); SB();
  SB();
  WAITVB(7); SB();
  PRIO(1); SB();
  MV16(acc[4], fw[1][0], p[5]); SB();
  LD16(p[4], vP1, sP, 0, "sc1");
  SB();
  MV16(acc[5], fw[1][1], p[5]); SB();
  *(LDS_AS f32x4*)(wa0 + 4096) = acc[0];
  SB();
  MV16(acc[6], fw[1][2], p[5]); SB();
  *(LDS_AS f32x4*)(wa1 + 4096) = acc[1];
  SB();
  MV16(acc[7], fw[1][3], p[5]); SB();
  { const char* f_ = sFU + 28 * FU; const unsigned l_ = lane_now() * 16; ST16X(1, l_, f_, nonce); }
  SB();
  SB();
  WAITVB(8); SB();
  PRIO(0); SB();
  MV16(acc[4], fw[2][0], p[6]); SB();
  LD16(p[5], vP2, sP, 0, "sc1");
  SB();
  MV16(acc[5], fw[2][1], p[6]); SB();
  *(LDS_AS f32x4*)(wa2 + 4096) = acc[2];
  SB();
  MV16(acc[6], fw[2][2], p[6]); SB();
  *(LDS_AS f32x4*)(wa3 + 4096) = acc[3];
  SB();
  MV16(acc[7], fw[2][3], p[6]); SB();
  BAR();
  STAMP(7);
  SB();
  SB();
  WAITVB(8); SB();
  PRIO(3); SB();
  MV16(acc[4], fw[3][0], p[7]); SB();
  LD16(p[6], vP3, sP, 0, "sc1"); sP += 5120; OPQ(sP);
  t4[0] = *(LDS_AS const f32x4*)(ra + 4096);
  SB();
  MV16(acc[5], fw[3][1], p[7]); SB();
  t4[1] = *(LDS_AS const f32x4*)(ra + 20480);
  SB();
  MV16(acc[6], fw[3][2], p[7]); SB();
  t4[2] = *(LDS_AS const f32x4*)(ra + 36864);
  SB();
  MV16(acc[7], fw[3][3], p[7]); SB();
  t4[3] = *(LDS_AS const f32x4*)(ra + 53248);
  SB();
  SB();
  WAITVB(8); SB();
  MV16(acc[4], fw[4][0], p[8]); SB();
  LD16(p[7], vP0, sP, 0, "sc1");
  t4[4] = *(LDS_AS const f32x4*)(rah + 4096);
  SB();
  MV16(acc[5], fw[4][1], p[8]); SB();
  t4[5] = *(LDS_AS const f32x4*)(rah + 20480);
  SB();
  MV16(acc[6], fw[4][2], p[8]); SB();
  t4[6] = *(LDS_AS const f32x4*)(rah + 36864);
  t4[7] = *(LDS_AS const f32x4*)(rah + 53248);
  SB();
  MV16(acc[7], fw[4][3], p[8]); SB();
  { const int ix = 28 + up; vU = (ix >> 2) * 5120 + frag(ix & 3, uL); PINV(vU); }
  SB();
  SB();
  WAITVB(8); SB();
  MV16(acc[4], fw[5][0], p[0]); SB();
  LD16(p[8], vP1, sP, 0, "sc1");
  OP4_3("v_add_f32", mm, t4[0], bias);
  SB();
  MV16(acc[5], fw[5][1], p[0]); SB();
  OP4_3("v_add_f32", zz, t4[1], t4[2]);
  SB();
  MV16(acc[6], fw[5][2], p[0]); SB();
  ACC4("v_add_f32", mm, t4[3]);
  SB();
  MV16(acc[7], fw[5][3], p[0]); SB();
  ACC4("v_add_f32", zz, t4[4]);
  SB();
  SB();
  WAITVB(8); SB();
  PRIO(2); SB();
  MV16(acc[4], fw[6][0], p[1]); SB();
  LD16(p[0], vP2, sP, 0, "sc1");
  ACC4("v_add_f32", mm, t4[5]);
  SB();
  MV16(acc[5], fw[6][1], p[1]); SB();
  ACC4("v_add_f32", zz, t4[6]);
  SB();
  MV16(acc[6], fw[6][2], p[1]); SB();
  ACC4("v_add_f32", mm, t4[7]);
  SB();
  MV16(acc[7], fw[6][3], p[1]); SB();
  ACC4("v_add_f32", mm, zz);
  SB();
  SB();
  WAITVB(8); SB();
  MV16(acc[4], fw[7][0], p[2]); SB();
  LD16(p[1], vP3, sP, 0, "sc1");
  OP4_3("v_mul_f32", zz, mm, mm);
  SB();
  MV16(acc[5], fw[7][1], p[2]); SB();
  POLY4(zz, c23);
  SB();
  MV16(acc[6], fw[7][2], p[2]); SB();
  ACC4("v_mul_f32", zz, mm);
  SB();
  MV16(acc[7], fw[7][3], p[2]); SB();
  UN4("v_exp_f32", "", "", zz);
  SB();
  SB();
  WAITVB(8); SB();
  MV16Z(acc[0], fw[0][0], p[3]); SB();
  bf16x8 fo[14];
  const char* sWo = A.ws + WS_WO + (size_t)(dg * 7 + (w < 7 ? w : 6)) * WO_B; unsigned long long mW = w < 7 ? ~0ull : 1ull; OPQ(sWo); OPQ(mW);
  UN4("v_add_f32", "1.0, ", "", zz);
  SB();
  MV16Z(acc[1], fw[0][1], p[3]); SB();
  UN4("v_rcp_f32", "", "", zz);
  SB();
  MV16Z(acc[2], fw[0][2], p[3]); SB();
  NFMA4(mm, zz);
  SB();
  MV16Z(acc[3], fw[0][3], p[3]); SB();
  { const unsigned x0 = pack2(mm[0], mm[1]), x1 = pack2(mm[2], mm[3]);
    const auto r0 = __builtin_amdgcn_permlane16_swap(x0, x0, false, false), r1 = __builtin_amdgcn_permlane16_swap(x1, x1, false, false);
    ST16E(0xffff, vU, sU, ((u32x4){r0[0], r1[0], r0[1], r1[1]})); }
  SB();
  SB();
  WAITVB(8); SB();
  PRIO(1); SB();
  MV16(acc[0], fw[1][0], p[4]); SB();
  *(LDS_AS f32x4*)(wa0 + 8192) = acc[4];
  SB();
  MV16(acc[1], fw[1][1], p[4]); SB();
  *(LDS_AS f32x4*)(wa1 + 8192) = acc[5];
  SB();
  MV16(acc[2], fw[1][2], p[4]); SB();
  MV16(acc[3], fw[1][3], p[4]); SB();
  SB();
  WAITVB(6); SB();
  PRIO(0); SB();
  MV16(acc[0], fw[2][0], p[5]); SB();
  *(LDS_AS f32x4*)(wa2 + 8192) = acc[6];
  SB();
  MV16(acc[1], fw[2][1], p[5]); SB();
  *(LDS_AS f32x4*)(wa3 + 8192) = acc[7];
  SB();
  MV16(acc[2], fw[2][2], p[5]); SB();
  BAR();
  SB();
  MV16(acc[3], fw[2][3], p[5]); SB();
  SB();
  WAITVB(5); SB();
  PRIO(2); SB();
  MV16(acc[0], fw[3][0], p[6]); SB();
  MV16(acc[1], fw[3][1], p[6]); SB();
  MV16(acc[2], fw[3][2], p[6]); SB();
  MV16(acc[3], fw[3][3], p[6]); SB();
  SB();
  WAITVB(4); SB();
  MV16(acc[0], fw[4][0], p[7]); SB();
  MV16(acc[1], fw[4][1], p[7]); SB();
  MV16(acc[2], fw[4][2], p[7]); SB();
  MV16(acc[3], fw[4][3], p[7]); SB();
  SB();
  WAITVB(3); SB();
  MV16(acc[0], fw[5][0], p[8]); SB();
  LD16M(fo[0], vP0, sWo, mW);
  LD16M(fo[1], vP1, sWo, mW);
  SB();
  MV16(acc[1], fw[5][1], p[8]); SB();
  MV16(acc[2], fw[5][2], p[8]); SB();
  MV16(acc[3], fw[5][3], p[8]); SB();
  SB();
  WAITVB(4); SB();
  PRIO(1); SB();
  MV16(acc[0], fw[6][0], p[0]); SB();
  LD16M(fo[2], vP2, sWo, mW);
  LD16M(fo[3], vP3, sWo, mW); sWo += 5120; OPQ(sWo);
  SB();
  MV16(acc[1], fw[6][1], p[0]); SB();
  MV16(acc[2], fw[6][2], p[0]); SB();
  MV16(acc[3], fw[6][3], p[0]); SB();
  SB();
  WAITVB(5); SB();
  PRIO(0); SB();
  MV16(acc[0], fw[7][0], p[1]); SB();
  LD16M(fo[4], vP0, sWo, mW);
  LD16M(fo[5], vP1, sWo, mW);
  SB();
  MV16(acc[1], fw[7][1], p[1]); SB();
  MV16(acc[2], fw[7][2], p[1]); SB();
  MV16(acc[3], fw[7][3], p[1]); SB();
  NOP(4); SB();
  *(LDS_AS f32x4*)(wa0 + 12288) = acc[0];
  SB();
  *(LDS_AS f32x4*)(wa1 + 12288) = acc[1];
  SB();
  *(LDS_AS f32x4*)(wa2 + 12288) = acc[2];
  SB();
  *(LDS_AS f32x4*)(wa3 + 12288) = acc[3];
  SB();
  BAR();
  STAMP(8);
  t4[0] = *(LDS_AS const f32x4*)(ra + 12288);
  t4[1] = *(LDS_AS const f32x4*)(ra + 28672);
  t4[2] = *(LDS_AS const f32x4*)(ra + 45056);
  t4[3] = *(LDS_AS const f32x4*)(ra + 61440);
  t4[4] = *(LDS_AS const f32x4*)(rah + 12288);
  t4[5] = *(LDS_AS const f32x4*)(rah + 28672);
  t4[6] = *(LDS_AS const f32x4*)(rah + 45056);
  t4[7] = *(LDS_AS const f32x4*)(rah + 61440);
  { const int ix = 42 + up; vU = (ix >> 2) * 5120 + frag(ix & 3, uL); PINV(vU); }
  WAITV(6);
  { const char* f_ = sFU + 56 * FU; const unsigned l_ = lane_now() * 16; ST16X(1, l_, f_, nonce); }
  { f32x4 y_ = ((t4[0] + (f32x4){bias[0], bias[1], bias[2], bias[3]}) + (t4[1] + t4[2])) + ((t4[3] + t4[4]) + ((t4[5] + t4[6]) + t4[7]));
    f32x4 z_ = y_ * (y_ * y_ * 0.10294324f + 2.3022082f);
    z_ = (f32x4){__builtin_amdgcn_exp2f(z_[0]), __builtin_amdgcn_exp2f(z_[1]), __builtin_amdgcn_exp2f(z_[2]), __builtin_amdgcn_exp2f(z_[3])} + 1.f;
    z_ = (f32x4){__builtin_amdgcn_rcpf(z_[0]), __builtin_amdgcn_rcpf(z_[1]), __builtin_amdgcn_rcpf(z_[2]), __builtin_amdgcn_rcpf(z_[3])};
    y_ = y_ - y_ * z_;
    mm[0] = y_[0]; mm[1] = y_[1]; mm[2] = y_[2]; mm[3] = y_[3]; }
  { const unsigned x0 = pack2(mm[0], mm[1]), x1 = pack2(mm[2], mm[3]);
    const auto r0 = __builtin_amdgcn_permlane16_swap(x0, x0, false, false), r1 = __builtin_amdgcn_permlane16_swap(x1, x1, false, false);
    ST16E(0xffff, vU, sU, ((u32x4){r0[0], r1[0], r0[1], r1[1]})); }
  STAMP(9);
  LD16M(fo[6], vP2, sWo, mW);
  LD16M(fo[7], vP3, sWo, mW); sWo += 5120; OPQ(sWo);
  WAITV(2);
  { const char* f_ = sFU + 84 * FU; const unsigned l_ = lane_now() * 16; ST16X(1, l_, f_, nonce); }
  LD16M(fo[8], vP0, sWo, mW);
  LD16M(fo[9], vP1, sWo, mW);
  LD16M(fo[10], vP2, sWo, mW);
  LD16M(fo[11], vP3, sWo, mW); sWo += 5120; OPQ(sWo);
  LD16M(fo[12], vP0, sWo, mW);
  LD16M(fo[13], vP1, sWo, mW);
  { int w_ = w; OPQ(w_); { const int w = w_ & 7, lane = lane_now(); const int n16 = lane & 15, g4 = lane >> 4, q4 = n16 >> 2, pq = lane & 3, r8 = lane >> 3; const unsigned vL16 = lane * 16;
  unsigned vO[7];
#pragma unroll
  for (int i = 0; i < 7; ++i) { vO[i] = line7(64 * i + 8 * w + r8) + (lane & 7) * 16; PINV(vO[i]); }
  const char* sO = A.ws + WS_OT + (size_t)st * OT_T;
  unsigned vFO = (((lane >> 2) & 7) * 8 + (lane & 3)) * FO, ro = lds0 + vL16, roh = ro + 65536;
  unsigned vS0 = pair3(q4) + (2 * (n16 & 3) + (g4 & 1)) * 32 + (g4 >> 1) * 16, vS1 = vS0 - pair3(q4) + pair3(4 + q4);
  const char* sFO = A.ws + WS_FLAGO + ((st >> 1) * 64 + (st & 1) * 4) * FO;
  const char* sS = A.ws + WS_ST + (size_t)(st * 4 + dg) * ST_T + w * 3072;
  const char* sFS = A.ws + WS_FLAGS + ((st * 4 + dg) * 8 + w) * FS;
  asm volatile("" : "+s"(sO), "+s"(sFO), "+s"(sS), "+s"(sFS)); asm volatile("" : "+v"(vFO), "+v"(ro), "+v"(roh), "+v"(vS0), "+v"(vS1));
  STAMP(10);
  STAMP(11);
  if (w == 7) poll1<3>(sFO, vFO, nonce);
  BAR();
  STAMP(12);
  bf16x8 of[3][4]; f32x4 as[4];
  DMA1(vO[0], sO, ldsP, 0, "sc1");
  DMA1(vO[1], sO, ldsP, 1024, "sc1");
  DMA1(vO[2], sO, ldsP, 2048, "sc1");
  DMA1(vO[3], sO, ldsP, 3072, "sc1");
  DMA1(vO[4], sO, ldsP, 4096, "sc1");
  DMA1(vO[5], sO, ldsP, 5120, "sc1");
  DMA1(vO[6], sO, ldsP, 6144, "sc1");
  WAITV(4);
  BAR0();
  if (w == 7) {
  WAITV(0);
  BAR0();
  } else {
  of[0][0] = *(LDS_AS const bf16x8*)(ro + 0);
  of[0][1] = *(LDS_AS const bf16x8*)(ro + 16384);
  of[0][2] = *(LDS_AS const bf16x8*)(ro + 32768);
  of[0][3] = *(LDS_AS const bf16x8*)(ro + 49152);
  STAMP(13);
  of[1][0] = *(LDS_AS const bf16x8*)(roh + 0);
  of[1][1] = *(LDS_AS const bf16x8*)(roh + 16384);
  of[1][2] = *(LDS_AS const bf16x8*)(roh + 32768);
  of[1][3] = *(LDS_AS const bf16x8*)(roh + 49152);
  SB();
  MV16Z(as[0], fo[0], of[0][0]); SB();
  of[2][0] = *(LDS_AS const bf16x8*)(ro + 1024);
  SB();
  MV16Z(as[1], fo[0], of[0][1]); SB();
  of[2][1] = *(LDS_AS const bf16x8*)(ro + 17408);
  SB();
  MV16Z(as[2], fo[0], of[0][2]); SB();
  of[2][2] = *(LDS_AS const bf16x8*)(ro + 33792);
  SB();
  MV16Z(as[3], fo[0], of[0][3]); SB();
  of[2][3] = *(LDS_AS const bf16x8*)(ro + 50176);
  SB();
  SB();
  MV16(as[0], fo[1], of[1][0]); SB();
  of[0][0] = *(LDS_AS const bf16x8*)(roh + 1024);
  SB();
  MV16(as[1], fo[1], of[1][1]); SB();
  of[0][1] = *(LDS_AS const bf16x8*)(roh + 17408);
  SB();
  MV16(as[2], fo[1], of[1][2]); SB();
  of[0][2] = *(LDS_AS const bf16x8*)(roh + 33792);
  SB();
  MV16(as[3], fo[1], of[1][3]); SB();
  of[0][3] = *(LDS_AS const bf16x8*)(roh + 50176);
  SB();
  SB();
  MV16(as[0], fo[2], of[2][0]); SB();
  of[1][0] = *(LDS_AS const bf16x8*)(ro + 2048);
  SB();
  MV16(as[1], fo[2], of[2][1]); SB();
  of[1][1] = *(LDS_AS const bf16x8*)(ro + 18432);
  SB();
  MV16(as[2], fo[2], of[2][2]); SB();
  of[1][2] = *(LDS_AS const bf16x8*)(ro + 34816);
  SB();
  MV16(as[3], fo[2], of[2][3]); SB();
  of[1][3] = *(LDS_AS const bf16x8*)(ro + 51200);
  SB();
  SB();
  MV16(as[0], fo[3], of[0][0]); SB();
  of[2][0] = *(LDS_AS const bf16x8*)(roh + 2048);
  SB();
  MV16(as[1], fo[3], of[0][1]); SB();
  of[2][1] = *(LDS_AS const bf16x8*)(roh + 18432);
  SB();
  MV16(as[2], fo[3], of[0][2]); SB();
  of[2][2] = *(LDS_AS const bf16x8*)(roh + 34816);
  SB();
  MV16(as[3], fo[3], of[0][3]); SB();
  of[2][3] = *(LDS_AS const bf16x8*)(roh + 51200);
  SB();
  SB();
  MV16(as[0], fo[4], of[1][0]); SB();
  WAITV(0);
  BAR0();
  of[0][0] = *(LDS_AS const bf16x8*)(ro + 3072);
  SB();
  MV16(as[1], fo[4], of[1][1]); SB();
  of[0][1] = *(LDS_AS const bf16x8*)(ro + 19456);
  SB();
  MV16(as[2], fo[4], of[1][2]); SB();
  of[0][2] = *(LDS_AS const bf16x8*)(ro + 35840);
  SB();
  MV16(as[3], fo[4], of[1][3]); SB();
  of[0][3] = *(LDS_AS const bf16x8*)(ro + 52224);
  SB();
  SB();
  MV16(as[0], fo[5], of[2][0]); SB();
  of[1][0] = *(LDS_AS const bf16x8*)(roh + 3072);
  SB();
  MV16(as[1], fo[5], of[2][1]); SB();
  of[1][1] = *(LDS_AS const bf16x8*)(roh + 19456);
  SB();
  MV16(as[2], fo[5], of[2][2]); SB();
  of[1][2] = *(LDS_AS const bf16x8*)(roh + 35840);
  SB();
  MV16(as[3], fo[5], of[2][3]); SB();
  of[1][3] = *(LDS_AS const bf16x8*)(roh + 52224);
  SB();
  SB();
  MV16(as[0], fo[6], of[0][0]); SB();
  of[2][0] = *(LDS_AS const bf16x8*)(ro + 4096);
  SB();
  MV16(as[1], fo[6], of[0][1]); SB();
  of[2][1] = *(LDS_AS const bf16x8*)(ro + 20480);
  SB();
  MV16(as[2], fo[6], of[0][2]); SB();
  of[2][2] = *(LDS_AS const bf16x8*)(ro + 36864);
  SB();
  MV16(as[3], fo[6], of[0][3]); SB();
  of[2][3] = *(LDS_AS const bf16x8*)(ro + 53248);
  SB();
  SB();
  MV16(as[0], fo[7], of[1][0]); SB();
  of[0][0] = *(LDS_AS const bf16x8*)(roh + 4096);
  SB();
  MV16(as[1], fo[7], of[1][1]); SB();
  of[0][1] = *(LDS_AS const bf16x8*)(roh + 20480);
  SB();
  MV16(as[2], fo[7], of[1][2]); SB();
  of[0][2] = *(LDS_AS const bf16x8*)(roh + 36864);
  SB();
  MV16(as[3], fo[7], of[1][3]); SB();
  of[0][3] = *(LDS_AS const bf16x8*)(roh + 53248);
  SB();
  SB();
  MV16(as[0], fo[8], of[2][0]); SB();
  of[1][0] = *(LDS_AS const bf16x8*)(ro + 5120);
  SB();
  MV16(as[1], fo[8], of[2][1]); SB();
  of[1][1] = *(LDS_AS const bf16x8*)(ro + 21504);
  SB();
  MV16(as[2], fo[8], of[2][2]); SB();
  of[1][2] = *(LDS_AS const bf16x8*)(ro + 37888);
  SB();
  MV16(as[3], fo[8], of[2][3]); SB();
  of[1][3] = *(LDS_AS const bf16x8*)(ro + 54272);
  SB();
  SB();
  MV16(as[0], fo[9], of[0][0]); SB();
  of[2][0] = *(LDS_AS const bf16x8*)(roh + 5120);
  SB();
  MV16(as[1], fo[9], of[0][1]); SB();
  of[2][1] = *(LDS_AS const bf16x8*)(roh + 21504);
  SB();
  MV16(as[2], fo[9], of[0][2]); SB();
  of[2][2] = *(LDS_AS const bf16x8*)(roh + 37888);
  SB();
  MV16(as[3], fo[9], of[0][3]); SB();
  of[2][3] = *(LDS_AS const bf16x8*)(roh + 54272);
  SB();
  SB();
  MV16(as[0], fo[10], of[1][0]); SB();
  of[0][0] = *(LDS_AS const bf16x8*)(ro + 6144);
  SB();
  MV16(as[1], fo[10], of[1][1]); SB();
  of[0][1] = *(LDS_AS const bf16x8*)(ro + 22528);
  SB();
  MV16(as[2], fo[10], of[1][2]); SB();
  of[0][2] = *(LDS_AS const bf16x8*)(ro + 38912);
  SB();
  MV16(as[3], fo[10], of[1][3]); SB();
  of[0][3] = *(LDS_AS const bf16x8*)(ro + 55296);
  SB();
  SB();
  MV16(as[0], fo[11], of[2][0]); SB();
  of[1][0] = *(LDS_AS const bf16x8*)(roh + 6144);
  SB();
  MV16(as[1], fo[11], of[2][1]); SB();
  of[1][1] = *(LDS_AS const bf16x8*)(roh + 22528);
  SB();
  MV16(as[2], fo[11], of[2][2]); SB();
  of[1][2] = *(LDS_AS const bf16x8*)(roh + 38912);
  SB();
  MV16(as[3], fo[11], of[2][3]); SB();
  of[1][3] = *(LDS_AS const bf16x8*)(roh + 55296);
  SB();
  SB();
  MV16(as[0], fo[12], of[0][0]); SB();
  MV16(as[1], fo[12], of[0][1]); SB();
  MV16(as[2], fo[12], of[0][2]); SB();
  MV16(as[3], fo[12], of[0][3]); SB();
  SB();
  MV16(as[0], fo[13], of[1][0]); SB();
  MV16(as[1], fo[13], of[1][1]); SB();
  MV16(as[2], fo[13], of[1][2]); SB();
  MV16(as[3], fo[13], of[1][3]); SB();
  NOP(5); SB();
  { const auto r0 = __builtin_amdgcn_permlane16_swap(pack2(as[0][0], as[0][1]), pack2(as[1][0], as[1][1]), false, false);
    const auto r1 = __builtin_amdgcn_permlane16_swap(pack2(as[0][2], as[0][3]), pack2(as[1][2], as[1][3]), false, false);
    ST16(vS0, sS, 0, ((u32x4){r0[0], r1[0], r0[1], r1[1]})); } SB();
  { const auto r0 = __builtin_amdgcn_permlane16_swap(pack2(as[2][0], as[2][1]), pack2(as[3][0], as[3][1]), false, false);
    const auto r1 = __builtin_amdgcn_permlane16_swap(pack2(as[2][2], as[2][3]), pack2(as[3][2], as[3][3]), false, false);
    ST16(vS1, sS, 0, ((u32x4){r0[0], r1[0], r0[1], r1[1]})); } SB();
  STAMP(14);
  WAITV(0);
  ST16X(15, vL16, sFS, nonce);
  STAMP(15);
  }
  STAMP_DUMP();
  }}}}
  } else {
  DMA2(vWa, vWb, sW, ldsW, 6144, ""); sW = wrap == 1 ? bW : sW + dW; OPQ(sW);
  { int w_ = w; OPQ(w_); { const int w = w_ & 7, lane = lane_now(); const int n16 = lane & 15, g4 = lane >> 4, q4 = n16 >> 2, pq = lane & 3, r8 = lane >> 3; const unsigned vL16 = lane * 16;
  const unsigned vFH = (lane & 31) * FA;
  bf16x8 fw[8][4], p[9]; f32x4 acc[8], t4[8]; float mm[4], zz[4], bias[4], c23 = 2.3022082f;
  unsigned rW0 = ldsP + (8 * g4 + q4) * 128 + (((q4 >> 1) | (2 * (g4 & 1))) * 32) + pq * 8, rW1 = rW0 ^ 32, rW2 = rW0 ^ 64, rW3 = rW0 ^ 96;
  asm volatile("" : "+v"(rW0), "+v"(rW1), "+v"(rW2), "+v"(rW3));
  unsigned vP0 = frag(0, lane), vP1 = frag(1, lane), vP2 = frag(2, lane), vP3 = frag(3, lane);
  const char* sP = A.ws + WS_HT + (size_t)(dg * 8 + w) * HT_W;
  asm volatile("" : "+s"(sP)); asm volatile("" : "+v"(vP0), "+v"(vP1), "+v"(vP2), "+v"(vP3));
  WAITV(5);
  fw[0][0] = tr8u(rW0 + 0, 512);
  fw[0][1] = tr8u(rW1 + 0, 512);
  fw[0][2] = tr8u(rW2 + 0, 512);
  fw[0][3] = tr8u(rW3 + 0, 512);
  DMA2(vWa, vWb, sW, ldsW, 8192, ""); sW += dW; OPQ(sW);
  DMA2(vWa, vWb, sW, ldsW, 10240, ""); sW = wrap == 2 ? bW : sW + dW; OPQ(sW);
  WAITV(4);
  fw[1][0] = tr8u(rW0 + 4096, 512);
  fw[1][1] = tr8u(rW1 + 4096, 512);
  fw[1][2] = tr8u(rW2 + 4096, 512);
  fw[1][3] = tr8u(rW3 + 4096, 512);
  DMA2(vWa, vWb, sW, ldsW, 12288, ""); sW += dW; OPQ(sW);
  DMA2(vWa, vWb, sW, ldsW, 14336, ""); sW = wrap == 3 ? bW : sW + dW; OPQ(sW);
  WAITV(4);
  fw[2][0] = tr8u(rW0 + 8192, 512);
  fw[2][1] = tr8u(rW1 + 8192, 512);
  fw[2][2] = tr8u(rW2 + 8192, 512);
  fw[2][3] = tr8u(rW3 + 8192, 512);
  WAITL(0);
  DMA2(vWa, vWb, sW, ldsW, 0, ""); sW += dW; OPQ(sW);
  DMA2(vWa, vWb, sW, ldsW, 2048, ""); sW = wrap == 4 ? bW : sW + dW; OPQ(sW);
  WAITV(4);
  fw[3][0] = tr8u(rW0 + 12288, 512);
  fw[3][1] = tr8u(rW1 + 12288, 512);
  fw[3][2] = tr8u(rW2 + 12288, 512);
  fw[3][3] = tr8u(rW3 + 12288, 512);
  STAMP(1);
  WAITL(0);
  DMA2(vWa, vWb, sW, ldsW, 4096, ""); sW += dW; OPQ(sW);
  DMA2(vWa, vWb, sW, ldsW, 6144, ""); sW = wrap == 5 ? bW : sW + dW; OPQ(sW);
  bias[0] = lo2f(bv[0]); bias[1] = hi2f(bv[0]); bias[2] = lo2f(bv[1]); bias[3] = hi2f(bv[1]);
  unsigned wa0 = ldsP + n16 * 256 + ((g4 ^ n16) << 4), wa1 = wa0 ^ 64, wa2 = wa0 ^ 128, wa3 = wa0 ^ 192;
  unsigned ra = lds0 + (16 * 0 + n16) * 256 + (((4 * (w & 3) + g4) ^ n16) << 4), rah = ra + 65536, uL = 32 * (w & 1) + 16 * (g4 >> 1) + n16, vU;
  const int kap = 2 * ft + ((w >> 1) & 1), wv = (kap * 37) >> 8, us = kap - 7 * wv, up_ = us - KROT2 * dg;
  int up = up_ < 0 ? up_ + 7 : up_, mU = ((w - 4) >> 31) & 0xffff;
  const char* sU = A.ws + WS_UT + (size_t)(dg * 8 + wv) * UT_W;
  const char* sFU = A.ws + WS_FLAGU + ((dg * 8 + wv) * 128 + 0 * 14 + us * 2 + (w & 1)) * FU;
  asm volatile("" : "+s"(sU), "+s"(sFU), "+s"(up), "+s"(mU)); asm volatile("" : "+v"(wa0), "+v"(wa1), "+v"(wa2), "+v"(wa3), "+v"(ra), "+v"(rah), "+v"(uL), "+v"(bias[0]), "+v"(bias[1]), "+v"(bias[2]), "+v"(bias[3]), "+v"(c23));
  WAITV(4);
  fw[4][0] = tr8u(rW0 + 0, 512);
  fw[4][1] = tr8u(rW1 + 0, 512);
  fw[4][2] = tr8u(rW2 + 0, 512);
  fw[4][3] = tr8u(rW3 + 0, 512);
  WAITL(0);
  DMA2(vWa, vWb, sW, ldsW, 8192, ""); sW += dW; OPQ(sW);
  LD4(fh, vFH, sFH, 0, "sc1");
  DMA2(vWa, vWb, sW, ldsW, 10240, ""); sW = wrap == 6 ? bW : sW + dW; OPQ(sW);
  WAITV(5);
  fw[5][0] = tr8u(rW0 + 4096, 512);
  fw[5][1] = tr8u(rW1 + 4096, 512);
  fw[5][2] = tr8u(rW2 + 4096, 512);
  fw[5][3] = tr8u(rW3 + 4096, 512);
  WAITL(0);
  DMA2(vWa, vWb, sW, ldsW, 12288, ""); sW += dW; OPQ(sW);
  DMA2(vWa, vWb, sW, ldsW, 14336, "");
  WAITV(4);
  fw[6][0] = tr8u(rW0 + 8192, 512);
  fw[6][1] = tr8u(rW1 + 8192, 512);
  fw[6][2] = tr8u(rW2 + 8192, 512);
  fw[6][3] = tr8u(rW3 + 8192, 512);
  WAITV1(6, fh);
  if (__ballot(fh != nonce) != 0ull) poll1<16>(sFH, vFH, nonce);
  STAMP(3);
  LD16(p[0], vP0, sP, 0, "sc1");
  LD16(p[1], vP1, sP, 0, "sc1");
  LD16(p[2], vP2, sP, 0, "sc1");
  LD16(p[3], vP3, sP, 0, "sc1"); sP += 5120; OPQ(sP);
  WAITV(4);
  fw[7][0] = tr8u(rW0 + 12288, 512);
  fw[7][1] = tr8u(rW1 + 12288, 512);
  fw[7][2] = tr8u(rW2 + 12288, 512);
  fw[7][3] = tr8u(rW3 + 12288, 512);
  STAMP(2);
  LD16(p[4], vP0, sP, 0, "sc1");
  LD16(p[5], vP1, sP, 0, "sc1");
  LD16(p[6], vP2, sP, 0, "sc1");
  LD16(p[7], vP3, sP, 0, "sc1"); sP += 5120; OPQ(sP);
  SB();
  WAITVB(7); SB();
  STAMP(4);
  PRIO(3); SB();
  MV16Z(acc[0], fw[0][0], p[0]); SB();
  LD16(p[8], vP0, sP, 0, "sc1");
  SB();
  MV16Z(acc[1], fw[0][1], p[0]); SB();
  MV16Z(acc[2], fw[0][2], p[0]); SB();
  MV16Z(acc[3], fw[0][3], p[0]); SB();
  SB();
  WAITVB(7); SB();
  MV16(acc[0], fw[1][0], p[1]); SB();
  LD16(p[0], vP1, sP, 0, "sc1");
  SB();
  MV16(acc[1], fw[1][1], p[1]); SB();
  MV16(acc[2], fw[1][2], p[1]); SB();
  MV16(acc[3], fw[1][3], p[1]); SB();
  SB();
  WAITVB(7); SB();
  MV16(acc[0], fw[2][0], p[2]); SB();
  LD16(p[1], vP2, sP, 0, "sc1");
  SB();
  MV16(acc[1], fw[2][1], p[2]); SB();
  MV16(acc[2], fw[2][2], p[2]); SB();
  MV16(acc[3], fw[2][3], p[2]); SB();
  SB();
  WAITVB(7); SB();
  MV16(acc[0], fw[3][0], p[3]); SB();
  LD16(p[2], vP3, sP, 0, "sc1"); sP += 5120; OPQ(sP);
  SB();
  MV16(acc[1], fw[3][1], p[3]); SB();
  MV16(acc[2], fw[3][2], p[3]); SB();
  MV16(acc[3], fw[3][3], p[3]); SB();
  SB();
  WAITVB(7); SB();
  MV16(acc[0], fw[4][0], p[4]); SB();
  LD16(p[3], vP0, sP, 0, "sc1");
  SB();
  MV16(acc[1], fw[4][1], p[4]); SB();
  MV16(acc[2], fw[4][2], p[4]); SB();
  MV16(acc[3], fw[4][3], p[4]); SB();
  SB();
  WAITVB(7); SB();
  MV16(acc[0], fw[5][0], p[5]); SB();
  LD16(p[4], vP1, sP, 0, "sc1");
  SB();
  MV16(acc[1], fw[5][1], p[5]); SB();
  MV16(acc[2], fw[5][2], p[5]); SB();
  MV16(acc[3], fw[5][3], p[5]); SB();
  SB();
  WAITVB(7); SB();
  PRIO(2); SB();
  MV16(acc[0], fw[6][0], p[6]); SB();
  LD16(p[5], vP2, sP, 0, "sc1");
  SB();
  MV16(acc[1], fw[6][1], p[6]); SB();
  MV16(acc[2], fw[6][2], p[6]); SB();
  MV16(acc[3], fw[6][3], p[6]); SB();
  SB();
  WAITVB(7); SB();
  MV16(acc[0], fw[7][0], p[7]); SB();
  LD16(p[6], vP3, sP, 0, "sc1"); sP += 5120; OPQ(sP);
  SB();
  MV16(acc[1], fw[7][1], p[7]); SB();
  MV16(acc[2], fw[7][2], p[7]); SB();
  MV16(acc[3], fw[7][3], p[7]); SB();
  SB();
  WAITVB(7); SB();
  MV16Z(acc[4], fw[0][0], p[8]); SB();
  LD16(p[7], vP0, sP, 0, "sc1");
  SB();
  MV16Z(acc[5], fw[0][1], p[8]); SB();
  MV16Z(acc[6], fw[0][2], p[8]); SB();
  MV16Z(acc[7], fw[0][3], p[8]); SB();
  SB();
  WAITVB(7); SB();
  PRIO(1); SB();
  MV16(acc[4], fw[1][0], p[0]); SB();
  LD16(p[8], vP1, sP, 0, "sc1");
  SB();
  MV16(acc[5], fw[1][1], p[0]); SB();
  *(LDS_AS f32x4*)(wa0 + 0) = acc[0];
  SB();
  MV16(acc[6], fw[1][2], p[0]); SB();
  *(LDS_AS f32x4*)(wa1 + 0) = acc[1];
  SB();
  MV16(acc[7], fw[1][3], p[0]); SB();
  SB();
  WAITVB(7); SB();
  PRIO(0); SB();
  MV16(acc[4], fw[2][0], p[1]); SB();
  LD16(p[0], vP2, sP, 0, "sc1");
  SB();
  MV16(acc[5], fw[2][1], p[1]); SB();
  *(LDS_AS f32x4*)(wa2 + 0) = acc[2];
  SB();
  MV16(acc[6], fw[2][2], p[1]); SB();
  *(LDS_AS f32x4*)(wa3 + 0) = acc[3];
  SB();
  MV16(acc[7], fw[2][3], p[1]); SB();
  BAR();
  STAMP(5);
  SB();
  SB();
  WAITVB(7); SB();
  PRIO(3); SB();
  MV16(acc[4], fw[3][0], p[2]); SB();
  LD16(p[1], vP3, sP, 0, "sc1"); sP += 5120; OPQ(sP);
  SB();
  MV16(acc[5], fw[3][1], p[2]); SB();
  MV16(acc[6], fw[3][2], p[2]); SB();
  MV16(acc[7], fw[3][3], p[2]); SB();
  SB();
  WAITVB(7); SB();
  MV16(acc[4], fw[4][0], p[3]); SB();
  LD16(p[2], vP0, sP, 0, "sc1");
  SB();
  MV16(acc[5], fw[4][1], p[3]); SB();
  MV16(acc[6], fw[4][2], p[3]); SB();
  MV16(acc[7], fw[4][3], p[3]); SB();
  SB();
  WAITVB(7); SB();
  MV16(acc[4], fw[5][0], p[4]); SB();
  LD16(p[3], vP1, sP, 0, "sc1");
  SB();
  MV16(acc[5], fw[5][1], p[4]); SB();
  MV16(acc[6], fw[5][2], p[4]); SB();
  MV16(acc[7], fw[5][3], p[4]); SB();
  SB();
  WAITVB(7); SB();
  PRIO(2); SB();
  MV16(acc[4], fw[6][0], p[5]); SB();
  LD16(p[4], vP2, sP, 0, "sc1");
  SB();
  MV16(acc[5], fw[6][1], p[5]); SB();
  MV16(acc[6], fw[6][2], p[5]); SB();
  MV16(acc[7], fw[6][3], p[5]); SB();
  SB();
  WAITVB(7); SB();
  MV16(acc[4], fw[7][0], p[6]); SB();
  LD16(p[5], vP3, sP, 0, "sc1"); sP += 5120; OPQ(sP);
  SB();
  MV16(acc[5], fw[7][1], p[6]); SB();
  MV16(acc[6], fw[7][2], p[6]); SB();
  MV16(acc[7], fw[7][3], p[6]); SB();
  SB();
  WAITVB(7); SB();
  MV16Z(acc[0], fw[0][0], p[7]); SB();
  LD16(p[6], vP0, sP, 0, "sc1");
  SB();
  MV16Z(acc[1], fw[0][1], p[7]); SB();
  MV16Z(acc[2], fw[0][2], p[7]); SB();
  MV16Z(acc[3], fw[0][3], p[7]); SB();
  SB();
  WAITVB(7); SB();
  PRIO(1); SB();
  MV16(acc[0], fw[1][0], p[8]); SB();
  LD16(p[7], vP1, sP, 0, "sc1");
  SB();
  MV16(acc[1], fw[1][1], p[8]); SB();
  *(LDS_AS f32x4*)(wa0 + 8192) = acc[4];
  SB();
  MV16(acc[2], fw[1][2], p[8]); SB();
  *(LDS_AS f32x4*)(wa1 + 8192) = acc[5];
  SB();
  MV16(acc[3], fw[1][3], p[8]); SB();
  SB();
  WAITVB(7); SB();
  PRIO(0); SB();
  MV16(acc[0], fw[2][0], p[0]); SB();
  LD16(p[8], vP2, sP, 0, "sc1");
  SB();
  MV16(acc[1], fw[2][1], p[0]); SB();
  *(LDS_AS f32x4*)(wa2 + 8192) = acc[6];
  SB();
  MV16(acc[2], fw[2][2], p[0]); SB();
  *(LDS_AS f32x4*)(wa3 + 8192) = acc[7];
  SB();
  MV16(acc[3], fw[2][3], p[0]); SB();
  BAR();
  SB();
  SB();
  WAITVB(7); SB();
  PRIO(3); SB();
  MV16(acc[0], fw[3][0], p[1]); SB();
  LD16(p[0], vP3, sP, 0, "sc1"); sP += 5120; OPQ(sP);
  t4[0] = *(LDS_AS const f32x4*)(ra + 8192);
  SB();
  MV16(acc[1], fw[3][1], p[1]); SB();
  t4[1] = *(LDS_AS const f32x4*)(ra + 24576);
  SB();
  MV16(acc[2], fw[3][2], p[1]); SB();
  t4[2] = *(LDS_AS const f32x4*)(ra + 40960);
  SB();
  MV16(acc[3], fw[3][3], p[1]); SB();
  t4[3] = *(LDS_AS const f32x4*)(ra + 57344);
  SB();
  SB();
  WAITVB(7); SB();
  MV16(acc[0], fw[4][0], p[2]); SB();
  LD16(p[1], vP0, sP, 0, "sc1");
  t4[4] = *(LDS_AS const f32x4*)(rah + 8192);
  SB();
  MV16(acc[1], fw[4][1], p[2]); SB();
  t4[5] = *(LDS_AS const f32x4*)(rah + 24576);
  SB();
  MV16(acc[2], fw[4][2], p[2]); SB();
  t4[6] = *(LDS_AS const f32x4*)(rah + 40960);
  t4[7] = *(LDS_AS const f32x4*)(rah + 57344);
  SB();
  MV16(acc[3], fw[4][3], p[2]); SB();
  { const int ix = 7 + up; vU = (ix >> 2) * 5120 + frag(ix & 3, uL); PINV(vU); }
  SB();
  SB();
  WAITVB(7); SB();
  MV16(acc[0], fw[5][0], p[3]); SB();
  LD16(p[2], vP1, sP, 0, "sc1");
  OP4_3("v_add_f32", mm, t4[0], bias);
  SB();
  MV16(acc[1], fw[5][1], p[3]); SB();
  OP4_3("v_add_f32", zz, t4[1], t4[2]);
  SB();
  MV16(acc[2], fw[5][2], p[3]); SB();
  ACC4("v_add_f32", mm, t4[3]);
  SB();
  MV16(acc[3], fw[5][3], p[3]); SB();
  ACC4("v_add_f32", zz, t4[4]);
  SB();
  SB();
  WAITVB(7); SB();
  PRIO(2); SB();
  MV16(acc[0], fw[6][0], p[4]); SB();
  LD16(p[3], vP2, sP, 0, "sc1");
  ACC4("v_add_f32", mm, t4[5]);
  SB();
  MV16(acc[1], fw[6][1], p[4]); SB();
  ACC4("v_add_f32", zz, t4[6]);
  SB();
  MV16(acc[2], fw[6][2], p[4]); SB();
  ACC4("v_add_f32", mm, t4[7]);
  SB();
  MV16(acc[3], fw[6][3], p[4]); SB();
  ACC4("v_add_f32", mm, zz);
  SB();
  SB();
  WAITVB(7); SB();
  MV16(acc[0], fw[7][0], p[5]); SB();
  LD16(p[4], vP3, sP, 0, "sc1"); sP += 5120; OPQ(sP);
  OP4_3("v_mul_f32", zz, mm, mm);
  SB();
  MV16(acc[1], fw[7][1], p[5]); SB();
  POLY4(zz, c23);
  SB();
  MV16(acc[2], fw[7][2], p[5]); SB();
  ACC4("v_mul_f32", zz, mm);
  SB();
  MV16(acc[3], fw[7][3], p[5]); SB();
  UN4("v_exp_f32", "", "", zz);
  SB();
  SB();
  WAITVB(7); SB();
  MV16Z(acc[4], fw[0][0], p[6]); SB();
  LD16(p[5], vP0, sP, 0, "sc1");
  UN4("v_add_f32", "1.0, ", "", zz);
  SB();
  MV16Z(acc[5], fw[0][1], p[6]); SB();
  UN4("v_rcp_f32", "", "", zz);
  SB();
  MV16Z(acc[6], fw[0][2], p[6]); SB();
  NFMA4(mm, zz);
  SB();
  MV16Z(acc[7], fw[0][3], p[6]); SB();
  { const unsigned x0 = pack2(mm[0], mm[1]), x1 = pack2(mm[2], mm[3]);
    const auto r0 = __builtin_amdgcn_permlane16_swap(x0, x0, false, false), r1 = __builtin_amdgcn_permlane16_swap(x1, x1, false, false);
    ST16E(0xffff, vU, sU, ((u32x4){r0[0], r1[0], r0[1], r1[1]})); }
  SB();
  SB();
  WAITVB(8); SB();
  PRIO(1); SB();
  MV16(acc[4], fw[1][0], p[7]); SB();
  LD16(p[6], vP1, sP, 0, "sc1");
  SB();
  MV16(acc[5], fw[1][1], p[7]); SB();
  *(LDS_AS f32x4*)(wa0 + 12288) = acc[0];
  SB();
  MV16(acc[6], fw[1][2], p[7]); SB();
  *(LDS_AS f32x4*)(wa1 + 12288) = acc[1];
  SB();
  MV16(acc[7], fw[1][3], p[7]); SB();
  SB();
  WAITVB(8); SB();
  PRIO(0); SB();
  MV16(acc[4], fw[2][0], p[8]); SB();
  LD16(p[7], vP2, sP, 0, "sc1");
  SB();
  MV16(acc[5], fw[2][1], p[8]); SB();
  *(LDS_AS f32x4*)(wa2 + 12288) = acc[2];
  SB();
  MV16(acc[6], fw[2][2], p[8]); SB();
  *(LDS_AS f32x4*)(wa3 + 12288) = acc[3];
  SB();
  MV16(acc[7], fw[2][3], p[8]); SB();
  BAR();
  STAMP(6);
  SB();
  SB();
  WAITVB(8); SB();
  PRIO(3); SB();
  MV16(acc[4], fw[3][0], p[0]); SB();
  LD16(p[8], vP3, sP, 0, "sc1"); sP += 5120; OPQ(sP);
  SB();
  MV16(acc[5], fw[3][1], p[0]); SB();
  MV16(acc[6], fw[3][2], p[0]); SB();
  MV16(acc[7], fw[3][3], p[0]); SB();
  SB();
  WAITVB(8); SB();
  MV16(acc[4], fw[4][0], p[1]); SB();
  LD16(p[0], vP0, sP, 0, "sc1");
  SB();
  MV16(acc[5], fw[4][1], p[1]); SB();
  MV16(acc[6], fw[4][2], p[1]); SB();
  MV16(acc[7], fw[4][3], p[1]); SB();
  SB();
  WAITVB(8); SB();
  MV16(acc[4], fw[5][0], p[2]); SB();
  LD16(p[1], vP1, sP, 0, "sc1");
  SB();
  MV16(acc[5], fw[5][1], p[2]); SB();
  MV16(acc[6], fw[5][2], p[2]); SB();
  MV16(acc[7], fw[5][3], p[2]); SB();
  SB();
  WAITVB(8); SB();
  PRIO(2); SB();
  MV16(acc[4], fw[6][0], p[3]); SB();
  LD16(p[2], vP2, sP, 0, "sc1");
  SB();
  MV16(acc[5], fw[6][1], p[3]); SB();
  MV16(acc[6], fw[6][2], p[3]); SB();
  MV16(acc[7], fw[6][3], p[3]); SB();
  SB();
  WAITVB(8); SB();
  MV16(acc[4], fw[7][0], p[4]); SB();
  LD16(p[3], vP3, sP, 0, "sc1"); sP += 5120; OPQ(sP);
  SB();
  MV16(acc[5], fw[7][1], p[4]); SB();
  MV16(acc[6], fw[7][2], p[4]); SB();
  MV16(acc[7], fw[7][3], p[4]); SB();
  SB();
  WAITVB(8); SB();
  MV16Z(acc[0], fw[0][0], p[5]); SB();
  LD16(p[4], vP0, sP, 0, "sc1");
  SB();
  MV16Z(acc[1], fw[0][1], p[5]); SB();
  MV16Z(acc[2], fw[0][2], p[5]); SB();
  MV16Z(acc[3], fw[0][3], p[5]); SB();
  SB();
  WAITVB(7); SB();
  PRIO(1); SB();
  MV16(acc[0], fw[1][0], p[6]); SB();
  LD16(p[5], vP1, sP, 0, "sc1");
  SB();
  MV16(acc[1], fw[1][1], p[6]); SB();
  *(LDS_AS f32x4*)(wa0 + 0) = acc[4];
  SB();
  MV16(acc[2], fw[1][2], p[6]); SB();
  *(LDS_AS f32x4*)(wa1 + 0) = acc[5];
  SB();
  MV16(acc[3], fw[1][3], p[6]); SB();
  { const char* f_ = sFU + 14 * FU; const unsigned l_ = lane_now() * 16; ST16X(1, l_, f_, nonce); }
  SB();
  SB();
  WAITVB(8); SB();
  PRIO(0); SB();
  MV16(acc[0], fw[2][0], p[7]); SB();
  LD16(p[6], vP2, sP, 0, "sc1");
  SB();
  MV16(acc[1], fw[2][1], p[7]); SB();
  *(LDS_AS f32x4*)(wa2 + 0) = acc[6];
  SB();
  MV16(acc[2], fw[2][2], p[7]); SB();
  *(LDS_AS f32x4*)(wa3 + 0) = acc[7];
  SB();
  MV16(acc[3], fw[2][3], p[7]); SB();
  BAR();
  SB();
  SB();
  WAITVB(8); SB();
  PRIO(3); SB();
  MV16(acc[0], fw[3][0], p[8]); SB();
  LD16(p[7], vP3, sP, 0, "sc1"); sP += 5120; OPQ(sP);
  t4[0] = *(LDS_AS const f32x4*)(ra + 0);
  SB();
  MV16(acc[1], fw[3][1], p[8]); SB();
  t4[1] = *(LDS_AS const f32x4*)(ra + 16384);
  SB();
  MV16(acc[2], fw[3][2], p[8]); SB();
  t4[2] = *(LDS_AS const f32x4*)(ra + 32768);
  SB();
  MV16(acc[3], fw[3][3], p[8]); SB();
  t4[3] = *(LDS_AS const f32x4*)(ra + 49152);
  SB();
  SB();
  WAITVB(8); SB();
  MV16(acc[0], fw[4][0], p[0]); SB();
  LD16(p[8], vP0, sP, 0, "sc1");
  t4[4] = *(LDS_AS const f32x4*)(rah + 0);
  SB();
  MV16(acc[1], fw[4][1], p[0]); SB();
  t4[5] = *(LDS_AS const f32x4*)(rah + 16384);
  SB();
  MV16(acc[2], fw[4][2], p[0]); SB();
  t4[6] = *(LDS_AS const f32x4*)(rah + 32768);
  t4[7] = *(LDS_AS const f32x4*)(rah + 49152);
  SB();
  MV16(acc[3], fw[4][3], p[0]); SB();
  { const int ix = 21 + up; vU = (ix >> 2) * 5120 + frag(ix & 3, uL); PINV(vU); }
  SB();
  SB();
  WAITVB(8); SB();
  MV16(acc[0], fw[5][0], p[1]); SB();
  LD16(p[0], vP1, sP, 0, "sc1");
  OP4_3("v_add_f32", mm, t4[0], bias);
  SB();
  MV16(acc[1], fw[5][1], p[1]); SB();
  OP4_3("v_add_f32", zz, t4[1], t4[2]);
  SB();
  MV16(acc[2], fw[5][2], p[1]); SB();
  ACC4("v_add_f32", mm, t4[3]);
  SB();
  MV16(acc[3], fw[5][3], p[1]); SB();
  ACC4("v_add_f32", zz, t4[4]);
  SB();
  SB();
  WAITVB(8); SB();
  PRIO(2); SB();
  MV16(acc[0], fw[6][0], p[2]); SB();
  LD16(p[1], vP2, sP, 0, "sc1");
  ACC4("v_add_f32", mm, t4[5]);
  SB();
  MV16(acc[1], fw[6][1], p[2]); SB();
  ACC4("v_add_f32", zz, t4[6]);
  SB();
  MV16(acc[2], fw[6][2], p[2]); SB();
  ACC4("v_add_f32", mm, t4[7]);
  SB();
  MV16(acc[3], fw[6][3], p[2]); SB();
  ACC4("v_add_f32", mm, zz);
  SB();
  SB();
  WAITVB(8); SB();
  MV16(acc[0], fw[7][0], p[3]); SB();
  LD16(p[2], vP3, sP, 0, "sc1"); sP += 5120; OPQ(sP);
  OP4_3("v_mul_f32", zz, mm, mm);
  SB();
  MV16(acc[1], fw[7][1], p[3]); SB();
  POLY4(zz, c23);
  SB();
  MV16(acc[2], fw[7][2], p[3]); SB();
  ACC4("v_mul_f32", zz, mm);
  SB();
  MV16(acc[3], fw[7][3], p[3]); SB();
  UN4("v_exp_f32", "", "", zz);
  SB();
  SB();
  WAITVB(8); SB();
  MV16Z(acc[4], fw[0][0], p[4]); SB();
  LD16(p[3], vP0, sP, 0, "sc1");
  UN4("v_add_f32", "1.0, ", "", zz);
  SB();
  MV16Z(acc[5], fw[0][1], p[4]); SB();
  UN4("v_rcp_f32", "", "", zz);
  SB();
  MV16Z(acc[6], fw[0][2], p[4]); SB();
  NFMA4(mm, zz);
  SB();
  MV16Z(acc[7], fw[0][3], p[4]); SB();
  { const unsigned x0 = pack2(mm[0], mm[1]), x1 = pack2(mm[2], mm[3]);
    const auto r0 = __builtin_amdgcn_permlane16_swap(x0, x0, false, false), r1 = __builtin_amdgcn_permlane16_swap(x1, x1, false, false);
    ST16E(0xffff, vU, sU, ((u32x4){r0[0], r1[0], r0[1], r1[1]})); }
  SB();
  SB();
  WAITVB(9); SB();
  PRIO(1); SB();
  MV16(acc[4], fw[1][0], p[5]); SB();
  LD16(p[4], vP1, sP, 0, "sc1");
  SB();
  MV16(acc[5], fw[1][1], p[5]); SB();
  *(LDS_AS f32x4*)(wa0 + 4096) = acc[0];
  SB();
  MV16(acc[6], fw[1][2], p[5]); SB();
  *(LDS_AS f32x4*)(wa1 + 4096) = acc[1];
  SB();
  MV16(acc[7], fw[1][3], p[5]); SB();
  SB();
  WAITVB(8); SB();
  PRIO(0); SB();
  MV16(acc[4], fw[2][0], p[6]); SB();
  LD16(p[5], vP2, sP, 0, "sc1");
  SB();
  MV16(acc[5], fw[2][1], p[6]); SB();
  *(LDS_AS f32x4*)(wa2 + 4096) = acc[2];
  SB();
  MV16(acc[6], fw[2][2], p[6]); SB();
  *(LDS_AS f32x4*)(wa3 + 4096) = acc[3];
  SB();
  MV16(acc[7], fw[2][3], p[6]); SB();
  BAR();
  STAMP(7);
  SB();
  SB();
  WAITVB(8); SB();
  PRIO(3); SB();
  MV16(acc[4], fw[3][0], p[7]); SB();
  LD16(p[6], vP3, sP, 0, "sc1"); sP += 5120; OPQ(sP);
  SB();
  MV16(acc[5], fw[3][1], p[7]); SB();
  MV16(acc[6], fw[3][2], p[7]); SB();
  MV16(acc[7], fw[3][3], p[7]); SB();
  SB();
  WAITVB(8); SB();
  MV16(acc[4], fw[4][0], p[8]); SB();
  LD16(p[7], vP0, sP, 0, "sc1");
  SB();
  MV16(acc[5], fw[4][1], p[8]); SB();
  MV16(acc[6], fw[4][2], p[8]); SB();
  MV16(acc[7], fw[4][3], p[8]); SB();
  SB();
  WAITVB(8); SB();
  MV16(acc[4], fw[5][0], p[0]); SB();
  LD16(p[8], vP1, sP, 0, "sc1");
  SB();
  MV16(acc[5], fw[5][1], p[0]); SB();
  MV16(acc[6], fw[5][2], p[0]); SB();
  MV16(acc[7], fw[5][3], p[0]); SB();
  SB();
  WAITVB(8); SB();
  PRIO(2); SB();
  MV16(acc[4], fw[6][0], p[1]); SB();
  LD16(p[0], vP2, sP, 0, "sc1");
  SB();
  MV16(acc[5], fw[6][1], p[1]); SB();
  MV16(acc[6], fw[6][2], p[1]); SB();
  MV16(acc[7], fw[6][3], p[1]); SB();
  SB();
  WAITVB(8); SB();
  MV16(acc[4], fw[7][0], p[2]); SB();
  LD16(p[1], vP3, sP, 0, "sc1");
  SB();
  MV16(acc[5], fw[7][1], p[2]); SB();
  MV16(acc[6], fw[7][2], p[2]); SB();
  MV16(acc[7], fw[7][3], p[2]); SB();
  SB();
  WAITVB(8); SB();
  MV16Z(acc[0], fw[0][0], p[3]); SB();
  bf16x8 fo[14];
  const char* sWo = A.ws + WS_WO + (size_t)(dg * 7 + (w < 7 ? w : 6)) * WO_B; unsigned long long mW = w < 7 ? ~0ull : 1ull; OPQ(sWo); OPQ(mW);
  SB();
  MV16Z(acc[1], fw[0][1], p[3]); SB();
  MV16Z(acc[2], fw[0][2], p[3]); SB();
  MV16Z(acc[3], fw[0][3], p[3]); SB();
  SB();
  WAITVB(6); SB();
  PRIO(1); SB();
  MV16(acc[0], fw[1][0], p[4]); SB();
  *(LDS_AS f32x4*)(wa0 + 8192) = acc[4];
  SB();
  MV16(acc[1], fw[1][1], p[4]); SB();
  *(LDS_AS f32x4*)(wa1 + 8192) = acc[5];
  SB();
  MV16(acc[2], fw[1][2], p[4]); SB();
  { const char* f_ = sFU + 42 * FU; const unsigned l_ = lane_now() * 16; ST16X(1, l_, f_, nonce); }
  SB();
  MV16(acc[3], fw[1][3], p[4]); SB();
  SB();
  WAITVB(6); SB();
  PRIO(0); SB();
  MV16(acc[0], fw[2][0], p[5]); SB();
  *(LDS_AS f32x4*)(wa2 + 8192) = acc[6];
  SB();
  MV16(acc[1], fw[2][1], p[5]); SB();
  *(LDS_AS f32x4*)(wa3 + 8192) = acc[7];
  SB();
  MV16(acc[2], fw[2][2], p[5]); SB();
  BAR();
  SB();
  MV16(acc[3], fw[2][3], p[5]); SB();
  SB();
  WAITVB(5); SB();
  PRIO(2); SB();
  MV16(acc[0], fw[3][0], p[6]); SB();
  t4[0] = *(LDS_AS const f32x4*)(ra + 8192);
  SB();
  MV16(acc[1], fw[3][1], p[6]); SB();
  t4[1] = *(LDS_AS const f32x4*)(ra + 24576);
  SB();
  MV16(acc[2], fw[3][2], p[6]); SB();
  t4[2] = *(LDS_AS const f32x4*)(ra + 40960);
  SB();
  MV16(acc[3], fw[3][3], p[6]); SB();
  t4[3] = *(LDS_AS const f32x4*)(ra + 57344);
  SB();
  SB();
  WAITVB(4); SB();
  MV16(acc[0], fw[4][0], p[7]); SB();
  t4[4] = *(LDS_AS const f32x4*)(rah + 8192);
  t4[5] = *(LDS_AS const f32x4*)(rah + 24576);
  SB();
  MV16(acc[1], fw[4][1], p[7]); SB();
  t4[6] = *(LDS_AS const f32x4*)(rah + 40960);
  SB();
  MV16(acc[2], fw[4][2], p[7]); SB();
  t4[7] = *(LDS_AS const f32x4*)(rah + 57344);
  SB();
  MV16(acc[3], fw[4][3], p[7]); SB();
  { const int ix = 35 + up; vU = (ix >> 2) * 5120 + frag(ix & 3, uL); PINV(vU); }
  SB();
  SB();
  WAITVB(3); SB();
  MV16(acc[0], fw[5][0], p[8]); SB();
  LD16M(fo[0], vP0, sWo, mW);
  LD16M(fo[1], vP1, sWo, mW);
  OP4_3("v_add_f32", mm, t4[0], bias);
  SB();
  MV16(acc[1], fw[5][1], p[8]); SB();
  OP4_3("v_add_f32", zz, t4[1], t4[2]);
  SB();
  MV16(acc[2], fw[5][2], p[8]); SB();
  ACC4("v_add_f32", mm, t4[3]);
  SB();
  MV16(acc[3], fw[5][3], p[8]); SB();
  ACC4("v_add_f32", zz, t4[4]);
  SB();
  SB();
  WAITVB(4); SB();
  PRIO(1); SB();
  MV16(acc[0], fw[6][0], p[0]); SB();
  LD16M(fo[2], vP2, sWo, mW);
  LD16M(fo[3], vP3, sWo, mW); sWo += 5120; OPQ(sWo);
  ACC4("v_add_f32", mm, t4[5]);
  SB();
  MV16(acc[1], fw[6][1], p[0]); SB();
  ACC4("v_add_f32", zz, t4[6]);
  SB();
  MV16(acc[2], fw[6][2], p[0]); SB();
  ACC4("v_add_f32", mm, t4[7]);
  SB();
  MV16(acc[3], fw[6][3], p[0]); SB();
  ACC4("v_add_f32", mm, zz);
  SB();
  SB();
  WAITVB(5); SB();
  PRIO(0); SB();
  MV16(acc[0], fw[7][0], p[1]); SB();
  LD16M(fo[4], vP0, sWo, mW);
  LD16M(fo[5], vP1, sWo, mW);
  OP4_3("v_mul_f32", zz, mm, mm);
  SB();
  MV16(acc[1], fw[7][1], p[1]); SB();
  POLY4(zz, c23);
  SB();
  MV16(acc[2], fw[7][2], p[1]); SB();
  ACC4("v_mul_f32", zz, mm);
  SB();
  MV16(acc[3], fw[7][3], p[1]); SB();
  UN4("v_exp_f32", "", "", zz);
  SB();
  NOP(4); SB();
  *(LDS_AS f32x4*)(wa0 + 12288) = acc[0];
  SB();
  *(LDS_AS f32x4*)(wa1 + 12288) = acc[1];
  SB();
  *(LDS_AS f32x4*)(wa2 + 12288) = acc[2];
  SB();
  *(LDS_AS f32x4*)(wa3 + 12288) = acc[3];
  SB();
  BAR();
  STAMP(8);
  UN4("v_add_f32", "1.0, ", "", zz);
  UN4("v_rcp_f32", "", "", zz);
  NFMA4(mm, zz);
  { const unsigned x0 = pack2(mm[0], mm[1]), x1 = pack2(mm[2], mm[3]);
    const auto r0 = __builtin_amdgcn_permlane16_swap(x0, x0, false, false), r1 = __builtin_amdgcn_permlane16_swap(x1, x1, false, false);
    ST16E(0xffff, vU, sU, ((u32x4){r0[0], r1[0], r0[1], r1[1]})); }
  STAMP(9);
  LD16M(fo[6], vP2, sWo, mW);
  LD16M(fo[7], vP3, sWo, mW); sWo += 5120; OPQ(sWo);
  WAITV(2);
  { const char* f_ = sFU + 70 * FU; const unsigned l_ = lane_now() * 16; ST16X(1, l_, f_, nonce); }
  LD16M(fo[8], vP0, sWo, mW);
  LD16M(fo[9], vP1, sWo, mW);
  LD16M(fo[10], vP2, sWo, mW);
  LD16M(fo[11], vP3, sWo, mW); sWo += 5120; OPQ(sWo);
  LD16M(fo[12], vP0, sWo, mW);
  LD16M(fo[13], vP1, sWo, mW);
  { int w_ = w; OPQ(w_); { const int w = w_ & 7, lane = lane_now(); const int n16 = lane & 15, g4 = lane >> 4, q4 = n16 >> 2, pq = lane & 3, r8 = lane >> 3; const unsigned vL16 = lane * 16;
  unsigned vO[7];
#pragma unroll
  for (int i = 0; i < 7; ++i) { vO[i] = line7(64 * i + 8 * w + r8) + (lane & 7) * 16; PINV(vO[i]); }
  const char* sO = A.ws + WS_OT + (size_t)st * OT_T;
  unsigned vFO = (((lane >> 2) & 7) * 8 + (lane & 3)) * FO, ro = lds0 + vL16, roh = ro + 65536;
  unsigned vS0 = pair3(q4) + (2 * (n16 & 3) + (g4 & 1)) * 32 + (g4 >> 1) * 16, vS1 = vS0 - pair3(q4) + pair3(4 + q4);
  const char* sFO = A.ws + WS_FLAGO + ((st >> 1) * 64 + (st & 1) * 4) * FO;
  const char* sS = A.ws + WS_ST + (size_t)(st * 4 + dg) * ST_T + w * 3072;
  const char* sFS = A.ws + WS_FLAGS + ((st * 4 + dg) * 8 + w) * FS;
  asm volatile("" : "+s"(sO), "+s"(sFO), "+s"(sS), "+s"(sFS)); asm volatile("" : "+v"(vFO), "+v"(ro), "+v"(roh), "+v"(vS0), "+v"(vS1));
  STAMP(10);
  STAMP(11);
  if (w == 7) poll1<3>(sFO, vFO, nonce);
  BAR();
  STAMP(12);
  bf16x8 of[3][4]; f32x4 as[4];
  DMA1(vO[0], sO, ldsP, 0, "sc1");
  DMA1(vO[1], sO, ldsP, 1024, "sc1");
  DMA1(vO[2], sO, ldsP, 2048, "sc1");
  DMA1(vO[3], sO, ldsP, 3072, "sc1");
  DMA1(vO[4], sO, ldsP, 4096, "sc1");
  DMA1(vO[5], sO, ldsP, 5120, "sc1");
  DMA1(vO[6], sO, ldsP, 6144, "sc1");
  WAITV(4);
  BAR0();
  if (w == 7) {
  WAITV(0);
  BAR0();
  } else {
  of[0][0] = *(LDS_AS const bf16x8*)(ro + 0);
  of[0][1] = *(LDS_AS const bf16x8*)(ro + 16384);
  of[0][2] = *(LDS_AS const bf16x8*)(ro + 32768);
  of[0][3] = *(LDS_AS const bf16x8*)(ro + 49152);
  STAMP(13);
  of[1][0] = *(LDS_AS const bf16x8*)(roh + 0);
  of[1][1] = *(LDS_AS const bf16x8*)(roh + 16384);
  of[1][2] = *(LDS_AS const bf16x8*)(roh + 32768);
  of[1][3] = *(LDS_AS const bf16x8*)(roh + 49152);
  SB();
  MV16Z(as[0], fo[0], of[0][0]); SB();
  of[2][0] = *(LDS_AS const bf16x8*)(ro + 1024);
  SB();
  MV16Z(as[1], fo[0], of[0][1]); SB();
  of[2][1] = *(LDS_AS const bf16x8*)(ro + 17408);
  SB();
  MV16Z(as[2], fo[0], of[0][2]); SB();
  of[2][2] = *(LDS_AS const bf16x8*)(ro + 33792);
  SB();
  MV16Z(as[3], fo[0], of[0][3]); SB();
  of[2][3] = *(LDS_AS const bf16x8*)(ro + 50176);
  SB();
  SB();
  MV16(as[0], fo[1], of[1][0]); SB();
  of[0][0] = *(LDS_AS const bf16x8*)(roh + 1024);
  SB();
  MV16(as[1], fo[1], of[1][1]); SB();
  of[0][1] = *(LDS_AS const bf16x8*)(roh + 17408);
  SB();
  MV16(as[2], fo[1], of[1][2]); SB();
  of[0][2] = *(LDS_AS const bf16x8*)(roh + 33792);
  SB();
  MV16(as[3], fo[1], of[1][3]); SB();
  of[0][3] = *(LDS_AS const bf16x8*)(roh + 50176);
  SB();
  SB();
  MV16(as[0], fo[2], of[2][0]); SB();
  of[1][0] = *(LDS_AS const bf16x8*)(ro + 2048);
  SB();
  MV16(as[1], fo[2], of[2][1]); SB();
  of[1][1] = *(LDS_AS const bf16x8*)(ro + 18432);
  SB();
  MV16(as[2], fo[2], of[2][2]); SB();
  of[1][2] = *(LDS_AS const bf16x8*)(ro + 34816);
  SB();
  MV16(as[3], fo[2], of[2][3]); SB();
  of[1][3] = *(LDS_AS const bf16x8*)(ro + 51200);
  SB();
  SB();
  MV16(as[0], fo[3], of[0][0]); SB();
  of[2][0] = *(LDS_AS const bf16x8*)(roh + 2048);
  SB();
  MV16(as[1], fo[3], of[0][1]); SB();
  of[2][1] = *(LDS_AS const bf16x8*)(roh + 18432);
  SB();
  MV16(as[2], fo[3], of[0][2]); SB();
  of[2][2] = *(LDS_AS const bf16x8*)(roh + 34816);
  SB();
  MV16(as[3], fo[3], of[0][3]); SB();
  of[2][3] = *(LDS_AS const bf16x8*)(roh + 51200);
  SB();
  SB();
  MV16(as[0], fo[4], of[1][0]); SB();
  WAITV(0);
  BAR0();
  of[0][0] = *(LDS_AS const bf16x8*)(ro + 3072);
  SB();
  MV16(as[1], fo[4], of[1][1]); SB();
  of[0][1] = *(LDS_AS const bf16x8*)(ro + 19456);
  SB();
  MV16(as[2], fo[4], of[1][2]); SB();
  of[0][2] = *(LDS_AS const bf16x8*)(ro + 35840);
  SB();
  MV16(as[3], fo[4], of[1][3]); SB();
  of[0][3] = *(LDS_AS const bf16x8*)(ro + 52224);
  SB();
  SB();
  MV16(as[0], fo[5], of[2][0]); SB();
  of[1][0] = *(LDS_AS const bf16x8*)(roh + 3072);
  SB();
  MV16(as[1], fo[5], of[2][1]); SB();
  of[1][1] = *(LDS_AS const bf16x8*)(roh + 19456);
  SB();
  MV16(as[2], fo[5], of[2][2]); SB();
  of[1][2] = *(LDS_AS const bf16x8*)(roh + 35840);
  SB();
  MV16(as[3], fo[5], of[2][3]); SB();
  of[1][3] = *(LDS_AS const bf16x8*)(roh + 52224);
  SB();
  SB();
  MV16(as[0], fo[6], of[0][0]); SB();
  of[2][0] = *(LDS_AS const bf16x8*)(ro + 4096);
  SB();
  MV16(as[1], fo[6], of[0][1]); SB();
  of[2][1] = *(LDS_AS const bf16x8*)(ro + 20480);
  SB();
  MV16(as[2], fo[6], of[0][2]); SB();
  of[2][2] = *(LDS_AS const bf16x8*)(ro + 36864);
  SB();
  MV16(as[3], fo[6], of[0][3]); SB();
  of[2][3] = *(LDS_AS const bf16x8*)(ro + 53248);
  SB();
  SB();
  MV16(as[0], fo[7], of[1][0]); SB();
  of[0][0] = *(LDS_AS const bf16x8*)(roh + 4096);
  SB();
  MV16(as[1], fo[7], of[1][1]); SB();
  of[0][1] = *(LDS_AS const bf16x8*)(roh + 20480);
  SB();
  MV16(as[2], fo[7], of[1][2]); SB();
  of[0][2] = *(LDS_AS const bf16x8*)(roh + 36864);
  SB();
  MV16(as[3], fo[7], of[1][3]); SB();
  of[0][3] = *(LDS_AS const bf16x8*)(roh + 53248);
  SB();
  SB();
  MV16(as[0], fo[8], of[2][0]); SB();
  of[1][0] = *(LDS_AS const bf16x8*)(ro + 5120);
  SB();
  MV16(as[1], fo[8], of[2][1]); SB();
  of[1][1] = *(LDS_AS const bf16x8*)(ro + 21504);
  SB();
  MV16(as[2], fo[8], of[2][2]); SB();
  of[1][2] = *(LDS_AS const bf16x8*)(ro + 37888);
  SB();
  MV16(as[3], fo[8], of[2][3]); SB();
  of[1][3] = *(LDS_AS const bf16x8*)(ro + 54272);
  SB();
  SB();
  MV16(as[0], fo[9], of[0][0]); SB();
  of[2][0] = *(LDS_AS const bf16x8*)(roh + 5120);
  SB();
  MV16(as[1], fo[9], of[0][1]); SB();
  of[2][1] = *(LDS_AS const bf16x8*)(roh + 21504);
  SB();
  MV16(as[2], fo[9], of[0][2]); SB();
  of[2][2] = *(LDS_AS const bf16x8*)(roh + 37888);
  SB();
  MV16(as[3], fo[9], of[0][3]); SB();
  of[2][3] = *(LDS_AS const bf16x8*)(roh + 54272);
  SB();
  SB();
  MV16(as[0], fo[10], of[1][0]); SB();
  of[0][0] = *(LDS_AS const bf16x8*)(ro + 6144);
  SB();
  MV16(as[1], fo[10], of[1][1]); SB();
  of[0][1] = *(LDS_AS const bf16x8*)(ro + 22528);
  SB();
  MV16(as[2], fo[10], of[1][2]); SB();
  of[0][2] = *(LDS_AS const bf16x8*)(ro + 38912);
  SB();
  MV16(as[3], fo[10], of[1][3]); SB();
  of[0][3] = *(LDS_AS const bf16x8*)(ro + 55296);
  SB();
  SB();
  MV16(as[0], fo[11], of[2][0]); SB();
  of[1][0] = *(LDS_AS const bf16x8*)(roh + 6144);
  SB();
  MV16(as[1], fo[11], of[2][1]); SB();
  of[1][1] = *(LDS_AS const bf16x8*)(roh + 22528);
  SB();
  MV16(as[2], fo[11], of[2][2]); SB();
  of[1][2] = *(LDS_AS const bf16x8*)(roh + 38912);
  SB();
  MV16(as[3], fo[11], of[2][3]); SB();
  of[1][3] = *(LDS_AS const bf16x8*)(roh + 55296);
  SB();
  SB();
  MV16(as[0], fo[12], of[0][0]); SB();
  MV16(as[1], fo[12], of[0][1]); SB();
  MV16(as[2], fo[12], of[0][2]); SB();
  MV16(as[3], fo[12], of[0][3]); SB();
  SB();
  MV16(as[0], fo[13], of[1][0]); SB();
  MV16(as[1], fo[13], of[1][1]); SB();
  MV16(as[2], fo[13], of[1][2]); SB();
  MV16(as[3], fo[13], of[1][3]); SB();
  NOP(5); SB();
  { const auto r0 = __builtin_amdgcn_permlane16_swap(pack2(as[0][0], as[0][1]), pack2(as[1][0], as[1][1]), false, false);
    const auto r1 = __builtin_amdgcn_permlane16_swap(pack2(as[0][2], as[0][3]), pack2(as[1][2], as[1][3]), false, false);
    ST16(vS0, sS, 0, ((u32x4){r0[0], r1[0], r0[1], r1[1]})); } SB();
  { const auto r0 = __builtin_amdgcn_permlane16_swap(pack2(as[2][0], as[2][1]), pack2(as[3][0], as[3][1]), false, false);
    const auto r1 = __builtin_amdgcn_permlane16_swap(pack2(as[2][2], as[2][3]), pack2(as[3][2], as[3][3]), false, false);
    ST16(vS1, sS, 0, ((u32x4){r0[0], r1[0], r0[1], r1[1]})); } SB();
  STAMP(14);
  WAITV(0);
  ST16X(15, vL16, sFS, nonce);
  STAMP(15);
  }
  STAMP_DUMP();
  }}}}
  }
  } else {
  WAITV(0);
  __builtin_amdgcn_s_sleep(40);
  poll1<16>(sFH, vFH, nonce);
  STAMP(3);
  __builtin_amdgcn_s_sleep(64);
  BAR();
  const unsigned vP0 = frag(0, lane), vP1 = frag(1, lane), vP2 = frag(2, lane), vP3 = frag(3, lane);
  bf16x8 fo[14];
  const char* sWo = A.ws + WS_WO + (size_t)(dg * 7 + (w < 7 ? w : 6)) * WO_B; unsigned long long mW = w < 7 ? ~0ull : 1ull; OPQ(sWo); OPQ(mW);
  LD16M(fo[0], vP0, sWo, mW);
  LD16M(fo[1], vP1, sWo, mW);
  LD16M(fo[2], vP2, sWo, mW);
  LD16M(fo[3], vP3, sWo, mW); sWo += 5120; OPQ(sWo);
  LD16M(fo[4], vP0, sWo, mW);
  LD16M(fo[5], vP1, sWo, mW);
  LD16M(fo[6], vP2, sWo, mW);
  LD16M(fo[7], vP3, sWo, mW); sWo += 5120; OPQ(sWo);
  LD16M(fo[8], vP0, sWo, mW);
  LD16M(fo[9], vP1, sWo, mW);
  LD16M(fo[10], vP2, sWo, mW);
  LD16M(fo[11], vP3, sWo, mW); sWo += 5120; OPQ(sWo);
  LD16M(fo[12], vP0, sWo, mW);
  LD16M(fo[13], vP1, sWo, mW);
  { int w_ = w; OPQ(w_); { const int w = w_ & 7, lane = lane_now(); const int n16 = lane & 15, g4 = lane >> 4, q4 = n16 >> 2, pq = lane & 3, r8 = lane >> 3; const unsigned vL16 = lane * 16;
  unsigned vO[7];
#pragma unroll
  for (int i = 0; i < 7; ++i) { vO[i] = line7(64 * i + 8 * w + r8) + (lane & 7) * 16; PINV(vO[i]); }
  const char* sO = A.ws + WS_OT + (size_t)st * OT_T;
  unsigned vFO = (((lane >> 2) & 7) * 8 + (lane & 3)) * FO, ro = lds0 + vL16, roh = ro + 65536;
  unsigned vS0 = pair3(q4) + (2 * (n16 & 3) + (g4 & 1)) * 32 + (g4 >> 1) * 16, vS1 = vS0 - pair3(q4) + pair3(4 + q4);
  const char* sFO = A.ws + WS_FLAGO + ((st >> 1) * 64 + (st & 1) * 4) * FO;
  const char* sS = A.ws + WS_ST + (size_t)(st * 4 + dg) * ST_T + w * 3072;
  const char* sFS = A.ws + WS_FLAGS + ((st * 4 + dg) * 8 + w) * FS;
  asm volatile("" : "+s"(sO), "+s"(sFO), "+s"(sS), "+s"(sFS)); asm volatile("" : "+v"(vFO), "+v"(ro), "+v"(roh), "+v"(vS0), "+v"(vS1));
  STAMP(11);
  if (w == 7) poll1<3>(sFO, vFO, nonce);
  BAR();
  STAMP(12);
  bf16x8 of[3][4]; f32x4 as[4];
  DMA1(vO[0], sO, ldsP, 0, "sc1");
  DMA1(vO[1], sO, ldsP, 1024, "sc1");
  DMA1(vO[2], sO, ldsP, 2048, "sc1");
  DMA1(vO[3], sO, ldsP, 3072, "sc1");
  DMA1(vO[4], sO, ldsP, 4096, "sc1");
  DMA1(vO[5], sO, ldsP, 5120, "sc1");
  DMA1(vO[6], sO, ldsP, 6144, "sc1");
  WAITV(4);
  BAR0();
  if (w == 7) {
  WAITV(0);
  BAR0();
  } else {
  of[0][0] = *(LDS_AS const bf16x8*)(ro + 0);
  of[0][1] = *(LDS_AS const bf16x8*)(ro + 16384);
  of[0][2] = *(LDS_AS const bf16x8*)(ro + 32768);
  of[0][3] = *(LDS_AS const bf16x8*)(ro + 49152);
  STAMP(13);
  of[1][0] = *(LDS_AS const bf16x8*)(roh + 0);
  of[1][1] = *(LDS_AS const bf16x8*)(roh + 16384);
  of[1][2] = *(LDS_AS const bf16x8*)(roh + 32768);
  of[1][3] = *(LDS_AS const bf16x8*)(roh + 49152);
  SB();
  MV16Z(as[0], fo[0], of[0][0]); SB();
  of[2][0] = *(LDS_AS const bf16x8*)(ro + 1024);
  SB();
  MV16Z(as[1], fo[0], of[0][1]); SB();
  of[2][1] = *(LDS_AS const bf16x8*)(ro + 17408);
  SB();
  MV16Z(as[2], fo[0], of[0][2]); SB();
  of[2][2] = *(LDS_AS const bf16x8*)(ro + 33792);
  SB();
  MV16Z(as[3], fo[0], of[0][3]); SB();
  of[2][3] = *(LDS_AS const bf16x8*)(ro + 50176);
  SB();
  SB();
  MV16(as[0], fo[1], of[1][0]); SB();
  of[0][0] = *(LDS_AS const bf16x8*)(roh + 1024);
  SB();
  MV16(as[1], fo[1], of[1][1]); SB();
  of[0][1] = *(LDS_AS const bf16x8*)(roh + 17408);
  SB();
  MV16(as[2], fo[1], of[1][2]); SB();
  of[0][2] = *(LDS_AS const bf16x8*)(roh + 33792);
  SB();
  MV16(as[3], fo[1], of[1][3]); SB();
  of[0][3] = *(LDS_AS const bf16x8*)(roh + 50176);
  SB();
  SB();
  MV16(as[0], fo[2], of[2][0]); SB();
  of[1][0] = *(LDS_AS const bf16x8*)(ro + 2048);
  SB();
  MV16(as[1], fo[2], of[2][1]); SB();
  of[1][1] = *(LDS_AS const bf16x8*)(ro + 18432);
  SB();
  MV16(as[2], fo[2], of[2][2]); SB();
  of[1][2] = *(LDS_AS const bf16x8*)(ro + 34816);
  SB();
  MV16(as[3], fo[2], of[2][3]); SB();
  of[1][3] = *(LDS_AS const bf16x8*)(ro + 51200);
  SB();
  SB();
  MV16(as[0], fo[3], of[0][0]); SB();
  of[2][0] = *(LDS_AS const bf16x8*)(roh + 2048);
  SB();
  MV16(as[1], fo[3], of[0][1]); SB();
  of[2][1] = *(LDS_AS const bf16x8*)(roh + 18432);
  SB();
  MV16(as[2], fo[3], of[0][2]); SB();
  of[2][2] = *(LDS_AS const bf16x8*)(roh + 34816);
  SB();
  MV16(as[3], fo[3], of[0][3]); SB();
  of[2][3] = *(LDS_AS const bf16x8*)(roh + 51200);
  SB();
  SB();
  MV16(as[0], fo[4], of[1][0]); SB();
  WAITV(0);
  BAR0();
  of[0][0] = *(LDS_AS const bf16x8*)(ro + 3072);
  SB();
  MV16(as[1], fo[4], of[1][1]); SB();
  of[0][1] = *(LDS_AS const bf16x8*)(ro + 19456);
  SB();
  MV16(as[2], fo[4], of[1][2]); SB();
  of[0][2] = *(LDS_AS const bf16x8*)(ro + 35840);
  SB();
  MV16(as[3], fo[4], of[1][3]); SB();
  of[0][3] = *(LDS_AS const bf16x8*)(ro + 52224);
  SB();
  SB();
  MV16(as[0], fo[5], of[2][0]); SB();
  of[1][0] = *(LDS_AS const bf16x8*)(roh + 3072);
  SB();
  MV16(as[1], fo[5], of[2][1]); SB();
  of[1][1] = *(LDS_AS const bf16x8*)(roh + 19456);
  SB();
  MV16(as[2], fo[5], of[2][2]); SB();
  of[1][2] = *(LDS_AS const bf16x8*)(roh + 35840);
  SB();
  MV16(as[3], fo[5], of[2][3]); SB();
  of[1][3] = *(LDS_AS const bf16x8*)(roh + 52224);
  SB();
  SB();
  MV16(as[0], fo[6], of[0][0]); SB();
  of[2][0] = *(LDS_AS const bf16x8*)(ro + 4096);
  SB();
  MV16(as[1], fo[6], of[0][1]); SB();
  of[2][1] = *(LDS_AS const bf16x8*)(ro + 20480);
  SB();
  MV16(as[2], fo[6], of[0][2]); SB();
  of[2][2] = *(LDS_AS const bf16x8*)(ro + 36864);
  SB();
  MV16(as[3], fo[6], of[0][3]); SB();
  of[2][3] = *(LDS_AS const bf16x8*)(ro + 53248);
  SB();
  SB();
  MV16(as[0], fo[7], of[1][0]); SB();
  of[0][0] = *(LDS_AS const bf16x8*)(roh + 4096);
  SB();
  MV16(as[1], fo[7], of[1][1]); SB();
  of[0][1] = *(LDS_AS const bf16x8*)(roh + 20480);
  SB();
  MV16(as[2], fo[7], of[1][2]); SB();
  of[0][2] = *(LDS_AS const bf16x8*)(roh + 36864);
  SB();
  MV16(as[3], fo[7], of[1][3]); SB();
  of[0][3] = *(LDS_AS const bf16x8*)(roh + 53248);
  SB();
  SB();
  MV16(as[0], fo[8], of[2][0]); SB();
  of[1][0] = *(LDS_AS const bf16x8*)(ro + 5120);
  SB();
  MV16(as[1], fo[8], of[2][1]); SB();
  of[1][1] = *(LDS_AS const bf16x8*)(ro + 21504);
  SB();
  MV16(as[2], fo[8], of[2][2]); SB();
  of[1][2] = *(LDS_AS const bf16x8*)(ro + 37888);
  SB();
  MV16(as[3], fo[8], of[2][3]); SB();
  of[1][3] = *(LDS_AS const bf16x8*)(ro + 54272);
  SB();
  SB();
  MV16(as[0], fo[9], of[0][0]); SB();
  of[2][0] = *(LDS_AS const bf16x8*)(roh + 5120);
  SB();
  MV16(as[1], fo[9], of[0][1]); SB();
  of[2][1] = *(LDS_AS const bf16x8*)(roh + 21504);
  SB();
  MV16(as[2], fo[9], of[0][2]); SB();
  of[2][2] = *(LDS_AS const bf16x8*)(roh + 37888);
  SB();
  MV16(as[3], fo[9], of[0][3]); SB();
  of[2][3] = *(LDS_AS const bf16x8*)(roh + 54272);
  SB();
  SB();
  MV16(as[0], fo[10], of[1][0]); SB();
  of[0][0] = *(LDS_AS const bf16x8*)(ro + 6144);
  SB();
  MV16(as[1], fo[10], of[1][1]); SB();
  of[0][1] = *(LDS_AS const bf16x8*)(ro + 22528);
  SB();
  MV16(as[2], fo[10], of[1][2]); SB();
  of[0][2] = *(LDS_AS const bf16x8*)(ro + 38912);
  SB();
  MV16(as[3], fo[10], of[1][3]); SB();
  of[0][3] = *(LDS_AS const bf16x8*)(ro + 55296);
  SB();
  SB();
  MV16(as[0], fo[11], of[2][0]); SB();
  of[1][0] = *(LDS_AS const bf16x8*)(roh + 6144);
  SB();
  MV16(as[1], fo[11], of[2][1]); SB();
  of[1][1] = *(LDS_AS const bf16x8*)(roh + 22528);
  SB();
  MV16(as[2], fo[11], of[2][2]); SB();
  of[1][2] = *(LDS_AS const bf16x8*)(roh + 38912);
  SB();
  MV16(as[3], fo[11], of[2][3]); SB();
  of[1][3] = *(LDS_AS const bf16x8*)(roh + 55296);
  SB();
  SB();
  MV16(as[0], fo[12], of[0][0]); SB();
  MV16(as[1], fo[12], of[0][1]); SB();
  MV16(as[2], fo[12], of[0][2]); SB();
  MV16(as[3], fo[12], of[0][3]); SB();
  SB();
  MV16(as[0], fo[13], of[1][0]); SB();
  MV16(as[1], fo[13], of[1][1]); SB();
  MV16(as[2], fo[13], of[1][2]); SB();
  MV16(as[3], fo[13], of[1][3]); SB();
  NOP(5); SB();
  { const auto r0 = __builtin_amdgcn_permlane16_swap(pack2(as[0][0], as[0][1]), pack2(as[1][0], as[1][1]), false, false);
    const auto r1 = __builtin_amdgcn_permlane16_swap(pack2(as[0][2], as[0][3]), pack2(as[1][2], as[1][3]), false, false);
    ST16(vS0, sS, 0, ((u32x4){r0[0], r1[0], r0[1], r1[1]})); } SB();
  { const auto r0 = __builtin_amdgcn_permlane16_swap(pack2(as[2][0], as[2][1]), pack2(as[3][0], as[3][1]), false, false);
    const auto r1 = __builtin_amdgcn_permlane16_swap(pack2(as[2][2], as[2][3]), pack2(as[3][2], as[3][3]), false, false);
    ST16(vS1, sS, 0, ((u32x4){r0[0], r1[0], r0[1], r1[1]})); } SB();
  STAMP(14);
  WAITV(0);
  ST16X(15, vL16, sFS, nonce);
  STAMP(15);
  }
  if (q == 0 && jb == 31 && w == 7) ST4X(0u, A.ws0, nonce);
  STAMP_DUMP();
  asm volatile(".Lcpf_e:");
  }}
  }
  }
}

extern "C" void kernel_launch(void* const* d_in, const int* in_sizes, int n_in,
                              void* d_out, int out_size, void* d_ws, size_t ws_size,
                              hipStream_t stream) {
  fused_kernel<<<256, 512, 0, stream>>>((const char*)d_in[0], (const char*)d_in[1], (const char*)d_in[2], (const char*)d_in[4],
                                        (const char*)d_in[5], (const char*)d_in[6], (const char*)d_in[7], (char*)d_ws, (char*)d_out);
}
```

```cpp
#include <hip/hip_runtime.h>
#include <hip/hip_bf16.h>
#include <stdint.h>

typedef short bf16x8 __attribute__((ext_vector_type(8)));
typedef short s16x4 __attribute__((ext_vector_type(4)));
typedef float f32x4 __attribute__((ext_vector_type(4)));
typedef float f32x16 __attribute__((ext_vector_type(16)));
typedef unsigned u32x4 __attribute__((ext_vector_type(4)));
typedef unsigned u32x2 __attribute__((ext_vector_type(2)));
typedef __bf16 bf16x2_t __attribute__((ext_vector_type(2)));
typedef float f32x2_t __attribute__((ext_vector_type(2)));
#define LDS_AS __attribute__((address_space(3)))
#define DEV __device__ __forceinline__

#define STAMP(i)
#define STAMP_DUMP()

#define SB() __builtin_amdgcn_sched_barrier(0)
#define WAITV(n) asm volatile("s_waitcnt vmcnt(" #n ")" ::: "memory")
#define WAITV1(n, a) asm volatile("s_waitcnt vmcnt(" #n ")" : "+v"(a) :: "memory")
#define WAITV2(n, a, b) asm volatile("s_waitcnt vmcnt(" #n ")" : "+v"(a), "+v"(b) :: "memory")
#define WAITV3(n, a, b, c) asm volatile("s_waitcnt vmcnt(" #n ")" : "+v"(a), "+v"(b), "+v"(c) :: "memory")
#define WAITV4(n, a, b, c, d) asm volatile("s_waitcnt vmcnt(" #n ")" : "+v"(a), "+v"(b), "+v"(c), "+v"(d) :: "memory")
#define WAITL(n) do { asm volatile("" ::: "memory"); __builtin_amdgcn_s_waitcnt(0xc07f | ((n) << 8)); asm volatile("" ::: "memory"); } while (0)
#define BAR() do { WAITL(0); __builtin_amdgcn_s_barrier(); asm volatile("" ::: "memory"); } while (0)
#define BAR0() do { asm volatile("s_barrier" ::: "memory"); } while (0)
#define OPQ(p) asm volatile("" : "+s"(p))
#define PINV(p) asm volatile("" : "+v"(p))
#define PINF(p) asm("" : "+v"(p))
#define PRIO(n) __builtin_amdgcn_s_setprio(n)
#define LD16(dst, voff, sb, imm, bits) asm volatile("global_load_dwordx4 %0, %1, %2 offset:" #imm " " bits : "=&v"(dst) : "v"(voff), "s"(sb) : "memory")
#define LD16M(dst, voff, sb, m) asm volatile("s_mov_b64 exec, %3\n\tglobal_load_dwordx4 %0, %1, %2 sc1\n\ts_mov_b64 exec, -1" : "=&v"(dst) : "v"(voff), "s"(sb), "s"(m) : "memory")
#define LD8(dst, voff, sb, imm, bits) asm volatile("global_load_dwordx2 %0, %1, %2 offset:" #imm " " bits : "=&v"(dst) : "v"(voff), "s"(sb) : "memory")
#define LD4(dst, voff, sb, imm, bits) asm volatile("global_load_dword %0, %1, %2 offset:" #imm " " bits : "=&v"(dst) : "v"(voff), "s"(sb) : "memory")
#define LD2(dst, voff, sb, imm, bits) asm volatile("global_load_ushort %0, %1, %2 offset:" #imm " " bits : "=&v"(dst) : "v"(voff), "s"(sb) : "memory")
#define DMA1(v0, sb, lds, limm, bits) \
  asm volatile("s_add_u32 m0, %2, %3\n\ts_nop 0\n\tglobal_load_lds_dwordx4 %0, %1 " bits :: "v"(v0), "s"(sb), "s"(lds), "n"(limm) : "memory", "scc")
#define DMA2(v0, v1, sb, lds, limm, bits) \
  asm volatile("s_add_u32 m0, %3, %4\n\ts_nop 0\n\tglobal_load_lds_dwordx4 %0, %2 " bits "\n\tglobal_load_lds_dwordx4 %1, %2 offset:1024 " bits \
               :: "v"(v0), "v"(v1), "s"(sb), "s"(lds), "n"(limm) : "memory", "scc")
#define TOUCH(v0, sb, lds) asm volatile("s_mov_b32 m0, %2\n\ts_nop 0\n\tglobal_load_lds_dword %0, %1 sc1" :: "v"(v0), "s"(sb), "s"(lds) : "memory")
#define ST16(voff, sb, imm, v) do { const u32x4 v_ = (v); asm volatile("global_store_dwordx4 %0, %1, %2 offset:" #imm " sc1\n\ts_nop 1" :: "v"(voff), "v"(v_), "s"(sb) : "memory"); } while (0)
#define ST16M(mask, voff, sb, v) do { const u32x4 v_ = (v); \
  asm volatile("s_mov_b32 exec_lo, %3\n\ts_mov_b32 exec_hi, %3\n\tglobal_store_dwordx4 %0, %1, %2 sc1\n\ts_mov_b64 exec, -1\n\ts_nop 0" :: "v"(voff), "v"(v_), "s"(sb), "s"(mask) : "memory"); } while (0)
#define ST16E(mask, voff, sb, v) do { const u32x4 v_ = (v); \
  asm volatile("s_mov_b32 exec_lo, " #mask "\n\ts_mov_b32 exec_hi, " #mask "\n\tglobal_store_dwordx4 %0, %1, %2 sc1\n\ts_mov_b64 exec, -1\n\ts_nop 0" :: "v"(voff), "v"(v_), "s"(sb) : "memory"); } while (0)
#define ST16L56(voff, sb, v) do { const u32x4 v_ = (v); \
  asm volatile("s_mov_b32 exec_hi, 0xffffff\n\tglobal_store_dwordx4 %0, %1, %2 sc1\n\ts_mov_b32 exec_hi, -1\n\ts_nop 0" :: "v"(voff), "v"(v_), "s"(sb) : "memory"); } while (0)
#define ST16L48(voff, sb, imm, v) do { const u32x4 v_ = (v); \
  asm volatile("s_mov_b32 exec_hi, 0xffff\n\tglobal_store_dwordx4 %0, %1, %2 offset:" #imm " sc1\n\ts_mov_b32 exec_hi, -1\n\ts_nop 0" :: "v"(voff), "v"(v_), "s"(sb) : "memory"); } while (0)
#define ST4(voff, sb, v) do { const unsigned v_ = (v); asm volatile("global_store_dword %0, %1, %2 sc1" :: "v"(voff), "v"(v_), "s"(sb) : "memory"); } while (0)
#define ST4M(mask, voff, sb) asm volatile("s_mov_b32 exec_lo, %2\n\ts_mov_b32 exec_hi, %2\n\tglobal_store_dword %0, %0, %1 sc1\n\ts_mov_b64 exec, -1" :: "v"(voff), "s"(sb), "s"(mask) : "memory")
#define ST4X(voff, sb, v) do { const unsigned v_ = (v); asm volatile("s_mov_b64 exec, 1\n\tglobal_store_dword %0, %1, %2 sc1\n\ts_mov_b64 exec, -1" :: "v"(voff), "v"(v_), "s"(sb) : "memory"); } while (0)
#define ST16X(mask, voff, sb, v) do { const u32x4 v_ = {(v), (v), (v), (v)}; asm volatile("s_mov_b64 exec, " #mask "\n\tglobal_store_dwordx4 %0, %1, %2 sc1\n\ts_mov_b64 exec, -1\n\ts_nop 0" :: "v"(voff), "v"(v_), "s"(sb) : "memory"); } while (0)

DEV float lo2f(unsigned v) { return __uint_as_float(v << 16); }
DEV float hi2f(unsigned v) { return __uint_as_float(v & 0xffff0000u); }
DEV unsigned pack2(float a, float b) { f32x2_t v = {a, b}; return __builtin_bit_cast(unsigned, __builtin_convertvector(v, bf16x2_t)); }
DEV float rbf(float a) { return __uint_as_float(pack2(0.f, a)); }
DEV f32x2_t rbf2(f32x2_t a) { return (f32x2_t){rbf(a[0]), rbf(a[1])}; }
DEV float dot2(unsigned a, unsigned b, float c) {
  return __builtin_amdgcn_fdot2_f32_bf16(__builtin_bit_cast(bf16x2_t, a), __builtin_bit_cast(bf16x2_t, b), c, false);
}
DEV bf16x8 cat8(s16x4 a, s16x4 b) { return __builtin_shufflevector(a, b, 0, 1, 2, 3, 4, 5, 6, 7); }
DEV bf16x8 tr8u(unsigned p, int second) {
  return cat8(__builtin_amdgcn_ds_read_tr16_b64_v4i16((LDS_AS s16x4*)p), __builtin_amdgcn_ds_read_tr16_b64_v4i16((LDS_AS s16x4*)(p + second)));
}
template <int CTRL> DEV float dpp_add(float v) {
  return v + __builtin_bit_cast(float, __builtin_amdgcn_update_dpp(0, __builtin_bit_cast(int, v), CTRL, 0xf, 0xf, true));
}
DEV float red32(float v) {
  v = dpp_add<0x140>(dpp_add<0x141>(dpp_add<0x4E>(dpp_add<0xB1>(v))));
  const auto r = __builtin_amdgcn_permlane16_swap(__float_as_uint(v), __float_as_uint(v), false, false);
  return __uint_as_float(r[0]) + __uint_as_float(r[1]);
}
DEV float fmx(float a, float b) { return __builtin_elementwise_maximum(a, b); }
DEV float xmax2(float a, float b) {
  const auto r = __builtin_amdgcn_permlane16_swap(__float_as_uint(a), __float_as_uint(b), false, false);
  const float v = fmx(__uint_as_float(r[0]), __uint_as_float(r[1]));
  const auto s = __builtin_amdgcn_permlane32_swap(__float_as_uint(v), __float_as_uint(v), false, false);
  return fmx(__uint_as_float(s[0]), __uint_as_float(s[1]));
}
DEV float xsum2(float a, float b) {
  const auto r = __builtin_amdgcn_permlane16_swap(__float_as_uint(a), __float_as_uint(b), false, false);
  const float v = __uint_as_float(r[0]) + __uint_as_float(r[1]);
  const auto s = __builtin_amdgcn_permlane32_swap(__float_as_uint(v), __float_as_uint(v), false, false);
  return __uint_as_float(s[0]) + __uint_as_float(s[1]);
}
DEV f32x2_t xbcast2(float v) {
  const auto r = __builtin_amdgcn_permlane16_swap(__float_as_uint(v), __float_as_uint(v), false, false);
  return (f32x2_t){__uint_as_float(r[0]), __uint_as_float(r[1])};
}
#define MFMA32(a, b, c) __builtin_amdgcn_mfma_f32_32x32x16_bf16(a, b, c, 0, 0, 0)
#define MFMA16(a, b, c) __builtin_amdgcn_mfma_f32_16x16x32_bf16(a, b, c, 0, 0, 0)
#define MV16(d, a, b) asm volatile("v_mfma_f32_16x16x32_bf16 %0, %1, %2, %0" : "+v"(d) : "v"(a), "v"(b))
#define MV16Z(d, a, b) asm volatile("v_mfma_f32_16x16x32_bf16 %0, %1, %2, 0" : "=&v"(d) : "v"(a), "v"(b))
#define MV32(d, a, b) asm volatile("v_mfma_f32_32x32x16_bf16 %0, %1, %2, %0" : "+v"(d) : "v"(a), "v"(b))
#define MV32Z(d, a, b) asm volatile("v_mfma_f32_32x32x16_bf16 %0, %1, %2, 0" : "=&v"(d) : "v"(a), "v"(b))
#define NOP(n) asm volatile("s_nop " #n)
DEV int lane_now() { int l; asm volatile("v_mbcnt_lo_u32_b32 %0, -1, 0\n\tv_mbcnt_hi_u32_b32 %0, -1, %0" : "=v"(l)); __builtin_assume(l >= 0 && l < 64); return l; }
#define SEL(h, i) __uint_as_float((m1[i] & __float_as_uint(sc[h][1][i])) | (~m1[i] & __float_as_uint(sc[h][0][i])))
#define Q4(a, i) (f32x4){a[4 * (i)], a[4 * (i) + 1], a[4 * (i) + 2], a[4 * (i) + 3]}

constexpr int kSpinCap = 20000;
template <int SLEEP> DEV void poll1(const char* sb, unsigned voff, unsigned nonce) {
#pragma unroll 1
  for (int spin = 0; spin < kSpinCap; ++spin) {
    __builtin_amdgcn_s_sleep(SLEEP);
    unsigned v;
    asm volatile("global_load_dword %0, %1, %2 sc1\n\ts_waitcnt vmcnt(0)" : "=&v"(v) : "v"(voff), "s"(sb) : "memory");
    if (__ballot(v != nonce) == 0ull) return;
  }
}
DEV unsigned bad4(const u32x4& v, unsigned n) { return (v[0] ^ n) | (v[1] ^ n) | (v[2] ^ n) | (v[3] ^ n); }
template <int SLEEP> DEV void poll4(const char* sb, unsigned voff, unsigned nonce) {
#pragma unroll 1
  for (int spin = 0; spin < kSpinCap; ++spin) {
    __builtin_amdgcn_s_sleep(SLEEP);
    u32x4 a;
    asm volatile("global_load_dwordx4 %0, %1, %2 sc1\n\ts_waitcnt vmcnt(0)" : "=&v"(a) : "v"(voff), "s"(sb) : "memory");
    if (__ballot(bad4(a, nonce) != 0u) == 0ull) return;
  }
}
#define LDS_ARRIVE(old, addr) do { unsigned v_; \
  asm volatile("s_mov_b64 exec, 1\n\tds_add_rtn_u32 %0, %2, %3\n\ts_mov_b64 exec, -1\n\ts_waitcnt lgkmcnt(0)\n\tv_readfirstlane_b32 %1, %0" \
               : "=&v"(v_), "=s"(old) : "v"(addr), "v"(1u) : "memory"); old = __builtin_amdgcn_readfirstlane(old); } while (0)
DEV unsigned frag(int h, int L) {
  return 512 + 1152 * h + L * 16 + (L >= 56 - 8 * h ? 128 : 0);
}

DEV unsigned line7(unsigned l) { return (l + ((l + 4) * 9363 >> 16)) * 128; }

DEV int pair3(int p) { const int g = (p * 11) >> 5, r = p - 3 * g; return g * 1024 + (r ? 256 + 256 * r : 0); }

DEV float ln_inv(float var) {
  const float sd = rbf(__builtin_amdgcn_sqrtf(rbf(rbf(fmaxf(var, 0.f)) + 1e-5f)));
  return __builtin_amdgcn_rcpf(sd);
}
DEV float red64(float v) {
  v = red32(v);
  const auto r = __builtin_amdgcn_permlane32_swap(__float_as_uint(v), __float_as_uint(v), false, false);
  return __uint_as_float(r[0]) + __uint_as_float(r[1]);
}
DEV void ln_1(u32x4& p, f32x2_t (&d)[4], bool act) {
  const float s = dot2(p[0], 0x3f803f80u, dot2(p[1], 0x3f803f80u, 0.f)) + dot2(p[2], 0x3f803f80u, dot2(p[3], 0x3f803f80u, 0.f));
  const float mean = red64(act ? s : 0.f) * (1.0f / 448.0f), mb = rbf(mean), dm = mean - mb;
  f32x2_t q = {0.f, 0.f};
#pragma unroll
  for (int i = 0; i < 4; ++i) { d[i] = (f32x2_t){lo2f(p[i]), hi2f(p[i])} - (f32x2_t){mb, mb}; q = d[i] * d[i] + q; }
  const float inv = ln_inv(red64(act ? q[0] + q[1] : 0.f) * (1.0f / 448.0f) - dm * dm);
#pragma unroll
  for (int i = 0; i < 4; ++i) { d[i] = rbf2(d[i]) * (f32x2_t){inv, inv}; p[i] = pack2(d[i][0], d[i][1]); }
}
template <bool EXACT> DEV void ln_2(u32x4& p, const f32x2_t (&d)[4], bool act) {
  const float s = dot2(p[0], 0x3f803f80u, dot2(p[1], 0x3f803f80u, 0.f)) + dot2(p[2], 0x3f803f80u, dot2(p[3], 0x3f803f80u, 0.f));
  const float q = dot2(p[0], p[0], dot2(p[1], p[1], 0.f)) + dot2(p[2], p[2], dot2(p[3], p[3], 0.f));
  const float mean = red64(act ? s : 0.f) * (1.0f / 448.0f), msq = red64(act ? q : 0.f) * (1.0f / 448.0f);
  const float inv = ln_inv(msq - mean * mean), mb = rbf(mean), c = -mb * inv;
#pragma unroll
  for (int i = 0; i < 4; ++i) {
    const f32x2_t r = EXACT ? rbf2((f32x2_t){lo2f(p[i]), hi2f(p[i])} - (f32x2_t){mb, mb}) * (f32x2_t){inv, inv}
                            : __builtin_elementwise_fma(d[i], (f32x2_t){inv, inv}, (f32x2_t){c, c});
    p[i] = pack2(r[0], r[1]);
  }
}
#define OP4_3(op, d, a, b) asm volatile(op " %0, %4, %8\n\t" op " %1, %5, %9\n\t" op " %2, %6, %10\n\t" op " %3, %7, %11" \
  : "=&v"(d[0]), "=&v"(d[1]), "=&v"(d[2]), "=v"(d[3]) : "v"(a[0]), "v"(a[1]), "v"(a[2]), "v"(a[3]), "v"(b[0]), "v"(b[1]), "v"(b[2]), "v"(b[3]))
#define ACC4(op, d, a) asm volatile(op " %0, %4, %0\n\t" op " %1, %5, %1\n\t" op " %2, %6, %2\n\t" op " %3, %7, %3" \
  : "+v"(d[0]), "+v"(d[1]), "+v"(d[2]), "+v"(d[3]) : "v"(a[0]), "v"(a[1]), "v"(a[2]), "v"(a[3]))
#define UN4(op, pre, post, d) asm volatile(op " %0, " pre "%0" post "\n\t" op " %1, " pre "%1" post "\n\t" op " %2, " pre "%2" post "\n\t" op " %3, " pre "%3" post \
  : "+v"(d[0]), "+v"(d[1]), "+v"(d[2]), "+v"(d[3]))
#define POLY4(d, c) asm volatile("v_fmamk_f32 %0, %0, 0x3dd2d3e8, %4\n\tv_fmamk_f32 %1, %1, 0x3dd2d3e8, %4\n\tv_fmamk_f32 %2, %2, 0x3dd2d3e8, %4\n\tv_fmamk_f32 %3, %3, 0x3dd2d3e8, %4" \
  : "+v"(d[0]), "+v"(d[1]), "+v"(d[2]), "+v"(d[3]) : "v"(c))
#define FMK4(d, k, c) asm volatile("v_fmamk_f32 %0, %0, " k ", %4\n\tv_fmamk_f32 %1, %1, " k ", %5\n\tv_fmamk_f32 %2, %2, " k ", %6\n\tv_fmamk_f32 %3, %3, " k ", %7" \
  : "+v"(d[0]), "+v"(d[1]), "+v"(d[2]), "+v"(d[3]) : "v"(c[0]), "v"(c[1]), "v"(c[2]), "v"(c[3]))
#define NFMA4(d, a) asm volatile("v_fma_f32 %0, -%0, %4, %0\n\tv_fma_f32 %1, -%1, %5, %1\n\tv_fma_f32 %2, -%2, %6, %2\n\tv_fma_f32 %3, -%3, %7, %3" \
  : "+v"(d[0]), "+v"(d[1]), "+v"(d[2]), "+v"(d[3]) : "v"(a[0]), "v"(a[1]), "v"(a[2]), "v"(a[3]))
#define SCL4(d, a) asm volatile("v_mul_f32 %0, 0xbfb8aa3b, %4\n\tv_mul_f32 %1, 0xbfb8aa3b, %5\n\tv_mul_f32 %2, 0xbfb8aa3b, %6\n\tv_mul_f32 %3, 0xbfb8aa3b, %7" \
  : "=&v"(d[0]), "=&v"(d[1]), "=&v"(d[2]), "=v"(d[3]) : "v"(a[0]), "v"(a[1]), "v"(a[2]), "v"(a[3]))
#define WAITVB(n) __builtin_amdgcn_s_waitcnt(0x0f70 | ((n) & 15) | (((n) >> 4) << 14))
DEV void xchg4(float (&a)[4], float (&b)[4]) {
#pragma unroll
  for (int i = 0; i < 4; ++i) {
    const int x = __float_as_int(a[i]), y = __float_as_int(b[i]);
    a[i] = __builtin_bit_cast(float, __builtin_amdgcn_update_dpp(x, y, 0x114, 0xf, 0xa, false));
    b[i] = __builtin_bit_cast(float, __builtin_amdgcn_update_dpp(y, x, 0x104, 0xf, 0x5, false));
  }
}
DEV unsigned fin2(unsigned s, float g0, float g1, f32x2_t x) {
  const f32x2_t r = __builtin_elementwise_fma((f32x2_t){lo2f(s), hi2f(s)}, (f32x2_t){g0, g1}, x);
  return pack2(r[0], r[1]);
}

constexpr unsigned WS_AL = 524288, WIN = 524288, WY = 131072, WX = 393216, WQ_S = 24576, ST_T = 21504;
__host__ __device__ constexpr unsigned o_h(int dg) { return (dg >> 1) * WIN + (dg & 1) * 81920; }
__host__ __device__ constexpr unsigned o_wo(int bk) { return (bk >> 1) * WIN + (bk & 1) * 20480; }
__host__ __device__ constexpr unsigned o_4(int i) { return (i >> 2) * WIN + (i & 3) * 65536; }
__host__ __device__ constexpr unsigned o_xn(int t) { return (t >> 8) * WIN + (t & 255) * 1024; }
constexpr unsigned WS_HT = WX, HU = 2 * WIN, WS_WO = WX + 163840, WS_OT = WX + 14 * WIN, WS_XN = WY + 512, WS_WQ = WY + 8 * WIN, WS_UT = WY + 16 * WIN;
constexpr int FA = 16, FU = 16, FO = 64, FS = 64;
constexpr unsigned WS_FLAGA = WX + 22 * WIN;
constexpr unsigned WS_FLAGU = WS_FLAGA + 32768;
constexpr unsigned WS_FLAGO = WS_FLAGU + 65536;
constexpr unsigned WS_FLAGS = WS_FLAGO + 65536;
constexpr unsigned WS_ST = WX + 23 * WIN;
static_assert(FA <= 128 && FU <= 16 && FO <= 64 && FS <= 64, "flags");
constexpr int KROT = 2, KROT2 = 2;
constexpr int PW = 16384;
constexpr int TH = 8 * PW, TP = 960, TQ = TH + 8 * TP, TQP = 1088;
constexpr int L_CNT = TH + 896, L_DUMP = TQ + 8 * TQP;
constexpr int SG = 39936, SGX = 21504;
constexpr int TB = 3 * SG;
constexpr int WU = 8 * PW;
constexpr int SVB = 4608, SKH = 9216, SK = 4 * SVB, SQ = SK + 2 * SKH - 512;
constexpr int LDS_BYTES = 163840;

struct Args { const char *x, *wqkv, *wo, *w1, *b1, *w2, *b2; char *ws0, *ws, *out; };

__global__ __launch_bounds__(512, 2) void fused_kernel(const char* __restrict__ x, const char* __restrict__ wqkv, const char* __restrict__ wo,
                                                       const char* __restrict__ w1, const char* __restrict__ b1,
                                                       const char* __restrict__ w2, const char* __restrict__ b2,
                                                       char* __restrict__ ws, char* __restrict__ out) {
  __shared__ __attribute__((aligned(256))) char smem[LDS_BYTES];
  const int w = __builtin_amdgcn_readfirstlane(threadIdx.x >> 6);
  const int b = blockIdx.x, jb = b >> 3, q = (b >> 1) & 3, side = b & 1;
  STAMP(0);
  const unsigned lds0 = (unsigned)(size_t)(LDS_AS char*)smem;
  { const char* pc_; unsigned vo_ = (threadIdx.x * 32 + ((b >> 3) & 31)) * 128;
    asm volatile("s_getpc_b64 %0\n.Lcpf_b:\n\ts_mov_b32 m0, %2\n\tv_min_u32 %1, .Lcpf_e-.Lcpf_b-4, %1\n\tglobal_load_lds_dword %1, %0"
                 : "=&s"(pc_), "+v"(vo_) : "s"(lds0 + L_DUMP) : "memory"); }
  Args A = {x, wqkv, wo, w1, b1, w2, b2, ws, ws, out};
  const int lane = threadIdx.x & 63, ci = (b & 7) * 32 + (b >> 3);
  const unsigned vQ = (lane < 56 ? lane : 55) * 16;
  unsigned oX = (8 * ci + w) * 896;
  asm volatile("" : "+s"(oX) : "v"(vQ));
  asm volatile("" :: "s"(A.x), "s"(A.wqkv), "s"(A.wo), "s"(A.w1), "s"(A.b1), "s"(A.w2), "s"(A.b2), "s"(A.out), "s"(A.ws0));
  u32x4 xr;
  const char* sX = A.x + oX;
  LD16(xr, vQ, sX, 0, "");
  SB();
  PRIO(3);
  const int n16 = lane & 15, g4 = lane >> 4, q4 = n16 >> 2, pq = lane & 3, r8 = lane >> 3; const unsigned vL16 = lane * 16;
  const int dg = jb & 3, pk = jb < 4 ? 0 : (2 * q + side) * 28 + jb - 4, qp = (pk * 293) >> 14, kg = pk - 56 * qp;
  const unsigned ldsP = lds0 + w * PW;
  { const int oh = (kg * 37) >> 8, oj = kg - 7 * oh;
    int oq_ = (8 * kg + w) * 2688 + qp * 896, oo_ = (56 * oh + 4 * oj + (w & 3) + 28 * (w >> 2)) * 896; OPQ(oq_); OPQ(oo_);
    const char* sQ = (qp < 3 ? A.wqkv : A.wo) + (qp < 3 ? oq_ : oo_);
    DMA1(vQ, sQ, lds0 + w * TQP, TQ, ""); }
  unsigned nonce; asm volatile("s_load_dword %0, %1, 0x0" : "=&s"(nonce) : "s"(A.ws0) : "memory");
  const int t7 = (jb < 28 ? jb >> 2 : 1) + ((3 - (int)((size_t)A.w1 >> 7) - 7 * q) & 3), ft = 7 * q + (t7 >= 7 ? t7 - 7 : t7);
  const int swp = 0 * (((side ? 35 - jb : jb + 4) >> 5) & 1), is2 = side | swp;
  const int st = 8 * ((q + (side ? 0 : 0) + swp) & 3) + ((((jb >> 2) ^ (7 * swp)) + 3 - (int)((size_t)A.w2 >> 7)) & 7);
  const int rot = side ? KROT2 * dg : KROT * dg + ((jb + 4) >> 5), ld = 3584 + 512 * side;
  int wrap = 7 - side - rot, dW = 16 * ld;
  unsigned vWa = r8 * ld + ((lane & 7) ^ (r8 & 2)) * 16, vWb = (vWa ^ 64) + (8 * ld - 1024);
  vWa &= (side & swp) - 1; vWb &= (side & swp) - 1;
  const char* bW = (side ? A.w2 : A.w1) + ((side ? st : ft) * 128u + w * (256u * 3584u));
  const char* sW = bW + (unsigned)(rot * 2 * dW);
  asm volatile("" : "+s"(wrap), "+s"(dW), "+s"(bW), "+s"(sW) : "v"(vWa), "v"(vWb));
  const unsigned ldsW = lds0 + (w << (14 - side * 1));
  OPQ(A.ws); A.ws = (char*)(((size_t)A.ws + WS_AL) & ~(size_t)(WS_AL - 1));
  *(LDS_AS unsigned*)(lds0 + L_CNT) = 0u;
  unsigned vH, tH, vq, tq, hw = lds0 + TH + w * TP + vQ;
  const int ks = kg >> 2;
  { const int task = w < 7 ? w : 6, bk = 4 * task + g4, d4 = (bk * 37) >> 8, b7 = bk - 7 * d4;
    const int pos = ((ci >> 2) - KROT * d4) & 7, ix = (b7 < 6 ? 8 * b7 + 8 : 0) + pos;
    vH = o_h(d4) + (ix >> 3) * HU + ((ix >> 2) & 1) * 5120 + frag(ix & 3, 16 * (ci & 3) + n16);
    tH = lds0 + TH + q4 * TP + bk * 32 + pq * 8;
    const int ti = w ? w - 1 : 0, c0 = 64 * ti + lane, hp0 = (c0 * 1171) >> 16, cc = c0 - 56 * hp0, kk = (kg >> 2) & 1, g_ = kg & 3;
    const unsigned off = (cc >> 4) * 6144 + qp * 1024 + (cc < 48 ? kk * 3072 + g_ * 256 : kk * 512 + g_ * 128) + (cc & 15) * 16;
    unsigned a_ = hp0 * WIN + line7(off >> 7) + (off & 127), b_ = o_wo(4 * ti + g4) + frag(ks & 3, 16 * g_ + n16);
    PINV(a_); PINV(b_);
    vq = qp < 3 ? a_ : b_;
    tq = lds0 + TQ + q4 * TQP + ti * 128 + g4 * 32 + pq * 8; }
  unsigned wq_ = WS_WQ + (kg >> 3) * WQ_S, wo_ = WS_WO + (ks >> 2) * 5120; OPQ(wq_); OPQ(wo_);
  const char* sN = A.ws + (WS_XN + o_xn(8 * ci + w));
  const char* sH = A.ws + (WS_HT + (ci >> 5) * 10240);
  const char* sWq = A.ws + (qp < 3 ? wq_ : wo_);
  const char* sFA = A.ws + WS_FLAGA + ci * FA;
  int j_ = 3 - jb, n_ = -w; OPQ(j_); OPQ(n_);
  int mH = (w - 7) >> 31, mQ = (j_ & n_) >> 31;
  asm volatile("" : "+s"(mH), "+s"(mQ), "+s"(sN), "+s"(sH), "+s"(sWq), "+s"(sFA)); asm volatile("" : "+v"(vH), "+v"(tH), "+v"(vq), "+v"(tq), "+v"(hw));
  u32x2 bv; { const char* sB = is2 ? A.b2 + st * 128 : A.b1 + ft * 128; const unsigned vB = is2 ? n16 * 8 : ((w & 3) * 16 + 4 * g4) * 2;
    LD8(bv, vB, sB, 0, ""); }
  PRIO(0);
  DMA2(vWa, vWb, sW, ldsW, 0, ""); sW += dW; OPQ(sW);
  DMA2(vWa, vWb, sW, ldsW, 2048, ""); sW = wrap == 0 ? bW : sW + dW; OPQ(sW);
  PRIO(3);
  WAITV1(6, xr);
  STAMP(24);
  f32x2_t dd[4];
  ln_1(xr, dd, lane < 56);
  ST16L56(vQ, sN, xr);
  ln_2<true>(xr, dd, lane < 56);
  *(LDS_AS u32x4*)hw = xr;
  WAITV(6);
  asm volatile("s_waitcnt lgkmcnt(0)" : "+s"(nonce) :: "memory"); nonce += 1u;
  BAR();
  STAMP(25);
  { const bf16x8 h0 = tr8u(tH, 4 * TP), q0 = tr8u(tq, 4 * TQP);
    ST16M(mH, vH, sH, __builtin_bit_cast(u32x4, h0));
    ST16M(mQ, vq, sWq, __builtin_bit_cast(u32x4, q0)); }
  SB();
  PRIO(0);
  DMA2(vWa, vWb, sW, ldsW, 4096, ""); sW += dW; OPQ(sW);
  PRIO(3);
  STAMP(26);
  unsigned fh; const char* sFH = A.ws + WS_FLAGA + w * (32 * FA); unsigned vFH = (lane & 31) * FA;
  asm volatile("" : "+s"(sFH)); asm volatile("" : "+v"(vFH));
  WAITV1(2, bv);
  BAR0(); if (w == 0) ST16X(1, vL16, sFA, nonce);
  PRIO(0);
  STAMP(27);

  if (__builtin_expect(side, 1)) {
  const unsigned ldsU = lds0 + WU + w * 4096, ldsL = lds0 + 2 * SG + w * 4096; bf16x8 fw[7][4];
  DMA2(vWa, vWb, sW, ldsW, 6144, ""); sW = wrap == 1 ? bW : sW + dW; OPQ(sW);
  unsigned rW0 = ldsW + (8 * g4 + q4) * 128 + (((q4 >> 1) | (2 * (g4 & 1))) * 32) + pq * 8, rW1 = rW0 ^ 32, rW2 = rW0 ^ 64, rW3 = rW0 ^ 96;
  asm volatile("" : "+v"(rW0), "+v"(rW1), "+v"(rW2), "+v"(rW3));
  unsigned rL0 = ldsL + (8 * g4 + q4) * 128 + (((q4 >> 1) | (2 * (g4 & 1))) * 32) + pq * 8, rL1 = rL0 ^ 32, rL2 = rL0 ^ 64, rL3 = rL0 ^ 96;
  asm volatile("" : "+v"(rL0), "+v"(rL1), "+v"(rL2), "+v"(rL3));
  unsigned rU0 = ldsU + (8 * g4 + q4) * 128 + (((q4 >> 1) | (2 * (g4 & 1))) * 32) + pq * 8, rU1 = rU0 ^ 32, rU2 = rU0 ^ 64, rU3 = rU0 ^ 96;
  asm volatile("" : "+v"(rU0), "+v"(rU1), "+v"(rU2), "+v"(rU3));
  const int tt = 4 * q + (jb >> 3), hp = jb & 7, t0 = 128 * tt, cb = w & 3;
  unsigned vD[5];
#pragma unroll
  for (int m = 0; m < 5; ++m) {
    const int i = m == 4 && w == 7 ? 31 : w + 8 * m, row = 8 * (i - 21) + r8, tok = t0 - 16 + row;
    unsigned a_ = line7(8 * i + r8) + (lane & 7) * 16, b_ = o_xn(tok < 0 ? 0 : tok) + ((lane & 7) ^ ((row >> 1) & 7)) * 16;
    vD[m] = m < 2 ? a_ : m > 2 ? b_ : i < 21 ? a_ : b_;
    PINV(vD[m]);
  }
  const char* sDq = A.ws + (WS_WQ + hp * WIN); const char* sDx = A.ws + WS_XN; const char* sDm = w < 5 ? sDq : sDx;
  int dm = w < 5 ? (int)WQ_S : 128;
  unsigned ldsD = lds0 + w * 1024, ldsE = w == 7 ? lds0 + 31 * 1024 : ldsD + 32768;
  asm volatile("" : "+s"(sDq), "+s"(sDx), "+s"(sDm), "+s"(dm), "+s"(ldsD), "+s"(ldsE));
  WAITV(5);
  fw[0][0] = tr8u(rW0 + 0, 512);
  fw[0][1] = tr8u(rW1 + 0, 512);
  fw[0][2] = tr8u(rW2 + 0, 512);
  fw[0][3] = tr8u(rW3 + 0, 512);
  WAITL(0);
  DMA2(vWa, vWb, sW, ldsW, 0, ""); sW += dW; OPQ(sW);
  DMA2(vWa, vWb, sW, ldsW, 2048, ""); sW = wrap == 2 ? bW : sW + dW; OPQ(sW);
  u32x4 tb0; u32x2 tb4; unsigned tba = lds0 + TB + (w < 4 ? w : w + 4) * 1024 + vL16, tbb = lds0 + TB + 4096 + w * 512 + lane * 8;
  { const int dk = 4 * g4 - n16;
    u32x4 m_;
#pragma unroll
    for (int i = 0; i < 4; ++i) m_[i] = (unsigned)((dk + i + 7) >> 31);
    tb0 = m_;
    if ((w & 3) == 0) {
#pragma unroll
      for (int i = 0; i < 4; ++i) tb0[i] = ~m_[i] & ((unsigned)(dk + i - 1) < 8u ? (w && lane < 32 ? 0xce6e0000u : 0u) : 0xff800000u);
    } else if (w == 2) {
      tb0 = (u32x4){frag(0, lane), frag(1, lane), frag(2, lane), frag(3, lane)};
    } else if (w == 3) {
      tb0 = (u32x4){(m_[0] & 0xffffu) | (m_[1] & 0xffff0000u), (m_[2] & 0xffffu) | (m_[3] & 0xffff0000u),
                    lds0 + SG + n16 * 64 + ((g4 ^ q4) << 4), lds0 + SG + (4 * g4 + q4) * 32 + pq * 8};
    } else if (w == 5) {
      tb0 = (u32x4){n16 * 128u + ((g4 ^ (n16 & 7)) << 4), r8 * 128u + (((2 * pq + ((lane >> 2) & 1)) ^ r8) << 4), r8 * 896u + (lane & 7) * 16,
                    ((lane & 7) >> 1) * 3072u + r8 * 32 + (lane & 1) * 16};
    }
    if (w < 4) {
      const int ga = 7 * hp + g4;
      const unsigned l0 = ((ga >> 2) * 4 + cb) * 8 + (ga & 3) * 2 + (n16 >> 3);
      tb4 = (u32x2){line7(l0) + (n16 & 7) * 16, line7(l0 + 32) + (n16 & 7) * 16};
    } else {
      const int c = 16 * cb + 4 * g4;
      const bool ok = c < 56;
      const int hd = ok ? (c >= 28) : (g4 & 1), d = ok ? c - 28 * hd : 28, row = (n16 >> 1) + 72 * (n16 & 1);
      tb4 = (u32x2){lds0 + SG + hd * SKH + row * 64 + (((d >> 3) ^ (n16 >> 3) ^ (2 * (n16 & 1))) << 4) + ((d >> 2) & 1) * 8,
                    lds0 + SG + (2 * hd + (d >> 4)) * SVB + row * 32 + (d & 15) * 2};
    } }
  unsigned xr0 = lds0 + SGX + n16 * 128 + ((g4 ^ (n16 >> 1)) << 4), xr1 = xr0 ^ 64;
  unsigned wr0 = lds0 + cb * 6144 + (cb < 3 ? vL16 : g4 * 128 + (n16 & 7) * 16);
  asm volatile("" : "+v"(tb0), "+v"(tb4), "+v"(tba), "+v"(tbb), "+v"(xr0), "+v"(xr1), "+v"(wr0));
  unsigned oP_ = WS_UT + o_4(dg * 8 + w), oW_ = WS_WO + o_wo(dg * 7 + (w < 7 ? w : 6)); OPQ(oP_); OPQ(oW_);
  const char* sP = A.ws + (swp & 1 ? oW_ : oP_);
  const char* sFU = A.ws + WS_FLAGU + (dg * 8 + w) * (128 * FU);
  const char* sO = A.ws + (WS_OT + o_4(2 * tt + (w >> 2)));
  const char* sFO = A.ws + WS_FLAGO + ((tt * 8 + hp) * 8 + w) * FO;
  const char* sS = A.ws + WS_ST + (size_t)(st * 4 + dg) * ST_T + pair3(w);
  const char* sFS = A.ws + WS_FLAGS + (st * 4 + dg) * (8 * FS);
  const char* sXo = A.x + ((64 * st + 8 * w) * 896 + dg * 224); const char* sOut = A.out + ((64 * st + 8 * w) * 896 + dg * 224);
  int kx = 32 * (w & 1), r0 = 72 * (w & 1) + 16 * (w >> 1), cmo = t0 + (w >> 1) == 0 ? 8192 : 0, rao = lds0 + w * 1024, bio = lds0 + TB + 10496 + 32 * w;
  asm volatile("" : "+s"(sP), "+s"(sFU), "+s"(sO), "+s"(sFO), "+s"(sS), "+s"(sFS), "+s"(sXo), "+s"(sOut), "+s"(kx), "+s"(r0), "+s"(cmo), "+s"(rao), "+s"(bio));
  u32x4 t2, t3, m1; f32x4 cm; u32x2 vS; unsigned rk, rv, mq_, vFU;
  *(LDS_AS u32x2*)(tbb) = tb4;
  if (w < 6) *(LDS_AS u32x4*)(tba) = tb0; else *(LDS_AS u32x4*)(lds0 + TB + 10496 + n16 * 16) = (u32x4){bv[0] << 16, bv[0] & 0xffff0000u, bv[1] << 16, bv[1] & 0xffff0000u};
  SB();
  WAITV(4);
  fw[1][0] = tr8u(rW0 + 4096, 512);
  fw[1][1] = tr8u(rW1 + 4096, 512);
  fw[1][2] = tr8u(rW2 + 4096, 512);
  fw[1][3] = tr8u(rW3 + 4096, 512);
  WAITL(0);
  DMA2(vWa, vWb, sW, ldsW, 4096, ""); sW += dW; OPQ(sW);
  DMA2(vWa, vWb, sW, ldsW, 6144, ""); sW = wrap == 3 ? bW : sW + dW; OPQ(sW);
  WAITV(4);
  fw[2][0] = tr8u(rW0 + 0, 512);
  fw[2][1] = tr8u(rW1 + 0, 512);
  fw[2][2] = tr8u(rW2 + 0, 512);
  fw[2][3] = tr8u(rW3 + 0, 512);
  STAMP(33);
  LD4(fh, vFH, sFH, 0, "sc1");
  DMA2(vWa, vWb, sW, ldsL, 0, ""); sW += dW; OPQ(sW);
  DMA2(vWa, vWb, sW, ldsL, 2048, ""); sW = wrap == 4 ? bW : sW + dW; OPQ(sW);
  WAITV(5);
  fw[3][0] = tr8u(rW0 + 4096, 512);
  fw[3][1] = tr8u(rW1 + 4096, 512);
  fw[3][2] = tr8u(rW2 + 4096, 512);
  fw[3][3] = tr8u(rW3 + 4096, 512);
  DMA2(vWa, vWb, sW, ldsU, 0, ""); sW += dW; OPQ(sW);
  DMA2(vWa, vWb, sW, ldsU, 2048, ""); sW = wrap == 5 ? bW : sW + dW; OPQ(sW);
  WAITV1(8, fh);
  if (__ballot(fh != nonce) != 0ull) poll1<4>(sFH, vFH, nonce);
  BAR();
  STAMP(34);
  DMA1(vD[0], sDq, ldsD, 0, "sc1");
  DMA1(vD[1], sDq, ldsD, 8192, "sc1"); sDq += WQ_S; OPQ(sDq);
  DMA1(vD[2], sDm, ldsD, 16384, "sc1"); sDm += dm; OPQ(sDm);
  DMA1(vD[3], sDx, ldsD, 24576, "sc1");
  DMA1(vD[4], sDx, ldsE, 0, "sc1"); sDx += 128; OPQ(sDx);
  BAR0();
  DMA1(vD[0], sDq, ldsD, 39936, "sc1");
  DMA1(vD[1], sDq, ldsD, 48128, "sc1"); sDq += WQ_S; OPQ(sDq);
  DMA1(vD[2], sDm, ldsD, 56320, "sc1"); sDm += dm; OPQ(sDm);
  DMA1(vD[3], sDx, ldsD, 64512, "sc1");
  DMA1(vD[4], sDx, ldsE, 39936, "sc1"); sDx += 128; OPQ(sDx);
  WAITV(14);
  fw[4][0] = tr8u(rL0 + 0, 512);
  fw[4][1] = tr8u(rL1 + 0, 512);
  fw[4][2] = tr8u(rL2 + 0, 512);
  fw[4][3] = tr8u(rL3 + 0, 512);
  WAITV(10);
  fw[5][0] = tr8u(rU0 + 0, 512);
  fw[5][1] = tr8u(rU1 + 0, 512);
  fw[5][2] = tr8u(rU2 + 0, 512);
  fw[5][3] = tr8u(rU3 + 0, 512);
  if (w < 3) {
  f32x4 aq[5], ak[5], av[5];
  bf16x8 xf[2][5], wf[2][5];
  unsigned sk0, sk1, sv;
  WAITV(5);
  BAR();
  STAMP(35);
  wf[0][1] = *(LDS_AS const bf16x8*)(wr0 + 1024);
  xf[0][1] = *(LDS_AS const bf16x8*)(xr0 + 2048);
  wf[0][2] = *(LDS_AS const bf16x8*)(wr0 + 2048);
  wf[0][0] = *(LDS_AS const bf16x8*)(wr0 + 0);
  xf[0][2] = *(LDS_AS const bf16x8*)(xr0 + 4096);
  xf[0][3] = *(LDS_AS const bf16x8*)(xr0 + 6144);
  xf[0][4] = *(LDS_AS const bf16x8*)(xr0 + 8192);
  SB();
  MV16Z(ak[1], wf[0][1], xf[0][1]); SB();
  wf[1][1] = *(LDS_AS const bf16x8*)(wr0 + 4096);
  SB();
  MV16Z(av[1], wf[0][2], xf[0][1]); SB();
  xf[1][1] = *(LDS_AS const bf16x8*)(xr1 + 2048);
  SB();
  MV16Z(aq[1], wf[0][0], xf[0][1]); SB();
  wf[1][2] = *(LDS_AS const bf16x8*)(wr0 + 5120);
  SB();
  MV16Z(ak[2], wf[0][1], xf[0][2]); SB();
  wf[1][0] = *(LDS_AS const bf16x8*)(wr0 + 3072);
  SB();
  MV16Z(av[2], wf[0][2], xf[0][2]); SB();
  xf[1][2] = *(LDS_AS const bf16x8*)(xr1 + 4096);
  SB();
  MV16Z(aq[2], wf[0][0], xf[0][2]); SB();
  xf[1][3] = *(LDS_AS const bf16x8*)(xr1 + 6144);
  SB();
  MV16Z(ak[3], wf[0][1], xf[0][3]); SB();
  xf[1][4] = *(LDS_AS const bf16x8*)(xr1 + 8192);
  SB();
  MV16Z(av[3], wf[0][2], xf[0][3]); SB();
  DMA1(vD[0], sDq, ldsD, 79872, "sc1");
  SB();
  MV16Z(aq[3], wf[0][0], xf[0][3]); SB();
  DMA1(vD[1], sDq, ldsD, 88064, "sc1"); sDq += WQ_S; OPQ(sDq);
  SB();
  MV16Z(ak[4], wf[0][1], xf[0][4]); SB();
  DMA1(vD[2], sDm, ldsD, 96256, "sc1"); sDm += dm; OPQ(sDm);
  SB();
  MV16Z(av[4], wf[0][2], xf[0][4]); SB();
  DMA1(vD[3], sDx, ldsD, 104448, "sc1");
  SB();
  MV16Z(aq[4], wf[0][0], xf[0][4]); SB();
  DMA1(vD[4], sDx, ldsE, 79872, "sc1"); sDx += 128; OPQ(sDx);
  SB();
  SB();
  MV16(ak[1], wf[1][1], xf[1][1]); SB();
  SB();
  MV16(av[1], wf[1][2], xf[1][1]); SB();
  WAITV(5);
  BAR();
  xr0 += 39936; wr0 += 39936; asm volatile("" : "+v"(xr0), "+v"(wr0));
  SB();
  MV16(aq[1], wf[1][0], xf[1][1]); SB();
  wf[0][1] = *(LDS_AS const bf16x8*)(wr0 + 1024);
  SB();
  MV16(ak[2], wf[1][1], xf[1][2]); SB();
  xf[0][1] = *(LDS_AS const bf16x8*)(xr0 + 2048);
  SB();
  MV16(av[2], wf[1][2], xf[1][2]); SB();
  wf[0][2] = *(LDS_AS const bf16x8*)(wr0 + 2048);
  SB();
  MV16(aq[2], wf[1][0], xf[1][2]); SB();
  wf[0][0] = *(LDS_AS const bf16x8*)(wr0 + 0);
  SB();
  MV16(ak[3], wf[1][1], xf[1][3]); SB();
  xf[0][2] = *(LDS_AS const bf16x8*)(xr0 + 4096);
  SB();
  MV16(av[3], wf[1][2], xf[1][3]); SB();
  xf[0][3] = *(LDS_AS const bf16x8*)(xr0 + 6144);
  SB();
  MV16(aq[3], wf[1][0], xf[1][3]); SB();
  xf[0][4] = *(LDS_AS const bf16x8*)(xr0 + 8192);
  SB();
  MV16(ak[4], wf[1][1], xf[1][4]); SB();
  MV16(av[4], wf[1][2], xf[1][4]); SB();
  MV16(aq[4], wf[1][0], xf[1][4]); SB();
  SB();
  MV16(ak[1], wf[0][1], xf[0][1]); SB();
  xr1 += 39936; PINV(xr1);
  wf[1][1] = *(LDS_AS const bf16x8*)(wr0 + 4096);
  SB();
  MV16(av[1], wf[0][2], xf[0][1]); SB();
  xf[1][1] = *(LDS_AS const bf16x8*)(xr1 + 2048);
  SB();
  MV16(aq[1], wf[0][0], xf[0][1]); SB();
  wf[1][2] = *(LDS_AS const bf16x8*)(wr0 + 5120);
  SB();
  MV16(ak[2], wf[0][1], xf[0][2]); SB();
  wf[1][0] = *(LDS_AS const bf16x8*)(wr0 + 3072);
  SB();
  MV16(av[2], wf[0][2], xf[0][2]); SB();
  xf[1][2] = *(LDS_AS const bf16x8*)(xr1 + 4096);
  SB();
  MV16(aq[2], wf[0][0], xf[0][2]); SB();
  xf[1][3] = *(LDS_AS const bf16x8*)(xr1 + 6144);
  SB();
  MV16(ak[3], wf[0][1], xf[0][3]); SB();
  xf[1][4] = *(LDS_AS const bf16x8*)(xr1 + 8192);
  SB();
  MV16(av[3], wf[0][2], xf[0][3]); SB();
  DMA1(vD[0], sDq, ldsD, 0, "sc1");
  SB();
  MV16(aq[3], wf[0][0], xf[0][3]); SB();
  DMA1(vD[1], sDq, ldsD, 8192, "sc1"); sDq += WQ_S; OPQ(sDq);
  SB();
  MV16(ak[4], wf[0][1], xf[0][4]); SB();
  DMA1(vD[2], sDm, ldsD, 16384, "sc1"); sDm += dm; OPQ(sDm);
  SB();
  MV16(av[4], wf[0][2], xf[0][4]); SB();
  DMA1(vD[3], sDx, ldsD, 24576, "sc1");
  SB();
  MV16(aq[4], wf[0][0], xf[0][4]); SB();
  DMA1(vD[4], sDx, ldsE, 0, "sc1"); sDx += 128; OPQ(sDx);
  SB();
  SB();
  MV16(ak[1], wf[1][1], xf[1][1]); SB();
  SB();
  MV16(av[1], wf[1][2], xf[1][1]); SB();
  WAITV(5);
  BAR();
  xr0 += 39936; wr0 += 39936; asm volatile("" : "+v"(xr0), "+v"(wr0));
  STAMP(60);
  SB();
  MV16(aq[1], wf[1][0], xf[1][1]); SB();
  wf[0][1] = *(LDS_AS const bf16x8*)(wr0 + 1024);
  SB();
  MV16(ak[2], wf[1][1], xf[1][2]); SB();
  xf[0][1] = *(LDS_AS const bf16x8*)(xr0 + 2048);
  SB();
  MV16(av[2], wf[1][2], xf[1][2]); SB();
  wf[0][2] = *(LDS_AS const bf16x8*)(wr0 + 2048);
  SB();
  MV16(aq[2], wf[1][0], xf[1][2]); SB();
  wf[0][0] = *(LDS_AS const bf16x8*)(wr0 + 0);
  SB();
  MV16(ak[3], wf[1][1], xf[1][3]); SB();
  xf[0][2] = *(LDS_AS const bf16x8*)(xr0 + 4096);
  SB();
  MV16(av[3], wf[1][2], xf[1][3]); SB();
  xf[0][3] = *(LDS_AS const bf16x8*)(xr0 + 6144);
  SB();
  MV16(aq[3], wf[1][0], xf[1][3]); SB();
  xf[0][4] = *(LDS_AS const bf16x8*)(xr0 + 8192);
  SB();
  MV16(ak[4], wf[1][1], xf[1][4]); SB();
  MV16(av[4], wf[1][2], xf[1][4]); SB();
  MV16(aq[4], wf[1][0], xf[1][4]); SB();
  SB();
  MV16(ak[1], wf[0][1], xf[0][1]); SB();
  xr1 += 39936; PINV(xr1);
  wf[1][1] = *(LDS_AS const bf16x8*)(wr0 + 4096);
  SB();
  MV16(av[1], wf[0][2], xf[0][1]); SB();
  xf[1][1] = *(LDS_AS const bf16x8*)(xr1 + 2048);
  SB();
  MV16(aq[1], wf[0][0], xf[0][1]); SB();
  wf[1][2] = *(LDS_AS const bf16x8*)(wr0 + 5120);
  SB();
  MV16(ak[2], wf[0][1], xf[0][2]); SB();
  wf[1][0] = *(LDS_AS const bf16x8*)(wr0 + 3072);
  SB();
  MV16(av[2], wf[0][2], xf[0][2]); SB();
  xf[1][2] = *(LDS_AS const bf16x8*)(xr1 + 4096);
  SB();
  MV16(aq[2], wf[0][0], xf[0][2]); SB();
  xf[1][3] = *(LDS_AS const bf16x8*)(xr1 + 6144);
  SB();
  MV16(ak[3], wf[0][1], xf[0][3]); SB();
  xf[1][4] = *(LDS_AS const bf16x8*)(xr1 + 8192);
  SB();
  MV16(av[3], wf[0][2], xf[0][3]); SB();
  DMA1(vD[0], sDq, ldsD, 39936, "sc1");
  SB();
  MV16(aq[3], wf[0][0], xf[0][3]); SB();
  DMA1(vD[1], sDq, ldsD, 48128, "sc1"); sDq += WQ_S; OPQ(sDq);
  SB();
  MV16(ak[4], wf[0][1], xf[0][4]); SB();
  DMA1(vD[2], sDm, ldsD, 56320, "sc1"); sDm += dm; OPQ(sDm);
  SB();
  MV16(av[4], wf[0][2], xf[0][4]); SB();
  DMA1(vD[3], sDx, ldsD, 64512, "sc1");
  SB();
  MV16(aq[4], wf[0][0], xf[0][4]); SB();
  DMA1(vD[4], sDx, ldsE, 39936, "sc1"); sDx += 128; OPQ(sDx);
  SB();
  SB();
  MV16(ak[1], wf[1][1], xf[1][1]); SB();
  SB();
  MV16(av[1], wf[1][2], xf[1][1]); SB();
  WAITV(5);
  BAR();
  xr0 += -79872; wr0 += -79872; asm volatile("" : "+v"(xr0), "+v"(wr0));
  SB();
  MV16(aq[1], wf[1][0], xf[1][1]); SB();
  wf[0][1] = *(LDS_AS const bf16x8*)(wr0 + 1024);
  SB();
  MV16(ak[2], wf[1][1], xf[1][2]); SB();
  xf[0][1] = *(LDS_AS const bf16x8*)(xr0 + 2048);
  SB();
  MV16(av[2], wf[1][2], xf[1][2]); SB();
  wf[0][2] = *(LDS_AS const bf16x8*)(wr0 + 2048);
  SB();
  MV16(aq[2], wf[1][0], xf[1][2]); SB();
  wf[0][0] = *(LDS_AS const bf16x8*)(wr0 + 0);
  SB();
  MV16(ak[3], wf[1][1], xf[1][3]); SB();
  xf[0][2] = *(LDS_AS const bf16x8*)(xr0 + 4096);
  SB();
  MV16(av[3], wf[1][2], xf[1][3]); SB();
  xf[0][3] = *(LDS_AS const bf16x8*)(xr0 + 6144);
  SB();
  MV16(aq[3], wf[1][0], xf[1][3]); SB();
  xf[0][4] = *(LDS_AS const bf16x8*)(xr0 + 8192);
  SB();
  MV16(ak[4], wf[1][1], xf[1][4]); SB();
  MV16(av[4], wf[1][2], xf[1][4]); SB();
  MV16(aq[4], wf[1][0], xf[1][4]); SB();
  SB();
  MV16(ak[1], wf[0][1], xf[0][1]); SB();
  xr1 += -79872; PINV(xr1);
  wf[1][1] = *(LDS_AS const bf16x8*)(wr0 + 4096);
  SB();
  MV16(av[1], wf[0][2], xf[0][1]); SB();
  xf[1][1] = *(LDS_AS const bf16x8*)(xr1 + 2048);
  SB();
  MV16(aq[1], wf[0][0], xf[0][1]); SB();
  wf[1][2] = *(LDS_AS const bf16x8*)(wr0 + 5120);
  SB();
  MV16(ak[2], wf[0][1], xf[0][2]); SB();
  wf[1][0] = *(LDS_AS const bf16x8*)(wr0 + 3072);
  SB();
  MV16(av[2], wf[0][2], xf[0][2]); SB();
  xf[1][2] = *(LDS_AS const bf16x8*)(xr1 + 4096);
  SB();
  MV16(aq[2], wf[0][0], xf[0][2]); SB();
  xf[1][3] = *(LDS_AS const bf16x8*)(xr1 + 6144);
  SB();
  MV16(ak[3], wf[0][1], xf[0][3]); SB();
  xf[1][4] = *(LDS_AS const bf16x8*)(xr1 + 8192);
  SB();
  MV16(av[3], wf[0][2], xf[0][3]); SB();
  DMA1(vD[0], sDq, ldsD, 79872, "sc1");
  SB();
  MV16(aq[3], wf[0][0], xf[0][3]); SB();
  DMA1(vD[1], sDq, ldsD, 88064, "sc1"); sDq += WQ_S; OPQ(sDq);
  SB();
  MV16(ak[4], wf[0][1], xf[0][4]); SB();
  DMA1(vD[2], sDm, ldsD, 96256, "sc1"); sDm += dm; OPQ(sDm);
  SB();
  MV16(av[4], wf[0][2], xf[0][4]); SB();
  DMA1(vD[3], sDx, ldsD, 104448, "sc1");
  SB();
  MV16(aq[4], wf[0][0], xf[0][4]); SB();
  DMA1(vD[4], sDx, ldsE, 79872, "sc1"); sDx += 128; OPQ(sDx);
  SB();
  SB();
  MV16(ak[1], wf[1][1], xf[1][1]); SB();
  SB();
  MV16(av[1], wf[1][2], xf[1][1]); SB();
  WAITV(5);
  BAR();
  xr0 += 39936; wr0 += 39936; asm volatile("" : "+v"(xr0), "+v"(wr0));
  STAMP(61);
  SB();
  MV16(aq[1], wf[1][0], xf[1][1]); SB();
  wf[0][1] = *(LDS_AS const bf16x8*)(wr0 + 1024);
  SB();
  MV16(ak[2], wf[1][1], xf[1][2]); SB();
  xf[0][1] = *(LDS_AS const bf16x8*)(xr0 + 2048);
  SB();
  MV16(av[2], wf[1][2], xf[1][2]); SB();
  wf[0][2] = *(LDS_AS const bf16x8*)(wr0 + 2048);
  SB();
  MV16(aq[2], wf[1][0], xf[1][2]); SB();
  wf[0][0] = *(LDS_AS const bf16x8*)(wr0 + 0);
  SB();
  MV16(ak[3], wf[1][1], xf[1][3]); SB();
  xf[0][2] = *(LDS_AS const bf16x8*)(xr0 + 4096);
  SB();
  MV16(av[3], wf[1][2], xf[1][3]); SB();
  xf[0][3] = *(LDS_AS const bf16x8*)(xr0 + 6144);
  SB();
  MV16(aq[3], wf[1][0], xf[1][3]); SB();
  xf[0][4] = *(LDS_AS const bf16x8*)(xr0 + 8192);
  SB();
  MV16(ak[4], wf[1][1], xf[1][4]); SB();
  MV16(av[4], wf[1][2], xf[1][4]); SB();
  MV16(aq[4], wf[1][0], xf[1][4]); SB();
  SB();
  MV16(ak[1], wf[0][1], xf[0][1]); SB();
  xr1 += 39936; PINV(xr1);
  wf[1][1] = *(LDS_AS const bf16x8*)(wr0 + 4096);
  SB();
  MV16(av[1], wf[0][2], xf[0][1]); SB();
  xf[1][1] = *(LDS_AS const bf16x8*)(xr1 + 2048);
  SB();
  MV16(aq[1], wf[0][0], xf[0][1]); SB();
  wf[1][2] = *(LDS_AS const bf16x8*)(wr0 + 5120);
  SB();
  MV16(ak[2], wf[0][1], xf[0][2]); SB();
  wf[1][0] = *(LDS_AS const bf16x8*)(wr0 + 3072);
  SB();
  MV16(av[2], wf[0][2], xf[0][2]); SB();
  xf[1][2] = *(LDS_AS const bf16x8*)(xr1 + 4096);
  SB();
  MV16(aq[2], wf[0][0], xf[0][2]); SB();
  xf[1][3] = *(LDS_AS const bf16x8*)(xr1 + 6144);
  SB();
  MV16(ak[3], wf[0][1], xf[0][3]); SB();
  xf[1][4] = *(LDS_AS const bf16x8*)(xr1 + 8192);
  SB();
  MV16(av[3], wf[0][2], xf[0][3]); SB();
  DMA1(vD[0], sDq, ldsD, 0, "sc1");
  SB();
  MV16(aq[3], wf[0][0], xf[0][3]); SB();
  DMA1(vD[1], sDq, ldsD, 8192, "sc1"); sDq += WQ_S; OPQ(sDq);
  SB();
  MV16(ak[4], wf[0][1], xf[0][4]); SB();
  DMA1(vD[2], sDm, ldsD, 16384, "sc1"); sDm += dm; OPQ(sDm);
  SB();
  MV16(av[4], wf[0][2], xf[0][4]); SB();
  DMA1(vD[3], sDx, ldsD, 24576, "sc1");
  SB();
  MV16(aq[4], wf[0][0], xf[0][4]); SB();
  DMA1(vD[4], sDx, ldsE, 0, "sc1"); sDx += 128; OPQ(sDx);
  SB();
  SB();
  MV16(ak[1], wf[1][1], xf[1][1]); SB();
  SB();
  MV16(av[1], wf[1][2], xf[1][1]); SB();
  WAITV(5);
  BAR();
  xr0 += 39936; wr0 += 39936; asm volatile("" : "+v"(xr0), "+v"(wr0));
  SB();
  MV16(aq[1], wf[1][0], xf[1][1]); SB();
  wf[0][1] = *(LDS_AS const bf16x8*)(wr0 + 1024);
  SB();
  MV16(ak[2], wf[1][1], xf[1][2]); SB();
  xf[0][1] = *(LDS_AS const bf16x8*)(xr0 + 2048);
  SB();
  MV16(av[2], wf[1][2], xf[1][2]); SB();
  wf[0][2] = *(LDS_AS const bf16x8*)(wr0 + 2048);
  SB();
  MV16(aq[2], wf[1][0], xf[1][2]); SB();
  wf[0][0] = *(LDS_AS const bf16x8*)(wr0 + 0);
  SB();
  MV16(ak[3], wf[1][1], xf[1][3]); SB();
  xf[0][2] = *(LDS_AS const bf16x8*)(xr0 + 4096);
  SB();
  MV16(av[3], wf[1][2], xf[1][3]); SB();
  xf[0][3] = *(LDS_AS const bf16x8*)(xr0 + 6144);
  SB();
  MV16(aq[3], wf[1][0], xf[1][3]); SB();
  xf[0][4] = *(LDS_AS const bf16x8*)(xr0 + 8192);
  SB();
  MV16(ak[4], wf[1][1], xf[1][4]); SB();
  MV16(av[4], wf[1][2], xf[1][4]); SB();
  MV16(aq[4], wf[1][0], xf[1][4]); SB();
  SB();
  MV16(ak[1], wf[0][1], xf[0][1]); SB();
  xr1 += 39936; PINV(xr1);
  SB();
  MV16(av[1], wf[0][2], xf[0][1]); SB();
  wf[1][1] = *(LDS_AS const bf16x8*)(wr0 + 4096);
  SB();
  MV16(aq[1], wf[0][0], xf[0][1]); SB();
  xf[1][1] = *(LDS_AS const bf16x8*)(xr1 + 2048);
  SB();
  MV16(ak[2], wf[0][1], xf[0][2]); SB();
  wf[1][2] = *(LDS_AS const bf16x8*)(wr0 + 5120);
  SB();
  MV16(av[2], wf[0][2], xf[0][2]); SB();
  wf[1][0] = *(LDS_AS const bf16x8*)(wr0 + 3072);
  SB();
  MV16(aq[2], wf[0][0], xf[0][2]); SB();
  xf[1][2] = *(LDS_AS const bf16x8*)(xr1 + 4096);
  SB();
  MV16(ak[3], wf[0][1], xf[0][3]); SB();
  xf[1][3] = *(LDS_AS const bf16x8*)(xr1 + 6144);
  SB();
  MV16(av[3], wf[0][2], xf[0][3]); SB();
  xf[1][4] = *(LDS_AS const bf16x8*)(xr1 + 8192);
  SB();
  MV16(aq[3], wf[0][0], xf[0][3]); SB();
  DMA2(vWa, vWb, sW, ldsU, 0, ""); sW += dW; OPQ(sW);
  SB();
  MV16(ak[4], wf[0][1], xf[0][4]); SB();
  DMA2(vWa, vWb, sW, ldsU, 2048, "");
  SB();
  MV16(av[4], wf[0][2], xf[0][4]); SB();
  MV16(aq[4], wf[0][0], xf[0][4]); SB();
  SB();
  MV16(ak[1], wf[1][1], xf[1][1]); SB();
  SB();
  MV16(av[1], wf[1][2], xf[1][1]); SB();
  WAITV(4);
  BAR();
  xr0 += -79872; wr0 += -79872; asm volatile("" : "+v"(xr0), "+v"(wr0));
  STAMP(62);
  SB();
  MV16(aq[1], wf[1][0], xf[1][1]); SB();
  wf[0][1] = *(LDS_AS const bf16x8*)(wr0 + 1024);
  SB();
  MV16(ak[2], wf[1][1], xf[1][2]); SB();
  xf[0][1] = *(LDS_AS const bf16x8*)(xr0 + 2048);
  SB();
  MV16(av[2], wf[1][2], xf[1][2]); SB();
  wf[0][2] = *(LDS_AS const bf16x8*)(wr0 + 2048);
  SB();
  MV16(aq[2], wf[1][0], xf[1][2]); SB();
  wf[0][0] = *(LDS_AS const bf16x8*)(wr0 + 0);
  SB();
  MV16(ak[3], wf[1][1], xf[1][3]); SB();
  xf[0][2] = *(LDS_AS const bf16x8*)(xr0 + 4096);
  SB();
  MV16(av[3], wf[1][2], xf[1][3]); SB();
  xf[0][3] = *(LDS_AS const bf16x8*)(xr0 + 6144);
  SB();
  MV16(aq[3], wf[1][0], xf[1][3]); SB();
  xf[0][4] = *(LDS_AS const bf16x8*)(xr0 + 8192);
  SB();
  MV16(ak[4], wf[1][1], xf[1][4]); SB();
  unsigned f0; { const int l_ = lane_now(); vFU = (l_ < 14 ? l_ : 13) * FU; PINV(vFU); }
  LD4(f0, vFU, sFU, 0, "sc1");
  SB();
  MV16(av[4], wf[1][2], xf[1][4]); SB();
  unsigned tl; { const int l_ = lane_now(); tl = lds0 + TB + l_ * 16; const u32x2 t_ = *(LDS_AS const u32x2*)(lds0 + TB + 6144 + cb * 512 + l_ * 8); sk0 = t_[0]; sv = t_[1];
    vS = *(LDS_AS const u32x2*)(lds0 + TB + 4096 + cb * 512 + l_ * 8); PINV(tl); }
  SB();
  MV16(aq[4], wf[1][0], xf[1][4]); SB();
  xr1 += -79872; PINV(xr1);
  MV16(ak[1], wf[0][1], xf[0][1]); SB();
  wf[1][1] = *(LDS_AS const bf16x8*)(wr0 + 4096);
  xf[1][1] = *(LDS_AS const bf16x8*)(xr1 + 2048);
  SB();
  MV16(av[1], wf[0][2], xf[0][1]); SB();
  wf[1][2] = *(LDS_AS const bf16x8*)(wr0 + 5120);
  SB();
  MV16(aq[1], wf[0][0], xf[0][1]); SB();
  wf[1][0] = *(LDS_AS const bf16x8*)(wr0 + 3072);
  SB();
  MV16(ak[2], wf[0][1], xf[0][2]); SB();
  xf[1][2] = *(LDS_AS const bf16x8*)(xr1 + 4096);
  xf[1][3] = *(LDS_AS const bf16x8*)(xr1 + 6144);
  SB();
  MV16(av[2], wf[0][2], xf[0][2]); SB();
  xf[1][4] = *(LDS_AS const bf16x8*)(xr1 + 8192);
  SB();
  MV16(aq[2], wf[0][0], xf[0][2]); SB();
  sk1 = sk0 ^ 32; asm volatile("" : "+v"(sk0), "+v"(sk1), "+v"(sv));
  SB();
  MV16(ak[1], wf[1][1], xf[1][1]); SB();
  SB();
  MV16(av[1], wf[1][2], xf[1][1]); SB();
  SB();
  MV16(aq[1], wf[1][0], xf[1][1]); SB();
  SB();
  MV16(ak[3], wf[0][1], xf[0][3]); SB();
  SB();
  MV16(av[3], wf[0][2], xf[0][3]); SB();
  SB();
  MV16(aq[3], wf[0][0], xf[0][3]); SB();
  SB();
  MV16(ak[2], wf[1][1], xf[1][2]); SB();
  t3 = *(LDS_AS const u32x4*)(tl + 3072); t2 = *(LDS_AS const u32x4*)(tl + 2048);
  SB();
  MV16(av[2], wf[1][2], xf[1][2]); SB();
  SB();
  MV16(aq[2], wf[1][0], xf[1][2]); SB();
  SB();
  MV16(ak[4], wf[0][1], xf[0][4]); SB();
  *(LDS_AS u32x2*)(sk1 + SK + 512) = (u32x2){pack2(ak[1][0], ak[1][1]), pack2(ak[1][2], ak[1][3])};
  SB();
  MV16(av[4], wf[0][2], xf[0][4]); SB();
  *(LDS_AS u32x2*)(sv + 256) = (u32x2){pack2(av[1][0], av[1][1]), pack2(av[1][2], av[1][3])};
  SB();
  MV16(aq[4], wf[0][0], xf[0][4]); SB();
  *(LDS_AS u32x2*)(sk0 + SQ + 512) = (u32x2){pack2(aq[1][0], aq[1][1]), pack2(aq[1][2], aq[1][3])};
  SB();
  MV16(ak[3], wf[1][1], xf[1][3]); SB();
  m1 = *(LDS_AS const u32x4*)(tl + 1024); cm = *(LDS_AS const f32x4*)(tl + cmo);
  SB();
  MV16(av[3], wf[1][2], xf[1][3]); SB();
  SB();
  MV16(aq[3], wf[1][0], xf[1][3]); SB();
  SB();
  MV16(ak[4], wf[1][1], xf[1][4]); SB();
  WAITV1(0, f0);
  if (__ballot(f0 != nonce) != 0ull) poll1<2>(sFU, vFU, nonce);
  STAMP(44);
  mq_ = lane_now() >= 48 ? 0u : ~0u; PINV(mq_); sFU += 14 * FU; OPQ(sFU);
  rk = (t3[2] ^ kx) + r0 * 64; rv = t3[3] + r0 * 32; asm volatile("" : "+v"(rk), "+v"(rv));
  SB();
  MV16(av[4], wf[1][2], xf[1][4]); SB();
  *(LDS_AS u32x2*)(sk0 + SK + 1024) = (u32x2){pack2(ak[2][0], ak[2][1]), pack2(ak[2][2], ak[2][3])};
  *(LDS_AS u32x2*)(sv + 512) = (u32x2){pack2(av[2][0], av[2][1]), pack2(av[2][2], av[2][3])};
  SB();
  MV16(aq[4], wf[1][0], xf[1][4]); SB();
  *(LDS_AS u32x2*)(sk1 + SQ + 1024) = (u32x2){pack2(aq[2][0], aq[2][1]), pack2(aq[2][2], aq[2][3])};
  SB();
  STAMP(36);
  *(LDS_AS u32x2*)(sk1 + SK + 1536) = (u32x2){pack2(ak[3][0], ak[3][1]), pack2(ak[3][2], ak[3][3])};
  *(LDS_AS u32x2*)(sv + 768) = (u32x2){pack2(av[3][0], av[3][1]), pack2(av[3][2], av[3][3])};
  *(LDS_AS u32x2*)(sk0 + SQ + 1536) = (u32x2){pack2(aq[3][0], aq[3][1]), pack2(aq[3][2], aq[3][3])};
  SB();
  *(LDS_AS u32x2*)(sk0 + SK + 2048) = (u32x2){pack2(ak[4][0], ak[4][1]), pack2(ak[4][2], ak[4][3])};
  *(LDS_AS u32x2*)(sv + 1024) = (u32x2){pack2(av[4][0], av[4][1]), pack2(av[4][2], av[4][3])};
  *(LDS_AS u32x2*)(sk1 + SQ + 2048) = (u32x2){pack2(aq[4][0], aq[4][1]), pack2(aq[4][2], aq[4][3])};
  SB();

  } else if (w == 3) {
  f32x4 aq[5], ak[5], av[5];
  bf16x8 xf[2][5], wf[2][5];
  unsigned sk0, sk1, sv;
  unsigned wk, wh, e0, e1, e2, hb[3]; { const int l_ = lane_now(); wk = wr0 + (l_ & 8) * 128; wh = lds0 + l_ * 16; asm volatile("" : "+v"(wk), "+v"(wh)); }
  const unsigned kz = t0 == 0 ? 0u : ~0u;
  WAITV(5);
  BAR();
  STAMP(35);
  xf[0][0] = *(LDS_AS const bf16x8*)(xr0 + 0);
  wf[0][2] = *(LDS_AS const bf16x8*)(wh + 1024);
  wf[0][3] = *(LDS_AS const bf16x8*)(wh + 7168);
  wf[0][4] = *(LDS_AS const bf16x8*)(wh + 13312);
  wf[0][1] = *(LDS_AS const bf16x8*)(wk + 1024);
  xf[0][1] = *(LDS_AS const bf16x8*)(xr0 + 2048);
  wf[0][0] = *(LDS_AS const bf16x8*)(wr0 + 0);
  xf[0][2] = *(LDS_AS const bf16x8*)(xr0 + 4096);
  xf[0][3] = *(LDS_AS const bf16x8*)(xr0 + 6144);
  xf[0][4] = *(LDS_AS const bf16x8*)(xr0 + 8192);
  SB();
  MV16Z(av[0], wf[0][2], xf[0][0]); SB();
  xf[1][0] = *(LDS_AS const bf16x8*)(xr1 + 0);
  wf[1][2] = *(LDS_AS const bf16x8*)(wh + 4096);
  SB();
  MV16Z(av[1], wf[0][3], xf[0][0]); SB();
  wf[1][3] = *(LDS_AS const bf16x8*)(wh + 10240);
  SB();
  MV16Z(av[2], wf[0][4], xf[0][0]); SB();
  wf[1][4] = *(LDS_AS const bf16x8*)(wh + 16384);
  SB();
  MV16Z(ak[0], wf[0][1], xf[0][0]); SB();
  wf[1][1] = *(LDS_AS const bf16x8*)(wk + 1536);
  SB();
  MV16Z(ak[1], wf[0][1], xf[0][1]); SB();
  xf[1][1] = *(LDS_AS const bf16x8*)(xr1 + 2048);
  wf[1][0] = *(LDS_AS const bf16x8*)(wr0 + 512);
  SB();
  MV16Z(aq[1], wf[0][0], xf[0][1]); SB();
  xf[1][2] = *(LDS_AS const bf16x8*)(xr1 + 4096);
  SB();
  MV16Z(ak[2], wf[0][1], xf[0][2]); SB();
  xf[1][3] = *(LDS_AS const bf16x8*)(xr1 + 6144);
  SB();
  MV16Z(aq[2], wf[0][0], xf[0][2]); SB();
  xf[1][4] = *(LDS_AS const bf16x8*)(xr1 + 8192);
  SB();
  MV16Z(ak[3], wf[0][1], xf[0][3]); SB();
  DMA1(vD[0], sDq, ldsD, 79872, "sc1");
  DMA1(vD[1], sDq, ldsD, 88064, "sc1"); sDq += WQ_S; OPQ(sDq);
  SB();
  MV16Z(aq[3], wf[0][0], xf[0][3]); SB();
  DMA1(vD[2], sDm, ldsD, 96256, "sc1"); sDm += dm; OPQ(sDm);
  SB();
  MV16Z(ak[4], wf[0][1], xf[0][4]); SB();
  DMA1(vD[3], sDx, ldsD, 104448, "sc1");
  SB();
  MV16Z(aq[4], wf[0][0], xf[0][4]); SB();
  DMA1(vD[4], sDx, ldsE, 79872, "sc1"); sDx += 128; OPQ(sDx);
  SB();
  SB();
  MV16(av[0], wf[1][2], xf[1][0]); SB();
  SB();
  MV16(av[1], wf[1][3], xf[1][0]); SB();
  WAITV(5);
  BAR();
  xr0 += 39936; wr0 += 39936; asm volatile("" : "+v"(xr0), "+v"(wr0));
  wk += 39936; wh += 39936; asm volatile("" : "+v"(wk), "+v"(wh));
  SB();
  MV16(av[2], wf[1][4], xf[1][0]); SB();
  xf[0][0] = *(LDS_AS const bf16x8*)(xr0 + 0);
  SB();
  MV16(ak[0], wf[1][1], xf[1][0]); SB();
  wf[0][2] = *(LDS_AS const bf16x8*)(wh + 1024);
  SB();
  MV16(ak[1], wf[1][1], xf[1][1]); SB();
  wf[0][3] = *(LDS_AS const bf16x8*)(wh + 7168);
  SB();
  MV16(aq[1], wf[1][0], xf[1][1]); SB();
  wf[0][4] = *(LDS_AS const bf16x8*)(wh + 13312);
  SB();
  MV16(ak[2], wf[1][1], xf[1][2]); SB();
  wf[0][1] = *(LDS_AS const bf16x8*)(wk + 1024);
  SB();
  MV16(aq[2], wf[1][0], xf[1][2]); SB();
  xf[0][1] = *(LDS_AS const bf16x8*)(xr0 + 2048);
  SB();
  MV16(ak[3], wf[1][1], xf[1][3]); SB();
  wf[0][0] = *(LDS_AS const bf16x8*)(wr0 + 0);
  SB();
  MV16(aq[3], wf[1][0], xf[1][3]); SB();
  xf[0][2] = *(LDS_AS const bf16x8*)(xr0 + 4096);
  SB();
  MV16(ak[4], wf[1][1], xf[1][4]); SB();
  xf[0][3] = *(LDS_AS const bf16x8*)(xr0 + 6144);
  SB();
  MV16(aq[4], wf[1][0], xf[1][4]); SB();
  xf[0][4] = *(LDS_AS const bf16x8*)(xr0 + 8192);
  SB();
  SB();
  MV16(av[0], wf[0][2], xf[0][0]); SB();
  xr1 += 39936; PINV(xr1);
  xf[1][0] = *(LDS_AS const bf16x8*)(xr1 + 0);
  SB();
  MV16(av[1], wf[0][3], xf[0][0]); SB();
  wf[1][2] = *(LDS_AS const bf16x8*)(wh + 4096);
  SB();
  MV16(av[2], wf[0][4], xf[0][0]); SB();
  wf[1][3] = *(LDS_AS const bf16x8*)(wh + 10240);
  SB();
  MV16(ak[0], wf[0][1], xf[0][0]); SB();
  wf[1][4] = *(LDS_AS const bf16x8*)(wh + 16384);
  wf[1][1] = *(LDS_AS const bf16x8*)(wk + 1536);
  SB();
  MV16(ak[1], wf[0][1], xf[0][1]); SB();
  xf[1][1] = *(LDS_AS const bf16x8*)(xr1 + 2048);
  SB();
  MV16(aq[1], wf[0][0], xf[0][1]); SB();
  wf[1][0] = *(LDS_AS const bf16x8*)(wr0 + 512);
  SB();
  MV16(ak[2], wf[0][1], xf[0][2]); SB();
  xf[1][2] = *(LDS_AS const bf16x8*)(xr1 + 4096);
  xf[1][3] = *(LDS_AS const bf16x8*)(xr1 + 6144);
  SB();
  MV16(aq[2], wf[0][0], xf[0][2]); SB();
  xf[1][4] = *(LDS_AS const bf16x8*)(xr1 + 8192);
  SB();
  MV16(ak[3], wf[0][1], xf[0][3]); SB();
  DMA1(vD[0], sDq, ldsD, 0, "sc1");
  SB();
  MV16(aq[3], wf[0][0], xf[0][3]); SB();
  DMA1(vD[1], sDq, ldsD, 8192, "sc1"); sDq += WQ_S; OPQ(sDq);
  DMA1(vD[2], sDm, ldsD, 16384, "sc1"); sDm += dm; OPQ(sDm);
  SB();
  MV16(ak[4], wf[0][1], xf[0][4]); SB();
  DMA1(vD[3], sDx, ldsD, 24576, "sc1");
  SB();
  MV16(aq[4], wf[0][0], xf[0][4]); SB();
  DMA1(vD[4], sDx, ldsE, 0, "sc1"); sDx += 128; OPQ(sDx);
  SB();
  SB();
  MV16(av[0], wf[1][2], xf[1][0]); SB();
  SB();
  MV16(av[1], wf[1][3], xf[1][0]); SB();
  WAITV(5);
  BAR();
  xr0 += 39936; wr0 += 39936; asm volatile("" : "+v"(xr0), "+v"(wr0));
  wk += 39936; wh += 39936; asm volatile("" : "+v"(wk), "+v"(wh));
  STAMP(60);
  SB();
  MV16(av[2], wf[1][4], xf[1][0]); SB();
  xf[0][0] = *(LDS_AS const bf16x8*)(xr0 + 0);
  SB();
  MV16(ak[0], wf[1][1], xf[1][0]); SB();
  wf[0][2] = *(LDS_AS const bf16x8*)(wh + 1024);
  SB();
  MV16(ak[1], wf[1][1], xf[1][1]); SB();
  wf[0][3] = *(LDS_AS const bf16x8*)(wh + 7168);
  SB();
  MV16(aq[1], wf[1][0], xf[1][1]); SB();
  wf[0][4] = *(LDS_AS const bf16x8*)(wh + 13312);
  SB();
  MV16(ak[2], wf[1][1], xf[1][2]); SB();
  wf[0][1] = *(LDS_AS const bf16x8*)(wk + 1024);
  SB();
  MV16(aq[2], wf[1][0], xf[1][2]); SB();
  xf[0][1] = *(LDS_AS const bf16x8*)(xr0 + 2048);
  SB();
  MV16(ak[3], wf[1][1], xf[1][3]); SB();
  wf[0][0] = *(LDS_AS const bf16x8*)(wr0 + 0);
  SB();
  MV16(aq[3], wf[1][0], xf[1][3]); SB();
  xf[0][2] = *(LDS_AS const bf16x8*)(xr0 + 4096);
  SB();
  MV16(ak[4], wf[1][1], xf[1][4]); SB();
  xf[0][3] = *(LDS_AS const bf16x8*)(xr0 + 6144);
  SB();
  MV16(aq[4], wf[1][0], xf[1][4]); SB();
  xf[0][4] = *(LDS_AS const bf16x8*)(xr0 + 8192);
  SB();
  SB();
  MV16(av[0], wf[0][2], xf[0][0]); SB();
  xr1 += 39936; PINV(xr1);
  xf[1][0] = *(LDS_AS const bf16x8*)(xr1 + 0);
  SB();
  MV16(av[1], wf[0][3], xf[0][0]); SB();
  wf[1][2] = *(LDS_AS const bf16x8*)(wh + 4096);
  SB();
  MV16(av[2], wf[0][4], xf[0][0]); SB();
  wf[1][3] = *(LDS_AS const bf16x8*)(wh + 10240);
  SB();
  MV16(ak[0], wf[0][1], xf[0][0]); SB();
  wf[1][4] = *(LDS_AS const bf16x8*)(wh + 16384);
  wf[1][1] = *(LDS_AS const bf16x8*)(wk + 1536);
  SB();
  MV16(ak[1], wf[0][1], xf[0][1]); SB();
  xf[1][1] = *(LDS_AS const bf16x8*)(xr1 + 2048);
  SB();
  MV16(aq[1], wf[0][0], xf[0][1]); SB();
  wf[1][0] = *(LDS_AS const bf16x8*)(wr0 + 512);
  SB();
  MV16(ak[2], wf[0][1], xf[0][2]); SB();
  xf[1][2] = *(LDS_AS const bf16x8*)(xr1 + 4096);
  xf[1][3] = *(LDS_AS const bf16x8*)(xr1 + 6144);
  SB();
  MV16(aq[2], wf[0][0], xf[0][2]); SB();
  xf[1][4] = *(LDS_AS const bf16x8*)(xr1 + 8192);
  SB();
  MV16(ak[3], wf[0][1], xf[0][3]); SB();
  DMA1(vD[0], sDq, ldsD, 39936, "sc1");
  SB();
  MV16(aq[3], wf[0][0], xf[0][3]); SB();
  DMA1(vD[1], sDq, ldsD, 48128, "sc1"); sDq += WQ_S; OPQ(sDq);
  DMA1(vD[2], sDm, ldsD, 56320, "sc1"); sDm += dm; OPQ(sDm);
  SB();
  MV16(ak[4], wf[0][1], xf[0][4]); SB();
  DMA1(vD[3], sDx, ldsD, 64512, "sc1");
  SB();
  MV16(aq[4], wf[0][0], xf[0][4]); SB();
  DMA1(vD[4], sDx, ldsE, 39936, "sc1"); sDx += 128; OPQ(sDx);
  SB();
  SB();
  MV16(av[0], wf[1][2], xf[1][0]); SB();
  SB();
  MV16(av[1], wf[1][3], xf[1][0]); SB();
  WAITV(5);
  BAR();
  xr0 += -79872; wr0 += -79872; asm volatile("" : "+v"(xr0), "+v"(wr0));
  wk += -79872; wh += -79872; asm volatile("" : "+v"(wk), "+v"(wh));
  SB();
  MV16(av[2], wf[1][4], xf[1][0]); SB();
  xf[0][0] = *(LDS_AS const bf16x8*)(xr0 + 0);
  SB();
  MV16(ak[0], wf[1][1], xf[1][0]); SB();
  wf[0][2] = *(LDS_AS const bf16x8*)(wh + 1024);
  SB();
  MV16(ak[1], wf[1][1], xf[1][1]); SB();
  wf[0][3] = *(LDS_AS const bf16x8*)(wh + 7168);
  SB();
  MV16(aq[1], wf[1][0], xf[1][1]); SB();
  wf[0][4] = *(LDS_AS const bf16x8*)(wh + 13312);
  SB();
  MV16(ak[2], wf[1][1], xf[1][2]); SB();
  wf[0][1] = *(LDS_AS const bf16x8*)(wk + 1024);
  SB();
  MV16(aq[2], wf[1][0], xf[1][2]); SB();
  xf[0][1] = *(LDS_AS const bf16x8*)(xr0 + 2048);
  SB();
  MV16(ak[3], wf[1][1], xf[1][3]); SB();
  wf[0][0] = *(LDS_AS const bf16x8*)(wr0 + 0);
  SB();
  MV16(aq[3], wf[1][0], xf[1][3]); SB();
  xf[0][2] = *(LDS_AS const bf16x8*)(xr0 + 4096);
  SB();
  MV16(ak[4], wf[1][1], xf[1][4]); SB();
  xf[0][3] = *(LDS_AS const bf16x8*)(xr0 + 6144);
  SB();
  MV16(aq[4], wf[1][0], xf[1][4]); SB();
  xf[0][4] = *(LDS_AS const bf16x8*)(xr0 + 8192);
  SB();
  SB();
  MV16(av[0], wf[0][2], xf[0][0]); SB();
  xr1 += -79872; PINV(xr1);
  xf[1][0] = *(LDS_AS const bf16x8*)(xr1 + 0);
  SB();
  MV16(av[1], wf[0][3], xf[0][0]); SB();
  wf[1][2] = *(LDS_AS const bf16x8*)(wh + 4096);
  SB();
  MV16(av[2], wf[0][4], xf[0][0]); SB();
  wf[1][3] = *(LDS_AS const bf16x8*)(wh + 10240);
  SB();
  MV16(ak[0], wf[0][1], xf[0][0]); SB();
  wf[1][4] = *(LDS_AS const bf16x8*)(wh + 16384);
  wf[1][1] = *(LDS_AS const bf16x8*)(wk + 1536);
  SB();
  MV16(ak[1], wf[0][1], xf[0][1]); SB();
  xf[1][1] = *(LDS_AS const bf16x8*)(xr1 + 2048);
  SB();
  MV16(aq[1], wf[0][0], xf[0][1]); SB();
  wf[1][0] = *(LDS_AS const bf16x8*)(wr0 + 512);
  SB();
  MV16(ak[2], wf[0][1], xf[0][2]); SB();
  xf[1][2] = *(LDS_AS const bf16x8*)(xr1 + 4096);
  xf[1][3] = *(LDS_AS const bf16x8*)(xr1 + 6144);
  SB();
  MV16(aq[2], wf[0][0], xf[0][2]); SB();
  xf[1][4] = *(LDS_AS const bf16x8*)(xr1 + 8192);
  SB();
  MV16(ak[3], wf[0][1], xf[0][3]); SB();
  DMA1(vD[0], sDq, ldsD, 79872, "sc1");
  SB();
  MV16(aq[3], wf[0][0], xf[0][3]); SB();
  DMA1(vD[1], sDq, ldsD, 88064, "sc1"); sDq += WQ_S; OPQ(sDq);
  DMA1(vD[2], sDm, ldsD, 96256, "sc1"); sDm += dm; OPQ(sDm);
  SB();
  MV16(ak[4], wf[0][1], xf[0][4]); SB();
  DMA1(vD[3], sDx, ldsD, 104448, "sc1");
  SB();
  MV16(aq[4], wf[0][0], xf[0][4]); SB();
  DMA1(vD[4], sDx, ldsE, 79872, "sc1"); sDx += 128; OPQ(sDx);
  SB();
  SB();
  MV16(av[0], wf[1][2], xf[1][0]); SB();
  SB();
  MV16(av[1], wf[1][3], xf[1][0]); SB();
  WAITV(5);
  BAR();
  xr0 += 39936; wr0 += 39936; asm volatile("" : "+v"(xr0), "+v"(wr0));
  wk += 39936; wh += 39936; asm volatile("" : "+v"(wk), "+v"(wh));
  STAMP(61);
  SB();
  MV16(av[2], wf[1][4], xf[1][0]); SB();
  xf[0][0] = *(LDS_AS const bf16x8*)(xr0 + 0);
  SB();
  MV16(ak[0], wf[1][1], xf[1][0]); SB();
  wf[0][2] = *(LDS_AS const bf16x8*)(wh + 1024);
  SB();
  MV16(ak[1], wf[1][1], xf[1][1]); SB();
  wf[0][3] = *(LDS_AS const bf16x8*)(wh + 7168);
  SB();
  MV16(aq[1], wf[1][0], xf[1][1]); SB();
  wf[0][4] = *(LDS_AS const bf16x8*)(wh + 13312);
  SB();
  MV16(ak[2], wf[1][1], xf[1][2]); SB();
  wf[0][1] = *(LDS_AS const bf16x8*)(wk + 1024);
  SB();
  MV16(aq[2], wf[1][0], xf[1][2]); SB();
  xf[0][1] = *(LDS_AS const bf16x8*)(xr0 + 2048);
  SB();
  MV16(ak[3], wf[1][1], xf[1][3]); SB();
  wf[0][0] = *(LDS_AS const bf16x8*)(wr0 + 0);
  SB();
  MV16(aq[3], wf[1][0], xf[1][3]); SB();
  xf[0][2] = *(LDS_AS const bf16x8*)(xr0 + 4096);
  SB();
  MV16(ak[4], wf[1][1], xf[1][4]); SB();
  xf[0][3] = *(LDS_AS const bf16x8*)(xr0 + 6144);
  SB();
  MV16(aq[4], wf[1][0], xf[1][4]); SB();
  xf[0][4] = *(LDS_AS const bf16x8*)(xr0 + 8192);
  SB();
  SB();
  MV16(av[0], wf[0][2], xf[0][0]); SB();
  xr1 += 39936; PINV(xr1);
  xf[1][0] = *(LDS_AS const bf16x8*)(xr1 + 0);
  SB();
  MV16(av[1], wf[0][3], xf[0][0]); SB();
  wf[1][2] = *(LDS_AS const bf16x8*)(wh + 4096);
  SB();
  MV16(av[2], wf[0][4], xf[0][0]); SB();
  wf[1][3] = *(LDS_AS const bf16x8*)(wh + 10240);
  SB();
  MV16(ak[0], wf[0][1], xf[0][0]); SB();
  wf[1][4] = *(LDS_AS const bf16x8*)(wh + 16384);
  wf[1][1] = *(LDS_AS const bf16x8*)(wk + 1536);
  SB();
  MV16(ak[1], wf[0][1], xf[0][1]); SB();
  xf[1][1] = *(LDS_AS const bf16x8*)(xr1 + 2048);
  SB();
  MV16(aq[1], wf[0][0], xf[0][1]); SB();
  wf[1][0] = *(LDS_AS const bf16x8*)(wr0 + 512);
  SB();
  MV16(ak[2], wf[0][1], xf[0][2]); SB();
  xf[1][2] = *(LDS_AS const bf16x8*)(xr1 + 4096);
  xf[1][3] = *(LDS_AS const bf16x8*)(xr1 + 6144);
  SB();
  MV16(aq[2], wf[0][0], xf[0][2]); SB();
  xf[1][4] = *(LDS_AS const bf16x8*)(xr1 + 8192);
  SB();
  MV16(ak[3], wf[0][1], xf[0][3]); SB();
  DMA1(vD[0], sDq, ldsD, 0, "sc1");
  SB();
  MV16(aq[3], wf[0][0], xf[0][3]); SB();
  DMA1(vD[1], sDq, ldsD, 8192, "sc1"); sDq += WQ_S; OPQ(sDq);
  DMA1(vD[2], sDm, ldsD, 16384, "sc1"); sDm += dm; OPQ(sDm);
  SB();
  MV16(ak[4], wf[0][1], xf[0][4]); SB();
  DMA1(vD[3], sDx, ldsD, 24576, "sc1");
  SB();
  MV16(aq[4], wf[0][0], xf[0][4]); SB();
  DMA1(vD[4], sDx, ldsE, 0, "sc1"); sDx += 128; OPQ(sDx);
  SB();
  SB();
  MV16(av[0], wf[1][2], xf[1][0]); SB();
  SB();
  MV16(av[1], wf[1][3], xf[1][0]); SB();
  WAITV(5);
  BAR();
  xr0 += 39936; wr0 += 39936; asm volatile("" : "+v"(xr0), "+v"(wr0));
  wk += 39936; wh += 39936; asm volatile("" : "+v"(wk), "+v"(wh));
  SB();
  MV16(av[2], wf[1][4], xf[1][0]); SB();
  xf[0][0] = *(LDS_AS const bf16x8*)(xr0 + 0);
  SB();
  MV16(ak[0], wf[1][1], xf[1][0]); SB();
  wf[0][2] = *(LDS_AS const bf16x8*)(wh + 1024);
  SB();
  MV16(ak[1], wf[1][1], xf[1][1]); SB();
  wf[0][3] = *(LDS_AS const bf16x8*)(wh + 7168);
  SB();
  MV16(aq[1], wf[1][0], xf[1][1]); SB();
  wf[0][4] = *(LDS_AS const bf16x8*)(wh + 13312);
  SB();
  MV16(ak[2], wf[1][1], xf[1][2]); SB();
  wf[0][1] = *(LDS_AS const bf16x8*)(wk + 1024);
  SB();
  MV16(aq[2], wf[1][0], xf[1][2]); SB();
  xf[0][1] = *(LDS_AS const bf16x8*)(xr0 + 2048);
  SB();
  MV16(ak[3], wf[1][1], xf[1][3]); SB();
  wf[0][0] = *(LDS_AS const bf16x8*)(wr0 + 0);
  SB();
  MV16(aq[3], wf[1][0], xf[1][3]); SB();
  xf[0][2] = *(LDS_AS const bf16x8*)(xr0 + 4096);
  SB();
  MV16(ak[4], wf[1][1], xf[1][4]); SB();
  xf[0][3] = *(LDS_AS const bf16x8*)(xr0 + 6144);
  SB();
  MV16(aq[4], wf[1][0], xf[1][4]); SB();
  xf[0][4] = *(LDS_AS const bf16x8*)(xr0 + 8192);
  SB();
  SB();
  MV16(av[0], wf[0][2], xf[0][0]); SB();
  xr1 += 39936; PINV(xr1);
  xf[1][0] = *(LDS_AS const bf16x8*)(xr1 + 0);
  SB();
  MV16(av[1], wf[0][3], xf[0][0]); SB();
  wf[1][2] = *(LDS_AS const bf16x8*)(wh + 4096);
  SB();
  MV16(av[2], wf[0][4], xf[0][0]); SB();
  wf[1][3] = *(LDS_AS const bf16x8*)(wh + 10240);
  SB();
  MV16(ak[0], wf[0][1], xf[0][0]); SB();
  wf[1][4] = *(LDS_AS const bf16x8*)(wh + 16384);
  SB();
  MV16(ak[1], wf[0][1], xf[0][1]); SB();
  wf[1][1] = *(LDS_AS const bf16x8*)(wk + 1536);
  SB();
  MV16(aq[1], wf[0][0], xf[0][1]); SB();
  xf[1][1] = *(LDS_AS const bf16x8*)(xr1 + 2048);
  SB();
  MV16(ak[2], wf[0][1], xf[0][2]); SB();
  wf[1][0] = *(LDS_AS const bf16x8*)(wr0 + 512);
  SB();
  MV16(aq[2], wf[0][0], xf[0][2]); SB();
  xf[1][2] = *(LDS_AS const bf16x8*)(xr1 + 4096);
  SB();
  MV16(ak[3], wf[0][1], xf[0][3]); SB();
  xf[1][3] = *(LDS_AS const bf16x8*)(xr1 + 6144);
  SB();
  MV16(aq[3], wf[0][0], xf[0][3]); SB();
  xf[1][4] = *(LDS_AS const bf16x8*)(xr1 + 8192);
  SB();
  MV16(ak[4], wf[0][1], xf[0][4]); SB();
  DMA2(vWa, vWb, sW, ldsU, 0, ""); sW += dW; OPQ(sW);
  SB();
  MV16(aq[4], wf[0][0], xf[0][4]); SB();
  DMA2(vWa, vWb, sW, ldsU, 2048, "");
  SB();
  SB();
  MV16(av[0], wf[1][2], xf[1][0]); SB();
  WAITV(4);
  BAR();
  xr0 += -79872; wr0 += -79872; asm volatile("" : "+v"(xr0), "+v"(wr0));
  wk += -79872; wh += -79872; asm volatile("" : "+v"(wk), "+v"(wh));
  STAMP(62);
  SB();
  MV16(av[1], wf[1][3], xf[1][0]); SB();
  xf[0][0] = *(LDS_AS const bf16x8*)(xr0 + 0);
  SB();
  MV16(av[2], wf[1][4], xf[1][0]); SB();
  wf[0][2] = *(LDS_AS const bf16x8*)(wh + 1024);
  SB();
  MV16(ak[0], wf[1][1], xf[1][0]); SB();
  wf[0][3] = *(LDS_AS const bf16x8*)(wh + 7168);
  SB();
  MV16(ak[1], wf[1][1], xf[1][1]); SB();
  wf[0][4] = *(LDS_AS const bf16x8*)(wh + 13312);
  wf[0][1] = *(LDS_AS const bf16x8*)(wk + 1024);
  SB();
  MV16(aq[1], wf[1][0], xf[1][1]); SB();
  xf[0][1] = *(LDS_AS const bf16x8*)(xr0 + 2048);
  SB();
  MV16(ak[2], wf[1][1], xf[1][2]); SB();
  wf[0][0] = *(LDS_AS const bf16x8*)(wr0 + 0);
  SB();
  MV16(aq[2], wf[1][0], xf[1][2]); SB();
  xf[0][2] = *(LDS_AS const bf16x8*)(xr0 + 4096);
  SB();
  MV16(ak[3], wf[1][1], xf[1][3]); SB();
  xf[0][3] = *(LDS_AS const bf16x8*)(xr0 + 6144);
  xf[0][4] = *(LDS_AS const bf16x8*)(xr0 + 8192);
  SB();
  MV16(aq[3], wf[1][0], xf[1][3]); SB();
  unsigned f0; { const int l_ = lane_now(); vFU = (l_ < 14 ? l_ : 13) * FU; PINV(vFU); }
  LD4(f0, vFU, sFU, 0, "sc1");
  SB();
  MV16(ak[4], wf[1][1], xf[1][4]); SB();
  unsigned tl; { const int l_ = lane_now(); tl = lds0 + TB + l_ * 16; const u32x2 t_ = *(LDS_AS const u32x2*)(lds0 + TB + 6144 + cb * 512 + l_ * 8); sk0 = t_[0]; sv = t_[1];
    vS = *(LDS_AS const u32x2*)(lds0 + TB + 4096 + cb * 512 + l_ * 8); PINV(tl); }
  SB();
  MV16(aq[4], wf[1][0], xf[1][4]); SB();
  { const int l_ = lane_now(); const unsigned a_ = lds0 + TB + 6144 + l_ * 8; sv = *(LDS_AS const unsigned*)(lds0 + TB + 7684 + (l_ & 31) * 8);
    hb[0] = *(LDS_AS const unsigned*)(a_ + 0); hb[1] = *(LDS_AS const unsigned*)(a_ + 512); hb[2] = *(LDS_AS const unsigned*)(a_ + 1024); }
  SB();
  xr1 += -79872; PINV(xr1);
  MV16(av[0], wf[0][2], xf[0][0]); SB();
  xf[1][0] = *(LDS_AS const bf16x8*)(xr1 + 0);
  wf[1][2] = *(LDS_AS const bf16x8*)(wh + 4096);
  SB();
  MV16(av[1], wf[0][3], xf[0][0]); SB();
  wf[1][3] = *(LDS_AS const bf16x8*)(wh + 10240);
  wf[1][4] = *(LDS_AS const bf16x8*)(wh + 16384);
  SB();
  MV16(av[2], wf[0][4], xf[0][0]); SB();
  wf[1][1] = *(LDS_AS const bf16x8*)(wk + 1536);
  xf[1][1] = *(LDS_AS const bf16x8*)(xr1 + 2048);
  SB();
  MV16(ak[0], wf[0][1], xf[0][0]); SB();
  wf[1][0] = *(LDS_AS const bf16x8*)(wr0 + 512);
  xf[1][2] = *(LDS_AS const bf16x8*)(xr1 + 4096);
  SB();
  MV16(ak[1], wf[0][1], xf[0][1]); SB();
  xf[1][3] = *(LDS_AS const bf16x8*)(xr1 + 6144);
  xf[1][4] = *(LDS_AS const bf16x8*)(xr1 + 8192);
  SB();
  MV16(aq[1], wf[0][0], xf[0][1]); SB();
  sk1 = sk0 ^ 32; asm volatile("" : "+v"(sk0), "+v"(sk1), "+v"(sv));
  const bool lo_ = lane_now() < 32;
  e0 = lo_ ? sk0 + SK : sv; e1 = lo_ ? sk1 + SK + 512 : sv + 256; e2 = lo_ ? 1024 : 512; asm volatile("" : "+v"(e0), "+v"(e1), "+v"(e2));
  const unsigned kl = lo_ ? kz : ~0u;
  SB();
  MV16(av[0], wf[1][2], xf[1][0]); SB();
  SB();
  MV16(av[1], wf[1][3], xf[1][0]); SB();
  SB();
  MV16(av[2], wf[1][4], xf[1][0]); SB();
  SB();
  MV16(ak[0], wf[1][1], xf[1][0]); SB();
  SB();
  MV16(ak[2], wf[0][1], xf[0][2]); SB();
  SB();
  MV16(aq[2], wf[0][0], xf[0][2]); SB();
  SB();
  MV16(ak[1], wf[1][1], xf[1][1]); SB();
  SB();
  MV16(aq[1], wf[1][0], xf[1][1]); SB();
  SB();
  MV16(ak[3], wf[0][1], xf[0][3]); SB();
  SB();
  MV16(aq[3], wf[0][0], xf[0][3]); SB();
  SB();
  MV16(ak[2], wf[1][1], xf[1][2]); SB();
  t3 = *(LDS_AS const u32x4*)(tl + 3072); t2 = *(LDS_AS const u32x4*)(tl + 2048);
  *(LDS_AS u32x2*)(hb[0] + SK) = (u32x2){pack2(av[0][0], av[0][1]) & kz, pack2(av[0][2], av[0][3]) & kz};
  SB();
  MV16(aq[2], wf[1][0], xf[1][2]); SB();
  *(LDS_AS u32x2*)(hb[1] + SK) = (u32x2){pack2(av[1][0], av[1][1]) & kz, pack2(av[1][2], av[1][3]) & kz};
  SB();
  MV16(ak[4], wf[0][1], xf[0][4]); SB();
  *(LDS_AS u32x2*)(hb[2] + SK) = (u32x2){pack2(av[2][0], av[2][1]) & kz, pack2(av[2][2], av[2][3]) & kz};
  SB();
  MV16(aq[4], wf[0][0], xf[0][4]); SB();
  *(LDS_AS u32x2*)(e0) = (u32x2){pack2(ak[0][0], ak[0][1]) & kl, pack2(ak[0][2], ak[0][3]) & kl};
  SB();
  MV16(ak[3], wf[1][1], xf[1][3]); SB();
  WAITV1(0, f0);
  if (__ballot(f0 != nonce) != 0ull) poll1<2>(sFU, vFU, nonce);
  STAMP(44);
  mq_ = lane_now() >= 48 ? 0u : ~0u; PINV(mq_); sFU += 14 * FU; OPQ(sFU);
  SB();
  MV16(aq[3], wf[1][0], xf[1][3]); SB();
  m1 = *(LDS_AS const u32x4*)(tl + 1024); cm = *(LDS_AS const f32x4*)(tl + cmo);
  SB();
  MV16(ak[4], wf[1][1], xf[1][4]); SB();
  rk = (t3[2] ^ kx) + r0 * 64; rv = t3[3] + r0 * 32; asm volatile("" : "+v"(rk), "+v"(rv));
  *(LDS_AS u32x2*)(e1 + 0 * e2) = (u32x2){pack2(ak[1][0], ak[1][1]), pack2(ak[1][2], ak[1][3])};
  SB();
  MV16(aq[4], wf[1][0], xf[1][4]); SB();
  *(LDS_AS u32x2*)(sk0 + SQ + 512) = (u32x2){pack2(aq[1][0], aq[1][1]), pack2(aq[1][2], aq[1][3])};
  SB();
  STAMP(36);
  *(LDS_AS u32x2*)(e0 + 1 * e2) = (u32x2){pack2(ak[2][0], ak[2][1]), pack2(ak[2][2], ak[2][3])};
  *(LDS_AS u32x2*)(sk1 + SQ + 1024) = (u32x2){pack2(aq[2][0], aq[2][1]), pack2(aq[2][2], aq[2][3])};
  SB();
  *(LDS_AS u32x2*)(e1 + 1 * e2) = (u32x2){pack2(ak[3][0], ak[3][1]), pack2(ak[3][2], ak[3][3])};
  *(LDS_AS u32x2*)(sk0 + SQ + 1536) = (u32x2){pack2(aq[3][0], aq[3][1]), pack2(aq[3][2], aq[3][3])};
  SB();
  *(LDS_AS u32x2*)(e0 + 2 * e2) = (u32x2){pack2(ak[4][0], ak[4][1]), pack2(ak[4][2], ak[4][3])};
  *(LDS_AS u32x2*)(sk1 + SQ + 2048) = (u32x2){pack2(aq[4][0], aq[4][1]), pack2(aq[4][2], aq[4][3])};
  SB();

  } else if (w < 7) {
  f32x4 aq[5], ak[5], av[5];
  bf16x8 xf[2][5], wf[2][5];
  unsigned sk0, sk1, sv;
  WAITV(5);
  BAR();
  STAMP(35);
  wf[0][1] = *(LDS_AS const bf16x8*)(wr0 + 1024);
  xf[0][1] = *(LDS_AS const bf16x8*)(xr0 + 10240);
  wf[0][2] = *(LDS_AS const bf16x8*)(wr0 + 2048);
  wf[0][0] = *(LDS_AS const bf16x8*)(wr0 + 0);
  xf[0][2] = *(LDS_AS const bf16x8*)(xr0 + 12288);
  xf[0][3] = *(LDS_AS const bf16x8*)(xr0 + 14336);
  xf[0][4] = *(LDS_AS const bf16x8*)(xr0 + 16384);
  SB();
  MV16Z(ak[1], wf[0][1], xf[0][1]); SB();
  wf[1][1] = *(LDS_AS const bf16x8*)(wr0 + 4096);
  SB();
  MV16Z(av[1], wf[0][2], xf[0][1]); SB();
  xf[1][1] = *(LDS_AS const bf16x8*)(xr1 + 10240);
  SB();
  MV16Z(aq[1], wf[0][0], xf[0][1]); SB();
  wf[1][2] = *(LDS_AS const bf16x8*)(wr0 + 5120);
  SB();
  MV16Z(ak[2], wf[0][1], xf[0][2]); SB();
  wf[1][0] = *(LDS_AS const bf16x8*)(wr0 + 3072);
  SB();
  MV16Z(av[2], wf[0][2], xf[0][2]); SB();
  xf[1][2] = *(LDS_AS const bf16x8*)(xr1 + 12288);
  SB();
  MV16Z(aq[2], wf[0][0], xf[0][2]); SB();
  xf[1][3] = *(LDS_AS const bf16x8*)(xr1 + 14336);
  SB();
  MV16Z(ak[3], wf[0][1], xf[0][3]); SB();
  xf[1][4] = *(LDS_AS const bf16x8*)(xr1 + 16384);
  SB();
  MV16Z(av[3], wf[0][2], xf[0][3]); SB();
  DMA1(vD[0], sDq, ldsD, 79872, "sc1");
  SB();
  MV16Z(aq[3], wf[0][0], xf[0][3]); SB();
  DMA1(vD[1], sDq, ldsD, 88064, "sc1"); sDq += WQ_S; OPQ(sDq);
  SB();
  MV16Z(ak[4], wf[0][1], xf[0][4]); SB();
  DMA1(vD[2], sDm, ldsD, 96256, "sc1"); sDm += dm; OPQ(sDm);
  SB();
  MV16Z(av[4], wf[0][2], xf[0][4]); SB();
  DMA1(vD[3], sDx, ldsD, 104448, "sc1");
  SB();
  MV16Z(aq[4], wf[0][0], xf[0][4]); SB();
  DMA1(vD[4], sDx, ldsE, 79872, "sc1"); sDx += 128; OPQ(sDx);
  SB();
  SB();
  MV16(ak[1], wf[1][1], xf[1][1]); SB();
  SB();
  MV16(av[1], wf[1][2], xf[1][1]); SB();
  WAITV(5);
  BAR();
  xr0 += 39936; wr0 += 39936; asm volatile("" : "+v"(xr0), "+v"(wr0));
  DMA1(vD[0], sDq, ldsD, 0, "sc1");
  DMA1(vD[1], sDq, ldsD, 8192, "sc1"); sDq += WQ_S; OPQ(sDq);
  DMA1(vD[2], sDm, ldsD, 16384, "sc1"); sDm += dm; OPQ(sDm);
  DMA1(vD[3], sDx, ldsD, 24576, "sc1");
  DMA1(vD[4], sDx, ldsE, 0, "sc1"); sDx += 128; OPQ(sDx);
  SB();
  MV16(aq[1], wf[1][0], xf[1][1]); SB();
  wf[0][1] = *(LDS_AS const bf16x8*)(wr0 + 1024);
  SB();
  MV16(ak[2], wf[1][1], xf[1][2]); SB();
  xf[0][1] = *(LDS_AS const bf16x8*)(xr0 + 10240);
  SB();
  MV16(av[2], wf[1][2], xf[1][2]); SB();
  wf[0][2] = *(LDS_AS const bf16x8*)(wr0 + 2048);
  SB();
  MV16(aq[2], wf[1][0], xf[1][2]); SB();
  wf[0][0] = *(LDS_AS const bf16x8*)(wr0 + 0);
  SB();
  MV16(ak[3], wf[1][1], xf[1][3]); SB();
  xf[0][2] = *(LDS_AS const bf16x8*)(xr0 + 12288);
  SB();
  MV16(av[3], wf[1][2], xf[1][3]); SB();
  xf[0][3] = *(LDS_AS const bf16x8*)(xr0 + 14336);
  SB();
  MV16(aq[3], wf[1][0], xf[1][3]); SB();
  xf[0][4] = *(LDS_AS const bf16x8*)(xr0 + 16384);
  SB();
  MV16(ak[4], wf[1][1], xf[1][4]); SB();
  MV16(av[4], wf[1][2], xf[1][4]); SB();
  MV16(aq[4], wf[1][0], xf[1][4]); SB();
  SB();
  MV16(ak[1], wf[0][1], xf[0][1]); SB();
  xr1 += 39936; PINV(xr1);
  SB();
  MV16(av[1], wf[0][2], xf[0][1]); SB();
  wf[1][1] = *(LDS_AS const bf16x8*)(wr0 + 4096);
  SB();
  MV16(aq[1], wf[0][0], xf[0][1]); SB();
  xf[1][1] = *(LDS_AS const bf16x8*)(xr1 + 10240);
  SB();
  MV16(ak[2], wf[0][1], xf[0][2]); SB();
  wf[1][2] = *(LDS_AS const bf16x8*)(wr0 + 5120);
  SB();
  MV16(av[2], wf[0][2], xf[0][2]); SB();
  wf[1][0] = *(LDS_AS const bf16x8*)(wr0 + 3072);
  SB();
  MV16(aq[2], wf[0][0], xf[0][2]); SB();
  xf[1][2] = *(LDS_AS const bf16x8*)(xr1 + 12288);
  SB();
  MV16(ak[3], wf[0][1], xf[0][3]); SB();
  xf[1][3] = *(LDS_AS const bf16x8*)(xr1 + 14336);
  SB();
  MV16(av[3], wf[0][2], xf[0][3]); SB();
  xf[1][4] = *(LDS_AS const bf16x8*)(xr1 + 16384);
  SB();
  MV16(aq[3], wf[0][0], xf[0][3]); SB();
  MV16(ak[4], wf[0][1], xf[0][4]); SB();
  MV16(av[4], wf[0][2], xf[0][4]); SB();
  MV16(aq[4], wf[0][0], xf[0][4]); SB();
  SB();
  MV16(ak[1], wf[1][1], xf[1][1]); SB();
  SB();
  MV16(av[1], wf[1][2], xf[1][1]); SB();
  WAITV(5);
  BAR();
  xr0 += 39936; wr0 += 39936; asm volatile("" : "+v"(xr0), "+v"(wr0));
  STAMP(60);
  DMA1(vD[0], sDq, ldsD, 39936, "sc1");
  DMA1(vD[1], sDq, ldsD, 48128, "sc1"); sDq += WQ_S; OPQ(sDq);
  DMA1(vD[2], sDm, ldsD, 56320, "sc1"); sDm += dm; OPQ(sDm);
  DMA1(vD[3], sDx, ldsD, 64512, "sc1");
  DMA1(vD[4], sDx, ldsE, 39936, "sc1"); sDx += 128; OPQ(sDx);
  SB();
  MV16(aq[1], wf[1][0], xf[1][1]); SB();
  wf[0][1] = *(LDS_AS const bf16x8*)(wr0 + 1024);
  SB();
  MV16(ak[2], wf[1][1], xf[1][2]); SB();
  xf[0][1] = *(LDS_AS const bf16x8*)(xr0 + 10240);
  SB();
  MV16(av[2], wf[1][2], xf[1][2]); SB();
  wf[0][2] = *(LDS_AS const bf16x8*)(wr0 + 2048);
  SB();
  MV16(aq[2], wf[1][0], xf[1][2]); SB();
  wf[0][0] = *(LDS_AS const bf16x8*)(wr0 + 0);
  SB();
  MV16(ak[3], wf[1][1], xf[1][3]); SB();
  xf[0][2] = *(LDS_AS const bf16x8*)(xr0 + 12288);
  SB();
  MV16(av[3], wf[1][2], xf[1][3]); SB();
  xf[0][3] = *(LDS_AS const bf16x8*)(xr0 + 14336);
  SB();
  MV16(aq[3], wf[1][0], xf[1][3]); SB();
  xf[0][4] = *(LDS_AS const bf16x8*)(xr0 + 16384);
  SB();
  MV16(ak[4], wf[1][1], xf[1][4]); SB();
  MV16(av[4], wf[1][2], xf[1][4]); SB();
  MV16(aq[4], wf[1][0], xf[1][4]); SB();
  SB();
  MV16(ak[1], wf[0][1], xf[0][1]); SB();
  xr1 += 39936; PINV(xr1);
  SB();
  MV16(av[1], wf[0][2], xf[0][1]); SB();
  wf[1][1] = *(LDS_AS const bf16x8*)(wr0 + 4096);
  SB();
  MV16(aq[1], wf[0][0], xf[0][1]); SB();
  xf[1][1] = *(LDS_AS const bf16x8*)(xr1 + 10240);
  SB();
  MV16(ak[2], wf[0][1], xf[0][2]); SB();
  wf[1][2] = *(LDS_AS const bf16x8*)(wr0 + 5120);
  SB();
  MV16(av[2], wf[0][2], xf[0][2]); SB();
  wf[1][0] = *(LDS_AS const bf16x8*)(wr0 + 3072);
  SB();
  MV16(aq[2], wf[0][0], xf[0][2]); SB();
  xf[1][2] = *(LDS_AS const bf16x8*)(xr1 + 12288);
  SB();
  MV16(ak[3], wf[0][1], xf[0][3]); SB();
  xf[1][3] = *(LDS_AS const bf16x8*)(xr1 + 14336);
  SB();
  MV16(av[3], wf[0][2], xf[0][3]); SB();
  xf[1][4] = *(LDS_AS const bf16x8*)(xr1 + 16384);
  SB();
  MV16(aq[3], wf[0][0], xf[0][3]); SB();
  MV16(ak[4], wf[0][1], xf[0][4]); SB();
  MV16(av[4], wf[0][2], xf[0][4]); SB();
  MV16(aq[4], wf[0][0], xf[0][4]); SB();
  SB();
  MV16(ak[1], wf[1][1], xf[1][1]); SB();
  SB();
  MV16(av[1], wf[1][2], xf[1][1]); SB();
  WAITV(5);
  BAR();
  xr0 += -79872; wr0 += -79872; asm volatile("" : "+v"(xr0), "+v"(wr0));
  DMA1(vD[0], sDq, ldsD, 79872, "sc1");
  DMA1(vD[1], sDq, ldsD, 88064, "sc1"); sDq += WQ_S; OPQ(sDq);
  DMA1(vD[2], sDm, ldsD, 96256, "sc1"); sDm += dm; OPQ(sDm);
  DMA1(vD[3], sDx, ldsD, 104448, "sc1");
  DMA1(vD[4], sDx, ldsE, 79872, "sc1"); sDx += 128; OPQ(sDx);
  SB();
  MV16(aq[1], wf[1][0], xf[1][1]); SB();
  wf[0][1] = *(LDS_AS const bf16x8*)(wr0 + 1024);
  SB();
  MV16(ak[2], wf[1][1], xf[1][2]); SB();
  xf[0][1] = *(LDS_AS const bf16x8*)(xr0 + 10240);
  SB();
  MV16(av[2], wf[1][2], xf[1][2]); SB();
  wf[0][2] = *(LDS_AS const bf16x8*)(wr0 + 2048);
  SB();
  MV16(aq[2], wf[1][0], xf[1][2]); SB();
  wf[0][0] = *(LDS_AS const bf16x8*)(wr0 + 0);
  SB();
  MV16(ak[3], wf[1][1], xf[1][3]); SB();
  xf[0][2] = *(LDS_AS const bf16x8*)(xr0 + 12288);
  SB();
  MV16(av[3], wf[1][2], xf[1][3]); SB();
  xf[0][3] = *(LDS_AS const bf16x8*)(xr0 + 14336);
  SB();
  MV16(aq[3], wf[1][0], xf[1][3]); SB();
  xf[0][4] = *(LDS_AS const bf16x8*)(xr0 + 16384);
  SB();
  MV16(ak[4], wf[1][1], xf[1][4]); SB();
  MV16(av[4], wf[1][2], xf[1][4]); SB();
  MV16(aq[4], wf[1][0], xf[1][4]); SB();
  SB();
  MV16(ak[1], wf[0][1], xf[0][1]); SB();
  xr1 += -79872; PINV(xr1);
  SB();
  MV16(av[1], wf[0][2], xf[0][1]); SB();
  wf[1][1] = *(LDS_AS const bf16x8*)(wr0 + 4096);
  SB();
  MV16(aq[1], wf[0][0], xf[0][1]); SB();
  xf[1][1] = *(LDS_AS const bf16x8*)(xr1 + 10240);
  SB();
  MV16(ak[2], wf[0][1], xf[0][2]); SB();
  wf[1][2] = *(LDS_AS const bf16x8*)(wr0 + 5120);
  SB();
  MV16(av[2], wf[0][2], xf[0][2]); SB();
  wf[1][0] = *(LDS_AS const bf16x8*)(wr0 + 3072);
  SB();
  MV16(aq[2], wf[0][0], xf[0][2]); SB();
  xf[1][2] = *(LDS_AS const bf16x8*)(xr1 + 12288);
  SB();
  MV16(ak[3], wf[0][1], xf[0][3]); SB();
  xf[1][3] = *(LDS_AS const bf16x8*)(xr1 + 14336);
  SB();
  MV16(av[3], wf[0][2], xf[0][3]); SB();
  xf[1][4] = *(LDS_AS const bf16x8*)(xr1 + 16384);
  SB();
  MV16(aq[3], wf[0][0], xf[0][3]); SB();
  MV16(ak[4], wf[0][1], xf[0][4]); SB();
  MV16(av[4], wf[0][2], xf[0][4]); SB();
  MV16(aq[4], wf[0][0], xf[0][4]); SB();
  SB();
  MV16(ak[1], wf[1][1], xf[1][1]); SB();
  SB();
  MV16(av[1], wf[1][2], xf[1][1]); SB();
  WAITV(5);
  BAR();
  xr0 += 39936; wr0 += 39936; asm volatile("" : "+v"(xr0), "+v"(wr0));
  STAMP(61);
  DMA1(vD[0], sDq, ldsD, 0, "sc1");
  DMA1(vD[1], sDq, ldsD, 8192, "sc1"); sDq += WQ_S; OPQ(sDq);
  DMA1(vD[2], sDm, ldsD, 16384, "sc1"); sDm += dm; OPQ(sDm);
  DMA1(vD[3], sDx, ldsD, 24576, "sc1");
  DMA1(vD[4], sDx, ldsE, 0, "sc1"); sDx += 128; OPQ(sDx);
  SB();
  MV16(aq[1], wf[1][0], xf[1][1]); SB();
  wf[0][1] = *(LDS_AS const bf16x8*)(wr0 + 1024);
  SB();
  MV16(ak[2], wf[1][1], xf[1][2]); SB();
  xf[0][1] = *(LDS_AS const bf16x8*)(xr0 + 10240);
  SB();
  MV16(av[2], wf[1][2], xf[1][2]); SB();
  wf[0][2] = *(LDS_AS const bf16x8*)(wr0 + 2048);
  SB();
  MV16(aq[2], wf[1][0], xf[1][2]); SB();
  wf[0][0] = *(LDS_AS const bf16x8*)(wr0 + 0);
  SB();
  MV16(ak[3], wf[1][1], xf[1][3]); SB();
  xf[0][2] = *(LDS_AS const bf16x8*)(xr0 + 12288);
  SB();
  MV16(av[3], wf[1][2], xf[1][3]); SB();
  xf[0][3] = *(LDS_AS const bf16x8*)(xr0 + 14336);
  SB();
  MV16(aq[3], wf[1][0], xf[1][3]); SB();
  xf[0][4] = *(LDS_AS const bf16x8*)(xr0 + 16384);
  SB();
  MV16(ak[4], wf[1][1], xf[1][4]); SB();
  MV16(av[4], wf[1][2], xf[1][4]); SB();
  MV16(aq[4], wf[1][0], xf[1][4]); SB();
  SB();
  MV16(ak[1], wf[0][1], xf[0][1]); SB();
  xr1 += 39936; PINV(xr1);
  SB();
  MV16(av[1], wf[0][2], xf[0][1]); SB();
  wf[1][1] = *(LDS_AS const bf16x8*)(wr0 + 4096);
  SB();
  MV16(aq[1], wf[0][0], xf[0][1]); SB();
  xf[1][1] = *(LDS_AS const bf16x8*)(xr1 + 10240);
  SB();
  MV16(ak[2], wf[0][1], xf[0][2]); SB();
  wf[1][2] = *(LDS_AS const bf16x8*)(wr0 + 5120);
  SB();
  MV16(av[2], wf[0][2], xf[0][2]); SB();
  wf[1][0] = *(LDS_AS const bf16x8*)(wr0 + 3072);
  SB();
  MV16(aq[2], wf[0][0], xf[0][2]); SB();
  xf[1][2] = *(LDS_AS const bf16x8*)(xr1 + 12288);
  SB();
  MV16(ak[3], wf[0][1], xf[0][3]); SB();
  xf[1][3] = *(LDS_AS const bf16x8*)(xr1 + 14336);
  SB();
  MV16(av[3], wf[0][2], xf[0][3]); SB();
  xf[1][4] = *(LDS_AS const bf16x8*)(xr1 + 16384);
  SB();
  MV16(aq[3], wf[0][0], xf[0][3]); SB();
  MV16(ak[4], wf[0][1], xf[0][4]); SB();
  MV16(av[4], wf[0][2], xf[0][4]); SB();
  MV16(aq[4], wf[0][0], xf[0][4]); SB();
  SB();
  MV16(ak[1], wf[1][1], xf[1][1]); SB();
  SB();
  MV16(av[1], wf[1][2], xf[1][1]); SB();
  WAITV(5);
  BAR();
  xr0 += 39936; wr0 += 39936; asm volatile("" : "+v"(xr0), "+v"(wr0));
  DMA2(vWa, vWb, sW, ldsU, 0, ""); sW += dW; OPQ(sW);
  DMA2(vWa, vWb, sW, ldsU, 2048, "");
  SB();
  MV16(aq[1], wf[1][0], xf[1][1]); SB();
  wf[0][1] = *(LDS_AS const bf16x8*)(wr0 + 1024);
  SB();
  MV16(ak[2], wf[1][1], xf[1][2]); SB();
  xf[0][1] = *(LDS_AS const bf16x8*)(xr0 + 10240);
  SB();
  MV16(av[2], wf[1][2], xf[1][2]); SB();
  wf[0][2] = *(LDS_AS const bf16x8*)(wr0 + 2048);
  SB();
  MV16(aq[2], wf[1][0], xf[1][2]); SB();
  wf[0][0] = *(LDS_AS const bf16x8*)(wr0 + 0);
  SB();
  MV16(ak[3], wf[1][1], xf[1][3]); SB();
  xf[0][2] = *(LDS_AS const bf16x8*)(xr0 + 12288);
  SB();
  MV16(av[3], wf[1][2], xf[1][3]); SB();
  xf[0][3] = *(LDS_AS const bf16x8*)(xr0 + 14336);
  SB();
  MV16(aq[3], wf[1][0], xf[1][3]); SB();
  xf[0][4] = *(LDS_AS const bf16x8*)(xr0 + 16384);
  SB();
  MV16(ak[4], wf[1][1], xf[1][4]); SB();
  MV16(av[4], wf[1][2], xf[1][4]); SB();
  MV16(aq[4], wf[1][0], xf[1][4]); SB();
  SB();
  MV16(ak[1], wf[0][1], xf[0][1]); SB();
  xr1 += 39936; PINV(xr1);
  SB();
  MV16(av[1], wf[0][2], xf[0][1]); SB();
  wf[1][1] = *(LDS_AS const bf16x8*)(wr0 + 4096);
  SB();
  MV16(aq[1], wf[0][0], xf[0][1]); SB();
  xf[1][1] = *(LDS_AS const bf16x8*)(xr1 + 10240);
  SB();
  MV16(ak[2], wf[0][1], xf[0][2]); SB();
  wf[1][2] = *(LDS_AS const bf16x8*)(wr0 + 5120);
  SB();
  MV16(av[2], wf[0][2], xf[0][2]); SB();
  wf[1][0] = *(LDS_AS const bf16x8*)(wr0 + 3072);
  SB();
  MV16(aq[2], wf[0][0], xf[0][2]); SB();
  xf[1][2] = *(LDS_AS const bf16x8*)(xr1 + 12288);
  SB();
  MV16(ak[3], wf[0][1], xf[0][3]); SB();
  xf[1][3] = *(LDS_AS const bf16x8*)(xr1 + 14336);
  SB();
  MV16(av[3], wf[0][2], xf[0][3]); SB();
  xf[1][4] = *(LDS_AS const bf16x8*)(xr1 + 16384);
  SB();
  MV16(aq[3], wf[0][0], xf[0][3]); SB();
  MV16(ak[4], wf[0][1], xf[0][4]); SB();
  MV16(av[4], wf[0][2], xf[0][4]); SB();
  MV16(aq[4], wf[0][0], xf[0][4]); SB();
  SB();
  MV16(ak[1], wf[1][1], xf[1][1]); SB();
  SB();
  MV16(av[1], wf[1][2], xf[1][1]); SB();
  WAITV(4);
  BAR();
  xr0 += -79872; wr0 += -79872; asm volatile("" : "+v"(xr0), "+v"(wr0));
  STAMP(62);
  SB();
  MV16(aq[1], wf[1][0], xf[1][1]); SB();
  wf[0][1] = *(LDS_AS const bf16x8*)(wr0 + 1024);
  SB();
  MV16(ak[2], wf[1][1], xf[1][2]); SB();
  xf[0][1] = *(LDS_AS const bf16x8*)(xr0 + 10240);
  SB();
  MV16(av[2], wf[1][2], xf[1][2]); SB();
  wf[0][2] = *(LDS_AS const bf16x8*)(wr0 + 2048);
  SB();
  MV16(aq[2], wf[1][0], xf[1][2]); SB();
  wf[0][0] = *(LDS_AS const bf16x8*)(wr0 + 0);
  SB();
  MV16(ak[3], wf[1][1], xf[1][3]); SB();
  xf[0][2] = *(LDS_AS const bf16x8*)(xr0 + 12288);
  SB();
  MV16(av[3], wf[1][2], xf[1][3]); SB();
  xf[0][3] = *(LDS_AS const bf16x8*)(xr0 + 14336);
  SB();
  MV16(aq[3], wf[1][0], xf[1][3]); SB();
  xf[0][4] = *(LDS_AS const bf16x8*)(xr0 + 16384);
  SB();
  MV16(ak[4], wf[1][1], xf[1][4]); SB();
  unsigned f0; { const int l_ = lane_now(); vFU = (l_ < 14 ? l_ : 13) * FU; PINV(vFU); }
  LD4(f0, vFU, sFU, 0, "sc1");
  SB();
  MV16(av[4], wf[1][2], xf[1][4]); SB();
  unsigned tl; { const int l_ = lane_now(); tl = lds0 + TB + l_ * 16; const u32x2 t_ = *(LDS_AS const u32x2*)(lds0 + TB + 6144 + cb * 512 + l_ * 8); sk0 = t_[0]; sv = t_[1];
    vS = *(LDS_AS const u32x2*)(lds0 + TB + 4096 + cb * 512 + l_ * 8); PINV(tl); }
  SB();
  MV16(aq[4], wf[1][0], xf[1][4]); SB();
  xr1 += -79872; PINV(xr1);
  MV16(ak[1], wf[0][1], xf[0][1]); SB();
  wf[1][1] = *(LDS_AS const bf16x8*)(wr0 + 4096);
  xf[1][1] = *(LDS_AS const bf16x8*)(xr1 + 10240);
  SB();
  MV16(av[1], wf[0][2], xf[0][1]); SB();
  wf[1][2] = *(LDS_AS const bf16x8*)(wr0 + 5120);
  SB();
  MV16(aq[1], wf[0][0], xf[0][1]); SB();
  wf[1][0] = *(LDS_AS const bf16x8*)(wr0 + 3072);
  SB();
  MV16(ak[2], wf[0][1], xf[0][2]); SB();
  xf[1][2] = *(LDS_AS const bf16x8*)(xr1 + 12288);
  xf[1][3] = *(LDS_AS const bf16x8*)(xr1 + 14336);
  SB();
  MV16(av[2], wf[0][2], xf[0][2]); SB();
  xf[1][4] = *(LDS_AS const bf16x8*)(xr1 + 16384);
  SB();
  MV16(aq[2], wf[0][0], xf[0][2]); SB();
  sk1 = sk0 ^ 32; asm volatile("" : "+v"(sk0), "+v"(sk1), "+v"(sv));
  SB();
  MV16(ak[1], wf[1][1], xf[1][1]); SB();
  SB();
  MV16(av[1], wf[1][2], xf[1][1]); SB();
  SB();
  MV16(aq[1], wf[1][0], xf[1][1]); SB();
  SB();
  MV16(ak[3], wf[0][1], xf[0][3]); SB();
  SB();
  MV16(av[3], wf[0][2], xf[0][3]); SB();
  SB();
  MV16(aq[3], wf[0][0], xf[0][3]); SB();
  SB();
  MV16(ak[2], wf[1][1], xf[1][2]); SB();
  t3 = *(LDS_AS const u32x4*)(tl + 3072); t2 = *(LDS_AS const u32x4*)(tl + 2048);
  SB();
  MV16(av[2], wf[1][2], xf[1][2]); SB();
  SB();
  MV16(aq[2], wf[1][0], xf[1][2]); SB();
  SB();
  MV16(ak[4], wf[0][1], xf[0][4]); SB();
  *(LDS_AS u32x2*)(sk1 + SK + 2560) = (u32x2){pack2(ak[1][0], ak[1][1]), pack2(ak[1][2], ak[1][3])};
  SB();
  MV16(av[4], wf[0][2], xf[0][4]); SB();
  *(LDS_AS u32x2*)(sv + 1280) = (u32x2){pack2(av[1][0], av[1][1]), pack2(av[1][2], av[1][3])};
  SB();
  MV16(aq[4], wf[0][0], xf[0][4]); SB();
  *(LDS_AS u32x2*)(sk0 + SQ + 2560) = (u32x2){pack2(aq[1][0], aq[1][1]), pack2(aq[1][2], aq[1][3])};
  SB();
  MV16(ak[3], wf[1][1], xf[1][3]); SB();
  m1 = *(LDS_AS const u32x4*)(tl + 1024); cm = *(LDS_AS const f32x4*)(tl + cmo);
  SB();
  MV16(av[3], wf[1][2], xf[1][3]); SB();
  SB();
  MV16(aq[3], wf[1][0], xf[1][3]); SB();
  SB();
  MV16(ak[4], wf[1][1], xf[1][4]); SB();
  WAITV1(0, f0);
  if (__ballot(f0 != nonce) != 0ull) poll1<2>(sFU, vFU, nonce);
  STAMP(44);
  mq_ = lane_now() >= 48 ? 0u : ~0u; PINV(mq_); sFU += 14 * FU; OPQ(sFU);
  rk = (t3[2] ^ kx) + r0 * 64; rv = t3[3] + r0 * 32; asm volatile("" : "+v"(rk), "+v"(rv));
  SB();
  MV16(av[4], wf[1][2], xf[1][4]); SB();
  *(LDS_AS u32x2*)(sk0 + SK + 3072) = (u32x2){pack2(ak[2][0], ak[2][1]), pack2(ak[2][2], ak[2][3])};
  *(LDS_AS u32x2*)(sv + 1536) = (u32x2){pack2(av[2][0], av[2][1]), pack2(av[2][2], av[2][3])};
  SB();
  MV16(aq[4], wf[1][0], xf[1][4]); SB();
  *(LDS_AS u32x2*)(sk1 + SQ + 3072) = (u32x2){pack2(aq[2][0], aq[2][1]), pack2(aq[2][2], aq[2][3])};
  SB();
  STAMP(36);
  *(LDS_AS u32x2*)(sk1 + SK + 3584) = (u32x2){pack2(ak[3][0], ak[3][1]), pack2(ak[3][2], ak[3][3])};
  *(LDS_AS u32x2*)(sv + 1792) = (u32x2){pack2(av[3][0], av[3][1]), pack2(av[3][2], av[3][3])};
  *(LDS_AS u32x2*)(sk0 + SQ + 3584) = (u32x2){pack2(aq[3][0], aq[3][1]), pack2(aq[3][2], aq[3][3])};
  SB();
  *(LDS_AS u32x2*)(sk0 + SK + 4096) = (u32x2){pack2(ak[4][0], ak[4][1]), pack2(ak[4][2], ak[4][3])};
  *(LDS_AS u32x2*)(sv + 2048) = (u32x2){pack2(av[4][0], av[4][1]), pack2(av[4][2], av[4][3])};
  *(LDS_AS u32x2*)(sk1 + SQ + 4096) = (u32x2){pack2(aq[4][0], aq[4][1]), pack2(aq[4][2], aq[4][3])};
  SB();

  } else {
  f32x4 aq[5], ak[5], av[5];
  bf16x8 xf[2][5], wf[2][5];
  unsigned sk0, sk1, sv;
  unsigned wk, wh, e0, e1, e2, hb[3]; { const int l_ = lane_now(); wk = wr0 + (l_ & 8) * 128; wh = lds0 + l_ * 16; asm volatile("" : "+v"(wk), "+v"(wh)); }
  WAITV(5);
  BAR();
  STAMP(35);
  xf[0][0] = *(LDS_AS const bf16x8*)(xr0 + 0);
  wf[0][2] = *(LDS_AS const bf16x8*)(wh + 2048);
  wf[0][3] = *(LDS_AS const bf16x8*)(wh + 8192);
  wf[0][4] = *(LDS_AS const bf16x8*)(wh + 14336);
  wf[0][1] = *(LDS_AS const bf16x8*)(wk + 1024);
  xf[0][1] = *(LDS_AS const bf16x8*)(xr0 + 10240);
  wf[0][0] = *(LDS_AS const bf16x8*)(wr0 + 0);
  xf[0][2] = *(LDS_AS const bf16x8*)(xr0 + 12288);
  xf[0][3] = *(LDS_AS const bf16x8*)(xr0 + 14336);
  xf[0][4] = *(LDS_AS const bf16x8*)(xr0 + 16384);
  SB();
  MV16Z(av[0], wf[0][2], xf[0][0]); SB();
  xf[1][0] = *(LDS_AS const bf16x8*)(xr1 + 0);
  wf[1][2] = *(LDS_AS const bf16x8*)(wh + 5120);
  SB();
  MV16Z(av[1], wf[0][3], xf[0][0]); SB();
  wf[1][3] = *(LDS_AS const bf16x8*)(wh + 11264);
  SB();
  MV16Z(av[2], wf[0][4], xf[0][0]); SB();
  wf[1][4] = *(LDS_AS const bf16x8*)(wh + 17408);
  wf[1][1] = *(LDS_AS const bf16x8*)(wk + 1536);
  SB();
  MV16Z(ak[1], wf[0][1], xf[0][1]); SB();
  xf[1][1] = *(LDS_AS const bf16x8*)(xr1 + 10240);
  SB();
  MV16Z(aq[1], wf[0][0], xf[0][1]); SB();
  wf[1][0] = *(LDS_AS const bf16x8*)(wr0 + 512);
  SB();
  MV16Z(ak[2], wf[0][1], xf[0][2]); SB();
  xf[1][2] = *(LDS_AS const bf16x8*)(xr1 + 12288);
  xf[1][3] = *(LDS_AS const bf16x8*)(xr1 + 14336);
  SB();
  MV16Z(aq[2], wf[0][0], xf[0][2]); SB();
  xf[1][4] = *(LDS_AS const bf16x8*)(xr1 + 16384);
  SB();
  MV16Z(ak[3], wf[0][1], xf[0][3]); SB();
  DMA1(vD[0], sDq, ldsD, 79872, "sc1");
  SB();
  MV16Z(aq[3], wf[0][0], xf[0][3]); SB();
  DMA1(vD[1], sDq, ldsD, 88064, "sc1"); sDq += WQ_S; OPQ(sDq);
  DMA1(vD[2], sDm, ldsD, 96256, "sc1"); sDm += dm; OPQ(sDm);
  SB();
  MV16Z(ak[4], wf[0][1], xf[0][4]); SB();
  DMA1(vD[3], sDx, ldsD, 104448, "sc1");
  SB();
  MV16Z(aq[4], wf[0][0], xf[0][4]); SB();
  DMA1(vD[4], sDx, ldsE, 79872, "sc1"); sDx += 128; OPQ(sDx);
  SB();
  SB();
  MV16(av[0], wf[1][2], xf[1][0]); SB();
  WAITV(5);
  BAR();
  xr0 += 39936; wr0 += 39936; asm volatile("" : "+v"(xr0), "+v"(wr0));
  wk += 39936; wh += 39936; asm volatile("" : "+v"(wk), "+v"(wh));
  DMA1(vD[0], sDq, ldsD, 0, "sc1");
  DMA1(vD[1], sDq, ldsD, 8192, "sc1"); sDq += WQ_S; OPQ(sDq);
  DMA1(vD[2], sDm, ldsD, 16384, "sc1"); sDm += dm; OPQ(sDm);
  DMA1(vD[3], sDx, ldsD, 24576, "sc1");
  DMA1(vD[4], sDx, ldsE, 0, "sc1"); sDx += 128; OPQ(sDx);
  SB();
  MV16(av[1], wf[1][3], xf[1][0]); SB();
  xf[0][0] = *(LDS_AS const bf16x8*)(xr0 + 0);
  SB();
  MV16(av[2], wf[1][4], xf[1][0]); SB();
  wf[0][2] = *(LDS_AS const bf16x8*)(wh + 2048);
  SB();
  MV16(ak[1], wf[1][1], xf[1][1]); SB();
  wf[0][3] = *(LDS_AS const bf16x8*)(wh + 8192);
  SB();
  MV16(aq[1], wf[1][0], xf[1][1]); SB();
  wf[0][4] = *(LDS_AS const bf16x8*)(wh + 14336);
  SB();
  MV16(ak[2], wf[1][1], xf[1][2]); SB();
  wf[0][1] = *(LDS_AS const bf16x8*)(wk + 1024);
  SB();
  MV16(aq[2], wf[1][0], xf[1][2]); SB();
  xf[0][1] = *(LDS_AS const bf16x8*)(xr0 + 10240);
  SB();
  MV16(ak[3], wf[1][1], xf[1][3]); SB();
  wf[0][0] = *(LDS_AS const bf16x8*)(wr0 + 0);
  SB();
  MV16(aq[3], wf[1][0], xf[1][3]); SB();
  xf[0][2] = *(LDS_AS const bf16x8*)(xr0 + 12288);
  SB();
  MV16(ak[4], wf[1][1], xf[1][4]); SB();
  xf[0][3] = *(LDS_AS const bf16x8*)(xr0 + 14336);
  SB();
  MV16(aq[4], wf[1][0], xf[1][4]); SB();
  xf[0][4] = *(LDS_AS const bf16x8*)(xr0 + 16384);
  SB();
  SB();
  MV16(av[0], wf[0][2], xf[0][0]); SB();
  xr1 += 39936; PINV(xr1);
  SB();
  MV16(av[1], wf[0][3], xf[0][0]); SB();
  xf[1][0] = *(LDS_AS const bf16x8*)(xr1 + 0);
  SB();
  MV16(av[2], wf[0][4], xf[0][0]); SB();
  wf[1][2] = *(LDS_AS const bf16x8*)(wh + 5120);
  SB();
  MV16(ak[1], wf[0][1], xf[0][1]); SB();
  wf[1][3] = *(LDS_AS const bf16x8*)(wh + 11264);
  SB();
  MV16(aq[1], wf[0][0], xf[0][1]); SB();
  wf[1][4] = *(LDS_AS const bf16x8*)(wh + 17408);
  SB();
  MV16(ak[2], wf[0][1], xf[0][2]); SB();
  wf[1][1] = *(LDS_AS const bf16x8*)(wk + 1536);
  SB();
  MV16(aq[2], wf[0][0], xf[0][2]); SB();
  xf[1][1] = *(LDS_AS const bf16x8*)(xr1 + 10240);
  SB();
  MV16(ak[3], wf[0][1], xf[0][3]); SB();
  wf[1][0] = *(LDS_AS const bf16x8*)(wr0 + 512);
  SB();
  MV16(aq[3], wf[0][0], xf[0][3]); SB();
  xf[1][2] = *(LDS_AS const bf16x8*)(xr1 + 12288);
  SB();
  MV16(ak[4], wf[0][1], xf[0][4]); SB();
  xf[1][3] = *(LDS_AS const bf16x8*)(xr1 + 14336);
  SB();
  MV16(aq[4], wf[0][0], xf[0][4]); SB();
  xf[1][4] = *(LDS_AS const bf16x8*)(xr1 + 16384);
  SB();
  SB();
  MV16(av[0], wf[1][2], xf[1][0]); SB();
  WAITV(5);
  BAR();
  xr0 += 39936; wr0 += 39936; asm volatile("" : "+v"(xr0), "+v"(wr0));
  wk += 39936; wh += 39936; asm volatile("" : "+v"(wk), "+v"(wh));
  STAMP(60);
  DMA1(vD[0], sDq, ldsD, 39936, "sc1");
  DMA1(vD[1], sDq, ldsD, 48128, "sc1"); sDq += WQ_S; OPQ(sDq);
  DMA1(vD[2], sDm, ldsD, 56320, "sc1"); sDm += dm; OPQ(sDm);
  DMA1(vD[3], sDx, ldsD, 64512, "sc1");
  DMA1(vD[4], sDx, ldsE, 39936, "sc1"); sDx += 128; OPQ(sDx);
  SB();
  MV16(av[1], wf[1][3], xf[1][0]); SB();
  xf[0][0] = *(LDS_AS const bf16x8*)(xr0 + 0);
  SB();
  MV16(av[2], wf[1][4], xf[1][0]); SB();
  wf[0][2] = *(LDS_AS const bf16x8*)(wh + 2048);
  SB();
  MV16(ak[1], wf[1][1], xf[1][1]); SB();
  wf[0][3] = *(LDS_AS const bf16x8*)(wh + 8192);
  SB();
  MV16(aq[1], wf[1][0], xf[1][1]); SB();
  wf[0][4] = *(LDS_AS const bf16x8*)(wh + 14336);
  SB();
  MV16(ak[2], wf[1][1], xf[1][2]); SB();
  wf[0][1] = *(LDS_AS const bf16x8*)(wk + 1024);
  SB();
  MV16(aq[2], wf[1][0], xf[1][2]); SB();
  xf[0][1] = *(LDS_AS const bf16x8*)(xr0 + 10240);
  SB();
  MV16(ak[3], wf[1][1], xf[1][3]); SB();
  wf[0][0] = *(LDS_AS const bf16x8*)(wr0 + 0);
  SB();
  MV16(aq[3], wf[1][0], xf[1][3]); SB();
  xf[0][2] = *(LDS_AS const bf16x8*)(xr0 + 12288);
  SB();
  MV16(ak[4], wf[1][1], xf[1][4]); SB();
  xf[0][3] = *(LDS_AS const bf16x8*)(xr0 + 14336);
  SB();
  MV16(aq[4], wf[1][0], xf[1][4]); SB();
  xf[0][4] = *(LDS_AS const bf16x8*)(xr0 + 16384);
  SB();
  SB();
  MV16(av[0], wf[0][2], xf[0][0]); SB();
  xr1 += 39936; PINV(xr1);
  SB();
  MV16(av[1], wf[0][3], xf[0][0]); SB();
  xf[1][0] = *(LDS_AS const bf16x8*)(xr1 + 0);
  SB();
  MV16(av[2], wf[0][4], xf[0][0]); SB();
  wf[1][2] = *(LDS_AS const bf16x8*)(wh + 5120);
  SB();
  MV16(ak[1], wf[0][1], xf[0][1]); SB();
  wf[1][3] = *(LDS_AS const bf16x8*)(wh + 11264);
  SB();
  MV16(aq[1], wf[0][0], xf[0][1]); SB();
  wf[1][4] = *(LDS_AS const bf16x8*)(wh + 17408);
  SB();
  MV16(ak[2], wf[0][1], xf[0][2]); SB();
  wf[1][1] = *(LDS_AS const bf16x8*)(wk + 1536);
  SB();
  MV16(aq[2], wf[0][0], xf[0][2]); SB();
  xf[1][1] = *(LDS_AS const bf16x8*)(xr1 + 10240);
  SB();
  MV16(ak[3], wf[0][1], xf[0][3]); SB();
  wf[1][0] = *(LDS_AS const bf16x8*)(wr0 + 512);
  SB();
  MV16(aq[3], wf[0][0], xf[0][3]); SB();
  xf[1][2] = *(LDS_AS const bf16x8*)(xr1 + 12288);
  SB();
  MV16(ak[4], wf[0][1], xf[0][4]); SB();
  xf[1][3] = *(LDS_AS const bf16x8*)(xr1 + 14336);
  SB();
  MV16(aq[4], wf[0][0], xf[0][4]); SB();
  xf[1][4] = *(LDS_AS const bf16x8*)(xr1 + 16384);
  SB();
  SB();
  MV16(av[0], wf[1][2], xf[1][0]); SB();
  WAITV(5);
  BAR();
  xr0 += -79872; wr0 += -79872; asm volatile("" : "+v"(xr0), "+v"(wr0));
  wk += -79872; wh += -79872; asm volatile("" : "+v"(wk), "+v"(wh));
  DMA1(vD[0], sDq, ldsD, 79872, "sc1");
  DMA1(vD[1], sDq, ldsD, 88064, "sc1"); sDq += WQ_S; OPQ(sDq);
  DMA1(vD[2], sDm, ldsD, 96256, "sc1"); sDm += dm; OPQ(sDm);
  DMA1(vD[3], sDx, ldsD, 104448, "sc1");
  DMA1(vD[4], sDx, ldsE, 79872, "sc1"); sDx += 128; OPQ(sDx);
  SB();
  MV16(av[1], wf[1][3], xf[1][0]); SB();
  xf[0][0] = *(LDS_AS const bf16x8*)(xr0 + 0);
  SB();
  MV16(av[2], wf[1][4], xf[1][0]); SB();
  wf[0][2] = *(LDS_AS const bf16x8*)(wh + 2048);
  SB();
  MV16(ak[1], wf[1][1], xf[1][1]); SB();
  wf[0][3] = *(LDS_AS const bf16x8*)(wh + 8192);
  SB();
  MV16(aq[1], wf[1][0], xf[1][1]); SB();
  wf[0][4] = *(LDS_AS const bf16x8*)(wh + 14336);
  SB();
  MV16(ak[2], wf[1][1], xf[1][2]); SB();
  wf[0][1] = *(LDS_AS const bf16x8*)(wk + 1024);
  SB();
  MV16(aq[2], wf[1][0], xf[1][2]); SB();
  xf[0][1] = *(LDS_AS const bf16x8*)(xr0 + 10240);
  SB();
  MV16(ak[3], wf[1][1], xf[1][3]); SB();
  wf[0][0] = *(LDS_AS const bf16x8*)(wr0 + 0);
  SB();
  MV16(aq[3], wf[1][0], xf[1][3]); SB();
  xf[0][2] = *(LDS_AS const bf16x8*)(xr0 + 12288);
  SB();
  MV16(ak[4], wf[1][1], xf[1][4]); SB();
  xf[0][3] = *(LDS_AS const bf16x8*)(xr0 + 14336);
  SB();
  MV16(aq[4], wf[1][0], xf[1][4]); SB();
  xf[0][4] = *(LDS_AS const bf16x8*)(xr0 + 16384);
  SB();
  SB();
  MV16(av[0], wf[0][2], xf[0][0]); SB();
  xr1 += -79872; PINV(xr1);
  SB();
  MV16(av[1], wf[0][3], xf[0][0]); SB();
  xf[1][0] = *(LDS_AS const bf16x8*)(xr1 + 0);
  SB();
  MV16(av[2], wf[0][4], xf[0][0]); SB();
  wf[1][2] = *(LDS_AS const bf16x8*)(wh + 5120);
  SB();
  MV16(ak[1], wf[0][1], xf[0][1]); SB();
  wf[1][3] = *(LDS_AS const bf16x8*)(wh + 11264);
  SB();
  MV16(aq[1], wf[0][0], xf[0][1]); SB();
  wf[1][4] = *(LDS_AS const bf16x8*)(wh + 17408);
  SB();
  MV16(ak[2], wf[0][1], xf[0][2]); SB();
  wf[1][1] = *(LDS_AS const bf16x8*)(wk + 1536);
  SB();
  MV16(aq[2], wf[0][0], xf[0][2]); SB();
  xf[1][1] = *(LDS_AS const bf16x8*)(xr1 + 10240);
  SB();
  MV16(ak[3], wf[0][1], xf[0][3]); SB();
  wf[1][0] = *(LDS_AS const bf16x8*)(wr0 + 512);
  SB();
  MV16(aq[3], wf[0][0], xf[0][3]); SB();
  xf[1][2] = *(LDS_AS const bf16x8*)(xr1 + 12288);
  SB();
  MV16(ak[4], wf[0][1], xf[0][4]); SB();
  xf[1][3] = *(LDS_AS const bf16x8*)(xr1 + 14336);
  SB();
  MV16(aq[4], wf[0][0], xf[0][4]); SB();
  xf[1][4] = *(LDS_AS const bf16x8*)(xr1 + 16384);
  SB();
  SB();
  MV16(av[0], wf[1][2], xf[1][0]); SB();
  WAITV(5);
  BAR();
  xr0 += 39936; wr0 += 39936; asm volatile("" : "+v"(xr0), "+v"(wr0));
  wk += 39936; wh += 39936; asm volatile("" : "+v"(wk), "+v"(wh));
  STAMP(61);
  DMA1(vD[0], sDq, ldsD, 0, "sc1");
  DMA1(vD[1], sDq, ldsD, 8192, "sc1"); sDq += WQ_S; OPQ(sDq);
  DMA1(vD[2], sDm, ldsD, 16384, "sc1"); sDm += dm; OPQ(sDm);
  DMA1(vD[3], sDx, ldsD, 24576, "sc1");
  DMA1(vD[4], sDx, ldsE, 0, "sc1"); sDx += 128; OPQ(sDx);
  SB();
  MV16(av[1], wf[1][3], xf[1][0]); SB();
  xf[0][0] = *(LDS_AS const bf16x8*)(xr0 + 0);
  SB();
  MV16(av[2], wf[1][4], xf[1][0]); SB();
  wf[0][2] = *(LDS_AS const bf16x8*)(wh + 2048);
  SB();
  MV16(ak[1], wf[1][1], xf[1][1]); SB();
  wf[0][3] = *(LDS_AS const bf16x8*)(wh + 8192);
  SB();
  MV16(aq[1], wf[1][0], xf[1][1]); SB();
  wf[0][4] = *(LDS_AS const bf16x8*)(wh + 14336);
  SB();
  MV16(ak[2], wf[1][1], xf[1][2]); SB();
  wf[0][1] = *(LDS_AS const bf16x8*)(wk + 1024);
  SB();
  MV16(aq[2], wf[1][0], xf[1][2]); SB();
  xf[0][1] = *(LDS_AS const bf16x8*)(xr0 + 10240);
  SB();
  MV16(ak[3], wf[1][1], xf[1][3]); SB();
  wf[0][0] = *(LDS_AS const bf16x8*)(wr0 + 0);
  SB();
  MV16(aq[3], wf[1][0], xf[1][3]); SB();
  xf[0][2] = *(LDS_AS const bf16x8*)(xr0 + 12288);
  SB();
  MV16(ak[4], wf[1][1], xf[1][4]); SB();
  xf[0][3] = *(LDS_AS const bf16x8*)(xr0 + 14336);
  SB();
  MV16(aq[4], wf[1][0], xf[1][4]); SB();
  xf[0][4] = *(LDS_AS const bf16x8*)(xr0 + 16384);
  SB();
  SB();
  MV16(av[0], wf[0][2], xf[0][0]); SB();
  xr1 += 39936; PINV(xr1);
  SB();
  MV16(av[1], wf[0][3], xf[0][0]); SB();
  xf[1][0] = *(LDS_AS const bf16x8*)(xr1 + 0);
  SB();
  MV16(av[2], wf[0][4], xf[0][0]); SB();
  wf[1][2] = *(LDS_AS const bf16x8*)(wh + 5120);
  SB();
  MV16(ak[1], wf[0][1], xf[0][1]); SB();
  wf[1][3] = *(LDS_AS const bf16x8*)(wh + 11264);
  SB();
  MV16(aq[1], wf[0][0], xf[0][1]); SB();
  wf[1][4] = *(LDS_AS const bf16x8*)(wh + 17408);
  SB();
  MV16(ak[2], wf[0][1], xf[0][2]); SB();
  wf[1][1] = *(LDS_AS const bf16x8*)(wk + 1536);
  SB();
  MV16(aq[2], wf[0][0], xf[0][2]); SB();
  xf[1][1] = *(LDS_AS const bf16x8*)(xr1 + 10240);
  SB();
  MV16(ak[3], wf[0][1], xf[0][3]); SB();
  wf[1][0] = *(LDS_AS const bf16x8*)(wr0 + 512);
  SB();
  MV16(aq[3], wf[0][0], xf[0][3]); SB();
  xf[1][2] = *(LDS_AS const bf16x8*)(xr1 + 12288);
  SB();
  MV16(ak[4], wf[0][1], xf[0][4]); SB();
  xf[1][3] = *(LDS_AS const bf16x8*)(xr1 + 14336);
  SB();
  MV16(aq[4], wf[0][0], xf[0][4]); SB();
  xf[1][4] = *(LDS_AS const bf16x8*)(xr1 + 16384);
  SB();
  SB();
  MV16(av[0], wf[1][2], xf[1][0]); SB();
  WAITV(5);
  BAR();
  xr0 += 39936; wr0 += 39936; asm volatile("" : "+v"(xr0), "+v"(wr0));
  wk += 39936; wh += 39936; asm volatile("" : "+v"(wk), "+v"(wh));
  DMA2(vWa, vWb, sW, ldsU, 0, ""); sW += dW; OPQ(sW);
  DMA2(vWa, vWb, sW, ldsU, 2048, "");
  SB();
  MV16(av[1], wf[1][3], xf[1][0]); SB();
  xf[0][0] = *(LDS_AS const bf16x8*)(xr0 + 0);
  SB();
  MV16(av[2], wf[1][4], xf[1][0]); SB();
  wf[0][2] = *(LDS_AS const bf16x8*)(wh + 2048);
  SB();
  MV16(ak[1], wf[1][1], xf[1][1]); SB();
  wf[0][3] = *(LDS_AS const bf16x8*)(wh + 8192);
  SB();
  MV16(aq[1], wf[1][0], xf[1][1]); SB();
  wf[0][4] = *(LDS_AS const bf16x8*)(wh + 14336);
  SB();
  MV16(ak[2], wf[1][1], xf[1][2]); SB();
  wf[0][1] = *(LDS_AS const bf16x8*)(wk + 1024);
  SB();
  MV16(aq[2], wf[1][0], xf[1][2]); SB();
  xf[0][1] = *(LDS_AS const bf16x8*)(xr0 + 10240);
  SB();
  MV16(ak[3], wf[1][1], xf[1][3]); SB();
  wf[0][0] = *(LDS_AS const bf16x8*)(wr0 + 0);
  SB();
  MV16(aq[3], wf[1][0], xf[1][3]); SB();
  xf[0][2] = *(LDS_AS const bf16x8*)(xr0 + 12288);
  SB();
  MV16(ak[4], wf[1][1], xf[1][4]); SB();
  xf[0][3] = *(LDS_AS const bf16x8*)(xr0 + 14336);
  SB();
  MV16(aq[4], wf[1][0], xf[1][4]); SB();
  xf[0][4] = *(LDS_AS const bf16x8*)(xr0 + 16384);
  SB();
  SB();
  MV16(av[0], wf[0][2], xf[0][0]); SB();
  xr1 += 39936; PINV(xr1);
  SB();
  MV16(av[1], wf[0][3], xf[0][0]); SB();
  xf[1][0] = *(LDS_AS const bf16x8*)(xr1 + 0);
  SB();
  MV16(av[2], wf[0][4], xf[0][0]); SB();
  wf[1][2] = *(LDS_AS const bf16x8*)(wh + 5120);
  SB();
  MV16(ak[1], wf[0][1], xf[0][1]); SB();
  wf[1][3] = *(LDS_AS const bf16x8*)(wh + 11264);
  SB();
  MV16(aq[1], wf[0][0], xf[0][1]); SB();
  wf[1][4] = *(LDS_AS const bf16x8*)(wh + 17408);
  SB();
  MV16(ak[2], wf[0][1], xf[0][2]); SB();
  wf[1][1] = *(LDS_AS const bf16x8*)(wk + 1536);
  SB();
  MV16(aq[2], wf[0][0], xf[0][2]); SB();
  xf[1][1] = *(LDS_AS const bf16x8*)(xr1 + 10240);
  SB();
  MV16(ak[3], wf[0][1], xf[0][3]); SB();
  wf[1][0] = *(LDS_AS const bf16x8*)(wr0 + 512);
  SB();
  MV16(aq[3], wf[0][0], xf[0][3]); SB();
  xf[1][2] = *(LDS_AS const bf16x8*)(xr1 + 12288);
  SB();
  MV16(ak[4], wf[0][1], xf[0][4]); SB();
  xf[1][3] = *(LDS_AS const bf16x8*)(xr1 + 14336);
  SB();
  MV16(aq[4], wf[0][0], xf[0][4]); SB();
  xf[1][4] = *(LDS_AS const bf16x8*)(xr1 + 16384);
  SB();
  SB();
  MV16(av[0], wf[1][2], xf[1][0]); SB();
  WAITV(4);
  BAR();
  xr0 += -79872; wr0 += -79872; asm volatile("" : "+v"(xr0), "+v"(wr0));
  wk += -79872; wh += -79872; asm volatile("" : "+v"(wk), "+v"(wh));
  STAMP(62);
  SB();
  MV16(av[1], wf[1][3], xf[1][0]); SB();
  xf[0][0] = *(LDS_AS const bf16x8*)(xr0 + 0);
  SB();
  MV16(av[2], wf[1][4], xf[1][0]); SB();
  wf[0][2] = *(LDS_AS const bf16x8*)(wh + 2048);
  wf[0][3] = *(LDS_AS const bf16x8*)(wh + 8192);
  SB();
  MV16(ak[1], wf[1][1], xf[1][1]); SB();
  wf[0][4] = *(LDS_AS const bf16x8*)(wh + 14336);
  SB();
  MV16(aq[1], wf[1][0], xf[1][1]); SB();
  wf[0][1] = *(LDS_AS const bf16x8*)(wk + 1024);
  SB();
  MV16(ak[2], wf[1][1], xf[1][2]); SB();
  xf[0][1] = *(LDS_AS const bf16x8*)(xr0 + 10240);
  wf[0][0] = *(LDS_AS const bf16x8*)(wr0 + 0);
  SB();
  MV16(aq[2], wf[1][0], xf[1][2]); SB();
  xf[0][2] = *(LDS_AS const bf16x8*)(xr0 + 12288);
  SB();
  MV16(ak[3], wf[1][1], xf[1][3]); SB();
  xf[0][3] = *(LDS_AS const bf16x8*)(xr0 + 14336);
  SB();
  MV16(aq[3], wf[1][0], xf[1][3]); SB();
  xf[0][4] = *(LDS_AS const bf16x8*)(xr0 + 16384);
  unsigned f0; { const int l_ = lane_now(); vFU = (l_ < 14 ? l_ : 13) * FU; PINV(vFU); }
  LD4(f0, vFU, sFU, 0, "sc1");
  SB();
  MV16(ak[4], wf[1][1], xf[1][4]); SB();
  unsigned tl; { const int l_ = lane_now(); tl = lds0 + TB + l_ * 16; const u32x2 t_ = *(LDS_AS const u32x2*)(lds0 + TB + 6144 + cb * 512 + l_ * 8); sk0 = t_[0]; sv = t_[1];
    vS = *(LDS_AS const u32x2*)(lds0 + TB + 4096 + cb * 512 + l_ * 8); PINV(tl); }
  SB();
  MV16(aq[4], wf[1][0], xf[1][4]); SB();
  { const int l_ = lane_now(); const unsigned a_ = lds0 + TB + 6144 + l_ * 8; sv = *(LDS_AS const unsigned*)(lds0 + TB + 7684 + (l_ & 31) * 8);
    hb[0] = *(LDS_AS const unsigned*)(a_ + 4); hb[1] = *(LDS_AS const unsigned*)(a_ + 516); hb[2] = *(LDS_AS const unsigned*)(a_ + 1028); }
  SB();
  xr1 += -79872; PINV(xr1);
  MV16(av[0], wf[0][2], xf[0][0]); SB();
  xf[1][0] = *(LDS_AS const bf16x8*)(xr1 + 0);
  wf[1][2] = *(LDS_AS const bf16x8*)(wh + 5120);
  wf[1][3] = *(LDS_AS const bf16x8*)(wh + 11264);
  SB();
  MV16(av[1], wf[0][3], xf[0][0]); SB();
  wf[1][4] = *(LDS_AS const bf16x8*)(wh + 17408);
  wf[1][1] = *(LDS_AS const bf16x8*)(wk + 1536);
  SB();
  MV16(av[2], wf[0][4], xf[0][0]); SB();
  xf[1][1] = *(LDS_AS const bf16x8*)(xr1 + 10240);
  wf[1][0] = *(LDS_AS const bf16x8*)(wr0 + 512);
  SB();
  MV16(ak[1], wf[0][1], xf[0][1]); SB();
  xf[1][2] = *(LDS_AS const bf16x8*)(xr1 + 12288);
  xf[1][3] = *(LDS_AS const bf16x8*)(xr1 + 14336);
  SB();
  MV16(aq[1], wf[0][0], xf[0][1]); SB();
  xf[1][4] = *(LDS_AS const bf16x8*)(xr1 + 16384);
  sk1 = sk0 ^ 32; asm volatile("" : "+v"(sk0), "+v"(sk1), "+v"(sv));
  const bool lo_ = lane_now() < 32;
  e0 = lo_ ? sk0 + SK : sv; e1 = lo_ ? sk1 + SK + 512 : sv + 256; e2 = lo_ ? 1024 : 512; asm volatile("" : "+v"(e0), "+v"(e1), "+v"(e2));
  SB();
  MV16(av[0], wf[1][2], xf[1][0]); SB();
  SB();
  MV16(av[1], wf[1][3], xf[1][0]); SB();
  SB();
  MV16(av[2], wf[1][4], xf[1][0]); SB();
  SB();
  MV16(ak[2], wf[0][1], xf[0][2]); SB();
  SB();
  MV16(aq[2], wf[0][0], xf[0][2]); SB();
  SB();
  MV16(ak[1], wf[1][1], xf[1][1]); SB();
  SB();
  MV16(aq[1], wf[1][0], xf[1][1]); SB();
  SB();
  MV16(ak[3], wf[0][1], xf[0][3]); SB();
  SB();
  MV16(aq[3], wf[0][0], xf[0][3]); SB();
  SB();
  MV16(ak[2], wf[1][1], xf[1][2]); SB();
  t3 = *(LDS_AS const u32x4*)(tl + 3072); t2 = *(LDS_AS const u32x4*)(tl + 2048);
  SB();
  MV16(aq[2], wf[1][0], xf[1][2]); SB();
  *(LDS_AS u32x2*)(hb[0]) = (u32x2){pack2(av[0][0], av[0][1]), pack2(av[0][2], av[0][3])};
  SB();
  MV16(ak[4], wf[0][1], xf[0][4]); SB();
  *(LDS_AS u32x2*)(hb[1]) = (u32x2){pack2(av[1][0], av[1][1]), pack2(av[1][2], av[1][3])};
  SB();
  MV16(aq[4], wf[0][0], xf[0][4]); SB();
  *(LDS_AS u32x2*)(hb[2]) = (u32x2){pack2(av[2][0], av[2][1]), pack2(av[2][2], av[2][3])};
  SB();
  MV16(ak[3], wf[1][1], xf[1][3]); SB();
  WAITV1(0, f0);
  if (__ballot(f0 != nonce) != 0ull) poll1<2>(sFU, vFU, nonce);
  STAMP(44);
  mq_ = lane_now() >= 48 ? 0u : ~0u; PINV(mq_); sFU += 14 * FU; OPQ(sFU);
  SB();
  MV16(aq[3], wf[1][0], xf[1][3]); SB();
  m1 = *(LDS_AS const u32x4*)(tl + 1024); cm = *(LDS_AS const f32x4*)(tl + cmo);
  SB();
  MV16(ak[4], wf[1][1], xf[1][4]); SB();
  rk = (t3[2] ^ kx) + r0 * 64; rv = t3[3] + r0 * 32; asm volatile("" : "+v"(rk), "+v"(rv));
  *(LDS_AS u32x2*)(e1 + 2 * e2) = (u32x2){pack2(ak[1][0], ak[1][1]), pack2(ak[1][2], ak[1][3])};
  SB();
  MV16(aq[4], wf[1][0], xf[1][4]); SB();
  *(LDS_AS u32x2*)(sk0 + SQ + 2560) = (u32x2){pack2(aq[1][0], aq[1][1]), pack2(aq[1][2], aq[1][3])};
  SB();
  STAMP(36);
  *(LDS_AS u32x2*)(e0 + 3 * e2) = (u32x2){pack2(ak[2][0], ak[2][1]), pack2(ak[2][2], ak[2][3])};
  *(LDS_AS u32x2*)(sk1 + SQ + 3072) = (u32x2){pack2(aq[2][0], aq[2][1]), pack2(aq[2][2], aq[2][3])};
  SB();
  *(LDS_AS u32x2*)(e1 + 3 * e2) = (u32x2){pack2(ak[3][0], ak[3][1]), pack2(ak[3][2], ak[3][3])};
  *(LDS_AS u32x2*)(sk0 + SQ + 3584) = (u32x2){pack2(aq[3][0], aq[3][1]), pack2(aq[3][2], aq[3][3])};
  SB();
  *(LDS_AS u32x2*)(e0 + 4 * e2) = (u32x2){pack2(ak[4][0], ak[4][1]), pack2(ak[4][2], ak[4][3])};
  *(LDS_AS u32x2*)(sk1 + SQ + 4096) = (u32x2){pack2(aq[4][0], aq[4][1]), pack2(aq[4][2], aq[4][3])};
  SB();

  }
  bf16x8 p[8]; unsigned fu;
  const unsigned vP0 = t2[0], vP1 = t2[1], vP2 = t2[2], vP3 = t2[3], vS0 = vS[0], vS1 = vS[1], mp[2] = {t3[0], t3[1]};
  PRIO(3);
  BAR();
  STAMP(37);
  u32x4 qf[2]; bf16x8 kf[2][2], vf[2][2];
  qf[0] = *(LDS_AS const u32x4*)(rk + SQ + 512); kf[0][0] = *(LDS_AS const bf16x8*)(rk + SK); kf[0][1] = *(LDS_AS const bf16x8*)(rk + SK + 1024);
  qf[1] = *(LDS_AS const u32x4*)(rk + SQ + 512 + SKH); kf[1][0] = *(LDS_AS const bf16x8*)(rk + SK + SKH); kf[1][1] = *(LDS_AS const bf16x8*)(rk + SK + SKH + 1024);
  SB();
  vf[0][0] = tr8u(rv, 512); vf[1][0] = tr8u(rv + 2 * SVB, 512); vf[0][1] = tr8u(rv + SVB, 512); vf[1][1] = tr8u(rv + 3 * SVB, 512);
  u32x4 t7; float bias[4];
  { const int l_ = lane_now(); t7 = *(LDS_AS const u32x4*)(lds0 + TB + 9216 + l_ * 16); bias[0] = bias[1] = bias[2] = bias[3] = *(LDS_AS const float*)(bio + (l_ >> 3) * 4); }
  SB();
  SB();
  LD16(p[0], vP0, sP, 0, "sc1");
  SB();
  f32x4 sc[2][2];
  const f32x4 z4 = {0.f, 0.f, 0.f, 0.f};
  qf[0][2] &= mq_; qf[0][3] &= mq_; qf[1][2] &= mq_; qf[1][3] &= mq_;
  sc[0][0] = MFMA16(kf[0][0], __builtin_bit_cast(bf16x8, qf[0]), z4); sc[0][1] = MFMA16(kf[0][1], __builtin_bit_cast(bf16x8, qf[0]), z4);
  sc[1][0] = MFMA16(kf[1][0], __builtin_bit_cast(bf16x8, qf[1]), z4); sc[1][1] = MFMA16(kf[1][1], __builtin_bit_cast(bf16x8, qf[1]), z4);
  SB();
  LD16(p[1], vP1, sP, 0, "sc1");
  LD16(p[2], vP2, sP, 0, "sc1");
  SB();
  float lg[2][4];
#pragma unroll
  for (int i = 0; i < 8; ++i) lg[i >> 2][i & 3] = rbf(SEL(i >> 2, i & 3));
  FMK4(lg[0], "0x3e41848f", cm); FMK4(lg[1], "0x3e41848f", cm);
#pragma unroll
  for (int i = 0; i < 8; ++i) lg[i >> 2][i & 3] = rbf(lg[i >> 2][i & 3]); asm volatile("" : "+v"(lg[0][0]), "+v"(lg[0][1]), "+v"(lg[0][2]), "+v"(lg[0][3]), "+v"(lg[1][0]), "+v"(lg[1][1]), "+v"(lg[1][2]), "+v"(lg[1][3]));
  SB();
  LD16(p[3], vP3, sP, 0, "sc1"); sP += 5120; OPQ(sP);
  LD16(p[4], vP0, sP, 0, "sc1");
  SB();
  f32x2_t mx = xbcast2(xmax2(fmx(fmx(lg[0][0], lg[0][1]), fmx(lg[0][2], lg[0][3])), fmx(fmx(lg[1][0], lg[1][1]), fmx(lg[1][2], lg[1][3])))); PINV(mx);
  SB();
  LD16(p[5], vP1, sP, 0, "sc1");
  LD16(p[6], vP2, sP, 0, "sc1");
  LD4(fu, vFU, sFU, 0, "sc1");
  SB();
  { const float m0[4] = {mx[0], mx[0], mx[0], mx[0]}, m1_[4] = {mx[1], mx[1], mx[1], mx[1]};
    ACC4("v_subrev_f32", lg[0], m0); ACC4("v_subrev_f32", lg[1], m1_); }
  UN4("v_mul_f32", "0x3fb8aa3b, ", "", lg[0]); UN4("v_mul_f32", "0x3fb8aa3b, ", "", lg[1]);
  UN4("v_exp_f32", "", "", lg[0]); UN4("v_exp_f32", "", "", lg[1]);
  SB();
  SB();
  float e0, e1;
  asm volatile("v_add_f32 %0, %2, %3\n\tv_add_f32 %1, %6, %7\n\tv_add_f32 %0, %0, %4\n\tv_add_f32 %1, %1, %8\n\tv_add_f32 %0, %0, %5\n\tv_add_f32 %1, %1, %9\n\ts_nop 1"
               : "=&v"(e0), "=&v"(e1) : "v"(lg[0][0]), "v"(lg[0][1]), "v"(lg[0][2]), "v"(lg[0][3]), "v"(lg[1][0]), "v"(lg[1][1]), "v"(lg[1][2]), "v"(lg[1][3]));
  f32x2_t rs = xbcast2(__builtin_amdgcn_rcpf(xsum2(e0, e1))); PINV(rs);
  SB();
  SB();
  u32x4 pf[2];
  { const float r0_[4] = {rs[0], rs[0], rs[0], rs[0]}, r1_[4] = {rs[1], rs[1], rs[1], rs[1]};
    ACC4("v_mul_f32", lg[0], r0_); ACC4("v_mul_f32", lg[1], r1_); }
#pragma unroll
  for (int h = 0; h < 2; ++h) {
    const unsigned wa = pack2(lg[h][0], lg[h][1]), wb = pack2(lg[h][2], lg[h][3]);
    pf[h] = (u32x4){wa & ~mp[0], wb & ~mp[1], wa & mp[0], wb & mp[1]};
  }
  const f32x4 oa0 = MFMA16(vf[0][0], __builtin_bit_cast(bf16x8, pf[0]), z4), ob0 = MFMA16(vf[1][0], __builtin_bit_cast(bf16x8, pf[1]), z4);
  const f32x4 oa1 = MFMA16(vf[0][1], __builtin_bit_cast(bf16x8, pf[0]), z4), ob1 = MFMA16(vf[1][1], __builtin_bit_cast(bf16x8, pf[1]), z4);
  SB();
  SB();
  const u32x4 od0 = {pack2(oa0[0], oa0[1]), pack2(oa0[2], oa0[3]), pack2(ob0[0], ob0[1]), pack2(ob0[2], ob0[3])};
  ST16(vS0, sO, 0, od0);
  const u32x4 od1 = {pack2(oa1[0], oa1[1]), pack2(oa1[2], oa1[3]), pack2(ob1[0], ob1[1]), pack2(ob1[2], ob1[3])};
  ST16L48(vS1, sO, 0, od1);
  STAMP(38);
  WAITV1(2, fu);
  if (__ballot(fu != nonce) != 0ull) { WAITV(0); { const unsigned l_ = lane_now() * 16; ST16X(15, l_, sFO, nonce); } poll1<2>(sFU, vFU, nonce); }
  STAMP(51);
  WAITV(11);
  fw[6][0] = tr8u(rU0 + 0, 512);
  fw[6][1] = tr8u(rU1 + 0, 512);
  fw[6][2] = tr8u(rU2 + 0, 512);
  fw[6][3] = tr8u(rU3 + 0, 512);
  WAITV(0);
  { const unsigned l_ = lane_now() * 16; ST16X(15, l_, sFO, nonce); }
  STAMP(39);
  unsigned wa0 = ldsP + t7[0], wa1 = wa0 ^ 64, ra = rao + t7[1], rah = ra + 65536, fs, vFS;
  const unsigned vOut = t7[2], vSt = t7[3];
  asm volatile("" : "+v"(wa0), "+v"(wa1), "+v"(ra), "+v"(rah));
  f32x4 acc[8], t4[8]; float gy[4][4], zz[4];
  u32x4 xv[2], sv4[2]; f32x2_t xf2[8];
  SB();
  WAITVB(10); SB();
  STAMP(45);
  PRIO(2); SB();
  MV16Z(acc[0], p[0], fw[0][0]); SB();
  LD16(p[7], vP3, sP, 0, "sc1"); sP += 5120; OPQ(sP);
  SB();
  MV16Z(acc[1], p[0], fw[0][1]); SB();
  MV16Z(acc[2], p[0], fw[0][2]); SB();
  MV16Z(acc[3], p[0], fw[0][3]); SB();
  SB();
  WAITVB(10); SB();
  MV16(acc[0], p[1], fw[1][0]); SB();
  LD16(p[0], vP0, sP, 0, "sc1");
  SB();
  MV16(acc[1], p[1], fw[1][1]); SB();
  LD4(fu, vFU, sFU, 224, "sc1");
  SB();
  MV16(acc[2], p[1], fw[1][2]); SB();
  MV16(acc[3], p[1], fw[1][3]); SB();
  SB();
  WAITVB(11); SB();
  MV16(acc[0], p[2], fw[2][0]); SB();
  LD16(p[1], vP1, sP, 0, "sc1");
  SB();
  MV16(acc[1], p[2], fw[2][1]); SB();
  MV16(acc[2], p[2], fw[2][2]); SB();
  MV16(acc[3], p[2], fw[2][3]); SB();
  SB();
  WAITVB(11); SB();
  MV16(acc[0], p[3], fw[3][0]); SB();
  LD16(p[2], vP2, sP, 0, "sc1");
  SB();
  MV16(acc[1], p[3], fw[3][1]); SB();
  MV16(acc[2], p[3], fw[3][2]); SB();
  MV16(acc[3], p[3], fw[3][3]); SB();
  SB();
  WAITVB(11); SB();
  MV16(acc[0], p[4], fw[4][0]); SB();
  LD16(p[3], vP3, sP, 0, "sc1"); sP += 5120; OPQ(sP);
  SB();
  MV16(acc[1], p[4], fw[4][1]); SB();
  MV16(acc[2], p[4], fw[4][2]); SB();
  MV16(acc[3], p[4], fw[4][3]); SB();
  SB();
  WAITVB(11); SB();
  MV16(acc[0], p[5], fw[5][0]); SB();
  LD16(p[4], vP0, sP, 0, "sc1");
  SB();
  MV16(acc[1], p[5], fw[5][1]); SB();
  MV16(acc[2], p[5], fw[5][2]); SB();
  MV16(acc[3], p[5], fw[5][3]); SB();
  SB();
  WAITVB(11); SB();
  MV16(acc[0], p[6], fw[6][0]); SB();
  LD16(p[5], vP1, sP, 0, "sc1");
  SB();
  MV16(acc[1], p[6], fw[6][1]); SB();
  BAR0();
  SB();
  MV16(acc[2], p[6], fw[6][2]); SB();
  MV16(acc[3], p[6], fw[6][3]); SB();
  SB();
  WAITVB(7); SB();
  PRIO(1); SB();
  MV16Z(acc[4], p[7], fw[0][0]); SB();
  WAITV1(5, fu);
  if (__ballot(fu != nonce) != 0ull) poll1<2>(sFU + 224, vFU, nonce);
  STAMP(53);
  LD16(p[6], vP2, sP, 0, "sc1");
  SB();
  MV16Z(acc[5], p[7], fw[0][1]); SB();
  MV16Z(acc[6], p[7], fw[0][2]); SB();
  MV16Z(acc[7], p[7], fw[0][3]); SB();
  SB();
  WAITVB(7); SB();
  MV16(acc[4], p[0], fw[1][0]); SB();
  LD16(p[7], vP3, sP, 0, "sc1"); sP += 5120; OPQ(sP);
  SB();
  MV16(acc[5], p[0], fw[1][1]); SB();
  *(LDS_AS f32x4*)(wa0 + 0) = acc[0];
  SB();
  MV16(acc[6], p[0], fw[1][2]); SB();
  *(LDS_AS f32x4*)(wa0 + 2048) = acc[1];
  SB();
  MV16(acc[7], p[0], fw[1][3]); SB();
  LD4(fu, vFU, sFU, 448, "sc1");
  SB();
  SB();
  WAITVB(7); SB();
  PRIO(0); SB();
  MV16(acc[4], p[1], fw[2][0]); SB();
  LD16(p[0], vP0, sP, 0, "sc1");
  SB();
  MV16(acc[5], p[1], fw[2][1]); SB();
  *(LDS_AS f32x4*)(wa0 + 4096) = acc[2];
  SB();
  MV16(acc[6], p[1], fw[2][2]); SB();
  *(LDS_AS f32x4*)(wa0 + 6144) = acc[3];
  SB();
  MV16(acc[7], p[1], fw[2][3]); SB();
  SB();
  WAITVB(7); SB();
  MV16(acc[4], p[2], fw[3][0]); SB();
  LD16(p[1], vP1, sP, 0, "sc1");
  SB();
  MV16(acc[5], p[2], fw[3][1]); SB();
  BAR();
  STAMP(46);
  SB();
  MV16(acc[6], p[2], fw[3][2]); SB();
  MV16(acc[7], p[2], fw[3][3]); SB();
  SB();
  WAITVB(7); SB();
  PRIO(3); SB();
  MV16(acc[4], p[3], fw[4][0]); SB();
  LD16(p[2], vP2, sP, 0, "sc1");
  t4[0] = *(LDS_AS const f32x4*)(ra + 0);
  SB();
  MV16(acc[5], p[3], fw[4][1]); SB();
  t4[1] = *(LDS_AS const f32x4*)(ra + 16384);
  SB();
  MV16(acc[6], p[3], fw[4][2]); SB();
  t4[2] = *(LDS_AS const f32x4*)(ra + 32768);
  SB();
  MV16(acc[7], p[3], fw[4][3]); SB();
  t4[3] = *(LDS_AS const f32x4*)(ra + 49152);
  SB();
  SB();
  WAITVB(7); SB();
  MV16(acc[4], p[4], fw[5][0]); SB();
  LD16(p[3], vP3, sP, 0, "sc1"); sP += 5120; OPQ(sP);
  t4[4] = *(LDS_AS const f32x4*)(rah + 0);
  SB();
  MV16(acc[5], p[4], fw[5][1]); SB();
  t4[5] = *(LDS_AS const f32x4*)(rah + 16384);
  SB();
  MV16(acc[6], p[4], fw[5][2]); SB();
  t4[6] = *(LDS_AS const f32x4*)(rah + 32768);
  SB();
  MV16(acc[7], p[4], fw[5][3]); SB();
  t4[7] = *(LDS_AS const f32x4*)(rah + 49152);
  SB();
  SB();
  WAITVB(7); SB();
  MV16(acc[4], p[5], fw[6][0]); SB();
  LD16(p[4], vP0, sP, 0, "sc1");
  SB();
  MV16(acc[5], p[5], fw[6][1]); SB();
  OP4_3("v_add_f32", gy[3], t4[0], bias);
  SB();
  MV16(acc[6], p[5], fw[6][2]); SB();
  MV16(acc[7], p[5], fw[6][3]); SB();
  SB();
  WAITVB(7); SB();
  MV16Z(acc[0], p[6], fw[0][0]); SB();
  WAITV1(5, fu);
  if (__ballot(fu != nonce) != 0ull) poll1<2>(sFU + 448, vFU, nonce);
  STAMP(53);
  LD16(p[5], vP1, sP, 0, "sc1");
  SB();
  MV16Z(acc[1], p[6], fw[0][1]); SB();
  OP4_3("v_add_f32", zz, t4[1], t4[2]);
  SB();
  MV16Z(acc[2], p[6], fw[0][2]); SB();
  MV16Z(acc[3], p[6], fw[0][3]); SB();
  SB();
  WAITVB(7); SB();
  MV16(acc[0], p[7], fw[1][0]); SB();
  LD16(p[6], vP2, sP, 0, "sc1");
  SB();
  MV16(acc[1], p[7], fw[1][1]); SB();
  *(LDS_AS f32x4*)(wa0 + 8192) = acc[4];
  SB();
  MV16(acc[2], p[7], fw[1][2]); SB();
  LD4(fu, vFU, sFU, 672, "sc1");
  SB();
  MV16(acc[3], p[7], fw[1][3]); SB();
  ACC4("v_add_f32", gy[3], t4[3]);
  SB();
  SB();
  WAITVB(7); SB();
  PRIO(2); SB();
  MV16(acc[0], p[0], fw[2][0]); SB();
  LD16(p[7], vP3, sP, 0, "sc1"); sP += 5120; OPQ(sP);
  SB();
  MV16(acc[1], p[0], fw[2][1]); SB();
  *(LDS_AS f32x4*)(wa0 + 10240) = acc[5];
  SB();
  MV16(acc[2], p[0], fw[2][2]); SB();
  ACC4("v_add_f32", zz, t4[4]);
  SB();
  MV16(acc[3], p[0], fw[2][3]); SB();
  SB();
  WAITVB(7); SB();
  MV16(acc[0], p[1], fw[3][0]); SB();
  LD16(p[0], vP0, sP, 0, "sc1");
  SB();
  MV16(acc[1], p[1], fw[3][1]); SB();
  *(LDS_AS f32x4*)(wa0 + 12288) = acc[6];
  SB();
  MV16(acc[2], p[1], fw[3][2]); SB();
  ACC4("v_add_f32", gy[3], t4[5]);
  SB();
  MV16(acc[3], p[1], fw[3][3]); SB();
  SB();
  WAITVB(7); SB();
  MV16(acc[0], p[2], fw[4][0]); SB();
  LD16(p[1], vP1, sP, 0, "sc1");
  SB();
  MV16(acc[1], p[2], fw[4][1]); SB();
  *(LDS_AS f32x4*)(wa0 + 14336) = acc[7];
  SB();
  MV16(acc[2], p[2], fw[4][2]); SB();
  ACC4("v_add_f32", zz, t4[6]);
  SB();
  MV16(acc[3], p[2], fw[4][3]); SB();
  SB();
  WAITVB(7); SB();
  MV16(acc[0], p[3], fw[5][0]); SB();
  LD16(p[2], vP2, sP, 0, "sc1");
  SB();
  MV16(acc[1], p[3], fw[5][1]); SB();
  ACC4("v_add_f32", gy[3], t4[7]);
  SB();
  MV16(acc[2], p[3], fw[5][2]); SB();
  MV16(acc[3], p[3], fw[5][3]); SB();
  SB();
  WAITVB(7); SB();
  MV16(acc[0], p[4], fw[6][0]); SB();
  LD16(p[3], vP3, sP, 0, "sc1"); sP += 5120; OPQ(sP);
  SB();
  MV16(acc[1], p[4], fw[6][1]); SB();
  ACC4("v_add_f32", gy[3], zz);
  SB();
  MV16(acc[2], p[4], fw[6][2]); SB();
  MV16(acc[3], p[4], fw[6][3]); SB();
  SB();
  WAITVB(7); SB();
  PRIO(1); SB();
  MV16Z(acc[4], p[5], fw[0][0]); SB();
  WAITV1(5, fu);
  if (__ballot(fu != nonce) != 0ull) poll1<2>(sFU + 672, vFU, nonce);
  STAMP(53);
  LD16(p[4], vP0, sP, 0, "sc1");
  SB();
  MV16Z(acc[5], p[5], fw[0][1]); SB();
  SCL4(zz, gy[3]);
  SB();
  MV16Z(acc[6], p[5], fw[0][2]); SB();
  MV16Z(acc[7], p[5], fw[0][3]); SB();
  SB();
  WAITVB(7); SB();
  MV16(acc[4], p[6], fw[1][0]); SB();
  LD16(p[5], vP1, sP, 0, "sc1");
  *(LDS_AS f32x4*)(wa1 + 8192) = acc[0];
  SB();
  MV16(acc[5], p[6], fw[1][1]); SB();
  *(LDS_AS f32x4*)(wa1 + 10240) = acc[1];
  SB();
  MV16(acc[6], p[6], fw[1][2]); SB();
  LD4(fu, vFU, sFU, 896, "sc1");
  SB();
  MV16(acc[7], p[6], fw[1][3]); SB();
  UN4("v_exp_f32", "", "", zz);
  SB();
  SB();
  WAITVB(7); SB();
  PRIO(0); SB();
  MV16(acc[4], p[7], fw[2][0]); SB();
  LD16(p[6], vP2, sP, 0, "sc1");
  SB();
  MV16(acc[5], p[7], fw[2][1]); SB();
  *(LDS_AS f32x4*)(wa1 + 12288) = acc[2];
  SB();
  MV16(acc[6], p[7], fw[2][2]); SB();
  *(LDS_AS f32x4*)(wa1 + 14336) = acc[3];
  SB();
  MV16(acc[7], p[7], fw[2][3]); SB();
  UN4("v_add_f32", "1.0, ", "", zz);
  SB();
  SB();
  WAITVB(7); SB();
  MV16(acc[4], p[0], fw[3][0]); SB();
  LD16(p[7], vP3, sP, 0, "sc1"); sP += 5120; OPQ(sP);
  SB();
  MV16(acc[5], p[0], fw[3][1]); SB();
  UN4("v_rcp_f32", "", "", zz);
  SB();
  MV16(acc[6], p[0], fw[3][2]); SB();
  BAR();
  STAMP(47);
  SB();
  MV16(acc[7], p[0], fw[3][3]); SB();
  SB();
  WAITVB(7); SB();
  PRIO(3); SB();
  MV16(acc[4], p[1], fw[4][0]); SB();
  LD16(p[0], vP0, sP, 0, "sc1");
  ACC4("v_mul_f32", gy[3], zz);
  SB();
  MV16(acc[5], p[1], fw[4][1]); SB();
  t4[0] = *(LDS_AS const f32x4*)(ra + 8192);
  SB();
  MV16(acc[6], p[1], fw[4][2]); SB();
  t4[1] = *(LDS_AS const f32x4*)(ra + 24576);
  t4[2] = *(LDS_AS const f32x4*)(ra + 40960);
  SB();
  MV16(acc[7], p[1], fw[4][3]); SB();
  t4[3] = *(LDS_AS const f32x4*)(ra + 57344);
  SB();
  SB();
  WAITVB(7); SB();
  MV16(acc[4], p[2], fw[5][0]); SB();
  LD16(p[1], vP1, sP, 0, "sc1");
  t4[4] = *(LDS_AS const f32x4*)(rah + 8192);
  SB();
  MV16(acc[5], p[2], fw[5][1]); SB();
  t4[5] = *(LDS_AS const f32x4*)(rah + 24576);
  SB();
  MV16(acc[6], p[2], fw[5][2]); SB();
  t4[6] = *(LDS_AS const f32x4*)(rah + 40960);
  SB();
  MV16(acc[7], p[2], fw[5][3]); SB();
  t4[7] = *(LDS_AS const f32x4*)(rah + 57344);
  SB();
  SB();
  WAITVB(7); SB();
  MV16(acc[4], p[3], fw[6][0]); SB();
  LD16(p[2], vP2, sP, 0, "sc1");
  SB();
  MV16(acc[5], p[3], fw[6][1]); SB();
  OP4_3("v_add_f32", gy[0], t4[0], bias);
  SB();
  MV16(acc[6], p[3], fw[6][2]); SB();
  MV16(acc[7], p[3], fw[6][3]); SB();
  SB();
  WAITVB(7); SB();
  MV16Z(acc[0], p[4], fw[0][0]); SB();
  WAITV1(5, fu);
  if (__ballot(fu != nonce) != 0ull) poll1<2>(sFU + 896, vFU, nonce);
  STAMP(53);
  LD16(p[3], vP3, sP, 0, "sc1"); sP += 5120; OPQ(sP);
  SB();
  MV16Z(acc[1], p[4], fw[0][1]); SB();
  OP4_3("v_add_f32", zz, t4[1], t4[2]);
  SB();
  MV16Z(acc[2], p[4], fw[0][2]); SB();
  MV16Z(acc[3], p[4], fw[0][3]); SB();
  SB();
  WAITVB(7); SB();
  MV16(acc[0], p[5], fw[1][0]); SB();
  LD16(p[4], vP0, sP, 0, "sc1");
  SB();
  MV16(acc[1], p[5], fw[1][1]); SB();
  *(LDS_AS f32x4*)(wa0 + 0) = acc[4];
  SB();
  MV16(acc[2], p[5], fw[1][2]); SB();
  LD4(fu, vFU, sFU, 1120, "sc1");
  SB();
  MV16(acc[3], p[5], fw[1][3]); SB();
  ACC4("v_add_f32", gy[0], t4[3]);
  SB();
  SB();
  WAITVB(7); SB();
  PRIO(2); SB();
  MV16(acc[0], p[6], fw[2][0]); SB();
  LD16(p[5], vP1, sP, 0, "sc1");
  SB();
  MV16(acc[1], p[6], fw[2][1]); SB();
  *(LDS_AS f32x4*)(wa0 + 2048) = acc[5];
  SB();
  MV16(acc[2], p[6], fw[2][2]); SB();
  ACC4("v_add_f32", zz, t4[4]);
  SB();
  MV16(acc[3], p[6], fw[2][3]); SB();
  SB();
  WAITVB(7); SB();
  MV16(acc[0], p[7], fw[3][0]); SB();
  LD16(p[6], vP2, sP, 0, "sc1");
  SB();
  MV16(acc[1], p[7], fw[3][1]); SB();
  *(LDS_AS f32x4*)(wa0 + 4096) = acc[6];
  SB();
  MV16(acc[2], p[7], fw[3][2]); SB();
  ACC4("v_add_f32", gy[0], t4[5]);
  SB();
  MV16(acc[3], p[7], fw[3][3]); SB();
  SB();
  WAITVB(7); SB();
  MV16(acc[0], p[0], fw[4][0]); SB();
  LD16(p[7], vP3, sP, 0, "sc1"); sP += 5120; OPQ(sP);
  SB();
  MV16(acc[1], p[0], fw[4][1]); SB();
  *(LDS_AS f32x4*)(wa0 + 6144) = acc[7];
  SB();
  MV16(acc[2], p[0], fw[4][2]); SB();
  ACC4("v_add_f32", zz, t4[6]);
  SB();
  MV16(acc[3], p[0], fw[4][3]); SB();
  SB();
  WAITVB(7); SB();
  MV16(acc[0], p[1], fw[5][0]); SB();
  LD16(p[0], vP0, sP, 0, "sc1");
  SB();
  MV16(acc[1], p[1], fw[5][1]); SB();
  ACC4("v_add_f32", gy[0], t4[7]);
  SB();
  MV16(acc[2], p[1], fw[5][2]); SB();
  MV16(acc[3], p[1], fw[5][3]); SB();
  SB();
  WAITVB(7); SB();
  MV16(acc[0], p[2], fw[6][0]); SB();
  LD16(p[1], vP1, sP, 0, "sc1");
  SB();
  MV16(acc[1], p[2], fw[6][1]); SB();
  ACC4("v_add_f32", gy[0], zz);
  SB();
  MV16(acc[2], p[2], fw[6][2]); SB();
  MV16(acc[3], p[2], fw[6][3]); SB();
  SB();
  WAITVB(7); SB();
  PRIO(1); SB();
  MV16Z(acc[4], p[3], fw[0][0]); SB();
  WAITV1(5, fu);
  if (__ballot(fu != nonce) != 0ull) poll1<2>(sFU + 1120, vFU, nonce);
  STAMP(43);
  LD16(p[2], vP2, sP, 0, "sc1");
  SB();
  MV16Z(acc[5], p[3], fw[0][1]); SB();
  SCL4(zz, gy[0]);
  SB();
  MV16Z(acc[6], p[3], fw[0][2]); SB();
  MV16Z(acc[7], p[3], fw[0][3]); SB();
  SB();
  WAITVB(7); SB();
  MV16(acc[4], p[4], fw[1][0]); SB();
  LD16(p[3], vP3, sP, 0, "sc1"); sP += 5120; OPQ(sP);
  *(LDS_AS f32x4*)(wa1 + 0) = acc[0];
  SB();
  MV16(acc[5], p[4], fw[1][1]); SB();
  *(LDS_AS f32x4*)(wa1 + 2048) = acc[1];
  SB();
  MV16(acc[6], p[4], fw[1][2]); SB();
  unsigned c6; { const int l_ = lane_now(); c6 = (l_ & 6) == 6 ? 32 : 0; vFS = (l_ < 7 ? l_ : 6) * FS; asm volatile("" : "+v"(c6), "+v"(vFS)); }
  LD16(xv[0], vOut, sXo, 0, "");
  LD16(xv[1], vOut - c6, sXo, 128, "");
  SB();
  MV16(acc[7], p[4], fw[1][3]); SB();
  UN4("v_exp_f32", "", "", zz);
  SB();
  SB();
  WAITVB(8); SB();
  PRIO(0); SB();
  MV16(acc[4], p[5], fw[2][0]); SB();
  LD16(p[4], vP0, sP, 0, "sc1");
  SB();
  MV16(acc[5], p[5], fw[2][1]); SB();
  *(LDS_AS f32x4*)(wa1 + 4096) = acc[2];
  SB();
  MV16(acc[6], p[5], fw[2][2]); SB();
  *(LDS_AS f32x4*)(wa1 + 6144) = acc[3];
  SB();
  MV16(acc[7], p[5], fw[2][3]); SB();
  UN4("v_add_f32", "1.0, ", "", zz);
  SB();
  SB();
  WAITVB(8); SB();
  MV16(acc[4], p[6], fw[3][0]); SB();
  LD16(p[5], vP1, sP, 0, "sc1");
  SB();
  MV16(acc[5], p[6], fw[3][1]); SB();
  UN4("v_rcp_f32", "", "", zz);
  SB();
  MV16(acc[6], p[6], fw[3][2]); SB();
  BAR();
  STAMP(48);
  SB();
  MV16(acc[7], p[6], fw[3][3]); SB();
  SB();
  WAITVB(8); SB();
  PRIO(3); SB();
  MV16(acc[4], p[7], fw[4][0]); SB();
  LD16(p[6], vP2, sP, 0, "sc1");
  ACC4("v_mul_f32", gy[0], zz);
  SB();
  MV16(acc[5], p[7], fw[4][1]); SB();
  t4[0] = *(LDS_AS const f32x4*)(ra + 0);
  SB();
  MV16(acc[6], p[7], fw[4][2]); SB();
  t4[1] = *(LDS_AS const f32x4*)(ra + 16384);
  t4[2] = *(LDS_AS const f32x4*)(ra + 32768);
  SB();
  MV16(acc[7], p[7], fw[4][3]); SB();
  t4[3] = *(LDS_AS const f32x4*)(ra + 49152);
  SB();
  SB();
  WAITVB(8); SB();
  PRIO(2); SB();
  MV16(acc[4], p[0], fw[5][0]); SB();
  LD16(p[7], vP3, sP, 0, "sc1"); sP += 5120; OPQ(sP);
  t4[4] = *(LDS_AS const f32x4*)(rah + 0);
  SB();
  MV16(acc[5], p[0], fw[5][1]); SB();
  t4[5] = *(LDS_AS const f32x4*)(rah + 16384);
  SB();
  MV16(acc[6], p[0], fw[5][2]); SB();
  t4[6] = *(LDS_AS const f32x4*)(rah + 32768);
  SB();
  MV16(acc[7], p[0], fw[5][3]); SB();
  t4[7] = *(LDS_AS const f32x4*)(rah + 49152);
  SB();
  SB();
  WAITVB(8); SB();
  MV16(acc[4], p[1], fw[6][0]); SB();
  LD16(p[0], vP0, sP, 0, "sc1");
  SB();
  MV16(acc[5], p[1], fw[6][1]); SB();
  LD4(fs, vFS, sFS, 0, "sc1");
  SB();
  MV16(acc[6], p[1], fw[6][2]); SB();
  OP4_3("v_add_f32", gy[1], t4[0], bias);
  SB();
  MV16(acc[7], p[1], fw[6][3]); SB();
  SB();
  WAITVB(9); SB();
  MV16Z(acc[0], p[2], fw[0][0]); SB();
  OP4_3("v_add_f32", zz, t4[1], t4[2]);
  SB();
  MV16Z(acc[1], p[2], fw[0][1]); SB();
  ACC4("v_add_f32", gy[1], t4[3]);
  SB();
  MV16Z(acc[2], p[2], fw[0][2]); SB();
  MV16Z(acc[3], p[2], fw[0][3]); SB();
  SB();
  WAITVB(8); SB();
  MV16(acc[0], p[3], fw[1][0]); SB();
  *(LDS_AS f32x4*)(wa0 + 8192) = acc[4];
  SB();
  MV16(acc[1], p[3], fw[1][1]); SB();
  ACC4("v_add_f32", zz, t4[4]);
  SB();
  MV16(acc[2], p[3], fw[1][2]); SB();
  ACC4("v_add_f32", gy[1], t4[5]);
  SB();
  MV16(acc[3], p[3], fw[1][3]); SB();
  SB();
  WAITVB(5); SB();
  MV16(acc[0], p[4], fw[2][0]); SB();
  *(LDS_AS f32x4*)(wa0 + 10240) = acc[5];
  SB();
  MV16(acc[1], p[4], fw[2][1]); SB();
  ACC4("v_add_f32", zz, t4[6]);
  SB();
  MV16(acc[2], p[4], fw[2][2]); SB();
  ACC4("v_add_f32", gy[1], t4[7]);
  SB();
  MV16(acc[3], p[4], fw[2][3]); SB();
  SB();
  WAITVB(4); SB();
  PRIO(1); SB();
  MV16(acc[0], p[5], fw[3][0]); SB();
  *(LDS_AS f32x4*)(wa0 + 12288) = acc[6];
  SB();
  MV16(acc[1], p[5], fw[3][1]); SB();
  ACC4("v_add_f32", gy[1], zz);
  SB();
  MV16(acc[2], p[5], fw[3][2]); SB();
  MV16(acc[3], p[5], fw[3][3]); SB();
  SB();
  WAITVB(3); SB();
  MV16(acc[0], p[6], fw[4][0]); SB();
  *(LDS_AS f32x4*)(wa0 + 14336) = acc[7];
  SB();
  MV16(acc[1], p[6], fw[4][1]); SB();
  SCL4(zz, gy[1]);
  SB();
  MV16(acc[2], p[6], fw[4][2]); SB();
  UN4("v_exp_f32", "", "", zz);
  SB();
  MV16(acc[3], p[6], fw[4][3]); SB();
  SB();
  WAITVB(2); SB();
  PRIO(0); SB();
  MV16(acc[0], p[7], fw[5][0]); SB();
  WAITV1(7, xv[0]);
  xf2[0] = (f32x2_t){lo2f(xv[0][0]), hi2f(xv[0][0])}; PINV(xf2[0]);
  xf2[1] = (f32x2_t){lo2f(xv[0][1]), hi2f(xv[0][1])}; PINV(xf2[1]);
  xf2[2] = (f32x2_t){lo2f(xv[0][2]), hi2f(xv[0][2])}; PINV(xf2[2]);
  xf2[3] = (f32x2_t){lo2f(xv[0][3]), hi2f(xv[0][3])}; PINV(xf2[3]);
  SB();
  MV16(acc[1], p[7], fw[5][1]); SB();
  WAITV1(0, fs);
  if (__ballot(fs != nonce) != 0ull) poll1<4>(sFS, vFS, nonce);
  STAMP(40);
  LD16(sv4[0], vSt, sS, 0, "sc1");
  { const char* s_ = sS + 12288; LD16(sv4[1], vSt - c6 * 96, s_, 0, "sc1"); }
  SB();
  MV16(acc[2], p[7], fw[5][2]); SB();
  UN4("v_add_f32", "1.0, ", "", zz);
  SB();
  MV16(acc[3], p[7], fw[5][3]); SB();
  UN4("v_rcp_f32", "", "", zz);
  SB();
  SB();
  WAITVB(3); SB();
  MV16(acc[0], p[0], fw[6][0]); SB();
  WAITV1(8, xv[1]);
  xf2[4] = (f32x2_t){lo2f(xv[1][0]), hi2f(xv[1][0])}; PINV(xf2[4]);
  xf2[5] = (f32x2_t){lo2f(xv[1][1]), hi2f(xv[1][1])}; PINV(xf2[5]);
  xf2[6] = (f32x2_t){lo2f(xv[1][2]), hi2f(xv[1][2])}; PINV(xf2[6]);
  xf2[7] = (f32x2_t){lo2f(xv[1][3]), hi2f(xv[1][3])}; PINV(xf2[7]);
  SB();
  MV16(acc[1], p[0], fw[6][1]); SB();
  ACC4("v_mul_f32", gy[1], zz);
  SB();
  MV16(acc[2], p[0], fw[6][2]); SB();
  xchg4(gy[0], gy[1]);
  SB();
  MV16(acc[3], p[0], fw[6][3]); SB();
  NOP(4); SB();
  *(LDS_AS f32x4*)(wa1 + 8192) = acc[0];
  SB();
  *(LDS_AS f32x4*)(wa1 + 10240) = acc[1];
  SB();
  *(LDS_AS f32x4*)(wa1 + 12288) = acc[2];
  SB();
  *(LDS_AS f32x4*)(wa1 + 14336) = acc[3];
  SB();
  BAR();
  STAMP(49);
  t4[0] = *(LDS_AS const f32x4*)(ra + 8192);
  t4[1] = *(LDS_AS const f32x4*)(ra + 24576);
  t4[2] = *(LDS_AS const f32x4*)(ra + 40960);
  t4[3] = *(LDS_AS const f32x4*)(ra + 57344);
  t4[4] = *(LDS_AS const f32x4*)(rah + 8192);
  t4[5] = *(LDS_AS const f32x4*)(rah + 24576);
  t4[6] = *(LDS_AS const f32x4*)(rah + 40960);
  t4[7] = *(LDS_AS const f32x4*)(rah + 57344);
  SB();
  WAITV1(1, sv4[0]);
  ST16(vOut, sOut, 0, ((u32x4){fin2(sv4[0][0], gy[0][0], gy[0][1], xf2[0]), fin2(sv4[0][1], gy[0][2], gy[0][3], xf2[1]), fin2(sv4[0][2], gy[1][0], gy[1][1], xf2[2]), fin2(sv4[0][3], gy[1][2], gy[1][3], xf2[3])}));
  SB();
  { f32x4 y_ = ((t4[0] + (f32x4){bias[0], bias[1], bias[2], bias[3]}) + (t4[1] + t4[2])) + ((t4[3] + t4[4]) + ((t4[5] + t4[6]) + t4[7]));
    f32x4 z_ = y_ * -1.442695041f;
    z_ = (f32x4){__builtin_amdgcn_exp2f(z_[0]), __builtin_amdgcn_exp2f(z_[1]), __builtin_amdgcn_exp2f(z_[2]), __builtin_amdgcn_exp2f(z_[3])} + 1.f;
    z_ = (f32x4){__builtin_amdgcn_rcpf(z_[0]), __builtin_amdgcn_rcpf(z_[1]), __builtin_amdgcn_rcpf(z_[2]), __builtin_amdgcn_rcpf(z_[3])};
    y_ = y_ * z_;
    gy[2][0] = y_[0]; gy[2][1] = y_[1]; gy[2][2] = y_[2]; gy[2][3] = y_[3]; }
  xchg4(gy[2], gy[3]);
  STAMP(41);
  WAITV1(1, sv4[1]);
  { const char* o_ = sOut + 128; ST16E(0x3f3f3f3f, vOut, o_, ((u32x4){fin2(sv4[1][0], gy[2][0], gy[2][1], xf2[4]), fin2(sv4[1][1], gy[2][2], gy[2][3], xf2[5]), fin2(sv4[1][2], gy[3][0], gy[3][1], xf2[6]), fin2(sv4[1][3], gy[3][2], gy[3][3], xf2[7])})); }
  STAMP(42);
  STAMP(50);
  STAMP_DUMP();

  } else {
  if (__builtin_expect(jb < 28, 1)) {
  if (w < 4) {
  DMA2(vWa, vWb, sW, ldsW, 6144, ""); sW = wrap == 1 ? bW : sW + dW; OPQ(sW);
  { int w_ = w; OPQ(w_); { const int w = w_ & 7, lane = lane_now(); const int n16 = lane & 15, g4 = lane >> 4, q4 = n16 >> 2, pq = lane & 3, r8 = lane >> 3; const unsigned vL16 = lane * 16;
  const unsigned vFH = (lane & 31) * FA;
  bf16x8 fw[8][4], p[9]; f32x4 acc[8], t4[8]; float mm[4], zz[4], bias[4], c23 = 2.3022082f;
  unsigned rW0 = ldsP + (8 * g4 + q4) * 128 + (((q4 >> 1) | (2 * (g4 & 1))) * 32) + pq * 8, rW1 = rW0 ^ 32, rW2 = rW0 ^ 64, rW3 = rW0 ^ 96;
  asm volatile("" : "+v"(rW0), "+v"(rW1), "+v"(rW2), "+v"(rW3));
  unsigned vP0 = frag(0, lane), vP1 = frag(1, lane), vP2 = frag(2, lane), vP3 = frag(3, lane);
  const char* sP = A.ws + (WS_HT + o_h(dg) + w * 10240);
  asm volatile("" : "+s"(sP)); asm volatile("" : "+v"(vP0), "+v"(vP1), "+v"(vP2), "+v"(vP3));
  WAITV(5);
  fw[0][0] = tr8u(rW0 + 0, 512);
  fw[0][1] = tr8u(rW1 + 0, 512);
  fw[0][2] = tr8u(rW2 + 0, 512);
  fw[0][3] = tr8u(rW3 + 0, 512);
  DMA2(vWa, vWb, sW, ldsW, 8192, ""); sW += dW; OPQ(sW);
  DMA2(vWa, vWb, sW, ldsW, 10240, ""); sW = wrap == 2 ? bW : sW + dW; OPQ(sW);
  WAITV(4);
  fw[1][0] = tr8u(rW0 + 4096, 512);
  fw[1][1] = tr8u(rW1 + 4096, 512);
  fw[1][2] = tr8u(rW2 + 4096, 512);
  fw[1][3] = tr8u(rW3 + 4096, 512);
  DMA2(vWa, vWb, sW, ldsW, 12288, ""); sW += dW; OPQ(sW);
  DMA2(vWa, vWb, sW, ldsW, 14336, ""); sW = wrap == 3 ? bW : sW + dW; OPQ(sW);
  WAITV(4);
  fw[2][0] = tr8u(rW0 + 8192, 512);
  fw[2][1] = tr8u(rW1 + 8192, 512);
  fw[2][2] = tr8u(rW2 + 8192, 512);
  fw[2][3] = tr8u(rW3 + 8192, 512);
  WAITL(0);
  DMA2(vWa, vWb, sW, ldsW, 0, ""); sW += dW; OPQ(sW);
  DMA2(vWa, vWb, sW, ldsW, 2048, ""); sW = wrap == 4 ? bW : sW + dW; OPQ(sW);
  WAITV(4);
  fw[3][0] = tr8u(rW0 + 12288, 512);
  fw[3][1] = tr8u(rW1 + 12288, 512);
  fw[3][2] = tr8u(rW2 + 12288, 512);
  fw[3][3] = tr8u(rW3 + 12288, 512);
  STAMP(1);
  WAITL(0);
  DMA2(vWa, vWb, sW, ldsW, 4096, ""); sW += dW; OPQ(sW);
  DMA2(vWa, vWb, sW, ldsW, 6144, ""); sW = wrap == 5 ? bW : sW + dW; OPQ(sW);
  bias[0] = lo2f(bv[0]); bias[1] = hi2f(bv[0]); bias[2] = lo2f(bv[1]); bias[3] = hi2f(bv[1]);
  unsigned wa0 = ldsP + n16 * 256 + ((g4 ^ n16) << 4), wa1 = wa0 ^ 64, wa2 = wa0 ^ 128, wa3 = wa0 ^ 192;
  unsigned ra = lds0 + (16 * 0 + n16) * 256 + (((4 * (w & 3) + g4) ^ n16) << 4), rah = ra + 65536, uL = 32 * (w & 1) + 16 * (g4 >> 1) + n16, vU;
  const int kap = 2 * ft + ((w >> 1) & 1), wv = (kap * 37) >> 8, us = kap - 7 * wv, up_ = us - KROT2 * dg;
  int up = up_ < 0 ? up_ + 7 : up_, mU = ((w - 4) >> 31) & 0xffff;
  const char* sU = A.ws + (WS_UT + o_4(dg * 8 + wv));
  const char* sFU = A.ws + WS_FLAGU + ((dg * 8 + wv) * 128 + 0 * 14 + us * 2 + (w & 1)) * FU;
  asm volatile("" : "+s"(sU), "+s"(sFU), "+s"(up), "+s"(mU)); asm volatile("" : "+v"(wa0), "+v"(wa1), "+v"(wa2), "+v"(wa3), "+v"(ra), "+v"(rah), "+v"(uL), "+v"(bias[0]), "+v"(bias[1]), "+v"(bias[2]), "+v"(bias[3]), "+v"(c23));
  WAITV(4);
  fw[4][0] = tr8u(rW0 + 0, 512);
  fw[4][1] = tr8u(rW1 + 0, 512);
  fw[4][2] = tr8u(rW2 + 0, 512);
  fw[4][3] = tr8u(rW3 + 0, 512);
  WAITL(0);
  DMA2(vWa, vWb, sW, ldsW, 8192, ""); sW += dW; OPQ(sW);
  LD4(fh, vFH, sFH, 0, "sc1");
  DMA2(vWa, vWb, sW, ldsW, 10240, ""); sW = wrap == 6 ? bW : sW + dW; OPQ(sW);
  WAITV(5);
  fw[5][0] = tr8u(rW0 + 4096, 512);
  fw[5][1] = tr8u(rW1 + 4096, 512);
  fw[5][2] = tr8u(rW2 + 4096, 512);
  fw[5][3] = tr8u(rW3 + 4096, 512);
  WAITL(0);
  DMA2(vWa, vWb, sW, ldsW, 12288, ""); sW += dW; OPQ(sW);
  DMA2(vWa, vWb, sW, ldsW, 14336, "");
  WAITV(4);
  fw[6][0] = tr8u(rW0 + 8192, 512);
  fw[6][1] = tr8u(rW1 + 8192, 512);
  fw[6][2] = tr8u(rW2 + 8192, 512);
  fw[6][3] = tr8u(rW3 + 8192, 512);
  WAITV1(6, fh);
  if (__ballot(fh != nonce) != 0ull) poll1<16>(sFH, vFH, nonce);
  STAMP(3);
  LD16(p[0], vP0, sP, 0, "sc1");
  LD16(p[1], vP1, sP, 0, "sc1");
  LD16(p[2], vP2, sP, 0, "sc1");
  LD16(p[3], vP3, sP, 0, "sc1"); sP += 5120; OPQ(sP);
  WAITV(4);
  fw[7][0] = tr8u(rW0 + 12288, 512);
  fw[7][1] = tr8u(rW1 + 12288, 512);
  fw[7][2] = tr8u(rW2 + 12288, 512);
  fw[7][3] = tr8u(rW3 + 12288, 512);
  STAMP(2);
  LD16(p[4], vP0, sP, 0, "sc1");
  LD16(p[5], vP1, sP, 0, "sc1");
  LD16(p[6], vP2, sP, 0, "sc1");
  LD16(p[7], vP3, sP, 0, "sc1"); sP += HU - 5120; OPQ(sP);
  SB();
  WAITVB(7); SB();
  STAMP(4);
  PRIO(3); SB();
  MV16Z(acc[0], fw[0][0], p[0]); SB();
  LD16(p[8], vP0, sP, 0, "sc1");
  SB();
  MV16Z(acc[1], fw[0][1], p[0]); SB();
  MV16Z(acc[2], fw[0][2], p[0]); SB();
  MV16Z(acc[3], fw[0][3], p[0]); SB();
  SB();
  WAITVB(7); SB();
  MV16(acc[0], fw[1][0], p[1]); SB();
  LD16(p[0], vP1, sP, 0, "sc1");
  SB();
  MV16(acc[1], fw[1][1], p[1]); SB();
  MV16(acc[2], fw[1][2], p[1]); SB();
  MV16(acc[3], fw[1][3], p[1]); SB();
  SB();
  WAITVB(7); SB();
  MV16(acc[0], fw[2][0], p[2]); SB();
  LD16(p[1], vP2, sP, 0, "sc1");
  SB();
  MV16(acc[1], fw[2][1], p[2]); SB();
  MV16(acc[2], fw[2][2], p[2]); SB();
  MV16(acc[3], fw[2][3], p[2]); SB();
  SB();
  WAITVB(7); SB();
  MV16(acc[0], fw[3][0], p[3]); SB();
  LD16(p[2], vP3, sP, 0, "sc1"); sP += 5120; OPQ(sP);
  SB();
  MV16(acc[1], fw[3][1], p[3]); SB();
  MV16(acc[2], fw[3][2], p[3]); SB();
  MV16(acc[3], fw[3][3], p[3]); SB();
  SB();
  WAITVB(7); SB();
  MV16(acc[0], fw[4][0], p[4]); SB();
  LD16(p[3], vP0, sP, 0, "sc1");
  SB();
  MV16(acc[1], fw[4][1], p[4]); SB();
  MV16(acc[2], fw[4][2], p[4]); SB();
  MV16(acc[3], fw[4][3], p[4]); SB();
  SB();
  WAITVB(7); SB();
  MV16(acc[0], fw[5][0], p[5]); SB();
  LD16(p[4], vP1, sP, 0, "sc1");
  SB();
  MV16(acc[1], fw[5][1], p[5]); SB();
  MV16(acc[2], fw[5][2], p[5]); SB();
  MV16(acc[3], fw[5][3], p[5]); SB();
  SB();
  WAITVB(7); SB();
  PRIO(2); SB();
  MV16(acc[0], fw[6][0], p[6]); SB();
  LD16(p[5], vP2, sP, 0, "sc1");
  SB();
  MV16(acc[1], fw[6][1], p[6]); SB();
  MV16(acc[2], fw[6][2], p[6]); SB();
  MV16(acc[3], fw[6][3], p[6]); SB();
  SB();
  WAITVB(7); SB();
  MV16(acc[0], fw[7][0], p[7]); SB();
  LD16(p[6], vP3, sP, 0, "sc1"); sP += HU - 5120; OPQ(sP);
  SB();
  MV16(acc[1], fw[7][1], p[7]); SB();
  MV16(acc[2], fw[7][2], p[7]); SB();
  MV16(acc[3], fw[7][3], p[7]); SB();
  SB();
  WAITVB(7); SB();
  MV16Z(acc[4], fw[0][0], p[8]); SB();
  LD16(p[7], vP0, sP, 0, "sc1");
  SB();
  MV16Z(acc[5], fw[0][1], p[8]); SB();
  MV16Z(acc[6], fw[0][2], p[8]); SB();
  MV16Z(acc[7], fw[0][3], p[8]); SB();
  SB();
  WAITVB(7); SB();
  PRIO(1); SB();
  MV16(acc[4], fw[1][0], p[0]); SB();
  LD16(p[8], vP1, sP, 0, "sc1");
  SB();
  MV16(acc[5], fw[1][1], p[0]); SB();
  *(LDS_AS f32x4*)(wa0 + 0) = acc[0];
  SB();
  MV16(acc[6], fw[1][2], p[0]); SB();
  *(LDS_AS f32x4*)(wa1 + 0) = acc[1];
  SB();
  MV16(acc[7], fw[1][3], p[0]); SB();
  SB();
  WAITVB(7); SB();
  PRIO(0); SB();
  MV16(acc[4], fw[2][0], p[1]); SB();
  LD16(p[0], vP2, sP, 0, "sc1");
  SB();
  MV16(acc[5], fw[2][1], p[1]); SB();
  *(LDS_AS f32x4*)(wa2 + 0) = acc[2];
  SB();
  MV16(acc[6], fw[2][2], p[1]); SB();
  *(LDS_AS f32x4*)(wa3 + 0) = acc[3];
  SB();
  MV16(acc[7], fw[2][3], p[1]); SB();
  BAR();
  STAMP(5);
  SB();
  SB();
  WAITVB(7); SB();
  PRIO(3); SB();
  MV16(acc[4], fw[3][0], p[2]); SB();
  LD16(p[1], vP3, sP, 0, "sc1"); sP += 5120; OPQ(sP);
  t4[0] = *(LDS_AS const f32x4*)(ra + 0);
  SB();
  MV16(acc[5], fw[3][1], p[2]); SB();
  t4[1] = *(LDS_AS const f32x4*)(ra + 16384);
  SB();
  MV16(acc[6], fw[3][2], p[2]); SB();
  t4[2] = *(LDS_AS const f32x4*)(ra + 32768);
  SB();
  MV16(acc[7], fw[3][3], p[2]); SB();
  t4[3] = *(LDS_AS const f32x4*)(ra + 49152);
  SB();
  SB();
  WAITVB(7); SB();
  MV16(acc[4], fw[4][0], p[3]); SB();
  LD16(p[2], vP0, sP, 0, "sc1");
  t4[4] = *(LDS_AS const f32x4*)(rah + 0);
  SB();
  MV16(acc[5], fw[4][1], p[3]); SB();
  t4[5] = *(LDS_AS const f32x4*)(rah + 16384);
  SB();
  MV16(acc[6], fw[4][2], p[3]); SB();
  t4[6] = *(LDS_AS const f32x4*)(rah + 32768);
  t4[7] = *(LDS_AS const f32x4*)(rah + 49152);
  SB();
  MV16(acc[7], fw[4][3], p[3]); SB();
  { const int ix = 0 + up; vU = (ix >> 2) * 5120 + frag(ix & 3, uL); PINV(vU); }
  SB();
  SB();
  WAITVB(7); SB();
  MV16(acc[4], fw[5][0], p[4]); SB();
  LD16(p[3], vP1, sP, 0, "sc1");
  OP4_3("v_add_f32", mm, t4[0], bias);
  SB();
  MV16(acc[5], fw[5][1], p[4]); SB();
  OP4_3("v_add_f32", zz, t4[1], t4[2]);
  SB();
  MV16(acc[6], fw[5][2], p[4]); SB();
  ACC4("v_add_f32", mm, t4[3]);
  SB();
  MV16(acc[7], fw[5][3], p[4]); SB();
  ACC4("v_add_f32", zz, t4[4]);
  SB();
  SB();
  WAITVB(7); SB();
  PRIO(2); SB();
  MV16(acc[4], fw[6][0], p[5]); SB();
  LD16(p[4], vP2, sP, 0, "sc1");
  ACC4("v_add_f32", mm, t4[5]);
  SB();
  MV16(acc[5], fw[6][1], p[5]); SB();
  ACC4("v_add_f32", zz, t4[6]);
  SB();
  MV16(acc[6], fw[6][2], p[5]); SB();
  ACC4("v_add_f32", mm, t4[7]);
  SB();
  MV16(acc[7], fw[6][3], p[5]); SB();
  ACC4("v_add_f32", mm, zz);
  SB();
  SB();
  WAITVB(7); SB();
  MV16(acc[4], fw[7][0], p[6]); SB();
  LD16(p[5], vP3, sP, 0, "sc1"); sP += HU - 5120; OPQ(sP);
  OP4_3("v_mul_f32", zz, mm, mm);
  SB();
  MV16(acc[5], fw[7][1], p[6]); SB();
  POLY4(zz, c23);
  SB();
  MV16(acc[6], fw[7][2], p[6]); SB();
  ACC4("v_mul_f32", zz, mm);
  SB();
  MV16(acc[7], fw[7][3], p[6]); SB();
  UN4("v_exp_f32", "", "", zz);
  SB();
  SB();
  WAITVB(7); SB();
  MV16Z(acc[0], fw[0][0], p[7]); SB();
  LD16(p[6], vP0, sP, 0, "sc1");
  UN4("v_add_f32", "1.0, ", "", zz);
  SB();
  MV16Z(acc[1], fw[0][1], p[7]); SB();
  UN4("v_rcp_f32", "", "", zz);
  SB();
  MV16Z(acc[2], fw[0][2], p[7]); SB();
  NFMA4(mm, zz);
  SB();
  MV16Z(acc[3], fw[0][3], p[7]); SB();
  { const unsigned x0 = pack2(mm[0], mm[1]), x1 = pack2(mm[2], mm[3]);
    const auto r0 = __builtin_amdgcn_permlane16_swap(x0, x0, false, false), r1 = __builtin_amdgcn_permlane16_swap(x1, x1, false, false);
    ST16E(0xffff, vU, sU, ((u32x4){r0[0], r1[0], r0[1], r1[1]})); }
  SB();
  SB();
  WAITVB(8); SB();
  PRIO(1); SB();
  MV16(acc[0], fw[1][0], p[8]); SB();
  LD16(p[7], vP1, sP, 0, "sc1");
  SB();
  MV16(acc[1], fw[1][1], p[8]); SB();
  *(LDS_AS f32x4*)(wa0 + 8192) = acc[4];
  SB();
  MV16(acc[2], fw[1][2], p[8]); SB();
  *(LDS_AS f32x4*)(wa1 + 8192) = acc[5];
  SB();
  MV16(acc[3], fw[1][3], p[8]); SB();
  SB();
  WAITVB(8); SB();
  PRIO(0); SB();
  MV16(acc[0], fw[2][0], p[0]); SB();
  LD16(p[8], vP2, sP, 0, "sc1");
  SB();
  MV16(acc[1], fw[2][1], p[0]); SB();
  *(LDS_AS f32x4*)(wa2 + 8192) = acc[6];
  SB();
  MV16(acc[2], fw[2][2], p[0]); SB();
  *(LDS_AS f32x4*)(wa3 + 8192) = acc[7];
  SB();
  MV16(acc[3], fw[2][3], p[0]); SB();
  BAR();
  SB();
  SB();
  WAITVB(8); SB();
  PRIO(3); SB();
  MV16(acc[0], fw[3][0], p[1]); SB();
  LD16(p[0], vP3, sP, 0, "sc1"); sP += 5120; OPQ(sP);
  SB();
  MV16(acc[1], fw[3][1], p[1]); SB();
  MV16(acc[2], fw[3][2], p[1]); SB();
  MV16(acc[3], fw[3][3], p[1]); SB();
  SB();
  WAITVB(8); SB();
  MV16(acc[0], fw[4][0], p[2]); SB();
  LD16(p[1], vP0, sP, 0, "sc1");
  SB();
  MV16(acc[1], fw[4][1], p[2]); SB();
  MV16(acc[2], fw[4][2], p[2]); SB();
  MV16(acc[3], fw[4][3], p[2]); SB();
  SB();
  WAITVB(8); SB();
  MV16(acc[0], fw[5][0], p[3]); SB();
  LD16(p[2], vP1, sP, 0, "sc1");
  SB();
  MV16(acc[1], fw[5][1], p[3]); SB();
  MV16(acc[2], fw[5][2], p[3]); SB();
  MV16(acc[3], fw[5][3], p[3]); SB();
  SB();
  WAITVB(8); SB();
  PRIO(2); SB();
  MV16(acc[0], fw[6][0], p[4]); SB();
  LD16(p[3], vP2, sP, 0, "sc1");
  SB();
  MV16(acc[1], fw[6][1], p[4]); SB();
  MV16(acc[2], fw[6][2], p[4]); SB();
  MV16(acc[3], fw[6][3], p[4]); SB();
  SB();
  WAITVB(8); SB();
  MV16(acc[0], fw[7][0], p[5]); SB();
  LD16(p[4], vP3, sP, 0, "sc1"); sP += HU - 5120; OPQ(sP);
  SB();
  MV16(acc[1], fw[7][1], p[5]); SB();
  MV16(acc[2], fw[7][2], p[5]); SB();
  MV16(acc[3], fw[7][3], p[5]); SB();
  SB();
  WAITVB(8); SB();
  MV16Z(acc[4], fw[0][0], p[6]); SB();
  LD16(p[5], vP0, sP, 0, "sc1");
  SB();
  MV16Z(acc[5], fw[0][1], p[6]); SB();
  MV16Z(acc[6], fw[0][2], p[6]); SB();
  MV16Z(acc[7], fw[0][3], p[6]); SB();
  SB();
  WAITVB(7); SB();
  PRIO(1); SB();
  MV16(acc[4], fw[1][0], p[7]); SB();
  LD16(p[6], vP1, sP, 0, "sc1");
  SB();
  MV16(acc[5], fw[1][1], p[7]); SB();
  *(LDS_AS f32x4*)(wa0 + 12288) = acc[0];
  SB();
  MV16(acc[6], fw[1][2], p[7]); SB();
  *(LDS_AS f32x4*)(wa1 + 12288) = acc[1];
  SB();
  MV16(acc[7], fw[1][3], p[7]); SB();
  { const char* f_ = sFU + 0 * FU; const unsigned l_ = lane_now() * 16; ST16X(1, l_, f_, nonce); }
  SB();
  SB();
  WAITVB(8); SB();
  PRIO(0); SB();
  MV16(acc[4], fw[2][0], p[8]); SB();
  LD16(p[7], vP2, sP, 0, "sc1");
  SB();
  MV16(acc[5], fw[2][1], p[8]); SB();
  *(LDS_AS f32x4*)(wa2 + 12288) = acc[2];
  SB();
  MV16(acc[6], fw[2][2], p[8]); SB();
  *(LDS_AS f32x4*)(wa3 + 12288) = acc[3];
  SB();
  MV16(acc[7], fw[2][3], p[8]); SB();
  BAR();
  STAMP(6);
  SB();
  SB();
  WAITVB(8); SB();
  PRIO(3); SB();
  MV16(acc[4], fw[3][0], p[0]); SB();
  LD16(p[8], vP3, sP, 0, "sc1"); sP += 5120; OPQ(sP);
  t4[0] = *(LDS_AS const f32x4*)(ra + 12288);
  SB();
  MV16(acc[5], fw[3][1], p[0]); SB();
  t4[1] = *(LDS_AS const f32x4*)(ra + 28672);
  SB();
  MV16(acc[6], fw[3][2], p[0]); SB();
  t4[2] = *(LDS_AS const f32x4*)(ra + 45056);
  SB();
  MV16(acc[7], fw[3][3], p[0]); SB();
  t4[3] = *(LDS_AS const f32x4*)(ra + 61440);
  SB();
  SB();
  WAITVB(8); SB();
  MV16(acc[4], fw[4][0], p[1]); SB();
  LD16(p[0], vP0, sP, 0, "sc1");
  t4[4] = *(LDS_AS const f32x4*)(rah + 12288);
  SB();
  MV16(acc[5], fw[4][1], p[1]); SB();
  t4[5] = *(LDS_AS const f32x4*)(rah + 28672);
  SB();
  MV16(acc[6], fw[4][2], p[1]); SB();
  t4[6] = *(LDS_AS const f32x4*)(rah + 45056);
  t4[7] = *(LDS_AS const f32x4*)(rah + 61440);
  SB();
  MV16(acc[7], fw[4][3], p[1]); SB();
  { const int ix = 14 + up; vU = (ix >> 2) * 5120 + frag(ix & 3, uL); PINV(vU); }
  SB();
  SB();
  WAITVB(8); SB();
  MV16(acc[4], fw[5][0], p[2]); SB();
  LD16(p[1], vP1, sP, 0, "sc1");
  OP4_3("v_add_f32", mm, t4[0], bias);
  SB();
  MV16(acc[5], fw[5][1], p[2]); SB();
  OP4_3("v_add_f32", zz, t4[1], t4[2]);
  SB();
  MV16(acc[6], fw[5][2], p[2]); SB();
  ACC4("v_add_f32", mm, t4[3]);
  SB();
  MV16(acc[7], fw[5][3], p[2]); SB();
  ACC4("v_add_f32", zz, t4[4]);
  SB();
  SB();
  WAITVB(8); SB();
  PRIO(2); SB();
  MV16(acc[4], fw[6][0], p[3]); SB();
  LD16(p[2], vP2, sP, 0, "sc1");
  ACC4("v_add_f32", mm, t4[5]);
  SB();
  MV16(acc[5], fw[6][1], p[3]); SB();
  ACC4("v_add_f32", zz, t4[6]);
  SB();
  MV16(acc[6], fw[6][2], p[3]); SB();
  ACC4("v_add_f32", mm, t4[7]);
  SB();
  MV16(acc[7], fw[6][3], p[3]); SB();
  ACC4("v_add_f32", mm, zz);
  SB();
  SB();
  WAITVB(8); SB();
  MV16(acc[4], fw[7][0], p[4]); SB();
  LD16(p[3], vP3, sP, 0, "sc1"); sP += HU - 5120; OPQ(sP);
  OP4_3("v_mul_f32", zz, mm, mm);
  SB();
  MV16(acc[5], fw[7][1], p[4]); SB();
  POLY4(zz, c23);
  SB();
  MV16(acc[6], fw[7][2], p[4]); SB();
  ACC4("v_mul_f32", zz, mm);
  SB();
  MV16(acc[7], fw[7][3], p[4]); SB();
  UN4("v_exp_f32", "", "", zz);
  SB();
  SB();
  WAITVB(8); SB();
  MV16Z(acc[0], fw[0][0], p[5]); SB();
  LD16(p[4], vP0, sP, 0, "sc1");
  UN4("v_add_f32", "1.0, ", "", zz);
  SB();
  MV16Z(acc[1], fw[0][1], p[5]); SB();
  UN4("v_rcp_f32", "", "", zz);
  SB();
  MV16Z(acc[2], fw[0][2], p[5]); SB();
  NFMA4(mm, zz);
  SB();
  MV16Z(acc[3], fw[0][3], p[5]); SB();
  { const unsigned x0 = pack2(mm[0], mm[1]), x1 = pack2(mm[2], mm[3]);
    const auto r0 = __builtin_amdgcn_permlane16_swap(x0, x0, false, false), r1 = __builtin_amdgcn_permlane16_swap(x1, x1, false, false);
    ST16E(0xffff, vU, sU, ((u32x4){r0[0], r1[0], r0[1], r1[1]})); }
  SB();
  SB();
  WAITVB(9); SB();
  PRIO(1); SB();
  MV16(acc[0], fw[1][0], p[6]); SB();
  LD16(p[5], vP1, sP, 0, "sc1");
  SB();
  MV16(acc[1], fw[1][1], p[6]); SB();
  *(LDS_AS f32x4*)(wa0 + 0) = acc[4];
  SB();
  MV16(acc[2], fw[1][2], p[6]); SB();
  *(LDS_AS f32x4*)(wa1 + 0) = acc[5];
  SB();
  MV16(acc[3], fw[1][3], p[6]); SB();
  SB();
  WAITVB(8); SB();
  PRIO(0); SB();
  MV16(acc[0], fw[2][0], p[7]); SB();
  LD16(p[6], vP2, sP, 0, "sc1");
  SB();
  MV16(acc[1], fw[2][1], p[7]); SB();
  *(LDS_AS f32x4*)(wa2 + 0) = acc[6];
  SB();
  MV16(acc[2], fw[2][2], p[7]); SB();
  *(LDS_AS f32x4*)(wa3 + 0) = acc[7];
  SB();
  MV16(acc[3], fw[2][3], p[7]); SB();
  BAR();
  SB();
  SB();
  WAITVB(8); SB();
  PRIO(3); SB();
  MV16(acc[0], fw[3][0], p[8]); SB();
  LD16(p[7], vP3, sP, 0, "sc1"); sP += 5120; OPQ(sP);
  SB();
  MV16(acc[1], fw[3][1], p[8]); SB();
  MV16(acc[2], fw[3][2], p[8]); SB();
  MV16(acc[3], fw[3][3], p[8]); SB();
  SB();
  WAITVB(8); SB();
  MV16(acc[0], fw[4][0], p[0]); SB();
  LD16(p[8], vP0, sP, 0, "sc1");
  SB();
  MV16(acc[1], fw[4][1], p[0]); SB();
  MV16(acc[2], fw[4][2], p[0]); SB();
  MV16(acc[3], fw[4][3], p[0]); SB();
  SB();
  WAITVB(8); SB();
  MV16(acc[0], fw[5][0], p[1]); SB();
  LD16(p[0], vP1, sP, 0, "sc1");
  SB();
  MV16(acc[1], fw[5][1], p[1]); SB();
  MV16(acc[2], fw[5][2], p[1]); SB();
  MV16(acc[3], fw[5][3], p[1]); SB();
  SB();
  WAITVB(8); SB();
  PRIO(2); SB();
  MV16(acc[0], fw[6][0], p[2]); SB();
  LD16(p[1], vP2, sP, 0, "sc1");
  SB();
  MV16(acc[1], fw[6][1], p[2]); SB();
  MV16(acc[2], fw[6][2], p[2]); SB();
  MV16(acc[3], fw[6][3], p[2]); SB();
  SB();
  WAITVB(8); SB();
  MV16(acc[0], fw[7][0], p[3]); SB();
  LD16(p[2], vP3, sP, 0, "sc1"); sP += HU - 5120; OPQ(sP);
  SB();
  MV16(acc[1], fw[7][1], p[3]); SB();
  MV16(acc[2], fw[7][2], p[3]); SB();
  MV16(acc[3], fw[7][3], p[3]); SB();
  SB();
  WAITVB(8); SB();
  MV16Z(acc[4], fw[0][0], p[4]); SB();
  LD16(p[3], vP0, sP, 0, "sc1");
  SB();
  MV16Z(acc[5], fw[0][1], p[4]); SB();
  MV16Z(acc[6], fw[0][2], p[4]); SB();
  MV16Z(acc[7], fw[0][3], p[4]); SB();
  SB();
  WAITVB(7); SB();
  PRIO(1); SB();
  MV16(acc[4], fw[1][0], p[5]); SB();
  LD16(p[4], vP1, sP, 0, "sc1");
  SB();
  MV16(acc[5], fw[1][1], p[5]); SB();
  *(LDS_AS f32x4*)(wa0 + 4096) = acc[0];
  SB();
  MV16(acc[6], fw[1][2], p[5]); SB();
  *(LDS_AS f32x4*)(wa1 + 4096) = acc[1];
  SB();
  MV16(acc[7], fw[1][3], p[5]); SB();
  { const char* f_ = sFU + 28 * FU; const unsigned l_ = lane_now() * 16; ST16X(1, l_, f_, nonce); }
  SB();
  SB();
  WAITVB(8); SB();
  PRIO(0); SB();
  MV16(acc[4], fw[2][0], p[6]); SB();
  LD16(p[5], vP2, sP, 0, "sc1");
  SB();
  MV16(acc[5], fw[2][1], p[6]); SB();
  *(LDS_AS f32x4*)(wa2 + 4096) = acc[2];
  SB();
  MV16(acc[6], fw[2][2], p[6]); SB();
  *(LDS_AS f32x4*)(wa3 + 4096) = acc[3];
  SB();
  MV16(acc[7], fw[2][3], p[6]); SB();
  BAR();
  STAMP(7);
  SB();
  SB();
  WAITVB(8); SB();
  PRIO(3); SB();
  MV16(acc[4], fw[3][0], p[7]); SB();
  LD16(p[6], vP3, sP, 0, "sc1"); sP += 5120; OPQ(sP);
  t4[0] = *(LDS_AS const f32x4*)(ra + 4096);
  SB();
  MV16(acc[5], fw[3][1], p[7]); SB();
  t4[1] = *(LDS_AS const f32x4*)(ra + 20480);
  SB();
  MV16(acc[6], fw[3][2], p[7]); SB();
  t4[2] = *(LDS_AS const f32x4*)(ra + 36864);
  SB();
  MV16(acc[7], fw[3][3], p[7]); SB();
  t4[3] = *(LDS_AS const f32x4*)(ra + 53248);
  SB();
  SB();
  WAITVB(8); SB();
  MV16(acc[4], fw[4][0], p[8]); SB();
  LD16(p[7], vP0, sP, 0, "sc1");
  t4[4] = *(LDS_AS const f32x4*)(rah + 4096);
  SB();
  MV16(acc[5], fw[4][1], p[8]); SB();
  t4[5] = *(LDS_AS const f32x4*)(rah + 20480);
  SB();
  MV16(acc[6], fw[4][2], p[8]); SB();
  t4[6] = *(LDS_AS const f32x4*)(rah + 36864);
  t4[7] = *(LDS_AS const f32x4*)(rah + 53248);
  SB();
  MV16(acc[7], fw[4][3], p[8]); SB();
  { const int ix = 28 + up; vU = (ix >> 2) * 5120 + frag(ix & 3, uL); PINV(vU); }
  SB();
  SB();
  WAITVB(8); SB();
  MV16(acc[4], fw[5][0], p[0]); SB();
  LD16(p[8], vP1, sP, 0, "sc1");
  OP4_3("v_add_f32", mm, t4[0], bias);
  SB();
  MV16(acc[5], fw[5][1], p[0]); SB();
  OP4_3("v_add_f32", zz, t4[1], t4[2]);
  SB();
  MV16(acc[6], fw[5][2], p[0]); SB();
  ACC4("v_add_f32", mm, t4[3]);
  SB();
  MV16(acc[7], fw[5][3], p[0]); SB();
  ACC4("v_add_f32", zz, t4[4]);
  SB();
  SB();
  WAITVB(8); SB();
  PRIO(2); SB();
  MV16(acc[4], fw[6][0], p[1]); SB();
  LD16(p[0], vP2, sP, 0, "sc1");
  ACC4("v_add_f32", mm, t4[5]);
  SB();
  MV16(acc[5], fw[6][1], p[1]); SB();
  ACC4("v_add_f32", zz, t4[6]);
  SB();
  MV16(acc[6], fw[6][2], p[1]); SB();
  ACC4("v_add_f32", mm, t4[7]);
  SB();
  MV16(acc[7], fw[6][3], p[1]); SB();
  ACC4("v_add_f32", mm, zz);
  SB();
  SB();
  WAITVB(8); SB();
  MV16(acc[4], fw[7][0], p[2]); SB();
  LD16(p[1], vP3, sP, 0, "sc1");
  OP4_3("v_mul_f32", zz, mm, mm);
  SB();
  MV16(acc[5], fw[7][1], p[2]); SB();
  POLY4(zz, c23);
  SB();
  MV16(acc[6], fw[7][2], p[2]); SB();
  ACC4("v_mul_f32", zz, mm);
  SB();
  MV16(acc[7], fw[7][3], p[2]); SB();
  UN4("v_exp_f32", "", "", zz);
  SB();
  SB();
  WAITVB(8); SB();
  MV16Z(acc[0], fw[0][0], p[3]); SB();
  bf16x8 fo[14];
  const char* sWo = A.ws + (WS_WO + o_wo(dg * 7 + (w < 7 ? w : 6))); unsigned long long mW = w < 7 ? ~0ull : 1ull; OPQ(sWo); OPQ(mW);
  UN4("v_add_f32", "1.0, ", "", zz);
  SB();
  MV16Z(acc[1], fw[0][1], p[3]); SB();
  UN4("v_rcp_f32", "", "", zz);
  SB();
  MV16Z(acc[2], fw[0][2], p[3]); SB();
  NFMA4(mm, zz);
  SB();
  MV16Z(acc[3], fw[0][3], p[3]); SB();
  { const unsigned x0 = pack2(mm[0], mm[1]), x1 = pack2(mm[2], mm[3]);
    const auto r0 = __builtin_amdgcn_permlane16_swap(x0, x0, false, false), r1 = __builtin_amdgcn_permlane16_swap(x1, x1, false, false);
    ST16E(0xffff, vU, sU, ((u32x4){r0[0], r1[0], r0[1], r1[1]})); }
  SB();
  SB();
  WAITVB(8); SB();
  PRIO(1); SB();
  MV16(acc[0], fw[1][0], p[4]); SB();
  *(LDS_AS f32x4*)(wa0 + 8192) = acc[4];
  SB();
  MV16(acc[1], fw[1][1], p[4]); SB();
  *(LDS_AS f32x4*)(wa1 + 8192) = acc[5];
  SB();
  MV16(acc[2], fw[1][2], p[4]); SB();
  MV16(acc[3], fw[1][3], p[4]); SB();
  SB();
  WAITVB(6); SB();
  PRIO(0); SB();
  MV16(acc[0], fw[2][0], p[5]); SB();
  *(LDS_AS f32x4*)(wa2 + 8192) = acc[6];
  SB();
  MV16(acc[1], fw[2][1], p[5]); SB();
  *(LDS_AS f32x4*)(wa3 + 8192) = acc[7];
  SB();
  MV16(acc[2], fw[2][2], p[5]); SB();
  BAR();
  SB();
  MV16(acc[3], fw[2][3], p[5]); SB();
  SB();
  WAITVB(5); SB();
  PRIO(2); SB();
  MV16(acc[0], fw[3][0], p[6]); SB();
  MV16(acc[1], fw[3][1], p[6]); SB();
  MV16(acc[2], fw[3][2], p[6]); SB();
  MV16(acc[3], fw[3][3], p[6]); SB();
  SB();
  WAITVB(4); SB();
  MV16(acc[0], fw[4][0], p[7]); SB();
  MV16(acc[1], fw[4][1], p[7]); SB();
  MV16(acc[2], fw[4][2], p[7]); SB();
  MV16(acc[3], fw[4][3], p[7]); SB();
  SB();
  WAITVB(3); SB();
  MV16(acc[0], fw[5][0], p[8]); SB();
  LD16M(fo[0], vP0, sWo, mW);
  LD16M(fo[1], vP1, sWo, mW);
  SB();
  MV16(acc[1], fw[5][1], p[8]); SB();
  MV16(acc[2], fw[5][2], p[8]); SB();
  MV16(acc[3], fw[5][3], p[8]); SB();
  SB();
  WAITVB(4); SB();
  PRIO(1); SB();
  MV16(acc[0], fw[6][0], p[0]); SB();
  LD16M(fo[2], vP2, sWo, mW);
  LD16M(fo[3], vP3, sWo, mW); sWo += 5120; OPQ(sWo);
  SB();
  MV16(acc[1], fw[6][1], p[0]); SB();
  MV16(acc[2], fw[6][2], p[0]); SB();
  MV16(acc[3], fw[6][3], p[0]); SB();
  SB();
  WAITVB(5); SB();
  PRIO(0); SB();
  MV16(acc[0], fw[7][0], p[1]); SB();
  LD16M(fo[4], vP0, sWo, mW);
  LD16M(fo[5], vP1, sWo, mW);
  SB();
  MV16(acc[1], fw[7][1], p[1]); SB();
  MV16(acc[2], fw[7][2], p[1]); SB();
  MV16(acc[3], fw[7][3], p[1]); SB();
  NOP(4); SB();
  *(LDS_AS f32x4*)(wa0 + 12288) = acc[0];
  SB();
  *(LDS_AS f32x4*)(wa1 + 12288) = acc[1];
  SB();
  *(LDS_AS f32x4*)(wa2 + 12288) = acc[2];
  SB();
  *(LDS_AS f32x4*)(wa3 + 12288) = acc[3];
  SB();
  BAR();
  STAMP(8);
  t4[0] = *(LDS_AS const f32x4*)(ra + 12288);
  t4[1] = *(LDS_AS const f32x4*)(ra + 28672);
  t4[2] = *(LDS_AS const f32x4*)(ra + 45056);
  t4[3] = *(LDS_AS const f32x4*)(ra + 61440);
  t4[4] = *(LDS_AS const f32x4*)(rah + 12288);
  t4[5] = *(LDS_AS const f32x4*)(rah + 28672);
  t4[6] = *(LDS_AS const f32x4*)(rah + 45056);
  t4[7] = *(LDS_AS const f32x4*)(rah + 61440);
  { const int ix = 42 + up; vU = (ix >> 2) * 5120 + frag(ix & 3, uL); PINV(vU); }
  WAITV(6);
  { const char* f_ = sFU + 56 * FU; const unsigned l_ = lane_now() * 16; ST16X(1, l_, f_, nonce); }
  { f32x4 y_ = ((t4[0] + (f32x4){bias[0], bias[1], bias[2], bias[3]}) + (t4[1] + t4[2])) + ((t4[3] + t4[4]) + ((t4[5] + t4[6]) + t4[7]));
    f32x4 z_ = y_ * (y_ * y_ * 0.10294324f + 2.3022082f);
    z_ = (f32x4){__builtin_amdgcn_exp2f(z_[0]), __builtin_amdgcn_exp2f(z_[1]), __builtin_amdgcn_exp2f(z_[2]), __builtin_amdgcn_exp2f(z_[3])} + 1.f;
    z_ = (f32x4){__builtin_amdgcn_rcpf(z_[0]), __builtin_amdgcn_rcpf(z_[1]), __builtin_amdgcn_rcpf(z_[2]), __builtin_amdgcn_rcpf(z_[3])};
    y_ = y_ - y_ * z_;
    mm[0] = y_[0]; mm[1] = y_[1]; mm[2] = y_[2]; mm[3] = y_[3]; }
  { const unsigned x0 = pack2(mm[0], mm[1]), x1 = pack2(mm[2], mm[3]);
    const auto r0 = __builtin_amdgcn_permlane16_swap(x0, x0, false, false), r1 = __builtin_amdgcn_permlane16_swap(x1, x1, false, false);
    ST16E(0xffff, vU, sU, ((u32x4){r0[0], r1[0], r0[1], r1[1]})); }
  STAMP(9);
  LD16M(fo[6], vP2, sWo, mW);
  LD16M(fo[7], vP3, sWo, mW); sWo += 5120; OPQ(sWo);
  WAITV(2);
  { const char* f_ = sFU + 84 * FU; const unsigned l_ = lane_now() * 16; ST16X(1, l_, f_, nonce); }
  LD16M(fo[8], vP0, sWo, mW);
  LD16M(fo[9], vP1, sWo, mW);
  LD16M(fo[10], vP2, sWo, mW);
  LD16M(fo[11], vP3, sWo, mW); sWo += 5120; OPQ(sWo);
  LD16M(fo[12], vP0, sWo, mW);
  LD16M(fo[13], vP1, sWo, mW);
  { int w_ = w; OPQ(w_); { const int w = w_ & 7, lane = lane_now(); const int n16 = lane & 15, g4 = lane >> 4, q4 = n16 >> 2, pq = lane & 3, r8 = lane >> 3; const unsigned vL16 = lane * 16;
  unsigned vO[7];
#pragma unroll
  for (int i = 0; i < 7; ++i) { vO[i] = line7(64 * i + 8 * w + r8) + (lane & 7) * 16; PINV(vO[i]); }
  const char* sO = A.ws + (WS_OT + o_4(st));
  unsigned vFO = (((lane >> 2) & 7) * 8 + (lane & 3)) * FO, ro = lds0 + vL16, roh = ro + 65536;
  unsigned vS0 = pair3(q4) + (2 * (n16 & 3) + (g4 & 1)) * 32 + (g4 >> 1) * 16, vS1 = vS0 - pair3(q4) + pair3(4 + q4);
  const char* sFO = A.ws + WS_FLAGO + ((st >> 1) * 64 + (st & 1) * 4) * FO;
  const char* sS = A.ws + WS_ST + (size_t)(st * 4 + dg) * ST_T + w * 3072;
  const char* sFS = A.ws + WS_FLAGS + ((st * 4 + dg) * 8 + w) * FS;
  asm volatile("" : "+s"(sO), "+s"(sFO), "+s"(sS), "+s"(sFS)); asm volatile("" : "+v"(vFO), "+v"(ro), "+v"(roh), "+v"(vS0), "+v"(vS1));
  STAMP(10);
  STAMP(11);
  if (w == 7) poll1<3>(sFO, vFO, nonce);
  BAR();
  STAMP(12);
  bf16x8 of[3][4]; f32x4 as[4];
  DMA1(vO[0], sO, ldsP, 0, "sc1");
  DMA1(vO[1], sO, ldsP, 1024, "sc1");
  DMA1(vO[2], sO, ldsP, 2048, "sc1");
  DMA1(vO[3], sO, ldsP, 3072, "sc1");
  DMA1(vO[4], sO, ldsP, 4096, "sc1");
  DMA1(vO[5], sO, ldsP, 5120, "sc1");
  DMA1(vO[6], sO, ldsP, 6144, "sc1");
  WAITV(4);
  BAR0();
  if (w == 7) {
  WAITV(0);
  BAR0();
  } else {
  of[0][0] = *(LDS_AS const bf16x8*)(ro + 0);
  of[0][1] = *(LDS_AS const bf16x8*)(ro + 16384);
  of[0][2] = *(LDS_AS const bf16x8*)(ro + 32768);
  of[0][3] = *(LDS_AS const bf16x8*)(ro + 49152);
  STAMP(13);
  of[1][0] = *(LDS_AS const bf16x8*)(roh + 0);
  of[1][1] = *(LDS_AS const bf16x8*)(roh + 16384);
  of[1][2] = *(LDS_AS const bf16x8*)(roh + 32768);
  of[1][3] = *(LDS_AS const bf16x8*)(roh + 49152);
  SB();
  MV16Z(as[0], fo[0], of[0][0]); SB();
  of[2][0] = *(LDS_AS const bf16x8*)(ro + 1024);
  SB();
  MV16Z(as[1], fo[0], of[0][1]); SB();
  of[2][1] = *(LDS_AS const bf16x8*)(ro + 17408);
  SB();
  MV16Z(as[2], fo[0], of[0][2]); SB();
  of[2][2] = *(LDS_AS const bf16x8*)(ro + 33792);
  SB();
  MV16Z(as[3], fo[0], of[0][3]); SB();
  of[2][3] = *(LDS_AS const bf16x8*)(ro + 50176);
  SB();
  SB();
  MV16(as[0], fo[1], of[1][0]); SB();
  of[0][0] = *(LDS_AS const bf16x8*)(roh + 1024);
  SB();
  MV16(as[1], fo[1], of[1][1]); SB();
  of[0][1] = *(LDS_AS const bf16x8*)(roh + 17408);
  SB();
  MV16(as[2], fo[1], of[1][2]); SB();
  of[0][2] = *(LDS_AS const bf16x8*)(roh + 33792);
  SB();
  MV16(as[3], fo[1], of[1][3]); SB();
  of[0][3] = *(LDS_AS const bf16x8*)(roh + 50176);
  SB();
  SB();
  MV16(as[0], fo[2], of[2][0]); SB();
  of[1][0] = *(LDS_AS const bf16x8*)(ro + 2048);
  SB();
  MV16(as[1], fo[2], of[2][1]); SB();
  of[1][1] = *(LDS_AS const bf16x8*)(ro + 18432);
  SB();
  MV16(as[2], fo[2], of[2][2]); SB();
  of[1][2] = *(LDS_AS const bf16x8*)(ro + 34816);
  SB();
  MV16(as[3], fo[2], of[2][3]); SB();
  of[1][3] = *(LDS_AS const bf16x8*)(ro + 51200);
  SB();
  SB();
  MV16(as[0], fo[3], of[0][0]); SB();
  of[2][0] = *(LDS_AS const bf16x8*)(roh + 2048);
  SB();
  MV16(as[1], fo[3], of[0][1]); SB();
  of[2][1] = *(LDS_AS const bf16x8*)(roh + 18432);
  SB();
  MV16(as[2], fo[3], of[0][2]); SB();
  of[2][2] = *(LDS_AS const bf16x8*)(roh + 34816);
  SB();
  MV16(as[3], fo[3], of[0][3]); SB();
  of[2][3] = *(LDS_AS const bf16x8*)(roh + 51200);
  SB();
  SB();
  MV16(as[0], fo[4], of[1][0]); SB();
  WAITV(0);
  BAR0();
  of[0][0] = *(LDS_AS const bf16x8*)(ro + 3072);
  SB();
  MV16(as[1], fo[4], of[1][1]); SB();
  of[0][1] = *(LDS_AS const bf16x8*)(ro + 19456);
  SB();
  MV16(as[2], fo[4], of[1][2]); SB();
  of[0][2] = *(LDS_AS const bf16x8*)(ro + 35840);
  SB();
  MV16(as[3], fo[4], of[1][3]); SB();
  of[0][3] = *(LDS_AS const bf16x8*)(ro + 52224);
  SB();
  SB();
  MV16(as[0], fo[5], of[2][0]); SB();
  of[1][0] = *(LDS_AS const bf16x8*)(roh + 3072);
  SB();
  MV16(as[1], fo[5], of[2][1]); SB();
  of[1][1] = *(LDS_AS const bf16x8*)(roh + 19456);
  SB();
  MV16(as[2], fo[5], of[2][2]); SB();
  of[1][2] = *(LDS_AS const bf16x8*)(roh + 35840);
  SB();
  MV16(as[3], fo[5], of[2][3]); SB();
  of[1][3] = *(LDS_AS const bf16x8*)(roh + 52224);
  SB();
  SB();
  MV16(as[0], fo[6], of[0][0]); SB();
  of[2][0] = *(LDS_AS const bf16x8*)(ro + 4096);
  SB();
  MV16(as[1], fo[6], of[0][1]); SB();
  of[2][1] = *(LDS_AS const bf16x8*)(ro + 20480);
  SB();
  MV16(as[2], fo[6], of[0][2]); SB();
  of[2][2] = *(LDS_AS const bf16x8*)(ro + 36864);
  SB();
  MV16(as[3], fo[6], of[0][3]); SB();
  of[2][3] = *(LDS_AS const bf16x8*)(ro + 53248);
  SB();
  SB();
  MV16(as[0], fo[7], of[1][0]); SB();
  of[0][0] = *(LDS_AS const bf16x8*)(roh + 4096);
  SB();
  MV16(as[1], fo[7], of[1][1]); SB();
  of[0][1] = *(LDS_AS const bf16x8*)(roh + 20480);
  SB();
  MV16(as[2], fo[7], of[1][2]); SB();
  of[0][2] = *(LDS_AS const bf16x8*)(roh + 36864);
  SB();
  MV16(as[3], fo[7], of[1][3]); SB();
  of[0][3] = *(LDS_AS const bf16x8*)(roh + 53248);
  SB();
  SB();
  MV16(as[0], fo[8], of[2][0]); SB();
  of[1][0] = *(LDS_AS const bf16x8*)(ro + 5120);
  SB();
  MV16(as[1], fo[8], of[2][1]); SB();
  of[1][1] = *(LDS_AS const bf16x8*)(ro + 21504);
  SB();
  MV16(as[2], fo[8], of[2][2]); SB();
  of[1][2] = *(LDS_AS const bf16x8*)(ro + 37888);
  SB();
  MV16(as[3], fo[8], of[2][3]); SB();
  of[1][3] = *(LDS_AS const bf16x8*)(ro + 54272);
  SB();
  SB();
  MV16(as[0], fo[9], of[0][0]); SB();
  of[2][0] = *(LDS_AS const bf16x8*)(roh + 5120);
  SB();
  MV16(as[1], fo[9], of[0][1]); SB();
  of[2][1] = *(LDS_AS const bf16x8*)(roh + 21504);
  SB();
  MV16(as[2], fo[9], of[0][2]); SB();
  of[2][2] = *(LDS_AS const bf16x8*)(roh + 37888);
  SB();
  MV16(as[3], fo[9], of[0][3]); SB();
  of[2][3] = *(LDS_AS const bf16x8*)(roh + 54272);
  SB();
  SB();
  MV16(as[0], fo[10], of[1][0]); SB();
  of[0][0] = *(LDS_AS const bf16x8*)(ro + 6144);
  SB();
  MV16(as[1], fo[10], of[1][1]); SB();
  of[0][1] = *(LDS_AS const bf16x8*)(ro + 22528);
  SB();
  MV16(as[2], fo[10], of[1][2]); SB();
  of[0][2] = *(LDS_AS const bf16x8*)(ro + 38912);
  SB();
  MV16(as[3], fo[10], of[1][3]); SB();
  of[0][3] = *(LDS_AS const bf16x8*)(ro + 55296);
  SB();
  SB();
  MV16(as[0], fo[11], of[2][0]); SB();
  of[1][0] = *(LDS_AS const bf16x8*)(roh + 6144);
  SB();
  MV16(as[1], fo[11], of[2][1]); SB();
  of[1][1] = *(LDS_AS const bf16x8*)(roh + 22528);
  SB();
  MV16(as[2], fo[11], of[2][2]); SB();
  of[1][2] = *(LDS_AS const bf16x8*)(roh + 38912);
  SB();
  MV16(as[3], fo[11], of[2][3]); SB();
  of[1][3] = *(LDS_AS const bf16x8*)(roh + 55296);
  SB();
  SB();
  MV16(as[0], fo[12], of[0][0]); SB();
  MV16(as[1], fo[12], of[0][1]); SB();
  MV16(as[2], fo[12], of[0][2]); SB();
  MV16(as[3], fo[12], of[0][3]); SB();
  SB();
  MV16(as[0], fo[13], of[1][0]); SB();
  MV16(as[1], fo[13], of[1][1]); SB();
  MV16(as[2], fo[13], of[1][2]); SB();
  MV16(as[3], fo[13], of[1][3]); SB();
  NOP(5); SB();
  { const auto r0 = __builtin_amdgcn_permlane16_swap(pack2(as[0][0], as[0][1]), pack2(as[1][0], as[1][1]), false, false);
    const auto r1 = __builtin_amdgcn_permlane16_swap(pack2(as[0][2], as[0][3]), pack2(as[1][2], as[1][3]), false, false);
    ST16(vS0, sS, 0, ((u32x4){r0[0], r1[0], r0[1], r1[1]})); } SB();
  { const auto r0 = __builtin_amdgcn_permlane16_swap(pack2(as[2][0], as[2][1]), pack2(as[3][0], as[3][1]), false, false);
    const auto r1 = __builtin_amdgcn_permlane16_swap(pack2(as[2][2], as[2][3]), pack2(as[3][2], as[3][3]), false, false);
    ST16(vS1, sS, 0, ((u32x4){r0[0], r1[0], r0[1], r1[1]})); } SB();
  STAMP(14);
  WAITV(0);
  ST16X(15, vL16, sFS, nonce);
  STAMP(15);
  }
  STAMP_DUMP();
  }}}}
  } else {
  DMA2(vWa, vWb, sW, ldsW, 6144, ""); sW = wrap == 1 ? bW : sW + dW; OPQ(sW);
  { int w_ = w; OPQ(w_); { const int w = w_ & 7, lane = lane_now(); const int n16 = lane & 15, g4 = lane >> 4, q4 = n16 >> 2, pq = lane & 3, r8 = lane >> 3; const unsigned vL16 = lane * 16;
  const unsigned vFH = (lane & 31) * FA;
  bf16x8 fw[8][4], p[9]; f32x4 acc[8], t4[8]; float mm[4], zz[4], bias[4], c23 = 2.3022082f;
  unsigned rW0 = ldsP + (8 * g4 + q4) * 128 + (((q4 >> 1) | (2 * (g4 & 1))) * 32) + pq * 8, rW1 = rW0 ^ 32, rW2 = rW0 ^ 64, rW3 = rW0 ^ 96;
  asm volatile("" : "+v"(rW0), "+v"(rW1), "+v"(rW2), "+v"(rW3));
  unsigned vP0 = frag(0, lane), vP1 = frag(1, lane), vP2 = frag(2, lane), vP3 = frag(3, lane);
  const char* sP = A.ws + (WS_HT + o_h(dg) + w * 10240);
  asm volatile("" : "+s"(sP)); asm volatile("" : "+v"(vP0), "+v"(vP1), "+v"(vP2), "+v"(vP3));
  WAITV(5);
  fw[0][0] = tr8u(rW0 + 0, 512);
  fw[0][1] = tr8u(rW1 + 0, 512);
  fw[0][2] = tr8u(rW2 + 0, 512);
  fw[0][3] = tr8u(rW3 + 0, 512);
  DMA2(vWa, vWb, sW, ldsW, 8192, ""); sW += dW; OPQ(sW);
  DMA2(vWa, vWb, sW, ldsW, 10240, ""); sW = wrap == 2 ? bW : sW + dW; OPQ(sW);
  WAITV(4);
  fw[1][0] = tr8u(rW0 + 4096, 512);
  fw[1][1] = tr8u(rW1 + 4096, 512);
  fw[1][2] = tr8u(rW2 + 4096, 512);
  fw[1][3] = tr8u(rW3 + 4096, 512);
  DMA2(vWa, vWb, sW, ldsW, 12288, ""); sW += dW; OPQ(sW);
  DMA2(vWa, vWb, sW, ldsW, 14336, ""); sW = wrap == 3 ? bW : sW + dW; OPQ(sW);
  WAITV(4);
  fw[2][0] = tr8u(rW0 + 8192, 512);
  fw[2][1] = tr8u(rW1 + 8192, 512);
  fw[2][2] = tr8u(rW2 + 8192, 512);
  fw[2][3] = tr8u(rW3 + 8192, 512);
  WAITL(0);
  DMA2(vWa, vWb, sW, ldsW, 0, ""); sW += dW; OPQ(sW);
  DMA2(vWa, vWb, sW, ldsW, 2048, ""); sW = wrap == 4 ? bW : sW + dW; OPQ(sW);
  WAITV(4);
  fw[3][0] = tr8u(rW0 + 12288, 512);
  fw[3][1] = tr8u(rW1 + 12288, 512);
  fw[3][2] = tr8u(rW2 + 12288, 512);
  fw[3][3] = tr8u(rW3 + 12288, 512);
  STAMP(1);
  WAITL(0);
  DMA2(vWa, vWb, sW, ldsW, 4096, ""); sW += dW; OPQ(sW);
  DMA2(vWa, vWb, sW, ldsW, 6144, ""); sW = wrap == 5 ? bW : sW + dW; OPQ(sW);
  bias[0] = lo2f(bv[0]); bias[1] = hi2f(bv[0]); bias[2] = lo2f(bv[1]); bias[3] = hi2f(bv[1]);
  unsigned wa0 = ldsP + n16 * 256 + ((g4 ^ n16) << 4), wa1 = wa0 ^ 64, wa2 = wa0 ^ 128, wa3 = wa0 ^ 192;
  unsigned ra = lds0 + (16 * 0 + n16) * 256 + (((4 * (w & 3) + g4) ^ n16) << 4), rah = ra + 65536, uL = 32 * (w & 1) + 16 * (g4 >> 1) + n16, vU;
  const int kap = 2 * ft + ((w >> 1) & 1), wv = (kap * 37) >> 8, us = kap - 7 * wv, up_ = us - KROT2 * dg;
  int up = up_ < 0 ? up_ + 7 : up_, mU = ((w - 4) >> 31) & 0xffff;
  const char* sU = A.ws + (WS_UT + o_4(dg * 8 + wv));
  const char* sFU = A.ws + WS_FLAGU + ((dg * 8 + wv) * 128 + 0 * 14 + us * 2 + (w & 1)) * FU;
  asm volatile("" : "+s"(sU), "+s"(sFU), "+s"(up), "+s"(mU)); asm volatile("" : "+v"(wa0), "+v"(wa1), "+v"(wa2), "+v"(wa3), "+v"(ra), "+v"(rah), "+v"(uL), "+v"(bias[0]), "+v"(bias[1]), "+v"(bias[2]), "+v"(bias[3]), "+v"(c23));
  WAITV(4);
  fw[4][0] = tr8u(rW0 + 0, 512);
  fw[4][1] = tr8u(rW1 + 0, 512);
  fw[4][2] = tr8u(rW2 + 0, 512);
  fw[4][3] = tr8u(rW3 + 0, 512);
  WAITL(0);
  DMA2(vWa, vWb, sW, ldsW, 8192, ""); sW += dW; OPQ(sW);
  LD4(fh, vFH, sFH, 0, "sc1");
  DMA2(vWa, vWb, sW, ldsW, 10240, ""); sW = wrap == 6 ? bW : sW + dW; OPQ(sW);
  WAITV(5);
  fw[5][0] = tr8u(rW0 + 4096, 512);
  fw[5][1] = tr8u(rW1 + 4096, 512);
  fw[5][2] = tr8u(rW2 + 4096, 512);
  fw[5][3] = tr8u(rW3 + 4096, 512);
  WAITL(0);
  DMA2(vWa, vWb, sW, ldsW, 12288, ""); sW += dW; OPQ(sW);
  DMA2(vWa, vWb, sW, ldsW, 14336, "");
  WAITV(4);
  fw[6][0] = tr8u(rW0 + 8192, 512);
  fw[6][1] = tr8u(rW1 + 8192, 512);
  fw[6][2] = tr8u(rW2 + 8192, 512);
  fw[6][3] = tr8u(rW3 + 8192, 512);
  WAITV1(6, fh);
  if (__ballot(fh != nonce) != 0ull) poll1<16>(sFH, vFH, nonce);
  STAMP(3);
  LD16(p[0], vP0, sP, 0, "sc1");
  LD16(p[1], vP1, sP, 0, "sc1");
  LD16(p[2], vP2, sP, 0, "sc1");
  LD16(p[3], vP3, sP, 0, "sc1"); sP += 5120; OPQ(sP);
  WAITV(4);
  fw[7][0] = tr8u(rW0 + 12288, 512);
  fw[7][1] = tr8u(rW1 + 12288, 512);
  fw[7][2] = tr8u(rW2 + 12288, 512);
  fw[7][3] = tr8u(rW3 + 12288, 512);
  STAMP(2);
  LD16(p[4], vP0, sP, 0, "sc1");
  LD16(p[5], vP1, sP, 0, "sc1");
  LD16(p[6], vP2, sP, 0, "sc1");
  LD16(p[7], vP3, sP, 0, "sc1"); sP += HU - 5120; OPQ(sP);
  SB();
  WAITVB(7); SB();
  STAMP(4);
  PRIO(3); SB();
  MV16Z(acc[0], fw[0][0], p[0]); SB();
  LD16(p[8], vP0, sP, 0, "sc1");
  SB();
  MV16Z(acc[1], fw[0][1], p[0]); SB();
  MV16Z(acc[2], fw[0][2], p[0]); SB();
  MV16Z(acc[3], fw[0][3], p[0]); SB();
  SB();
  WAITVB(7); SB();
  MV16(acc[0], fw[1][0], p[1]); SB();
  LD16(p[0], vP1, sP, 0, "sc1");
  SB();
  MV16(acc[1], fw[1][1], p[1]); SB();
  MV16(acc[2], fw[1][2], p[1]); SB();
  MV16(acc[3], fw[1][3], p[1]); SB();
  SB();
  WAITVB(7); SB();
  MV16(acc[0], fw[2][0], p[2]); SB();
  LD16(p[1], vP2, sP, 0, "sc1");
  SB();
  MV16(acc[1], fw[2][1], p[2]); SB();
  MV16(acc[2], fw[2][2], p[2]); SB();
  MV16(acc[3], fw[2][3], p[2]); SB();
  SB();
  WAITVB(7); SB();
  MV16(acc[0], fw[3][0], p[3]); SB();
  LD16(p[2], vP3, sP, 0, "sc1"); sP += 5120; OPQ(sP);
  SB();
  MV16(acc[1], fw[3][1], p[3]); SB();
  MV16(acc[2], fw[3][2], p[3]); SB();
  MV16(acc[3], fw[3][3], p[3]); SB();
  SB();
  WAITVB(7); SB();
  MV16(acc[0], fw[4][0], p[4]); SB();
  LD16(p[3], vP0, sP, 0, "sc1");
  SB();
  MV16(acc[1], fw[4][1], p[4]); SB();
  MV16(acc[2], fw[4][2], p[4]); SB();
  MV16(acc[3], fw[4][3], p[4]); SB();
  SB();
  WAITVB(7); SB();
  MV16(acc[0], fw[5][0], p[5]); SB();
  LD16(p[4], vP1, sP, 0, "sc1");
  SB();
  MV16(acc[1], fw[5][1], p[5]); SB();
  MV16(acc[2], fw[5][2], p[5]); SB();
  MV16(acc[3], fw[5][3], p[5]); SB();
  SB();
  WAITVB(7); SB();
  PRIO(2); SB();
  MV16(acc[0], fw[6][0], p[6]); SB();
  LD16(p[5], vP2, sP, 0, "sc1");
  SB();
  MV16(acc[1], fw[6][1], p[6]); SB();
  MV16(acc[2], fw[6][2], p[6]); SB();
  MV16(acc[3], fw[6][3], p[6]); SB();
  SB();
  WAITVB(7); SB();
  MV16(acc[0], fw[7][0], p[7]); SB();
  LD16(p[6], vP3, sP, 0, "sc1"); sP += HU - 5120; OPQ(sP);
  SB();
  MV16(acc[1], fw[7][1], p[7]); SB();
  MV16(acc[2], fw[7][2], p[7]); SB();
  MV16(acc[3], fw[7][3], p[7]); SB();
  SB();
  WAITVB(7); SB();
  MV16Z(acc[4], fw[0][0], p[8]); SB();
  LD16(p[7], vP0, sP, 0, "sc1");
  SB();
  MV16Z(acc[5], fw[0][1], p[8]); SB();
  MV16Z(acc[6], fw[0][2], p[8]); SB();
  MV16Z(acc[7], fw[0][3], p[8]); SB();
  SB();
  WAITVB(7); SB();
  PRIO(1); SB();
  MV16(acc[4], fw[1][0], p[0]); SB();
  LD16(p[8], vP1, sP, 0, "sc1");
  SB();
  MV16(acc[5], fw[1][1], p[0]); SB();
  *(LDS_AS f32x4*)(wa0 + 0) = acc[0];
  SB();
  MV16(acc[6], fw[1][2], p[0]); SB();
  *(LDS_AS f32x4*)(wa1 + 0) = acc[1];
  SB();
  MV16(acc[7], fw[1][3], p[0]); SB();
  SB();
  WAITVB(7); SB();
  PRIO(0); SB();
  MV16(acc[4], fw[2][0], p[1]); SB();
  LD16(p[0], vP2, sP, 0, "sc1");
  SB();
  MV16(acc[5], fw[2][1], p[1]); SB();
  *(LDS_AS f32x4*)(wa2 + 0) = acc[2];
  SB();
  MV16(acc[6], fw[2][2], p[1]); SB();
  *(LDS_AS f32x4*)(wa3 + 0) = acc[3];
  SB();
  MV16(acc[7], fw[2][3], p[1]); SB();
  BAR();
  STAMP(5);
  SB();
  SB();
  WAITVB(7); SB();
  PRIO(3); SB();
  MV16(acc[4], fw[3][0], p[2]); SB();
  LD16(p[1], vP3, sP, 0, "sc1"); sP += 5120; OPQ(sP);
  SB();
  MV16(acc[5], fw[3][1], p[2]); SB();
  MV16(acc[6], fw[3][2], p[2]); SB();
  MV16(acc[7], fw[3][3], p[2]); SB();
  SB();
  WAITVB(7); SB();
  MV16(acc[4], fw[4][0], p[3]); SB();
  LD16(p[2], vP0, sP, 0, "sc1");
  SB();
  MV16(acc[5], fw[4][1], p[3]); SB();
  MV16(acc[6], fw[4][2], p[3]); SB();
  MV16(acc[7], fw[4][3], p[3]); SB();
  SB();
  WAITVB(7); SB();
  MV16(acc[4], fw[5][0], p[4]); SB();
  LD16(p[3], vP1, sP, 0, "sc1");
  SB();
  MV16(acc[5], fw[5][1], p[4]); SB();
  MV16(acc[6], fw[5][2], p[4]); SB();
  MV16(acc[7], fw[5][3], p[4]); SB();
  SB();
  WAITVB(7); SB();
  PRIO(2); SB();
  MV16(acc[4], fw[6][0], p[5]); SB();
  LD16(p[4], vP2, sP, 0, "sc1");
  SB();
  MV16(acc[5], fw[6][1], p[5]); SB();
  MV16(acc[6], fw[6][2], p[5]); SB();
  MV16(acc[7], fw[6][3], p[5]); SB();
  SB();
  WAITVB(7); SB();
  MV16(acc[4], fw[7][0], p[6]); SB();
  LD16(p[5], vP3, sP, 0, "sc1"); sP += HU - 5120; OPQ(sP);
  SB();
  MV16(acc[5], fw[7][1], p[6]); SB();
  MV16(acc[6], fw[7][2], p[6]); SB();
  MV16(acc[7], fw[7][3], p[6]); SB();
  SB();
  WAITVB(7); SB();
  MV16Z(acc[0], fw[0][0], p[7]); SB();
  LD16(p[6], vP0, sP, 0, "sc1");
  SB();
  MV16Z(acc[1], fw[0][1], p[7]); SB();
  MV16Z(acc[2], fw[0][2], p[7]); SB();
  MV16Z(acc[3], fw[0][3], p[7]); SB();
  SB();
  WAITVB(7); SB();
  PRIO(1); SB();
  MV16(acc[0], fw[1][0], p[8]); SB();
  LD16(p[7], vP1, sP, 0, "sc1");
  SB();
  MV16(acc[1], fw[1][1], p[8]); SB();
  *(LDS_AS f32x4*)(wa0 + 8192) = acc[4];
  SB();
  MV16(acc[2], fw[1][2], p[8]); SB();
  *(LDS_AS f32x4*)(wa1 + 8192) = acc[5];
  SB();
  MV16(acc[3], fw[1][3], p[8]); SB();
  SB();
  WAITVB(7); SB();
  PRIO(0); SB();
  MV16(acc[0], fw[2][0], p[0]); SB();
  LD16(p[8], vP2, sP, 0, "sc1");
  SB();
  MV16(acc[1], fw[2][1], p[0]); SB();
  *(LDS_AS f32x4*)(wa2 + 8192) = acc[6];
  SB();
  MV16(acc[2], fw[2][2], p[0]); SB();
  *(LDS_AS f32x4*)(wa3 + 8192) = acc[7];
  SB();
  MV16(acc[3], fw[2][3], p[0]); SB();
  BAR();
  SB();
  SB();
  WAITVB(7); SB();
  PRIO(3); SB();
  MV16(acc[0], fw[3][0], p[1]); SB();
  LD16(p[0], vP3, sP, 0, "sc1"); sP += 5120; OPQ(sP);
  t4[0] = *(LDS_AS const f32x4*)(ra + 8192);
  SB();
  MV16(acc[1], fw[3][1], p[1]); SB();
  t4[1] = *(LDS_AS const f32x4*)(ra + 24576);
  SB();
  MV16(acc[2], fw[3][2], p[1]); SB();
  t4[2] = *(LDS_AS const f32x4*)(ra + 40960);
  SB();
  MV16(acc[3], fw[3][3], p[1]); SB();
  t4[3] = *(LDS_AS const f32x4*)(ra + 57344);
  SB();
  SB();
  WAITVB(7); SB();
  MV16(acc[0], fw[4][0], p[2]); SB();
  LD16(p[1], vP0, sP, 0, "sc1");
  t4[4] = *(LDS_AS const f32x4*)(rah + 8192);
  SB();
  MV16(acc[1], fw[4][1], p[2]); SB();
  t4[5] = *(LDS_AS const f32x4*)(rah + 24576);
  SB();
  MV16(acc[2], fw[4][2], p[2]); SB();
  t4[6] = *(LDS_AS const f32x4*)(rah + 40960);
  t4[7] = *(LDS_AS const f32x4*)(rah + 57344);
  SB();
  MV16(acc[3], fw[4][3], p[2]); SB();
  { const int ix = 7 + up; vU = (ix >> 2) * 5120 + frag(ix & 3, uL); PINV(vU); }
  SB();
  SB();
  WAITVB(7); SB();
  MV16(acc[0], fw[5][0], p[3]); SB();
  LD16(p[2], vP1, sP, 0, "sc1");
  OP4_3("v_add_f32", mm, t4[0], bias);
  SB();
  MV16(acc[1], fw[5][1], p[3]); SB();
  OP4_3("v_add_f32", zz, t4[1], t4[2]);
  SB();
  MV16(acc[2], fw[5][2], p[3]); SB();
  ACC4("v_add_f32", mm, t4[3]);
  SB();
  MV16(acc[3], fw[5][3], p[3]); SB();
  ACC4("v_add_f32", zz, t4[4]);
  SB();
  SB();
  WAITVB(7); SB();
  PRIO(2); SB();
  MV16(acc[0], fw[6][0], p[4]); SB();
  LD16(p[3], vP2, sP, 0, "sc1");
  ACC4("v_add_f32", mm, t4[5]);
  SB();
  MV16(acc[1], fw[6][1], p[4]); SB();
  ACC4("v_add_f32", zz, t4[6]);
  SB();
  MV16(acc[2], fw[6][2], p[4]); SB();
  ACC4("v_add_f32", mm, t4[7]);
  SB();
  MV16(acc[3], fw[6][3], p[4]); SB();
  ACC4("v_add_f32", mm, zz);
  SB();
  SB();
  WAITVB(7); SB();
  MV16(acc[0], fw[7][0], p[5]); SB();
  LD16(p[4], vP3, sP, 0, "sc1"); sP += HU - 5120; OPQ(sP);
  OP4_3("v_mul_f32", zz, mm, mm);
  SB();
  MV16(acc[1], fw[7][1], p[5]); SB();
  POLY4(zz, c23);
  SB();
  MV16(acc[2], fw[7][2], p[5]); SB();
  ACC4("v_mul_f32", zz, mm);
  SB();
  MV16(acc[3], fw[7][3], p[5]); SB();
  UN4("v_exp_f32", "", "", zz);
  SB();
  SB();
  WAITVB(7); SB();
  MV16Z(acc[4], fw[0][0], p[6]); SB();
  LD16(p[5], vP0, sP, 0, "sc1");
  UN4("v_add_f32", "1.0, ", "", zz);
  SB();
  MV16Z(acc[5], fw[0][1], p[6]); SB();
  UN4("v_rcp_f32", "", "", zz);
  SB();
  MV16Z(acc[6], fw[0][2], p[6]); SB();
  NFMA4(mm, zz);
  SB();
  MV16Z(acc[7], fw[0][3], p[6]); SB();
  { const unsigned x0 = pack2(mm[0], mm[1]), x1 = pack2(mm[2], mm[3]);
    const auto r0 = __builtin_amdgcn_permlane16_swap(x0, x0, false, false), r1 = __builtin_amdgcn_permlane16_swap(x1, x1, false, false);
    ST16E(0xffff, vU, sU, ((u32x4){r0[0], r1[0], r0[1], r1[1]})); }
  SB();
  SB();
  WAITVB(8); SB();
  PRIO(1); SB();
  MV16(acc[4], fw[1][0], p[7]); SB();
  LD16(p[6], vP1, sP, 0, "sc1");
  SB();
  MV16(acc[5], fw[1][1], p[7]); SB();
  *(LDS_AS f32x4*)(wa0 + 12288) = acc[0];
  SB();
  MV16(acc[6], fw[1][2], p[7]); SB();
  *(LDS_AS f32x4*)(wa1 + 12288) = acc[1];
  SB();
  MV16(acc[7], fw[1][3], p[7]); SB();
  SB();
  WAITVB(8); SB();
  PRIO(0); SB();
  MV16(acc[4], fw[2][0], p[8]); SB();
  LD16(p[7], vP2, sP, 0, "sc1");
  SB();
  MV16(acc[5], fw[2][1], p[8]); SB();
  *(LDS_AS f32x4*)(wa2 + 12288) = acc[2];
  SB();
  MV16(acc[6], fw[2][2], p[8]); SB();
  *(LDS_AS f32x4*)(wa3 + 12288) = acc[3];
  SB();
  MV16(acc[7], fw[2][3], p[8]); SB();
  BAR();
  STAMP(6);
  SB();
  SB();
  WAITVB(8); SB();
  PRIO(3); SB();
  MV16(acc[4], fw[3][0], p[0]); SB();
  LD16(p[8], vP3, sP, 0, "sc1"); sP += 5120; OPQ(sP);
  SB();
  MV16(acc[5], fw[3][1], p[0]); SB();
  MV16(acc[6], fw[3][2], p[0]); SB();
  MV16(acc[7], fw[3][3], p[0]); SB();
  SB();
  WAITVB(8); SB();
  MV16(acc[4], fw[4][0], p[1]); SB();
  LD16(p[0], vP0, sP, 0, "sc1");
  SB();
  MV16(acc[5], fw[4][1], p[1]); SB();
  MV16(acc[6], fw[4][2], p[1]); SB();
  MV16(acc[7], fw[4][3], p[1]); SB();
  SB();
  WAITVB(8); SB();
  MV16(acc[4], fw[5][0], p[2]); SB();
  LD16(p[1], vP1, sP, 0, "sc1");
  SB();
  MV16(acc[5], fw[5][1], p[2]); SB();
  MV16(acc[6], fw[5][2], p[2]); SB();
  MV16(acc[7], fw[5][3], p[2]); SB();
  SB();
  WAITVB(8); SB();
  PRIO(2); SB();
  MV16(acc[4], fw[6][0], p[3]); SB();
  LD16(p[2], vP2, sP, 0, "sc1");
  SB();
  MV16(acc[5], fw[6][1], p[3]); SB();
  MV16(acc[6], fw[6][2], p[3]); SB();
  MV16(acc[7], fw[6][3], p[3]); SB();
  SB();
  WAITVB(8); SB();
  MV16(acc[4], fw[7][0], p[4]); SB();
  LD16(p[3], vP3, sP, 0, "sc1"); sP += HU - 5120; OPQ(sP);
  SB();
  MV16(acc[5], fw[7][1], p[4]); SB();
  MV16(acc[6], fw[7][2], p[4]); SB();
  MV16(acc[7], fw[7][3], p[4]); SB();
  SB();
  WAITVB(8); SB();
  MV16Z(acc[0], fw[0][0], p[5]); SB();
  LD16(p[4], vP0, sP, 0, "sc1");
  SB();
  MV16Z(acc[1], fw[0][1], p[5]); SB();
  MV16Z(acc[2], fw[0][2], p[5]); SB();
  MV16Z(acc[3], fw[0][3], p[5]); SB();
  SB();
  WAITVB(7); SB();
  PRIO(1); SB();
  MV16(acc[0], fw[1][0], p[6]); SB();
  LD16(p[5], vP1, sP, 0, "sc1");
  SB();
  MV16(acc[1], fw[1][1], p[6]); SB();
  *(LDS_AS f32x4*)(wa0 + 0) = acc[4];
  SB();
  MV16(acc[2], fw[1][2], p[6]); SB();
  *(LDS_AS f32x4*)(wa1 + 0) = acc[5];
  SB();
  MV16(acc[3], fw[1][3], p[6]); SB();
  { const char* f_ = sFU + 14 * FU; const unsigned l_ = lane_now() * 16; ST16X(1, l_, f_, nonce); }
  SB();
  SB();
  WAITVB(8); SB();
  PRIO(0); SB();
  MV16(acc[0], fw[2][0], p[7]); SB();
  LD16(p[6], vP2, sP, 0, "sc1");
  SB();
  MV16(acc[1], fw[2][1], p[7]); SB();
  *(LDS_AS f32x4*)(wa2 + 0) = acc[6];
  SB();
  MV16(acc[2], fw[2][2], p[7]); SB();
  *(LDS_AS f32x4*)(wa3 + 0) = acc[7];
  SB();
  MV16(acc[3], fw[2][3], p[7]); SB();
  BAR();
  SB();
  SB();
  WAITVB(8); SB();
  PRIO(3); SB();
  MV16(acc[0], fw[3][0], p[8]); SB();
  LD16(p[7], vP3, sP, 0, "sc1"); sP += 5120; OPQ(sP);
  t4[0] = *(LDS_AS const f32x4*)(ra + 0);
  SB();
  MV16(acc[1], fw[3][1], p[8]); SB();
  t4[1] = *(LDS_AS const f32x4*)(ra + 16384);
  SB();
  MV16(acc[2], fw[3][2], p[8]); SB();
  t4[2] = *(LDS_AS const f32x4*)(ra + 32768);
  SB();
  MV16(acc[3], fw[3][3], p[8]); SB();
  t4[3] = *(LDS_AS const f32x4*)(ra + 49152);
  SB();
  SB();
  WAITVB(8); SB();
  MV16(acc[0], fw[4][0], p[0]); SB();
  LD16(p[8], vP0, sP, 0, "sc1");
  t4[4] = *(LDS_AS const f32x4*)(rah + 0);
  SB();
  MV16(acc[1], fw[4][1], p[0]); SB();
  t4[5] = *(LDS_AS const f32x4*)(rah + 16384);
  SB();
  MV16(acc[2], fw[4][2], p[0]); SB();
  t4[6] = *(LDS_AS const f32x4*)(rah + 32768);
  t4[7] = *(LDS_AS const f32x4*)(rah + 49152);
  SB();
  MV16(acc[3], fw[4][3], p[0]); SB();
  { const int ix = 21 + up; vU = (ix >> 2) * 5120 + frag(ix & 3, uL); PINV(vU); }
  SB();
  SB();
  WAITVB(8); SB();
  MV16(acc[0], fw[5][0], p[1]); SB();
  LD16(p[0], vP1, sP, 0, "sc1");
  OP4_3("v_add_f32", mm, t4[0], bias);
  SB();
  MV16(acc[1], fw[5][1], p[1]); SB();
  OP4_3("v_add_f32", zz, t4[1], t4[2]);
  SB();
  MV16(acc[2], fw[5][2], p[1]); SB();
  ACC4("v_add_f32", mm, t4[3]);
  SB();
  MV16(acc[3], fw[5][3], p[1]); SB();
  ACC4("v_add_f32", zz, t4[4]);
  SB();
  SB();
  WAITVB(8); SB();
  PRIO(2); SB();
  MV16(acc[0], fw[6][0], p[2]); SB();
  LD16(p[1], vP2, sP, 0, "sc1");
  ACC4("v_add_f32", mm, t4[5]);
  SB();
  MV16(acc[1], fw[6][1], p[2]); SB();
  ACC4("v_add_f32", zz, t4[6]);
  SB();
  MV16(acc[2], fw[6][2], p[2]); SB();
  ACC4("v_add_f32", mm, t4[7]);
  SB();
  MV16(acc[3], fw[6][3], p[2]); SB();
  ACC4("v_add_f32", mm, zz);
  SB();
  SB();
  WAITVB(8); SB();
  MV16(acc[0], fw[7][0], p[3]); SB();
  LD16(p[2], vP3, sP, 0, "sc1"); sP += HU - 5120; OPQ(sP);
  OP4_3("v_mul_f32", zz, mm, mm);
  SB();
  MV16(acc[1], fw[7][1], p[3]); SB();
  POLY4(zz, c23);
  SB();
  MV16(acc[2], fw[7][2], p[3]); SB();
  ACC4("v_mul_f32", zz, mm);
  SB();
  MV16(acc[3], fw[7][3], p[3]); SB();
  UN4("v_exp_f32", "", "", zz);
  SB();
  SB();
  WAITVB(8); SB();
  MV16Z(acc[4], fw[0][0], p[4]); SB();
  LD16(p[3], vP0, sP, 0, "sc1");
  UN4("v_add_f32", "1.0, ", "", zz);
  SB();
  MV16Z(acc[5], fw[0][1], p[4]); SB();
  UN4("v_rcp_f32", "", "", zz);
  SB();
  MV16Z(acc[6], fw[0][2], p[4]); SB();
  NFMA4(mm, zz);
  SB();
  MV16Z(acc[7], fw[0][3], p[4]); SB();
  { const unsigned x0 = pack2(mm[0], mm[1]), x1 = pack2(mm[2], mm[3]);
    const auto r0 = __builtin_amdgcn_permlane16_swap(x0, x0, false, false), r1 = __builtin_amdgcn_permlane16_swap(x1, x1, false, false);
    ST16E(0xffff, vU, sU, ((u32x4){r0[0], r1[0], r0[1], r1[1]})); }
  SB();
  SB();
  WAITVB(9); SB();
  PRIO(1); SB();
  MV16(acc[4], fw[1][0], p[5]); SB();
  LD16(p[4], vP1, sP, 0, "sc1");
  SB();
  MV16(acc[5], fw[1][1], p[5]); SB();
  *(LDS_AS f32x4*)(wa0 + 4096) = acc[0];
  SB();
  MV16(acc[6], fw[1][2], p[5]); SB();
  *(LDS_AS f32x4*)(wa1 + 4096) = acc[1];
  SB();
  MV16(acc[7], fw[1][3], p[5]); SB();
  SB();
  WAITVB(8); SB();
  PRIO(0); SB();
  MV16(acc[4], fw[2][0], p[6]); SB();
  LD16(p[5], vP2, sP, 0, "sc1");
  SB();
  MV16(acc[5], fw[2][1], p[6]); SB();
  *(LDS_AS f32x4*)(wa2 + 4096) = acc[2];
  SB();
  MV16(acc[6], fw[2][2], p[6]); SB();
  *(LDS_AS f32x4*)(wa3 + 4096) = acc[3];
  SB();
  MV16(acc[7], fw[2][3], p[6]); SB();
  BAR();
  STAMP(7);
  SB();
  SB();
  WAITVB(8); SB();
  PRIO(3); SB();
  MV16(acc[4], fw[3][0], p[7]); SB();
  LD16(p[6], vP3, sP, 0, "sc1"); sP += 5120; OPQ(sP);
  SB();
  MV16(acc[5], fw[3][1], p[7]); SB();
  MV16(acc[6], fw[3][2], p[7]); SB();
  MV16(acc[7], fw[3][3], p[7]); SB();
  SB();
  WAITVB(8); SB();
  MV16(acc[4], fw[4][0], p[8]); SB();
  LD16(p[7], vP0, sP, 0, "sc1");
  SB();
  MV16(acc[5], fw[4][1], p[8]); SB();
  MV16(acc[6], fw[4][2], p[8]); SB();
  MV16(acc[7], fw[4][3], p[8]); SB();
  SB();
  WAITVB(8); SB();
  MV16(acc[4], fw[5][0], p[0]); SB();
  LD16(p[8], vP1, sP, 0, "sc1");
  SB();
  MV16(acc[5], fw[5][1], p[0]); SB();
  MV16(acc[6], fw[5][2], p[0]); SB();
  MV16(acc[7], fw[5][3], p[0]); SB();
  SB();
  WAITVB(8); SB();
  PRIO(2); SB();
  MV16(acc[4], fw[6][0], p[1]); SB();
  LD16(p[0], vP2, sP, 0, "sc1");
  SB();
  MV16(acc[5], fw[6][1], p[1]); SB();
  MV16(acc[6], fw[6][2], p[1]); SB();
  MV16(acc[7], fw[6][3], p[1]); SB();
  SB();
  WAITVB(8); SB();
  MV16(acc[4], fw[7][0], p[2]); SB();
  LD16(p[1], vP3, sP, 0, "sc1");
  SB();
  MV16(acc[5], fw[7][1], p[2]); SB();
  MV16(acc[6], fw[7][2], p[2]); SB();
  MV16(acc[7], fw[7][3], p[2]); SB();
  SB();
  WAITVB(8); SB();
  MV16Z(acc[0], fw[0][0], p[3]); SB();
  bf16x8 fo[14];
  const char* sWo = A.ws + (WS_WO + o_wo(dg * 7 + (w < 7 ? w : 6))); unsigned long long mW = w < 7 ? ~0ull : 1ull; OPQ(sWo); OPQ(mW);
  SB();
  MV16Z(acc[1], fw[0][1], p[3]); SB();
  MV16Z(acc[2], fw[0][2], p[3]); SB();
  MV16Z(acc[3], fw[0][3], p[3]); SB();
  SB();
  WAITVB(6); SB();
  PRIO(1); SB();
  MV16(acc[0], fw[1][0], p[4]); SB();
  *(LDS_AS f32x4*)(wa0 + 8192) = acc[4];
  SB();
  MV16(acc[1], fw[1][1], p[4]); SB();
  *(LDS_AS f32x4*)(wa1 + 8192) = acc[5];
  SB();
  MV16(acc[2], fw[1][2], p[4]); SB();
  { const char* f_ = sFU + 42 * FU; const unsigned l_ = lane_now() * 16; ST16X(1, l_, f_, nonce); }
  SB();
  MV16(acc[3], fw[1][3], p[4]); SB();
  SB();
  WAITVB(6); SB();
  PRIO(0); SB();
  MV16(acc[0], fw[2][0], p[5]); SB();
  *(LDS_AS f32x4*)(wa2 + 8192) = acc[6];
  SB();
  MV16(acc[1], fw[2][1], p[5]); SB();
  *(LDS_AS f32x4*)(wa3 + 8192) = acc[7];
  SB();
  MV16(acc[2], fw[2][2], p[5]); SB();
  BAR();
  SB();
  MV16(acc[3], fw[2][3], p[5]); SB();
  SB();
  WAITVB(5); SB();
  PRIO(2); SB();
  MV16(acc[0], fw[3][0], p[6]); SB();
  t4[0] = *(LDS_AS const f32x4*)(ra + 8192);
  SB();
  MV16(acc[1], fw[3][1], p[6]); SB();
  t4[1] = *(LDS_AS const f32x4*)(ra + 24576);
  SB();
  MV16(acc[2], fw[3][2], p[6]); SB();
  t4[2] = *(LDS_AS const f32x4*)(ra + 40960);
  SB();
  MV16(acc[3], fw[3][3], p[6]); SB();
  t4[3] = *(LDS_AS const f32x4*)(ra + 57344);
  SB();
  SB();
  WAITVB(4); SB();
  MV16(acc[0], fw[4][0], p[7]); SB();
  t4[4] = *(LDS_AS const f32x4*)(rah + 8192);
  t4[5] = *(LDS_AS const f32x4*)(rah + 24576);
  SB();
  MV16(acc[1], fw[4][1], p[7]); SB();
  t4[6] = *(LDS_AS const f32x4*)(rah + 40960);
  SB();
  MV16(acc[2], fw[4][2], p[7]); SB();
  t4[7] = *(LDS_AS const f32x4*)(rah + 57344);
  SB();
  MV16(acc[3], fw[4][3], p[7]); SB();
  { const int ix = 35 + up; vU = (ix >> 2) * 5120 + frag(ix & 3, uL); PINV(vU); }
  SB();
  SB();
  WAITVB(3); SB();
  MV16(acc[0], fw[5][0], p[8]); SB();
  LD16M(fo[0], vP0, sWo, mW);
  LD16M(fo[1], vP1, sWo, mW);
  OP4_3("v_add_f32", mm, t4[0], bias);
  SB();
  MV16(acc[1], fw[5][1], p[8]); SB();
  OP4_3("v_add_f32", zz, t4[1], t4[2]);
  SB();
  MV16(acc[2], fw[5][2], p[8]); SB();
  ACC4("v_add_f32", mm, t4[3]);
  SB();
  MV16(acc[3], fw[5][3], p[8]); SB();
  ACC4("v_add_f32", zz, t4[4]);
  SB();
  SB();
  WAITVB(4); SB();
  PRIO(1); SB();
  MV16(acc[0], fw[6][0], p[0]); SB();
  LD16M(fo[2], vP2, sWo, mW);
  LD16M(fo[3], vP3, sWo, mW); sWo += 5120; OPQ(sWo);
  ACC4("v_add_f32", mm, t4[5]);
  SB();
  MV16(acc[1], fw[6][1], p[0]); SB();
  ACC4("v_add_f32", zz, t4[6]);
  SB();
  MV16(acc[2], fw[6][2], p[0]); SB();
  ACC4("v_add_f32", mm, t4[7]);
  SB();
  MV16(acc[3], fw[6][3], p[0]); SB();
  ACC4("v_add_f32", mm, zz);
  SB();
  SB();
  WAITVB(5); SB();
  PRIO(0); SB();
  MV16(acc[0], fw[7][0], p[1]); SB();
  LD16M(fo[4], vP0, sWo, mW);
  LD16M(fo[5], vP1, sWo, mW);
  OP4_3("v_mul_f32", zz, mm, mm);
  SB();
  MV16(acc[1], fw[7][1], p[1]); SB();
  POLY4(zz, c23);
  SB();
  MV16(acc[2], fw[7][2], p[1]); SB();
  ACC4("v_mul_f32", zz, mm);
  SB();
  MV16(acc[3], fw[7][3], p[1]); SB();
  UN4("v_exp_f32", "", "", zz);
  SB();
  NOP(4); SB();
  *(LDS_AS f32x4*)(wa0 + 12288) = acc[0];
  SB();
  *(LDS_AS f32x4*)(wa1 + 12288) = acc[1];
  SB();
  *(LDS_AS f32x4*)(wa2 + 12288) = acc[2];
  SB();
  *(LDS_AS f32x4*)(wa3 + 12288) = acc[3];
  SB();
  BAR();
  STAMP(8);
  UN4("v_add_f32", "1.0, ", "", zz);
  UN4("v_rcp_f32", "", "", zz);
  NFMA4(mm, zz);
  { const unsigned x0 = pack2(mm[0], mm[1]), x1 = pack2(mm[2], mm[3]);
    const auto r0 = __builtin_amdgcn_permlane16_swap(x0, x0, false, false), r1 = __builtin_amdgcn_permlane16_swap(x1, x1, false, false);
    ST16E(0xffff, vU, sU, ((u32x4){r0[0], r1[0], r0[1], r1[1]})); }
  STAMP(9);
  LD16M(fo[6], vP2, sWo, mW);
  LD16M(fo[7], vP3, sWo, mW); sWo += 5120; OPQ(sWo);
  WAITV(2);
  { const char* f_ = sFU + 70 * FU; const unsigned l_ = lane_now() * 16; ST16X(1, l_, f_, nonce); }
  LD16M(fo[8], vP0, sWo, mW);
  LD16M(fo[9], vP1, sWo, mW);
  LD16M(fo[10], vP2, sWo, mW);
  LD16M(fo[11], vP3, sWo, mW); sWo += 5120; OPQ(sWo);
  LD16M(fo[12], vP0, sWo, mW);
  LD16M(fo[13], vP1, sWo, mW);
  { int w_ = w; OPQ(w_); { const int w = w_ & 7, lane = lane_now(); const int n16 = lane & 15, g4 = lane >> 4, q4 = n16 >> 2, pq = lane & 3, r8 = lane >> 3; const unsigned vL16 = lane * 16;
  unsigned vO[7];
#pragma unroll
  for (int i = 0; i < 7; ++i) { vO[i] = line7(64 * i + 8 * w + r8) + (lane & 7) * 16; PINV(vO[i]); }
  const char* sO = A.ws + (WS_OT + o_4(st));
  unsigned vFO = (((lane >> 2) & 7) * 8 + (lane & 3)) * FO, ro = lds0 + vL16, roh = ro + 65536;
  unsigned vS0 = pair3(q4) + (2 * (n16 & 3) + (g4 & 1)) * 32 + (g4 >> 1) * 16, vS1 = vS0 - pair3(q4) + pair3(4 + q4);
  const char* sFO = A.ws + WS_FLAGO + ((st >> 1) * 64 + (st & 1) * 4) * FO;
  const char* sS = A.ws + WS_ST + (size_t)(st * 4 + dg) * ST_T + w * 3072;
  const char* sFS = A.ws + WS_FLAGS + ((st * 4 + dg) * 8 + w) * FS;
  asm volatile("" : "+s"(sO), "+s"(sFO), "+s"(sS), "+s"(sFS)); asm volatile("" : "+v"(vFO), "+v"(ro), "+v"(roh), "+v"(vS0), "+v"(vS1));
  STAMP(10);
  STAMP(11);
  if (w == 7) poll1<3>(sFO, vFO, nonce);
  BAR();
  STAMP(12);
  bf16x8 of[3][4]; f32x4 as[4];
  DMA1(vO[0], sO, ldsP, 0, "sc1");
  DMA1(vO[1], sO, ldsP, 1024, "sc1");
  DMA1(vO[2], sO, ldsP, 2048, "sc1");
  DMA1(vO[3], sO, ldsP, 3072, "sc1");
  DMA1(vO[4], sO, ldsP, 4096, "sc1");
  DMA1(vO[5], sO, ldsP, 5120, "sc1");
  DMA1(vO[6], sO, ldsP, 6144, "sc1");
  WAITV(4);
  BAR0();
  if (w == 7) {
  WAITV(0);
  BAR0();
  } else {
  of[0][0] = *(LDS_AS const bf16x8*)(ro + 0);
  of[0][1] = *(LDS_AS const bf16x8*)(ro + 16384);
  of[0][2] = *(LDS_AS const bf16x8*)(ro + 32768);
  of[0][3] = *(LDS_AS const bf16x8*)(ro + 49152);
  STAMP(13);
  of[1][0] = *(LDS_AS const bf16x8*)(roh + 0);
  of[1][1] = *(LDS_AS const bf16x8*)(roh + 16384);
  of[1][2] = *(LDS_AS const bf16x8*)(roh + 32768);
  of[1][3] = *(LDS_AS const bf16x8*)(roh + 49152);
  SB();
  MV16Z(as[0], fo[0], of[0][0]); SB();
  of[2][0] = *(LDS_AS const bf16x8*)(ro + 1024);
  SB();
  MV16Z(as[1], fo[0], of[0][1]); SB();
  of[2][1] = *(LDS_AS const bf16x8*)(ro + 17408);
  SB();
  MV16Z(as[2], fo[0], of[0][2]); SB();
  of[2][2] = *(LDS_AS const bf16x8*)(ro + 33792);
  SB();
  MV16Z(as[3], fo[0], of[0][3]); SB();
  of[2][3] = *(LDS_AS const bf16x8*)(ro + 50176);
  SB();
  SB();
  MV16(as[0], fo[1], of[1][0]); SB();
  of[0][0] = *(LDS_AS const bf16x8*)(roh + 1024);
  SB();
  MV16(as[1], fo[1], of[1][1]); SB();
  of[0][1] = *(LDS_AS const bf16x8*)(roh + 17408);
  SB();
  MV16(as[2], fo[1], of[1][2]); SB();
  of[0][2] = *(LDS_AS const bf16x8*)(roh + 33792);
  SB();
  MV16(as[3], fo[1], of[1][3]); SB();
  of[0][3] = *(LDS_AS const bf16x8*)(roh + 50176);
  SB();
  SB();
  MV16(as[0], fo[2], of[2][0]); SB();
  of[1][0] = *(LDS_AS const bf16x8*)(ro + 2048);
  SB();
  MV16(as[1], fo[2], of[2][1]); SB();
  of[1][1] = *(LDS_AS const bf16x8*)(ro + 18432);
  SB();
  MV16(as[2], fo[2], of[2][2]); SB();
  of[1][2] = *(LDS_AS const bf16x8*)(ro + 34816);
  SB();
  MV16(as[3], fo[2], of[2][3]); SB();
  of[1][3] = *(LDS_AS const bf16x8*)(ro + 51200);
  SB();
  SB();
  MV16(as[0], fo[3], of[0][0]); SB();
  of[2][0] = *(LDS_AS const bf16x8*)(roh + 2048);
  SB();
  MV16(as[1], fo[3], of[0][1]); SB();
  of[2][1] = *(LDS_AS const bf16x8*)(roh + 18432);
  SB();
  MV16(as[2], fo[3], of[0][2]); SB();
  of[2][2] = *(LDS_AS const bf16x8*)(roh + 34816);
  SB();
  MV16(as[3], fo[3], of[0][3]); SB();
  of[2][3] = *(LDS_AS const bf16x8*)(roh + 51200);
  SB();
  SB();
  MV16(as[0], fo[4], of[1][0]); SB();
  WAITV(0);
  BAR0();
  of[0][0] = *(LDS_AS const bf16x8*)(ro + 3072);
  SB();
  MV16(as[1], fo[4], of[1][1]); SB();
  of[0][1] = *(LDS_AS const bf16x8*)(ro + 19456);
  SB();
  MV16(as[2], fo[4], of[1][2]); SB();
  of[0][2] = *(LDS_AS const bf16x8*)(ro + 35840);
  SB();
  MV16(as[3], fo[4], of[1][3]); SB();
  of[0][3] = *(LDS_AS const bf16x8*)(ro + 52224);
  SB();
  SB();
  MV16(as[0], fo[5], of[2][0]); SB();
  of[1][0] = *(LDS_AS const bf16x8*)(roh + 3072);
  SB();
  MV16(as[1], fo[5], of[2][1]); SB();
  of[1][1] = *(LDS_AS const bf16x8*)(roh + 19456);
  SB();
  MV16(as[2], fo[5], of[2][2]); SB();
  of[1][2] = *(LDS_AS const bf16x8*)(roh + 35840);
  SB();
  MV16(as[3], fo[5], of[2][3]); SB();
  of[1][3] = *(LDS_AS const bf16x8*)(roh + 52224);
  SB();
  SB();
  MV16(as[0], fo[6], of[0][0]); SB();
  of[2][0] = *(LDS_AS const bf16x8*)(ro + 4096);
  SB();
  MV16(as[1], fo[6], of[0][1]); SB();
  of[2][1] = *(LDS_AS const bf16x8*)(ro + 20480);
  SB();
  MV16(as[2], fo[6], of[0][2]); SB();
  of[2][2] = *(LDS_AS const bf16x8*)(ro + 36864);
  SB();
  MV16(as[3], fo[6], of[0][3]); SB();
  of[2][3] = *(LDS_AS const bf16x8*)(ro + 53248);
  SB();
  SB();
  MV16(as[0], fo[7], of[1][0]); SB();
  of[0][0] = *(LDS_AS const bf16x8*)(roh + 4096);
  SB();
  MV16(as[1], fo[7], of[1][1]); SB();
  of[0][1] = *(LDS_AS const bf16x8*)(roh + 20480);
  SB();
  MV16(as[2], fo[7], of[1][2]); SB();
  of[0][2] = *(LDS_AS const bf16x8*)(roh + 36864);
  SB();
  MV16(as[3], fo[7], of[1][3]); SB();
  of[0][3] = *(LDS_AS const bf16x8*)(roh + 53248);
  SB();
  SB();
  MV16(as[0], fo[8], of[2][0]); SB();
  of[1][0] = *(LDS_AS const bf16x8*)(ro + 5120);
  SB();
  MV16(as[1], fo[8], of[2][1]); SB();
  of[1][1] = *(LDS_AS const bf16x8*)(ro + 21504);
  SB();
  MV16(as[2], fo[8], of[2][2]); SB();
  of[1][2] = *(LDS_AS const bf16x8*)(ro + 37888);
  SB();
  MV16(as[3], fo[8], of[2][3]); SB();
  of[1][3] = *(LDS_AS const bf16x8*)(ro + 54272);
  SB();
  SB();
  MV16(as[0], fo[9], of[0][0]); SB();
  of[2][0] = *(LDS_AS const bf16x8*)(roh + 5120);
  SB();
  MV16(as[1], fo[9], of[0][1]); SB();
  of[2][1] = *(LDS_AS const bf16x8*)(roh + 21504);
  SB();
  MV16(as[2], fo[9], of[0][2]); SB();
  of[2][2] = *(LDS_AS const bf16x8*)(roh + 37888);
  SB();
  MV16(as[3], fo[9], of[0][3]); SB();
  of[2][3] = *(LDS_AS const bf16x8*)(roh + 54272);
  SB();
  SB();
  MV16(as[0], fo[10], of[1][0]); SB();
  of[0][0] = *(LDS_AS const bf16x8*)(ro + 6144);
  SB();
  MV16(as[1], fo[10], of[1][1]); SB();
  of[0][1] = *(LDS_AS const bf16x8*)(ro + 22528);
  SB();
  MV16(as[2], fo[10], of[1][2]); SB();
  of[0][2] = *(LDS_AS const bf16x8*)(ro + 38912);
  SB();
  MV16(as[3], fo[10], of[1][3]); SB();
  of[0][3] = *(LDS_AS const bf16x8*)(ro + 55296);
  SB();
  SB();
  MV16(as[0], fo[11], of[2][0]); SB();
  of[1][0] = *(LDS_AS const bf16x8*)(roh + 6144);
  SB();
  MV16(as[1], fo[11], of[2][1]); SB();
  of[1][1] = *(LDS_AS const bf16x8*)(roh + 22528);
  SB();
  MV16(as[2], fo[11], of[2][2]); SB();
  of[1][2] = *(LDS_AS const bf16x8*)(roh + 38912);
  SB();
  MV16(as[3], fo[11], of[2][3]); SB();
  of[1][3] = *(LDS_AS const bf16x8*)(roh + 55296);
  SB();
  SB();
  MV16(as[0], fo[12], of[0][0]); SB();
  MV16(as[1], fo[12], of[0][1]); SB();
  MV16(as[2], fo[12], of[0][2]); SB();
  MV16(as[3], fo[12], of[0][3]); SB();
  SB();
  MV16(as[0], fo[13], of[1][0]); SB();
  MV16(as[1], fo[13], of[1][1]); SB();
  MV16(as[2], fo[13], of[1][2]); SB();
  MV16(as[3], fo[13], of[1][3]); SB();
  NOP(5); SB();
  { const auto r0 = __builtin_amdgcn_permlane16_swap(pack2(as[0][0], as[0][1]), pack2(as[1][0], as[1][1]), false, false);
    const auto r1 = __builtin_amdgcn_permlane16_swap(pack2(as[0][2], as[0][3]), pack2(as[1][2], as[1][3]), false, false);
    ST16(vS0, sS, 0, ((u32x4){r0[0], r1[0], r0[1], r1[1]})); } SB();
  { const auto r0 = __builtin_amdgcn_permlane16_swap(pack2(as[2][0], as[2][1]), pack2(as[3][0], as[3][1]), false, false);
    const auto r1 = __builtin_amdgcn_permlane16_swap(pack2(as[2][2], as[2][3]), pack2(as[3][2], as[3][3]), false, false);
    ST16(vS1, sS, 0, ((u32x4){r0[0], r1[0], r0[1], r1[1]})); } SB();
  STAMP(14);
  WAITV(0);
  ST16X(15, vL16, sFS, nonce);
  STAMP(15);
  }
  STAMP_DUMP();
  }}}}
  }
  } else {
  WAITV(0);
  __builtin_amdgcn_s_sleep(40);
  poll1<16>(sFH, vFH, nonce);
  STAMP(3);
  __builtin_amdgcn_s_sleep(64);
  BAR();
  const unsigned vP0 = frag(0, lane), vP1 = frag(1, lane), vP2 = frag(2, lane), vP3 = frag(3, lane);
  bf16x8 fo[14];
  const char* sWo = A.ws + (WS_WO + o_wo(dg * 7 + (w < 7 ? w : 6))); unsigned long long mW = w < 7 ? ~0ull : 1ull; OPQ(sWo); OPQ(mW);
  LD16M(fo[0], vP0, sWo, mW);
  LD16M(fo[1], vP1, sWo, mW);
  LD16M(fo[2], vP2, sWo, mW);
  LD16M(fo[3], vP3, sWo, mW); sWo += 5120; OPQ(sWo);
  LD16M(fo[4], vP0, sWo, mW);
  LD16M(fo[5], vP1, sWo, mW);
  LD16M(fo[6], vP2, sWo, mW);
  LD16M(fo[7], vP3, sWo, mW); sWo += 5120; OPQ(sWo);
  LD16M(fo[8], vP0, sWo, mW);
  LD16M(fo[9], vP1, sWo, mW);
  LD16M(fo[10], vP2, sWo, mW);
  LD16M(fo[11], vP3, sWo, mW); sWo += 5120; OPQ(sWo);
  LD16M(fo[12], vP0, sWo, mW);
  LD16M(fo[13], vP1, sWo, mW);
  { int w_ = w; OPQ(w_); { const int w = w_ & 7, lane = lane_now(); const int n16 = lane & 15, g4 = lane >> 4, q4 = n16 >> 2, pq = lane & 3, r8 = lane >> 3; const unsigned vL16 = lane * 16;
  unsigned vO[7];
#pragma unroll
  for (int i = 0; i < 7; ++i) { vO[i] = line7(64 * i + 8 * w + r8) + (lane & 7) * 16; PINV(vO[i]); }
  const char* sO = A.ws + (WS_OT + o_4(st));
  unsigned vFO = (((lane >> 2) & 7) * 8 + (lane & 3)) * FO, ro = lds0 + vL16, roh = ro + 65536;
  unsigned vS0 = pair3(q4) + (2 * (n16 & 3) + (g4 & 1)) * 32 + (g4 >> 1) * 16, vS1 = vS0 - pair3(q4) + pair3(4 + q4);
  const char* sFO = A.ws + WS_FLAGO + ((st >> 1) * 64 + (st & 1) * 4) * FO;
  const char* sS = A.ws + WS_ST + (size_t)(st * 4 + dg) * ST_T + w * 3072;
  const char* sFS = A.ws + WS_FLAGS + ((st * 4 + dg) * 8 + w) * FS;
  asm volatile("" : "+s"(sO), "+s"(sFO), "+s"(sS), "+s"(sFS)); asm volatile("" : "+v"(vFO), "+v"(ro), "+v"(roh), "+v"(vS0), "+v"(vS1));
  STAMP(11);
  if (w == 7) poll1<3>(sFO, vFO, nonce);
  BAR();
  STAMP(12);
  bf16x8 of[3][4]; f32x4 as[4];
  DMA1(vO[0], sO, ldsP, 0, "sc1");
  DMA1(vO[1], sO, ldsP, 1024, "sc1");
  DMA1(vO[2], sO, ldsP, 2048, "sc1");
  DMA1(vO[3], sO, ldsP, 3072, "sc1");
  DMA1(vO[4], sO, ldsP, 4096, "sc1");
  DMA1(vO[5], sO, ldsP, 5120, "sc1");
  DMA1(vO[6], sO, ldsP, 6144, "sc1");
  WAITV(4);
  BAR0();
  if (w == 7) {
  WAITV(0);
  BAR0();
  } else {
  of[0][0] = *(LDS_AS const bf16x8*)(ro + 0);
  of[0][1] = *(LDS_AS const bf16x8*)(ro + 16384);
  of[0][2] = *(LDS_AS const bf16x8*)(ro + 32768);
  of[0][3] = *(LDS_AS const bf16x8*)(ro + 49152);
  STAMP(13);
  of[1][0] = *(LDS_AS const bf16x8*)(roh + 0);
  of[1][1] = *(LDS_AS const bf16x8*)(roh + 16384);
  of[1][2] = *(LDS_AS const bf16x8*)(roh + 32768);
  of[1][3] = *(LDS_AS const bf16x8*)(roh + 49152);
  SB();
  MV16Z(as[0], fo[0], of[0][0]); SB();
  of[2][0] = *(LDS_AS const bf16x8*)(ro + 1024);
  SB();
  MV16Z(as[1], fo[0], of[0][1]); SB();
  of[2][1] = *(LDS_AS const bf16x8*)(ro + 17408);
  SB();
  MV16Z(as[2], fo[0], of[0][2]); SB();
  of[2][2] = *(LDS_AS const bf16x8*)(ro + 33792);
  SB();
  MV16Z(as[3], fo[0], of[0][3]); SB();
  of[2][3] = *(LDS_AS const bf16x8*)(ro + 50176);
  SB();
  SB();
  MV16(as[0], fo[1], of[1][0]); SB();
  of[0][0] = *(LDS_AS const bf16x8*)(roh + 1024);
  SB();
  MV16(as[1], fo[1], of[1][1]); SB();
  of[0][1] = *(LDS_AS const bf16x8*)(roh + 17408);
  SB();
  MV16(as[2], fo[1], of[1][2]); SB();
  of[0][2] = *(LDS_AS const bf16x8*)(roh + 33792);
  SB();
  MV16(as[3], fo[1], of[1][3]); SB();
  of[0][3] = *(LDS_AS const bf16x8*)(roh + 50176);
  SB();
  SB();
  MV16(as[0], fo[2], of[2][0]); SB();
  of[1][0] = *(LDS_AS const bf16x8*)(ro + 2048);
  SB();
  MV16(as[1], fo[2], of[2][1]); SB();
  of[1][1] = *(LDS_AS const bf16x8*)(ro + 18432);
  SB();
  MV16(as[2], fo[2], of[2][2]); SB();
  of[1][2] = *(LDS_AS const bf16x8*)(ro + 34816);
  SB();
  MV16(as[3], fo[2], of[2][3]); SB();
  of[1][3] = *(LDS_AS const bf16x8*)(ro + 51200);
  SB();
  SB();
  MV16(as[0], fo[3], of[0][0]); SB();
  of[2][0] = *(LDS_AS const bf16x8*)(roh + 2048);
  SB();
  MV16(as[1], fo[3], of[0][1]); SB();
  of[2][1] = *(LDS_AS const bf16x8*)(roh + 18432);
  SB();
  MV16(as[2], fo[3], of[0][2]); SB();
  of[2][2] = *(LDS_AS const bf16x8*)(roh + 34816);
  SB();
  MV16(as[3], fo[3], of[0][3]); SB();
  of[2][3] = *(LDS_AS const bf16x8*)(roh + 51200);
  SB();
  SB();
  MV16(as[0], fo[4], of[1][0]); SB();
  WAITV(0);
  BAR0();
  of[0][0] = *(LDS_AS const bf16x8*)(ro + 3072);
  SB();
  MV16(as[1], fo[4], of[1][1]); SB();
  of[0][1] = *(LDS_AS const bf16x8*)(ro + 19456);
  SB();
  MV16(as[2], fo[4], of[1][2]); SB();
  of[0][2] = *(LDS_AS const bf16x8*)(ro + 35840);
  SB();
  MV16(as[3], fo[4], of[1][3]); SB();
  of[0][3] = *(LDS_AS const bf16x8*)(ro + 52224);
  SB();
  SB();
  MV16(as[0], fo[5], of[2][0]); SB();
  of[1][0] = *(LDS_AS const bf16x8*)(roh + 3072);
  SB();
  MV16(as[1], fo[5], of[2][1]); SB();
  of[1][1] = *(LDS_AS const bf16x8*)(roh + 19456);
  SB();
  MV16(as[2], fo[5], of[2][2]); SB();
  of[1][2] = *(LDS_AS const bf16x8*)(roh + 35840);
  SB();
  MV16(as[3], fo[5], of[2][3]); SB();
  of[1][3] = *(LDS_AS const bf16x8*)(roh + 52224);
  SB();
  SB();
  MV16(as[0], fo[6], of[0][0]); SB();
  of[2][0] = *(LDS_AS const bf16x8*)(ro + 4096);
  SB();
  MV16(as[1], fo[6], of[0][1]); SB();
  of[2][1] = *(LDS_AS const bf16x8*)(ro + 20480);
  SB();
  MV16(as[2], fo[6], of[0][2]); SB();
  of[2][2] = *(LDS_AS const bf16x8*)(ro + 36864);
  SB();
  MV16(as[3], fo[6], of[0][3]); SB();
  of[2][3] = *(LDS_AS const bf16x8*)(ro + 53248);
  SB();
  SB();
  MV16(as[0], fo[7], of[1][0]); SB();
  of[0][0] = *(LDS_AS const bf16x8*)(roh + 4096);
  SB();
  MV16(as[1], fo[7], of[1][1]); SB();
  of[0][1] = *(LDS_AS const bf16x8*)(roh + 20480);
  SB();
  MV16(as[2], fo[7], of[1][2]); SB();
  of[0][2] = *(LDS_AS const bf16x8*)(roh + 36864);
  SB();
  MV16(as[3], fo[7], of[1][3]); SB();
  of[0][3] = *(LDS_AS const bf16x8*)(roh + 53248);
  SB();
  SB();
  MV16(as[0], fo[8], of[2][0]); SB();
  of[1][0] = *(LDS_AS const bf16x8*)(ro + 5120);
  SB();
  MV16(as[1], fo[8], of[2][1]); SB();
  of[1][1] = *(LDS_AS const bf16x8*)(ro + 21504);
  SB();
  MV16(as[2], fo[8], of[2][2]); SB();
  of[1][2] = *(LDS_AS const bf16x8*)(ro + 37888);
  SB();
  MV16(as[3], fo[8], of[2][3]); SB();
  of[1][3] = *(LDS_AS const bf16x8*)(ro + 54272);
  SB();
  SB();
  MV16(as[0], fo[9], of[0][0]); SB();
  of[2][0] = *(LDS_AS const bf16x8*)(roh + 5120);
  SB();
  MV16(as[1], fo[9], of[0][1]); SB();
  of[2][1] = *(LDS_AS const bf16x8*)(roh + 21504);
  SB();
  MV16(as[2], fo[9], of[0][2]); SB();
  of[2][2] = *(LDS_AS const bf16x8*)(roh + 37888);
  SB();
  MV16(as[3], fo[9], of[0][3]); SB();
  of[2][3] = *(LDS_AS const bf16x8*)(roh + 54272);
  SB();
  SB();
  MV16(as[0], fo[10], of[1][0]); SB();
  of[0][0] = *(LDS_AS const bf16x8*)(ro + 6144);
  SB();
  MV16(as[1], fo[10], of[1][1]); SB();
  of[0][1] = *(LDS_AS const bf16x8*)(ro + 22528);
  SB();
  MV16(as[2], fo[10], of[1][2]); SB();
  of[0][2] = *(LDS_AS const bf16x8*)(ro + 38912);
  SB();
  MV16(as[3], fo[10], of[1][3]); SB();
  of[0][3] = *(LDS_AS const bf16x8*)(ro + 55296);
  SB();
  SB();
  MV16(as[0], fo[11], of[2][0]); SB();
  of[1][0] = *(LDS_AS const bf16x8*)(roh + 6144);
  SB();
  MV16(as[1], fo[11], of[2][1]); SB();
  of[1][1] = *(LDS_AS const bf16x8*)(roh + 22528);
  SB();
  MV16(as[2], fo[11], of[2][2]); SB();
  of[1][2] = *(LDS_AS const bf16x8*)(roh + 38912);
  SB();
  MV16(as[3], fo[11], of[2][3]); SB();
  of[1][3] = *(LDS_AS const bf16x8*)(roh + 55296);
  SB();
  SB();
  MV16(as[0], fo[12], of[0][0]); SB();
  MV16(as[1], fo[12], of[0][1]); SB();
  MV16(as[2], fo[12], of[0][2]); SB();
  MV16(as[3], fo[12], of[0][3]); SB();
  SB();
  MV16(as[0], fo[13], of[1][0]); SB();
  MV16(as[1], fo[13], of[1][1]); SB();
  MV16(as[2], fo[13], of[1][2]); SB();
  MV16(as[3], fo[13], of[1][3]); SB();
  NOP(5); SB();
  { const auto r0 = __builtin_amdgcn_permlane16_swap(pack2(as[0][0], as[0][1]), pack2(as[1][0], as[1][1]), false, false);
    const auto r1 = __builtin_amdgcn_permlane16_swap(pack2(as[0][2], as[0][3]), pack2(as[1][2], as[1][3]), false, false);
    ST16(vS0, sS, 0, ((u32x4){r0[0], r1[0], r0[1], r1[1]})); } SB();
  { const auto r0 = __builtin_amdgcn_permlane16_swap(pack2(as[2][0], as[2][1]), pack2(as[3][0], as[3][1]), false, false);
    const auto r1 = __builtin_amdgcn_permlane16_swap(pack2(as[2][2], as[2][3]), pack2(as[3][2], as[3][3]), false, false);
    ST16(vS1, sS, 0, ((u32x4){r0[0], r1[0], r0[1], r1[1]})); } SB();
  STAMP(14);
  WAITV(0);
  ST16X(15, vL16, sFS, nonce);
  STAMP(15);
  }
  if (q == 0 && jb == 31 && w == 7) ST4X(0u, A.ws0, nonce);
  STAMP_DUMP();
  asm volatile(".Lcpf_e:");
  }}
  }
  }
}

extern "C" void kernel_launch(void* const* d_in, const int* in_sizes, int n_in,
                              void* d_out, int out_size, void* d_ws, size_t ws_size,
                              hipStream_t stream) {
  fused_kernel<<<256, 512, 0, stream>>>((const char*)d_in[0], (const char*)d_in[1], (const char*)d_in[2], (const char*)d_in[4],
                                        (const char*)d_in[5], (const char*)d_in[6], (const char*)d_in[7], (char*)d_ws, (char*)d_out);
}
```
